```python
import torch

def _softmax(x, axis=-1):
    return torch.softmax(x, dim=axis)

def _gelu(x):
    return torch.nn.functional.gelu(x, approximate='tanh')

def _silu(x):
    return torch.nn.functional.silu(x)

def _sigmoid(x):
    return torch.sigmoid(x)

def _layernorm(x, axis=-1, eps=1e-05):
    m = x.mean(dim=axis, keepdim=True)
    v = x.var(dim=axis, keepdim=True, unbiased=False)
    return (x - m) / torch.sqrt(v + eps)

def _scatter_add(tgt, idx, src, axis=0):
    return tgt.clone().index_add_(axis, idx, src)

def _segment_sum(src, seg, n):
    out = torch.zeros((n,) + src.shape[1:], dtype=src.dtype)
    return out.index_add_(0, seg, src)

def _pad(x, pw):
    flat = tuple((v for pair in reversed(pw) for v in pair))
    return torch.nn.functional.pad(x, flat)
kernel_name = 'concat_heads__sliding_local_dilated__memory_aug_recurrent_mem_tr__prompt'

def setup_inputs():

    _w = lambda a: (lambda t: t.to(torch.bfloat16) if t.is_floating_point() else t)(torch.as_tensor(a))

    def _A_setup():
        B, S, D = (1, 2048, 640)
        return {'x': _w((lambda *s: torch.randn(*s, dtype=torch.bfloat16) * 0.2)(B, S, D)), 'wqkv': _w((lambda *s: torch.randn(*s, dtype=torch.bfloat16) * 0.2)(D, 3 * D)), 'wo': _w((lambda *s: torch.randn(*s, dtype=torch.bfloat16) * 0.05)(D, D))}
    _A_in = _A_setup()

    def _B_setup():
        B, S, Dm = (1, 2048, 640)
        H, Dh = (32, 192)
        M, seg = (8, 32)
        seg = min(seg, S)
        S = S // seg * seg
        return {'x': _w((lambda *s: torch.randn(*s, dtype=torch.bfloat16) * 0.05)(B, S, Dm)), 'mem0': _w((lambda *s: torch.randn(*s, dtype=torch.bfloat16) * 1.6)(B, M, Dm)), 'Wq': _w((lambda *s: torch.randn(*s, dtype=torch.bfloat16) * 0.8)(Dm, H, Dh) * Dm ** (-0.5)), 'Wk': _w((lambda *s: torch.randn(*s, dtype=torch.bfloat16) * 0.8)(Dm, H, Dh) * Dm ** (-0.5)), 'Wv': _w((lambda *s: torch.randn(*s, dtype=torch.bfloat16) * 1.6)(Dm, H, Dh) * Dm ** (-0.5)), 'Wo': _w((lambda *s: torch.randn(*s, dtype=torch.bfloat16) * 1.6)(H, Dh, Dm) * (H * Dh) ** (-0.5)), 'seg_len': seg}
    _B_in = _B_setup()
    return {'A_x': _A_in['x'], 'A_wqkv': _A_in['wqkv'], 'A_wo': _A_in['wo'], 'B_x': _B_in['x'], 'B_mem0': _B_in['mem0'], 'B_Wq': _B_in['Wq'], 'B_Wk': _B_in['Wk'], 'B_Wv': _B_in['Wv'], 'B_Wo': _B_in['Wo'], 'B_seg_len': _B_in['seg_len']}

def reference(A_x, A_wqkv, A_wo, B_x, B_mem0, B_Wq, B_Wk, B_Wv, B_Wo, B_seg_len):

    def _A_ref(x, wqkv, wo):
        B, S, D = x.shape
        H = 32
        Dh = D // H
        W, dil = (8, 2)
        qkv = (lambda x, *d: x.permute(*d))((x @ wqkv).reshape(B, S, 3, H, Dh), 2, 0, 3, 1, 4)
        q, k, v = (qkv[0], qkv[1], qkv[2])
        scale = Dh ** (-0.5)
        pos = torch.arange(S)
        off = torch.arange(W) * dil
        idx = pos[:, None] - off[None, :]
        mask = idx >= 0
        idx_c = torch.where(mask, idx, 0).reshape(-1)
        kg = k[:, :, idx_c, :].reshape(B, H, S, W, Dh)
        vg = v[:, :, idx_c, :].reshape(B, H, S, W, Dh)
        logits = torch.einsum('bhsd,bhswd->bhsw', q, kg) * scale
        logits = torch.where(mask[None, None, :, :], logits, -1000000000.0)
        attn = _softmax(logits, axis=-1)
        o = torch.einsum('bhsw,bhswd->bhsd', attn, vg)
        return (lambda x, *d: x.permute(*d))(o, 0, 2, 1, 3).reshape(B, S, D) @ wo

    def _B_ref(x, mem0, Wq, Wk, Wv, Wo, seg_len):
        B, S, Dm = x.shape
        H, Dh = (Wq.shape[1], Wq.shape[2])
        M = mem0.shape[1]
        scale = Dh ** (-0.5)
        n_seg = S // seg_len
        L = M + seg_len
        idx = torch.arange(L)
        mask = (idx[:, None] < M) | (idx[:, None] >= idx[None, :])
        mem = mem0
        outs = []
        for s in range(n_seg):
            xs = x[:, s * seg_len:(s + 1) * seg_len, :].clone()
            h = torch.concatenate([mem, xs], axis=1)
            q = torch.einsum('btd,dhe->bhte', h, Wq)
            k = torch.einsum('btd,dhe->bhte', h, Wk)
            v = torch.einsum('btd,dhe->bhte', h, Wv)
            scores = torch.einsum('bhqe,bhke->bhqk', q, k) * scale
            scores = torch.where(mask[None, None], scores, -1000000000.0)
            p = _softmax(scores, axis=-1)
            o = torch.einsum('bhqk,bhke->bhqe', p, v)
            o = torch.einsum('bhte,hed->btd', o, Wo)
            mem = o[:, :M, :].clone()
            outs.append(o[:, M:, :])
        return torch.concatenate(outs, axis=1)
    a = _A_ref(A_x, A_wqkv, A_wo)
    b = _B_ref(B_x, B_mem0, B_Wq, B_Wk, B_Wv, B_Wo, B_seg_len)
    return torch.concatenate([a, b], axis=-1)
```

```cpp
#include <hip/hip_runtime.h>
#include <hip/hip_bf16.h>
#include <stdint.h>

typedef unsigned short u16;
typedef unsigned long long u64;
typedef __attribute__((ext_vector_type(4))) float f32x4;
typedef __attribute__((address_space(1))) u64 gu64;
typedef __attribute__((address_space(1))) unsigned gu32;
typedef unsigned u32x8 __attribute__((ext_vector_type(8)));
typedef unsigned u32x4 __attribute__((ext_vector_type(4)));
typedef unsigned u32x2 __attribute__((ext_vector_type(2)));
typedef __attribute__((ext_vector_type(4))) short bf16x4;
typedef __attribute__((ext_vector_type(8))) __bf16 bfv8;
typedef __attribute__((ext_vector_type(16))) __bf16 bfv16;
typedef __bf16 bf16x2_t __attribute__((ext_vector_type(2)));
typedef float f32x2_t __attribute__((ext_vector_type(2)));
typedef unsigned v2u_t __attribute__((ext_vector_type(2)));
#define RLX_AGENT __ATOMIC_RELAXED, __HIP_MEMORY_SCOPE_AGENT
#define LAS __attribute__((address_space(3)))

__device__ __forceinline__ float bf2f(u16 v) { return __uint_as_float(((unsigned)v) << 16); }
__device__ __forceinline__ u16 f2bf(float f) { return __builtin_bit_cast(u16, (__bf16)f); }
__device__ __forceinline__ float rbf(float f) { return bf2f(f2bf(f)); }
__device__ __forceinline__ unsigned cvtpk(float lo, float hi) { f32x2_t v = {lo, hi}; return __builtin_bit_cast(unsigned, __builtin_convertvector(v, bf16x2_t)); }
__device__ __forceinline__ __amdgpu_buffer_rsrc_t mkrs(const void* p) { return __builtin_amdgcn_make_buffer_rsrc((void*)p, 0, 0x7fffffff, 0x00020000); }
__device__ __forceinline__ u32x4 ld16(__amdgpu_buffer_rsrc_t rs, unsigned voff, unsigned soff) { return __builtin_amdgcn_raw_buffer_load_b128(rs, (int)voff, (int)soff, 16); }
#define OLA 0
__device__ __forceinline__ u32x4 ld16c(__amdgpu_buffer_rsrc_t rs, unsigned voff, unsigned soff) { return __builtin_amdgcn_raw_buffer_load_b128(rs, (int)voff, (int)soff, OLA); }
__device__ __forceinline__ u32x2 ld8(__amdgpu_buffer_rsrc_t rs, unsigned voff, unsigned soff) { return __builtin_amdgcn_raw_buffer_load_b64(rs, (int)voff, (int)soff, 16); }
__device__ __forceinline__ void st16(__amdgpu_buffer_rsrc_t rs, unsigned voff, unsigned soff, u32x4 w) { __builtin_amdgcn_raw_buffer_store_b128(w, rs, (int)voff, (int)soff, 16); asm volatile("s_nop 1" :: "v"(w) : "memory"); }
__device__ __forceinline__ void st8(__amdgpu_buffer_rsrc_t rs, unsigned voff, unsigned soff, unsigned lo, unsigned hi) { u32x2 w = {lo, hi}; __builtin_amdgcn_raw_buffer_store_b64(w, rs, (int)voff, (int)soff, 16); }
__device__ __forceinline__ bool full16(u32x4 v) { return ((v.x & v.y) != ~0u) & ((v.z & v.w) != ~0u); }
__device__ __forceinline__ float max16(float x) { v2u_t r = __builtin_amdgcn_permlane16_swap(__float_as_uint(x), __float_as_uint(x), false, false); return fmaxf(__uint_as_float(r[0]), __uint_as_float(r[1])); }
__device__ __forceinline__ float max32(float x) { v2u_t r = __builtin_amdgcn_permlane32_swap(__float_as_uint(x), __float_as_uint(x), false, false); return fmaxf(__uint_as_float(r[0]), __uint_as_float(r[1])); }
__device__ __forceinline__ float sum16(float x) { v2u_t r = __builtin_amdgcn_permlane16_swap(__float_as_uint(x), __float_as_uint(x), false, false); return __uint_as_float(r[0]) + __uint_as_float(r[1]); }
__device__ __forceinline__ float sum32(float x) { v2u_t r = __builtin_amdgcn_permlane32_swap(__float_as_uint(x), __float_as_uint(x), false, false); return __uint_as_float(r[0]) + __uint_as_float(r[1]); }
__device__ __forceinline__ float tred32(float a, float b) { v2u_t r = __builtin_amdgcn_permlane32_swap(__float_as_uint(a), __float_as_uint(b), false, false); return __uint_as_float(r[0]) + __uint_as_float(r[1]); }
__device__ __forceinline__ float tred16(float a, float b) { v2u_t r = __builtin_amdgcn_permlane16_swap(__float_as_uint(a), __float_as_uint(b), false, false); return __uint_as_float(r[0]) + __uint_as_float(r[1]); }
__device__ __forceinline__ unsigned ror8(unsigned x) { return (unsigned)__builtin_amdgcn_mov_dpp((int)x, 0x128, 0xf, 0xf, true); }
template <int N> __device__ __forceinline__ void nap() { if constexpr (N > 0) { if constexpr (N > 127) { __builtin_amdgcn_s_sleep(127); nap<N - 127>(); } else __builtin_amdgcn_s_sleep(N); } }
__device__ __forceinline__ unsigned half_of(const char* base, unsigned off, unsigned side, unsigned pol) {
  return off + ((((unsigned)(((size_t)base + off) >> 21) ^ side ^ pol) & 1u) << 13);
}
__device__ __forceinline__ unsigned side_map(const void* base, unsigned L, unsigned side, unsigned pol) { return half_of((const char*)base, (L >> 13) << 14, side, pol) + (L & 8191u); }
constexpr unsigned CH_SPIN_CAP = 1u << 18;
__device__ __forceinline__ bool spin_check(unsigned& spins, unsigned* tmop) {
  asm volatile("" ::: "memory");
  ++spins;
  if ((spins & 63u) == 0u) {
    gu32* tmo = (gu32*)tmop;
    if (__builtin_amdgcn_readfirstlane(__hip_atomic_load(tmo, RLX_AGENT))) return false;
    if (spins > CH_SPIN_CAP) { __hip_atomic_store(tmo, 1u, RLX_AGENT); return false; }
  }
  return true;
}
__device__ __forceinline__ unsigned xcc_side() { return ((unsigned)__builtin_amdgcn_s_getreg((3 << 11) | 20) >> 2) & 1u; }
__device__ __forceinline__ f32x4 mfma(u32x4 a, u32x4 b, f32x4 c) { return __builtin_amdgcn_mfma_f32_16x16x32_bf16(__builtin_bit_cast(bfv8, a), __builtin_bit_cast(bfv8, b), c, 0, 0, 0); }
__device__ __forceinline__ f32x4 smf(u32x4 a, u32x8 b, f32x4 c, int ix) { return __builtin_amdgcn_smfmac_f32_16x16x64_bf16(__builtin_bit_cast(bfv8, a), __builtin_bit_cast(bfv16, b), c, ix, 0, 0); }
__device__ __forceinline__ u32x4 lo4(u32x8 v) { return __builtin_shufflevector(v, v, 0, 1, 2, 3); }
__device__ __forceinline__ u32x4 hi4(u32x8 v) { return __builtin_shufflevector(v, v, 4, 5, 6, 7); }
__device__ __forceinline__ u32x8 cat8(u32x4 a, u32x4 b) { return __builtin_shufflevector(a, b, 0, 1, 2, 3, 4, 5, 6, 7); }
#define BAR() do { asm volatile("s_waitcnt lgkmcnt(0)" ::: "memory"); __builtin_amdgcn_s_barrier(); asm volatile("" ::: "memory"); } while (0)
#define SB() __builtin_amdgcn_sched_barrier(0)
#define VMW(n) asm volatile("s_waitcnt vmcnt(" #n ")" ::: "memory")
#define ZERO4 ((f32x4){0.f, 0.f, 0.f, 0.f})
__device__ __forceinline__ u32x2 trrd(const unsigned char* lds, unsigned off) { return __builtin_bit_cast(u32x2, __builtin_amdgcn_ds_read_tr16_b64_v4i16((LAS bf16x4*)(lds + off))); }
__device__ __forceinline__ u32x4 trrd2(const unsigned char* lds, unsigned off, unsigned d) { const u32x2 lo = trrd(lds, off), hi = trrd(lds, off + d); return (u32x4){lo.x, lo.y, hi.x, hi.y}; }


#include <stdlib.h>
#define SAG 16
#define DDV 80,80,8,0,30,0,127,99,105,115,25,30,20,12,1,0
#define XLN 4
#define XLS 2
struct Args {
  const u16* mem0; const u16* Wq; const u16* Wk; const u16* Wv; const u16* Wo; const u16* Bx; const u16* Ax; const u16* Awqkv; const u16* Awo;
  u16* out; char* ws;
  int d[16];
};
constexpr size_t MB = 1u << 20;
#define WOFF 0
#define NK 64
#define NU 16
#define NZ 16
#define NP 16
constexpr unsigned X1_STEP = 32768, Z_STEP = 1u << 20, K_STEP = 1u << 19;
constexpr size_t O_PROF = 0, O_CNT = (WOFF + 1) * MB, O_CAL = O_CNT + 65536, O_TS = O_CAL + 65536, O_X1 = (WOFF + 2) * MB, O_PS = O_X1 + 2 * MB, O_KF = O_PS + 4 * MB, O_QF = O_KF + NK * (size_t)K_STEP, O_VF = O_QF + NK * (size_t)K_STEP,
                 O_KM = O_VF + NK * (size_t)K_STEP, O_PT = O_KM + 6 * MB, O_OA = O_PT + 5 * MB, O_ZB = O_OA + 3 * MB, O_UO = O_ZB + NZ * MB, O_END = O_UO + NU * 40 * (size_t)32768;
static_assert(O_PS % (2 * MB) == 0 && O_ZB % (2 * MB) == 0 && NP <= 32, "");
#define KSLOT(s_) (((unsigned)(s_) & (NK - 1u)) * K_STEP)
#define ZSLOT(s_) (((unsigned)(s_) & (NZ - 1u)) * Z_STEP)
#define XSLOT(s_) (((unsigned)(s_) & (NP - 1u)) * X1_STEP)
#define CNT(i) (32 * (i))
__device__ __forceinline__ unsigned u_off(const unsigned s, const unsigned c) { return ((s & (NU - 1u)) * 40u + c) << 15; }
constexpr int PRE = 4;
#define WGATE 11
static_assert(NK == 64 && WGATE <= NU && WGATE + PRE + 1 <= NZ && NP >= PRE + 4, "");
constexpr unsigned SENT = 0xFF81FF81u;
#define SENT4 ((u32x4){SENT, SENT, SENT, SENT})
#define PSLOT(s_) (((s_) & (NP - 1)) * 65536)
constexpr int C_SLOT = 0, C_TMO = 8, C_POL = 9, C_FILL = 10, C_PO = 12, C_PT_ = 32, C_PV = 64, C_PS = 96, C_PW = 128, C_PZ = 160, C_TOK = 192, C_AO = 464, C_TOTAL = 480;
__device__ __forceinline__ float nopk(float x) { asm("" : "+v"(x)); return x; }
__device__ __forceinline__ float fmx(float a, float b) { float big = __builtin_inff(); asm("" : "+s"(big)); return __builtin_amdgcn_fmed3f(a, b, big); }
__device__ __forceinline__ float mx16(float x) { v2u_t r = __builtin_amdgcn_permlane16_swap(__float_as_uint(x), __float_as_uint(x), false, false); return fmx(__uint_as_float(r[0]), __uint_as_float(r[1])); }
__device__ __forceinline__ float mx32(float x) { v2u_t r = __builtin_amdgcn_permlane32_swap(__float_as_uint(x), __float_as_uint(x), false, false); return fmx(__uint_as_float(r[0]), __uint_as_float(r[1])); }
__device__ __forceinline__ int lane_id() { int l = (int)__builtin_amdgcn_mbcnt_hi(~0u, __builtin_amdgcn_mbcnt_lo(~0u, 0u)); asm volatile("" : "+v"(l)); return l; }
__device__ __forceinline__ unsigned sent_min(unsigned m, unsigned x, unsigned y) { unsigned r = min(min(m, x ^ SENT), y ^ SENT); asm("" : "+v"(r)); return r; }
__device__ __forceinline__ unsigned sent_min(unsigned m, u32x4 v) { return sent_min(sent_min(m, v.x, v.y), v.z, v.w); }
#define RT_MARK(t_) asm volatile("s_memrealtime %0\n s_waitcnt lgkmcnt(0)" : "=s"(t_) :: "memory")
__device__ __forceinline__ unsigned wait_rt(unsigned target) {
  unsigned t;
  target = (unsigned)__builtin_amdgcn_readfirstlane(target);
  asm volatile("s_branch .Lwb%=\n.Lwa%=:\n s_sleep 1\n.Lwb%=:\n s_memrealtime vcc\n s_waitcnt lgkmcnt(0)\n s_sub_i32 %0, %1, vcc_lo\n s_add_i32 vcc_hi, %0, -9\n s_cmp_lt_u32 vcc_hi, 392\n s_cbranch_scc1 .Lwa%=\n"
               " s_add_i32 %0, %0, -1\n s_cmp_lt_u32 %0, 8\n s_cbranch_scc1 .Lwb%=\n s_mov_b32 %0, vcc_lo" : "=&s"(t) : "s"(target) : "vcc", "scc", "memory");
  return t;
}
__device__ __forceinline__ unsigned ts_max(unsigned v) {
  if ((unsigned)__ballot(v == ~0u)) return ~0u;
  const unsigned base = (unsigned)__builtin_amdgcn_readfirstlane(v) - 4096u;
  unsigned d = v == ~0u ? 0u : v - base;
  d = max(d, (unsigned)__builtin_amdgcn_mov_dpp((int)d, 0x121, 0xf, 0xf, true)); d = max(d, (unsigned)__builtin_amdgcn_mov_dpp((int)d, 0x122, 0xf, 0xf, true));
  d = max(d, (unsigned)__builtin_amdgcn_mov_dpp((int)d, 0x124, 0xf, 0xf, true)); d = max(d, (unsigned)__builtin_amdgcn_mov_dpp((int)d, 0x128, 0xf, 0xf, true));
  return (base + max(max((unsigned)__builtin_amdgcn_readlane(d, 0), (unsigned)__builtin_amdgcn_readlane(d, 16)), max((unsigned)__builtin_amdgcn_readlane(d, 32), (unsigned)__builtin_amdgcn_readlane(d, 48)))) & ~1u;
}
#define TS_OFF(l_) (((l_) & 31) * 256 + ((l_) >> 5) * 16384)
#define TS_STEP(s_) (((s_) & 1) * 128 + ((s_) >> 1) * 4)
__device__ __forceinline__ unsigned ts_settle(const __amdgpu_buffer_rsrc_t rs_ts, const int tss, unsigned t, const int lead) {
  for (int n = 0; n < 48; ++n) {
    unsigned now;
    if (t == ~0u) { __builtin_amdgcn_s_sleep(4); u64 t_; RT_MARK(t_); now = (unsigned)t_; } else now = wait_rt(t - (unsigned)lead);
    t = ts_max(__builtin_amdgcn_raw_buffer_load_b32(rs_ts, TS_OFF(lane_id()), tss, 16));
    if (t != ~0u && (int)(t - now) <= lead + 4) break;
  }
  return t;
}
#define ADAPT(d_, failed_) do { if (s > 3) d_ += (failed_) ? a.d[5] : -(int)((s & a.d[6]) == 0); } while (0)
__device__ __forceinline__ void nap_rt(int n) { for (int i = 0; i < n; ++i) __builtin_amdgcn_s_sleep(1); }
#define PINV(x) asm volatile("" : "+v"(x))
#define PINS(x) asm volatile("" : "+s"(x))
#define PINU(x) asm volatile("" :: "v"(x))
#define IL(nm, nv) do { _Pragma("unroll") for (int il_ = 0; il_ < (nm); ++il_) { __builtin_amdgcn_sched_group_barrier(0x008, 1, 0); __builtin_amdgcn_sched_group_barrier(0x7f6, nv, 0); } } while (0)
constexpr int L_MEM = 0, L_QI = 10240;
constexpr int L_B0 = 16384, L_B1 = 65536, L_B2 = 114688;
constexpr int L_QA = L_B1, L_XV = L_B1 + 9216, L_ST = L_B1 + 10240, L_FLG = L_B1 + 10752, L_XR = L_B1 + 12288;

#define TR8_ASM "ds_read_b64_tr_b16 %0, %8\n ds_read_b64_tr_b16 %1, %8 offset:1536\n ds_read_b64_tr_b16 %2, %8 offset:12288\n ds_read_b64_tr_b16 %3, %8 offset:13824\n" \
               "ds_read_b64_tr_b16 %4, %8 offset:24576\n ds_read_b64_tr_b16 %5, %8 offset:26112\n ds_read_b64_tr_b16 %6, %8 offset:36864\n ds_read_b64_tr_b16 %7, %8 offset:38400\n s_waitcnt lgkmcnt(0)"
__device__ __forceinline__ void tr8(const bool agpr, const unsigned ad, u32x8& f0, u32x8& f1) {
  u32x2 a, b, c, d, e, f, g, h;
  if (agpr) asm volatile(TR8_ASM : "=&a"(a), "=&a"(b), "=&a"(c), "=&a"(d), "=&a"(e), "=&a"(f), "=&a"(g), "=&a"(h) : "v"(ad) : "memory");
  else asm volatile(TR8_ASM : "=&v"(a), "=&v"(b), "=&v"(c), "=&v"(d), "=&v"(e), "=&v"(f), "=&v"(g), "=&v"(h) : "v"(ad) : "memory");
  f0 = (u32x8){a.x, a.y, b.x, b.y, c.x, c.y, d.x, d.y}; f1 = (u32x8){e.x, e.y, f.x, f.y, g.x, g.y, h.x, h.y};
}
__device__ __forceinline__ void tr8d(const bool agpr, const unsigned ad, u32x4& f0, u32x4& f1, u32x4& f2, u32x4& f3) {
  u32x2 a, b, c, d, e, f, g, h;
  if (agpr) asm volatile(TR8_ASM : "=&a"(a), "=&a"(b), "=&a"(c), "=&a"(d), "=&a"(e), "=&a"(f), "=&a"(g), "=&a"(h) : "v"(ad) : "memory");
  else asm volatile(TR8_ASM : "=&v"(a), "=&v"(b), "=&v"(c), "=&v"(d), "=&v"(e), "=&v"(f), "=&v"(g), "=&v"(h) : "v"(ad) : "memory");
  f0 = (u32x4){a.x, a.y, b.x, b.y}; f1 = (u32x4){c.x, c.y, d.x, d.y}; f2 = (u32x4){e.x, e.y, f.x, f.y}; f3 = (u32x4){g.x, g.y, h.x, h.y};
}
#define TR4_ASM "ds_read_b64_tr_b16 %0, %4 offset:%5\n ds_read_b64_tr_b16 %1, %4 offset:%6\n ds_read_b64_tr_b16 %2, %4 offset:%7\n ds_read_b64_tr_b16 %3, %4 offset:%8\n s_waitcnt lgkmcnt(0)"
template <int J> __device__ __forceinline__ void tr4(const bool agpr, const unsigned ad, u32x8& f) {
  u32x2 a, b, c, d;
  if (agpr) asm volatile(TR4_ASM : "=&a"(a), "=&a"(b), "=&a"(c), "=&a"(d) : "v"(ad), "n"(16384 * J), "n"(16384 * J + 4096), "n"(16384 * J + 8192), "n"(16384 * J + 12288) : "memory");
  else asm volatile(TR4_ASM : "=&v"(a), "=&v"(b), "=&v"(c), "=&v"(d) : "v"(ad), "n"(16384 * J), "n"(16384 * J + 4096), "n"(16384 * J + 8192), "n"(16384 * J + 12288) : "memory");
  f = (u32x8){a.x, a.y, b.x, b.y, c.x, c.y, d.x, d.y};
}
template <int J> __device__ __forceinline__ void tr4d(const bool agpr, const unsigned ad, u32x4& f0, u32x4& f1) {
  u32x2 a, b, c, d;
  if (agpr) asm volatile(TR4_ASM : "=&a"(a), "=&a"(b), "=&a"(c), "=&a"(d) : "v"(ad), "n"(16384 * J), "n"(16384 * J + 4096), "n"(16384 * J + 8192), "n"(16384 * J + 12288) : "memory");
  else asm volatile(TR4_ASM : "=&v"(a), "=&v"(b), "=&v"(c), "=&v"(d) : "v"(ad), "n"(16384 * J), "n"(16384 * J + 4096), "n"(16384 * J + 8192), "n"(16384 * J + 12288) : "memory");
  f0 = (u32x4){a.x, a.y, b.x, b.y}; f1 = (u32x4){c.x, c.y, d.x, d.y};
}
__device__ __forceinline__ void glds(const void* sbase, unsigned voff, unsigned ldsdst) {
  unsigned keep;
  asm volatile("s_mov_b32 %0, m0\n s_mov_b32 m0, %3\n s_nop 0\n global_load_lds_dwordx4 %1, %2\n s_mov_b32 m0, %0" : "=&s"(keep) : "v"(voff), "s"(sbase), "s"(ldsdst) : "memory");
}
__device__ __forceinline__ void m0_set(unsigned ldsdst) { asm volatile("s_mov_b32 m0, %0\n s_nop 0" :: "s"(ldsdst) : "memory"); }
template <int OFF> __device__ __forceinline__ void glds_o(const void* sbase, unsigned voff) { asm volatile("global_load_lds_dwordx4 %0, %1 offset:%2" :: "v"(voff), "s"(sbase), "n"(OFF) : "memory"); }
template <int OFF> __device__ __forceinline__ void glds_x(const void* sbase, unsigned voff) { asm volatile("global_load_lds_dwordx4 %0, %1 offset:%2 sc1" :: "v"(voff), "s"(sbase), "n"(OFF) : "memory"); }
__device__ __forceinline__ unsigned lds_addr(const unsigned char* p) { return (unsigned)(size_t)(const LAS unsigned char*)p; }
__device__ __forceinline__ unsigned ld_word(const unsigned* p) { return (unsigned)__builtin_amdgcn_readfirstlane(__hip_atomic_load((const gu32*)p, RLX_AGENT)); }
template <int SLEEP>
__device__ __forceinline__ unsigned wait_ge(const unsigned* p, unsigned target, unsigned* tmo) {
  unsigned spins = 0, v;
  while ((v = ld_word(p)) < target) { __builtin_amdgcn_s_sleep(SLEEP); if (!spin_check(spins, tmo)) break; }
  asm volatile("" ::: "memory");
  return v;
}
__device__ __forceinline__ unsigned min_t(const u32x4 w, const int kv) {
  unsigned v = min(min(w.x, w.y), w.z); if (!kv) v = min(v, w.w);
  return (unsigned)__builtin_amdgcn_readfirstlane(v);
}
template <int SLEEP>
__device__ __forceinline__ unsigned wait_t(const unsigned* p, const int first, unsigned target, unsigned* tmo) {
  unsigned spins = 0, v;
  for (;;) {
    v = min_t(ld16(mkrs(p), 0u, 0u), first);
    if (v >= target) break;
    __builtin_amdgcn_s_sleep(SLEEP); if (!spin_check(spins, tmo)) break;
  }
  asm volatile("" ::: "memory");
  return v;
}
template <int SLEEP>
__device__ __forceinline__ void wait_heads(const unsigned* p, const int second, unsigned target, unsigned* tmo) {
  unsigned spins = 0; const int ln = lane_id();
  while (!__all(__builtin_amdgcn_raw_buffer_load_b32(mkrs(p), (ln & 31) * 128 + (ln >> 5) * second * 4, 0, 16) >= target)) { __builtin_amdgcn_s_sleep(SLEEP); if (!spin_check(spins, tmo)) break; }
  asm volatile("" ::: "memory");
}
#define WNAP 12
template <int N> __device__ __forceinline__ void nap4() { if constexpr (N > 16) { asm volatile("s_nop 15"); nap4<N - 16>(); } else if constexpr (N > 0) asm volatile("s_nop %0" :: "n"(N - 1)); }
#define DPPM(d_, s_, ctl_) asm volatile("v_max_f32_dpp %0, %1, %1 " ctl_ " row_mask:0xf bank_mask:0xf" : "=v"(d_) : "v"(s_))
#define XOR1 "quad_perm:[1,0,3,2]"
#define XOR2 "quad_perm:[2,3,0,1]"
__device__ __forceinline__ void light(const __amdgpu_buffer_rsrc_t rs, unsigned vo, const unsigned so, unsigned* tmo, const int slow = 0) {
  unsigned spins = 0;
  for (;;) {
    const bool empty = __builtin_amdgcn_raw_buffer_load_b32(rs, (int)vo, (int)so, 16) == SENT;
    if (!__any(empty) || !spin_check(spins, tmo)) break;
    vo = empty ? vo : 0x80000000u;
    if (slow) __builtin_amdgcn_s_sleep(16); else __builtin_amdgcn_s_sleep(XLS);
  }
}
__device__ __forceinline__ void x1_light(const __amdgpu_buffer_rsrc_t rs_x1, const unsigned so, unsigned* tmo) {
  const unsigned ln = (unsigned)lane_id();
  light(rs_x1, ln < 40u ? (ln >> 5) * 16384u + (ln & 31u) * 256u + 252u : 0x80000000u, so, tmo);
}
#define DS(x) do { x; SB(); } while (0)
template <int V> struct IC { static constexpr int v = V; };
template <int I, int N, class F> __device__ __forceinline__ void sfor(F&& f) { if constexpr (I < N) { f(IC<I>{}); sfor<I + 1, N>(f); } }
#define SFOR(i_, n_) sfor<0, n_>([&](auto i_##_c) __attribute__((always_inline)) { constexpr int i_ = decltype(i_##_c)::v;
#define SEND })
typedef unsigned u32x32 __attribute__((ext_vector_type(32)));
typedef unsigned u32x16 __attribute__((ext_vector_type(16)));
struct WReg { u32x32 b[11]; u32x16 e; };
#define WR_SW(f_, X) switch ((f_) >> 2) { case 0: X("a", "{a[0:31]}", W.b[0]); break; case 1: X("a", "{a[32:63]}", W.b[1]); break; case 2: X("a", "{a[64:95]}", W.b[2]); break; case 3: X("a", "{a[96:127]}", W.b[3]); break; \
    case 4: X("a", "{a[128:159]}", W.b[4]); break; case 5: X("a", "{a[160:191]}", W.b[5]); break; case 6: X("a", "{a[192:223]}", W.b[6]); break; case 7: X("a", "{a[224:255]}", W.b[7]); break; \
    case 8: X("v", "{v[142:173]}", W.b[8]); break; case 9: X("v", "{v[174:205]}", W.b[9]); break; case 10: X("v", "{v[206:237]}", W.b[10]); break; default: X("v", "{v[238:253]}", W.e); break; }
__device__ __forceinline__ void smf_w(f32x4& c, const u32x4 a, const int f, const int ix, const WReg& W) {
  const int r = f < 32 ? 8 * f : 8 * f - 114;
#define X_(rf_, cs_, v_) asm volatile("v_smfmac_f32_16x16x64_bf16 %0, %1, " rf_ "[%c3:%c4], %2" : "+v"(c) : "v"(a), "v"(ix), "n"(r), "n"(r + 7), cs_(v_))
  WR_SW(f, X_);
#undef X_
}
template <bool WA> __device__ __forceinline__ void mf_w(f32x4& c, const u32x4 x, const int d, const bool first, const WReg& W) {
  const int r = d < 64 ? 4 * d : 4 * d - 114;
#define X_(rf_, cs_, v_) if (WA) { if (first) asm volatile("v_mfma_f32_16x16x32_bf16 %0, " rf_ "[%c2:%c3], %1, 0" : "=&v"(c) : "v"(x), "n"(r), "n"(r + 3), cs_(v_)); \
      else asm volatile("v_mfma_f32_16x16x32_bf16 %0, " rf_ "[%c2:%c3], %1, %0" : "+v"(c) : "v"(x), "n"(r), "n"(r + 3), cs_(v_)); } \
    else { if (first) asm volatile("v_mfma_f32_16x16x32_bf16 %0, %1, " rf_ "[%c2:%c3], 0" : "=&v"(c) : "v"(x), "n"(r), "n"(r + 3), cs_(v_)); \
      else asm volatile("v_mfma_f32_16x16x32_bf16 %0, %1, " rf_ "[%c2:%c3], %0" : "+v"(c) : "v"(x), "n"(r), "n"(r + 3), cs_(v_)); }
  WR_SW(d >> 1, X_);
#undef X_
}
__device__ __forceinline__ void ld_w(const u32x4 rs, const unsigned vo, const unsigned so, const int off, const int f, const bool def, WReg& W) {
  const int r = f < 32 ? 8 * f : 8 * f - 114;
#define Y_(md_, rf_, cs_, v_) asm volatile("s_nop 4\n buffer_load_dwordx4 " rf_ "[%c6:%c7], %1, %2, %3 offen offset:%4 sc1\n buffer_load_dwordx4 " rf_ "[%c8:%c9], %1, %2, %3 offen offset:%5 sc1" \
    : md_ cs_(v_) : "v"(vo), "s"(rs), "s"(so), "n"(off), "n"(off + 1024), "n"(r), "n"(r + 3), "n"(r + 4), "n"(r + 7) : "memory")
#define X_(rf_, cs_, v_) if (def) Y_("=&", rf_, cs_, v_); else Y_("+", rf_, cs_, v_)
  WR_SW(f, X_);
#undef X_
#undef Y_
}
__device__ __forceinline__ u32x4 mkdesc(const void* p) { const size_t b = (size_t)p; return (u32x4){(unsigned)b, (unsigned)(b >> 32) & 0xffffu, 0x7fffffffu, 0x00020000u}; }
__device__ __forceinline__ void tr4_w(const unsigned ad, const int o0, const int o1, const int o2, const int o3, const int f, WReg& W) {
  const int r = f < 32 ? 8 * f : 8 * f - 114;
#define Y_(md_, rf_, cs_, v_) asm volatile("ds_read_b64_tr_b16 " rf_ "[%c6:%c7], %1 offset:%2\n ds_read_b64_tr_b16 " rf_ "[%c8:%c9], %1 offset:%3\n ds_read_b64_tr_b16 " rf_ "[%c10:%c11], %1 offset:%4\n ds_read_b64_tr_b16 " rf_ "[%c12:%c13], %1 offset:%5\n s_waitcnt lgkmcnt(0)" \
    : md_ cs_(v_) : "v"(ad), "n"(o0), "n"(o1), "n"(o2), "n"(o3), "n"(r), "n"(r + 1), "n"(r + 2), "n"(r + 3), "n"(r + 4), "n"(r + 5), "n"(r + 6), "n"(r + 7) : "memory")
#define X_(rf_, cs_, v_) if ((f & 3) == 0) Y_("=&", rf_, cs_, v_); else Y_("+", rf_, cs_, v_)
  WR_SW(f, X_);
#undef X_
#undef Y_
}
#define TRS0(ad_, f_) tr4_w(ad_, 0, 1536, 12288, 13824, f_, W)
#define TRS1(ad_, f_) tr4_w(ad_, 24576, 26112, 36864, 38400, f_, W)
#define TRO4(ad_, j_, f_) tr4_w(ad_, 16384 * (j_), 16384 * (j_) + 4096, 16384 * (j_) + 8192, 16384 * (j_) + 12288, f_, W)
#define SMFV(c_, a_, b_) do { asm volatile("v_smfmac_f32_16x16x64_bf16 %0, %1, %2, %3" : "+v"(c_) : "v"(a_), "v"(b_), "v"(ix)); SB(); } while (0)
#define SMFW(c_, a_, f_) do { smf_w(c_, a_, f_, ix, W); SB(); } while (0)
#define ADD1(d_, x_, y_) do { asm volatile("v_add_f32 %0, %1, %2" : "=v"(d_) : "v"(x_), "v"(y_)); SB(); } while (0)
#define ADD2(d0_, d1_, c_) do { asm volatile("v_add_f32 %0, %2, %3\n v_add_f32 %1, %4, %5" : "=&v"(d0_), "=v"(d1_) : "v"(c_[0]), "v"(c_[2]), "v"(c_[1]), "v"(c_[3])); SB(); } while (0)
#define CVT1(d_, x_, y_) do { asm volatile("v_cvt_pk_bf16_f32 %0, %1, %2" : "=v"(d_) : "v"(x_), "v"(y_)); SB(); } while (0)
#define EPI3(d_, c_) do { float e_; asm volatile("v_add_f32 %1, %2, %3\n v_add_f32 %0, %4, %5\n v_cvt_pk_bf16_f32 %0, %1, %0" : "=&v"(d_), "=&v"(e_) : "v"(c_[0]), "v"(c_[2]), "v"(c_[1]), "v"(c_[3])); SB(); } while (0)
#define EPI4(d_, m_, c_) do { float e_, f_; asm volatile("v_add_f32 %1, %4, %5\n v_add_f32 %2, %6, %7\n v_cvt_pk_bf16_f32 %0, %1, %2\n v_cmp_u_f32_e64 %3, %1, %2" : "=&v"(d_), "=&v"(e_), "=&v"(f_), "=s"(m_) : "v"(c_[0]), "v"(c_[2]), "v"(c_[1]), "v"(c_[3])); SB(); } while (0)
#define CVTN(d_, m_, x_, y_) do { NANM(m_, x_, y_); CVT1(d_, x_, y_); } while (0)
#define ADDCVT(d_, x_, c_) do { asm volatile("v_add_f32 %0, %2, %3\n v_cvt_pk_bf16_f32 %0, %1, %0" : "=&v"(d_) : "v"(x_), "v"(c_[1]), "v"(c_[3])); SB(); } while (0)
#define LGW(n_) do { __builtin_amdgcn_s_waitcnt(0xC07F | ((n_) << 8)); SB(); } while (0)
#define LBAR(n_) do { asm volatile("s_waitcnt lgkmcnt(" #n_ ")" ::: "memory"); __builtin_amdgcn_s_barrier(); asm volatile("" ::: "memory"); SB(); } while (0)
#define FILL() do { __builtin_amdgcn_s_setprio(0); SB(); } while (0)
#define NANM(m_, x_, y_) do { asm volatile("v_cmp_u_f32_e64 %0, %1, %2" : "=s"(m_) : "v"(x_), "v"(y_)); SB(); } while (0)
#define LW32(ad_, off_, v_) (*(unsigned*)(lds + (ad_) + (off_)) = (v_))
#define MEMB() asm volatile("" ::: "memory")
#define LG0() __builtin_amdgcn_s_waitcnt(0xC07F)
#define FLAG(i_) do { MEMB(); *(volatile LAS unsigned char*)(LAS unsigned char*)(lds + fla + 4 * (i_)) = (unsigned char)snv; SB(); } while (0)
#define FLRD(i_) (*(const volatile LAS unsigned*)(LAS unsigned char*)(lds + flr + 4 * (i_)))
#define SBUF(i_) (((i_) % 3) == 0 ? L_B0 : (((i_) % 3) == 1 ? L_B1 : L_B2))
#define SDMA_INIT() unsigned svo[12]; const int sq0 = 3 * ((h >> 2) & 3); \
    _Pragma("unroll") for (int q_ = 0; q_ < 12; ++q_) { const int qr_ = q_ + sq0, ins = wave + 4 * (qr_ < 12 ? qr_ : qr_ - 12); const unsigned c = (unsigned)(ins * 64 + lane), r = c / 24u, pc = c - r * 24u, sw = ((r >> 1) & 1u) * 2u + ((r >> 3) & 1u) * 4u; \
      svo[q_] = r * 12288u + 16u * (pc ^ sw); PINV(svo[q_]); }
#define KROT(kr_) ((kr_) < 4 ? (((kr_) + h) & 3) : 4)
#define SDMA(W_m, kr_, i_) do { const char* W_ = (const char*)((W_m) + (size_t)(128 * KROT(kr_)) * 6144 + 192 * h); \
    _Pragma("unroll") for (int q_ = 0; q_ < 12; ++q_) { const int qr_ = q_ + sq0; glds(W_, svo[q_], lds_addr(lds) + (unsigned)(SBUF(i_) + (wave + 4 * (qr_ < 12 ? qr_ : qr_ - 12)) * 1024)); } SB(); } while (0)
#define SDMAQK(i_) SDMA(((i_) & 1) ? a.Wk : a.Wq, (i_) >> 1, i_)
#define TRO(tile_) (trow * 384u + (((unsigned)(2 * (tile_) + ((l15 & 3) >> 1))) ^ tswz) * 16u + (unsigned)(l15 & 1) * 8u)
#define TRO_INIT() const unsigned trow = (unsigned)(8 * lq + (l15 >> 2)), tswz = ((trow >> 1) & 1u) * 2u + ((trow >> 3) & 1u) * 4u
#define SXDMA_INIT() unsigned xvo = (unsigned)((4 * wave + lq) * 1280 + (l15 ^ ((4 * wave + lq) & 15)) * 16); PINV(xvo)
#define SXDMA(kr_, xb_) do { const char* X_ = (const char*)a.Bx + 256 * KROT(kr_); \
    glds(X_, xvo, lds_addr(lds) + (unsigned)((xb_) + wave * 1024)); glds(X_ + 16 * 1280, xvo, lds_addr(lds) + (unsigned)((xb_) + 4096 + wave * 1024)); SB(); } while (0)
#define SXRD(xb_) _Pragma("unroll") for (int kk = 0; kk < 4; ++kk) _Pragma("unroll") for (int rt = 0; rt < 2; ++rt) xa[rt][kk] = *(const u32x4*)(lds + (xb_) + rt * 4096 + l15 * 256 + ((4 * kk + lq) ^ l15) * 16)
#define MFV(c_, a_, b_, first_) do { if (first_) asm volatile("v_mfma_f32_16x16x32_bf16 %0, %1, %2, 0" : "=&v"(c_) : "v"(a_), "v"(b_)); else asm volatile("v_mfma_f32_16x16x32_bf16 %0, %1, %2, %0" : "+v"(c_) : "v"(a_), "v"(b_)); } while (0)
#define HALF(f_, hi_) ((hi_) ? hi4(f_) : lo4(f_))
#define ODMA_INIT() unsigned ovo = (unsigned)((4 * wave + lq) * 1280 + (l15 ^ (((4 * wave + lq) & 7) << 1)) * 16); PINV(ovo); \
    const unsigned orow = (unsigned)(4 * lq + (l15 >> 2)); \
    unsigned otr[2]; _Pragma("unroll") for (int t = 0; t < 2; ++t) otr[t] = orow * 256u + (((unsigned)(2 * (2 * wave + t) + ((l15 & 3) >> 1))) ^ ((orow & 7u) << 1)) * 16u + (unsigned)(l15 & 1) * 8u
#define OROT(cr_) (((cr_) + h) % 5)
#define ODMA(cr_, i_) do { const char* W_ = (const char*)(a.Wo + (size_t)(192 * h) * 640) + 256 * OROT(cr_); \
    _Pragma("unroll") for (int q_ = 0; q_ < 12; ++q_) glds(W_ + q_ * (16 * 1280), ovo, lds_addr(lds) + (unsigned)(SBUF(i_) + (wave + 4 * q_) * 1024)); SB(); } while (0)

#define KXL 8
#define SAL 2
#define SOF 0
#define MNAP 0
#define SF0(m_, t_, k_) (6 * (2 * ((k_) >> 1) + (m_)) + 2 * (t_) + ((k_) & 1))
#define SFR(m_, t_, k_) (SF0(m_, t_, k_) - (SF0(m_, t_, k_) > 39) - (SF0(m_, t_, k_) > 41))
constexpr int L_KX = L_B1 + 28672;
template <int wave>
__device__ __forceinline__ void s_loop(const Args& a, unsigned char* lds, const int h, const unsigned pol, const WReg& W) {
  constexpr int KIND = wave < 2 ? 0 : wave - 1;
  char* ws = a.ws; asm volatile("" : "+s"(ws));
  unsigned* const cnt = (unsigned*)(ws + O_CNT); unsigned* const tmo = cnt + CNT(C_TMO);
  const int lane = lane_id(), l15 = lane & 15, lq = lane >> 4;
  char* const X1 = ws + O_X1; char* const P = ws + O_PS;
  u32x8 kxf[3];
  if (KIND == 0) {
#pragma unroll
    for (int t = 0; t < 3; ++t) kxf[t] = cat8(*(const u32x4*)(lds + L_KX + wave * 6144 + t * 2048 + lane * 16), *(const u32x4*)(lds + L_KX + wave * 6144 + t * 2048 + 1024 + lane * 16));
  }
  const __amdgpu_buffer_rsrc_t rs_x1 = mkrs(X1), rs_p = mkrs(P), rs_kf = mkrs(ws + O_KF);
  unsigned tprog = 0;
#define KX_LOAD(sg_) do { PINV(xv); const unsigned kb_ = KSLOT(sg_) + (unsigned)(h * 12288 + wave * 6144); \
    _Pragma("unroll") for (int t_ = 0; t_ < 3; ++t_) kxf[t_] = cat8(ld16(rs_kf, xv + (unsigned)((t_ & 1) * 2048), kb_ + (unsigned)((t_ >> 1) * 4096)), ld16(rs_kf, xv + (unsigned)((t_ & 1) * 2048 + 1024), kb_ + (unsigned)((t_ >> 1) * 4096))); } while (0)
  unsigned trx = (unsigned)(L_XR + wave * 4096 + lane * 16); PINV(trx);
  unsigned trq = (unsigned)(L_B2 + wave * 12288 + lane * 16); PINV(trq);
  unsigned trk = (unsigned)(L_B0 + wave * 12288 + lane * 16); PINV(trk);
  int ix = (l15 & 2) ? 0xEEEE : 0x4444; PINV(ix);
  const float CS = 0.07216878364870323f * 1.4426950408889634f;
  unsigned xv = (unsigned)lane * 16u;
  unsigned ib = (unsigned)(L_QI + (16 * wave + l15) * 16 + lq * 4);
  unsigned ia = (unsigned)(L_QA + wave * 384 + ((l15 >> 2) & 1) * 128 + (l15 >> 3) * 64 + (l15 & 1) * 32 + lq * 8 + ((l15 >> 1) & 1) * 4);
  unsigned rda = (unsigned)(L_QA + (lq >> 1) * 768 + (l15 >> 3) * 384 + (lq & 1) * 64 + ((l15 >> 2) & 1) * 32 + (l15 & 3) * 8);
  unsigned qk_rd = (unsigned)(L_QI + (4 * lq + (l15 >> 2)) * 16 + (l15 & 1) * 8);
  unsigned flr = (unsigned)L_FLG;
  unsigned xva = KIND == 0 ? (unsigned)(L_XV + lq * 256 + (l15 >> 1) * 16 + wave * 8 + (l15 & 1) * 4) : (unsigned)(L_XV + lane * 16);
  PINV(xv); PINV(ib); PINV(ia);
  if (KIND != 2) { PINV(rda); PINV(xva); } if (KIND == 1) PINV(qk_rd); if (KIND != 0) PINV(flr);
  const bool hi8 = l15 >= 8;
  const __amdgpu_buffer_rsrc_t rs_ts = mkrs(ws + O_TS + half_of(ws + O_TS, 0u, 0u, pol));
  int tso = lane == 0 ? 256 * h : (int)0x80000000u; if (KIND != 0) PINV(tso);
  int tsa = TS_OFF(lane); if (KIND == 2) PINV(tsa);
  const __amdgpu_buffer_rsrc_t rs_ps = mkrs(cnt + CNT(C_PS) - 32 * h);
  unsigned po = side_map(P, (unsigned)(h * 512 + ((l15 >> 2) & 1) * 256 + (4 * lq + 2 * (l15 & 1) + (l15 >> 3)) * 16 + ((l15 >> 1) & 1) * 8), 0u, pol);
  unsigned pm = !(l15 & 1) ? side_map(P, (unsigned)(16384 + h * 128 + (4 * lq + 2 * ((l15 >> 2) & 1) + (l15 >> 3)) * 8 + ((l15 >> 1) & 1) * 4), 0u, pol) : 0x80000000u;
  u32x4 p_desc = mkdesc(P); if (KIND == 1) PINS(p_desc);
  unsigned pfo = lane < 32 ? side_map(P, (unsigned)(h * 512 + lane * 16), 0u, pol) : (lane < 40 ? side_map(P, (unsigned)(16384 + h * 128 + (lane - 32) * 16), 0u, pol) : 0x80000000u);
  if (KIND == 1) { PINV(po); PINV(pm); }
  if (KIND == 1) PINV(pfo);
  unsigned dur = 130, tnx = 0, tpr = 0, adj = 0; int dly = a.d[0]; PINS(dly);
#define MQ(t_, f_, w_) SMFV(aq[t_], f_, w_)
#define MK(t_, f_, w_) SMFV(ak[t_], f_, w_)
#define QW(t_, k_) SMFW(aq[t_], A[k_], SFR(0, t_, k_))
#define KW(t_, k_) SMFW(ak[t_], A[k_], SFR(1, t_, k_))
#define RLO(tb_, t_, k2_) (*(const u32x4*)(lds + (tb_) + (t_) * 4096 + (k2_) * 2048))
#define RHI(tb_, t_, k2_) (*(const u32x4*)(lds + (tb_) + (t_) * 4096 + (k2_) * 2048 + 1024))
#define RFR(tb_, t_, k2_) cat8(RLO(tb_, t_, k2_), RHI(tb_, t_, k2_))
#define XLO(j_) (*(const u32x4*)(lds + trx + (j_) * 2048))
#define XHI(j_) (*(const u32x4*)(lds + trx + (j_) * 2048 + 1024))
#define ALD(k_) DS(A[k_] = ld16(rs_x1, xv + (unsigned)(((k_) & 1) * 1024), sx[(k_) >> 1]))
#define APACE 10
#define ALP(k_) do { ALD(k_); nap4<APACE>(); SB(); } while (0)
#define A_LOAD() do { PINV(xv); ALP(0); ALP(8); ALP(1); ALP(9); ALP(2); ALP(7); ALP(3); ALP(4); ALP(5); ALD(6); SB(); } while (0)
  u32x4 ts_desc = mkdesc(ws + O_TS + half_of(ws + O_TS, 0u, 0u, pol) + 256 * h);
  unsigned fad = lds_addr(lds) + (unsigned)L_FLG; PINV(fad); PINS(ts_desc);
  if (KIND == 2) LW32(flr, 24, dur);
  for (int s = 0; s < 64; ++s) {
    int pss = PSLOT(s), tss = TS_STEP(s), tsn = TS_STEP(s + 1); PINS(pss); PINS(tss); if (KIND == 2) PINS(tsn);
    unsigned snp = (unsigned)(s + 1) * 0x01010101u, snv = snp, tries = 0; PINS(snp); PINV(snv);
    f32x4 sc = ZERO4; if (KIND != 2) PINV(sc);
    unsigned sx[5];
    { const unsigned so = half_of(X1, XSLOT(s), 0u, pol);
#pragma unroll
      for (int j = 0; j < 5; ++j) { sx[j] = so + (j < 4 ? (unsigned)((j + h) & 3) * 2048u : 16384u); PINS(sx[j]); }
      if (KIND == 2 && s == 1) x1_light(rs_x1, so, tmo); }
    SB();
    asm volatile("s_branch .Lsre%c6\n"
                 ".Lsbad%c6:\n s_add_u32 %0, %0, 1\n s_cmp_lt_u32 %0, 4\n s_cbranch_scc1 .Lsnow%c6\n s_sleep 8\n"
                 ".Lsnow%c6:\n s_memrealtime vcc\n ds_write_b32 %5, %1 offset:28\n ds_read_b32 %1, %5 offset:24\n s_waitcnt lgkmcnt(0)\n v_add_u32 %1, vcc_lo, %1\n v_and_b32 %1, -2, %1\n"
                 " s_mov_b64 exec, 1\n buffer_store_dword %1, off, %3, %4 sc1\n s_mov_b64 exec, -1\n v_mov_b32 %1, %2\n"
                 ".Lsre%c6:" : "+s"(tries) : "v"(snv), "s"(snp), "s"(ts_desc), "s"(tss), "v"(fad), "n"(wave) : "vcc", "scc", "memory");
    SB();
    u32x8 ra = RFR(trq, 0, 0), rb = RFR(trq, 1, 0), rc = RFR(trq, 2, 0);
    f32x4 aq[3] = {ZERO4, ZERO4, ZERO4}, ak[3] = {ZERO4, ZERO4, ZERO4};
    asm volatile("s_cmp_lg_u32 %6, 0\n s_cbranch_scc1 .Lsgo%c7" : "+v"(aq[0]), "+v"(aq[1]), "+v"(aq[2]), "+v"(ak[0]), "+v"(ak[1]), "+v"(ak[2]) : "s"(tries), "n"(wave) : "scc", "memory");
    SB();
    unsigned tis = 0, tsl = 0; bool calm = false;
    asm volatile(".p2align %c0\n .fill %c1, 4, 0xbf800000" :: "n"(SAL), "n"(SOF));
    if (KIND == 2) tis = wait_rt(tnx);
    BAR();
    asm volatile(".Lsgo%c0:" :: "n"(wave) : "memory");
    SB();
    u32x4 A[10], lo, hi;
    A_LOAD();
    unsigned w0;
    float ta, tb_;
    u64 nanm;
    QW(0, 0); QW(1, 0); QW(2, 0); KW(0, 0);
    MQ(0, A[8], ra); DS(lo = RLO(trq, 0, 1));
    KW(1, 0); DS(hi = RHI(trq, 0, 1)); ra = cat8(lo, hi);
    MQ(1, A[8], rb); DS(lo = RLO(trq, 1, 1));
    KW(2, 0); DS(hi = RHI(trq, 1, 1)); rb = cat8(lo, hi);
    QW(0, 1);
    MQ(2, A[8], rc); DS(lo = RLO(trq, 2, 1));
    QW(1, 1); DS(hi = RHI(trq, 2, 1)); rc = cat8(lo, hi);
    QW(2, 1);
    MQ(0, A[9], ra); DS(lo = RLO(trk, 0, 0));
    KW(0, 1); DS(hi = RHI(trk, 0, 0)); ra = cat8(lo, hi);
    MQ(1, A[9], rb); DS(lo = XLO(0));
    KW(1, 1); DS(hi = XHI(0)); rb = cat8(lo, hi);
    QW(0, 2);
    MQ(2, A[9], rc); DS(lo = XLO(1));
    QW(1, 2); DS(hi = XHI(1)); rc = cat8(lo, hi);
    QW(2, 2);
    MK(0, A[8], ra); DS(lo = RLO(trk, 1, 0));
    QW(0, 7); DS(hi = RHI(trk, 1, 0)); ra = cat8(lo, hi);
    MQ(1, A[7], rb); DS(lo = RLO(trk, 0, 1));
    KW(0, 2); DS(hi = RHI(trk, 0, 1)); rb = cat8(lo, hi);
    QW(0, 3);
    MQ(2, A[7], rc); DS(lo = RLO(trk, 2, 0));
    QW(1, 3); DS(hi = RHI(trk, 2, 0)); rc = cat8(lo, hi);
    QW(2, 3);
    QW(0, 4);
    MK(1, A[8], ra); DS(lo = RLO(trk, 1, 1));
    QW(1, 4); DS(hi = RHI(trk, 1, 1)); ra = cat8(lo, hi);
    QW(2, 4);
    QW(0, 5);
    MK(0, A[9], rb); DS(lo = RLO(trk, 2, 1));
    QW(1, 5); DS(hi = RHI(trk, 2, 1)); rb = cat8(lo, hi);
    QW(2, 5);
    QW(0, 6); QW(1, 6); QW(2, 6); LG0(); SB();
    KW(0, 3); FILL(); ADD1(ta, aq[0][0], aq[0][2]);
    KW(0, 4); FILL(); ADD1(tb_, aq[0][1], aq[0][3]);
    KW(0, 5); FILL(); CVTN(w0, nanm, ta, tb_);
    KW(0, 6); DS(LW32(ia, 0, w0)); ADD1(ta, aq[1][0], aq[1][2]);
    KW(0, 7); ADD1(tb_, aq[1][1], aq[1][3]);
    KW(1, 2);
    if (__builtin_expect(nanm != 0ull, 0)) {
      unsigned m = ~0u;
#define S_BACK() asm volatile("v_cmp_eq_u32 vcc, 0, %0\n s_cmp_lt_u32 %1, %3\n s_cselect_b64 vcc, vcc, 0\n s_cbranch_vccnz .Lsbad%c2" : "+v"(m) : "s"(tries), "n"(wave), "n"(CH_SPIN_CAP) : "vcc", "scc")
#pragma unroll
      for (int k = 0; k < 10; k += 2) m = sent_min(m, A[k].x, A[k + 1].x);
      S_BACK();
#pragma unroll
      for (int k = 0; k < 10; ++k) m = sent_min(m, A[k]);
      S_BACK();
    }
    asm volatile("" :: "s"(sx[0]), "s"(sx[1]), "s"(sx[2]), "s"(sx[3]), "s"(sx[4]), "s"(tries), "s"(tss), "s"(snp), "s"(ts_desc), "v"(fad), "v"(snv));
    CVT1(w0, ta, tb_);
    KW(1, 3); DS(LW32(ia, 1536, w0)); ADD1(ta, aq[2][0], aq[2][2]);
    KW(1, 4); FILL(); ADD1(tb_, aq[2][1], aq[2][3]);
    KW(1, 5); FILL(); CVT1(w0, ta, tb_);
    KW(1, 6); DS(LW32(ia, 3072, w0)); ADD1(ta, ak[0][0], ak[0][2]);
    KW(1, 7); FILL(); ADD1(tb_, ak[0][1], ak[0][3]);
    MK(1, A[9], ra); CVT1(w0, ta, tb_);
    KW(2, 1); DS(LW32(ib, 0, w0));
    u32x2 g[6];
#define QAR(i_) do { if (KIND != 2) DS(g[i_] = trrd(lds, rda + (unsigned)(((i_) >> 1) * 1536 + ((i_) & 1) * 128))); else if ((i_) >= 1 && (i_) <= 3) FILL(); } while (0)
#define QA(t_) ((u32x4){g[2 * (t_)].x, g[2 * (t_)].y, g[2 * (t_) + 1].x, g[2 * (t_) + 1].y})
    KW(2, 2); LBAR(1); QAR(0);
    KW(2, 3); QAR(1); ADD1(ta, ak[1][0], ak[1][2]);
    KW(2, 4); QAR(2); ADD1(tb_, ak[1][1], ak[1][3]);
    KW(2, 5); QAR(3); CVT1(w0, ta, tb_);
    KW(2, 6); DS(LW32(ib, 1024, w0));
    KW(2, 7); QAR(4); QAR(5);
    if (KIND == 2) DS(tsl = __builtin_amdgcn_raw_buffer_load_b32(rs_ts, tsa, tss, 16));
    MK(2, A[8], rc);
    MK(2, A[9], rb);
    if (KIND == 0) {
      float la, lb;
      { const u32x4 q_ = QA(0); SMFV(sc, q_, kxf[0]); } LBAR(2);
      { const u32x4 q_ = QA(1); SMFV(sc, q_, kxf[1]); }
      { const u32x4 q_ = QA(2); SMFV(sc, q_, kxf[2]); }
      EPI3(w0, ak[2]); DS(LW32(ib, 2048, w0));
      asm volatile("s_nop 3\n v_add_f32 %0, %2, %3\n v_add_f32 %1, %4, %5" : "=&v"(la), "=v"(lb) : "v"(sc[0]), "v"(sc[2]), "v"(sc[1]), "v"(sc[3])); SB();
      DS(LW32(xva, 0, __float_as_uint(la)); LW32(xva, 128, __float_as_uint(lb)));
      LBAR(1); LBAR(0);
    } else if (KIND == 2) {
      LBAR(0);
      asm volatile("s_nop 5"); SB();
      EPI3(w0, ak[2]); DS(LW32(ib, 2048, w0));
      LBAR(0); LBAR(0);
      tsl = ts_max(tsl);
      calm = s > 1 && tsl != ~0u && (int)(tsl - tpr) <= a.d[10];
      if (calm && s < 63) __builtin_amdgcn_raw_buffer_store_b32((tsl + (unsigned)dly + dur) & ~1u, rs_ts, tso, tsn, 16);
    } else {
      u32x2 kb[12];
#define KBR(i_) DS(kb[i_] = trrd(lds, qk_rd + (unsigned)((i_) * 256)))
#define ML(t_) do { const u32x4 a_ = QA(t_); \
        const u32x8 b_ = {kb[4 * (t_)].x, kb[4 * (t_)].y, kb[4 * (t_) + 1].x, kb[4 * (t_) + 1].y, kb[4 * (t_) + 2].x, kb[4 * (t_) + 2].y, kb[4 * (t_) + 3].x, kb[4 * (t_) + 3].y}; SMFV(sc, a_, b_); } while (0)
      LBAR(2);
      KBR(0); KBR(1); KBR(2); KBR(3); KBR(4); KBR(5);
      EPI3(w0, ak[2]); DS(LW32(ib, 2048, w0));
      KBR(6); KBR(7);
      LBAR(2);
      KBR(8); KBR(9); KBR(10); KBR(11);
      ML(0);
      asm volatile("s_barrier" ::: "memory"); SB();
      f32x4 t; DS(t = *(const f32x4*)(lds + xva));
      ML(1); ML(2);
      float tl, nl, x, m, u, e, d;
      asm volatile("v_max3_f32 %4, %0, %1, %2\n v_max_f32 %4, %4, %3\n v_mul_f32 %5, %15, %4\n v_fma_f32 %0, %16, %0, %5\n v_fma_f32 %1, %16, %1, %5\n v_fma_f32 %2, %16, %2, %5\n v_fma_f32 %3, %16, %3, %5\n s_nop 0\n"
                   "v_add_f32_dpp %6, %11, %13 quad_perm:[0,1,2,3] row_mask:0xf bank_mask:0x3\n v_add_f32_dpp %6, %12, %14 quad_perm:[0,1,2,3] row_mask:0xf bank_mask:0xc\n v_max_f32 %7, %6, %4\n"
                   "v_exp_f32 %0, %0\n v_exp_f32 %1, %1\n v_max_f32_dpp %7, %7, %7 quad_perm:[1,0,3,2] row_mask:0xf bank_mask:0xf\n"
                   "v_exp_f32 %2, %2\n v_exp_f32 %3, %3\n v_max_f32_dpp %7, %7, %7 quad_perm:[2,3,0,1] row_mask:0xf bank_mask:0xf\n"
                   "v_add_f32 %8, %0, %1\n v_add_f32 %10, %2, %3\n v_max_f32_dpp %7, %7, %7 row_half_mirror row_mask:0xf bank_mask:0xf\n"
                   "v_mul_f32 %7, %15, %7\n v_sub_f32 %4, %7, %5\n v_fma_f32 %9, %16, %6, %7\n v_exp_f32 %4, %4\n v_exp_f32 %9, %9\n v_add_f32 %8, %8, %10\n v_fma_f32 %10, %4, %8, %9\n"
                   "v_mul_f32 %0, %0, %4\n v_mul_f32 %1, %1, %4\n v_add_f32_dpp %10, %10, %10 quad_perm:[1,0,3,2] row_mask:0xf bank_mask:0xf\n"
                   "v_mul_f32 %2, %2, %4\n v_mul_f32 %3, %3, %4\n v_add_f32_dpp %10, %10, %10 quad_perm:[2,3,0,1] row_mask:0xf bank_mask:0xf\n"
                   "v_mov_b32_dpp %6, %9 quad_perm:[1,0,3,2] row_mask:0xf bank_mask:0xf\n s_nop 0\n v_add_f32_dpp %10, %10, %10 row_half_mirror row_mask:0xf bank_mask:0xf\n v_rcp_f32 %10, %10\n s_nop 0\n"
                   "v_mul_f32 %0, %0, %10\n v_mul_f32 %1, %1, %10\n v_cvt_pk_bf16_f32 v0, %0, %1\n v_mul_f32 %2, %2, %10\n v_mul_f32 %3, %3, %10\n v_cvt_pk_bf16_f32 v1, %2, %3\n"
                   "buffer_store_dwordx2 v[0:1], %17, %19, %20 offen sc1\n v_mul_f32 %9, %9, %10\n v_mul_f32 %6, %6, %10\n v_cvt_pk_bf16_f32 %9, %9, %6\n buffer_store_dword %9, %18, %19, %20 offen sc1"
                   : "+v"(t[0]), "+v"(t[1]), "+v"(t[2]), "+v"(t[3]), "=&v"(tl), "=&v"(nl), "=&v"(x), "=&v"(m), "=&v"(u), "=&v"(e), "=&v"(d)
                   : "v"(sc[0]), "v"(sc[1]), "v"(sc[2]), "v"(sc[3]), "s"(-CS), "s"(CS), "v"(po), "v"(pm), "s"(p_desc), "s"(pss) : "v0", "v1", "memory");
      SB();
      { u64 t_; RT_MARK(t_); LW32(flr, 20, (unsigned)t_); }
    }
    BAR();
    if (KIND == 0) {
      nap_rt(KXL - 1);
      if (s < 63) {
        const unsigned tneed = (unsigned)(s + 1);
        if (tprog < tneed) tprog = wait_t<4>(cnt + CNT(C_PT_ + h), 1, tneed, tmo);
        KX_LOAD(s + 1);
      }
    }
    if (KIND == 0) tprog = min_t(ld16(mkrs(cnt + CNT(C_PT_ + h)), 0u, 0u), 1);
    if (KIND == 2 && s < 63) {
      const unsigned f7 = FLRD(7), f5 = FLRD(5);
      const unsigned tb2 = (unsigned)__builtin_amdgcn_readfirstlane(f5);
      adj = __builtin_amdgcn_readfirstlane(f7) == snp;
      if (__builtin_expect(!calm || adj, 0)) {
        if (s < 2) __builtin_amdgcn_raw_buffer_store_b32(((unsigned)tb2 + (unsigned)a.d[11]) & ~1u, rs_ts, tso, tss, 16);
        tsl = ts_settle(rs_ts, tss, tsl, a.d[4]) + (unsigned)a.d[12];
        if (s > 0) __builtin_amdgcn_raw_buffer_store_b32((tsl + (unsigned)dly + dur) & ~1u, rs_ts, tso, tsn, 16);
      }
      tnx = tsl + (unsigned)dly; tpr = tnx + dur;
      dur = min(dur + 1u, (unsigned)tb2 - tis); PINS(dur);
      LW32(flr, 24, dur + (unsigned)a.d[13]);
      ADAPT(dly, adj);
    }
    if (KIND == 1) {
      const int ln = lane_id();
      const __amdgpu_buffer_rsrc_t rs_km = mkrs(ws + O_KM);
      if (s + PRE < 63) st16(rs_p, pfo, (unsigned)PSLOT(s + PRE), SENT4);
#pragma unroll
      for (int kk = 0; kk < 6; kk += 2) {
        const u32x4 v = trrd2(lds, qk_rd + kk * 512, 256u), v2 = trrd2(lds, qk_rd + (kk + 1) * 512, 256u);
        st16(rs_km, (unsigned)((kk + ((ln >> 3) & 1)) * 512 + (ln >> 4) * 128 + (ln & 7) * 16), (unsigned)(s * 32 + h) * 3072u, (u32x4){min(hi8 ? v2.x : v.x, 0xfffffffeu), hi8 ? v2.y : v.y, min(hi8 ? v2.z : v.z, 0xfffffffeu), hi8 ? v2.w : v.w});
      }
      if (s >= 60) VMW(0);
      __builtin_amdgcn_raw_buffer_store_b32((unsigned)(s >= 60 ? s + 1 : s), rs_ps, tso, 0, 16);
    }
  }
}
__device__ __forceinline__ void s_role(const Args& a, unsigned char* lds, const int h, const int wave) {
  char* ws = a.ws; asm volatile("" : "+s"(ws));
  unsigned* const cnt = (unsigned*)(ws + O_CNT); unsigned* const tmo = cnt + CNT(C_TMO);
  const int lane = lane_id(), l15 = lane & 15, lq = lane >> 4;
  WReg W; u32x8 wx[3];
  unsigned tro[3];
  { TRO_INIT();
#pragma unroll
    for (int t = 0; t < 3; ++t) tro[t] = TRO(4 * t + wave); }
  SDMA_INIT();
  SXDMA_INIT();
  SXDMA(0, 0); SDMAQK(0); SDMAQK(1); SXDMA(1, 8192); SDMAQK(2);
  f32x4 pac[2][2][3];
#define kacc pac[1]
#define qacc pac[0]
  SFOR(i, 10)
    if (i < 2 || i == 3 || i == 5 || i == 7) VMW(26); else if (i < 8) VMW(24); else if (i == 8) VMW(12); else VMW(0);
    BAR();
    u32x8 res[3][2];
    SFOR(t, 3)
      const unsigned ad = lds_addr(lds) + tro[t] + (unsigned)SBUF(i);
      if (i < 8) { TRS0(ad, SFR(i & 1, t, i & 6)); if (i == 6 && t > 0) tr8(false, ad, res[t][0], wx[t]); else TRS1(ad, SFR(i & 1, t, (i & 6) + 1)); }
      else tr8(false, ad, res[t][0], res[t][1]);
    SEND;
    BAR();
    if (i == 2 || i == 4 || i == 6) SXDMA((i >> 1) + 1, (((i >> 1) + 1) & 1) * 8192);
    if (i + 3 < 10) SDMAQK(i + 3);
    if (i >= 8) {
#pragma unroll
      for (int t = 0; t < 3; ++t)
#pragma unroll
        for (int k = 0; k < 2; ++k) {
          unsigned char* dst = lds + SBUF(i) + wave * 12288 + t * 4096 + k * 2048 + lane * 16;
          *(u32x4*)dst = lo4(res[t][k]); *(u32x4*)(dst + 1024) = hi4(res[t][k]);
        }
    }
    if (i == 7) {
#pragma unroll
      for (int t = 1; t < 3; ++t) { unsigned char* dst = lds + L_XR + wave * 4096 + lane * 16 + (t - 1) * 2048; *(u32x4*)dst = lo4(wx[t]); *(u32x4*)(dst + 1024) = hi4(wx[t]); }
    }
    SB();
    SFOR(kk, 4)
      u32x4 xa[2];
#pragma unroll
      for (int rt = 0; rt < 2; ++rt) xa[rt] = *(const u32x4*)(lds + ((i >> 1) & 1) * 8192 + rt * 4096 + l15 * 256 + ((4 * kk + lq) ^ l15) * 16);
      SFOR(n, 6)
        constexpr int t = n >> 1, rt = n & 1;
        if (i >= 8) { const u32x4 w_ = HALF(res[t][kk >> 1], kk & 1); MFV(pac[i & 1][rt][t], w_, xa[rt], false); }
        else if (i == 6 && t > 0 && kk >= 2) { const u32x4 w_ = HALF(wx[t], kk & 1); MFV(pac[0][rt][t], w_, xa[rt], false); }
        else mf_w<true>(pac[i & 1][rt][t], xa[rt], 2 * SFR(i & 1, t, (i & 6) + (kk >> 1)) + (kk & 1), i < 2 && kk == 0, W);
      SEND;
    SEND;
    SB();
  SEND;
  unsigned pol;
  {
    const u32x2 pf = ld8(mkrs(cnt + CNT(C_POL)), lane < 32 ? 0u : 128u, 0u);
    asm volatile("s_nop 7\n s_nop 7" : "+v"(kacc[0][0]), "+v"(kacc[0][1]), "+v"(kacc[0][2]), "+v"(kacc[1][0]), "+v"(kacc[1][1]), "+v"(kacc[1][2]));
#pragma unroll
    for (int rt = 0; rt < 2; ++rt)
#pragma unroll
      for (int t = 0; t < 3; ++t) { const int ix = 4 * t + wave; const unsigned o_ = (unsigned)(rt * 6144 + (ix >> 1) * 1024 + lane * 16 + (ix & 1) * 8);
        const unsigned k0 = cvtpk(kacc[rt][t][0], kacc[rt][t][1]), k1 = cvtpk(kacc[rt][t][2], kacc[rt][t][3]);
        *(u32x2*)(lds + L_KX + o_) = (u32x2){k0, k1};
        st8(mkrs(ws + O_KF), o_, (unsigned)h * 12288u, k0, k1);
        st8(mkrs(ws + O_QF), o_, (unsigned)h * 12288u, cvtpk(qacc[rt][t][0], qacc[rt][t][1]), cvtpk(qacc[rt][t][2], qacc[rt][t][3])); }
    if (wave == 2) { if (lane < 8) *(unsigned*)(lds + L_FLG + 4 * lane) = 0u; }
    BAR();
    pol = (unsigned)__builtin_amdgcn_readlane((int)pf.x, 0);
    if (__builtin_expect((unsigned)__builtin_amdgcn_readlane((int)pf.x, 32) < 40u || pol == 0u, 0)) { wait_ge<8>(cnt + CNT(C_FILL), 40u, tmo); pol = ld_word(cnt + CNT(C_POL)); }
    pol &= 1u;
  }
  if (wave == 0) s_loop<0>(a, lds, h, pol, W); else if (wave == 1) s_loop<1>(a, lds, h, pol, W); else if (wave == 2) s_loop<2>(a, lds, h, pol, W); else s_loop<3>(a, lds, h, pol, W);
}
#undef kacc
#undef qacc

__device__ __forceinline__ void z_role(const Args& a, unsigned char* lds, const int h, const int wave) {
  char* ws = a.ws; asm volatile("" : "+s"(ws));
  unsigned* const cnt = (unsigned*)(ws + O_CNT); unsigned* const tmo = cnt + CNT(C_TMO);
  char* const X1 = ws + O_X1;
#define ZFV(t_, k_) (8 * (t_) + (k_))
#define ZFO(n_, j_) ((n_) < 6 ? 24 + 3 * (n_) + (j_) : 30 + 2 * (n_) + ((j_) >> 1))
  WReg W;
  const __amdgpu_buffer_rsrc_t rs_u = mkrs(ws + O_UO + h * 1024);
  unsigned xv = (unsigned)lane_id() * 16u; PINV(xv);
  unsigned pkv, pk3, pk4;
  {
    const int lane = lane_id(), l15 = lane & 15, lq = lane >> 4;
    unsigned tro[3];
    { TRO_INIT();
#pragma unroll
      for (int t = 0; t < 3; ++t) tro[t] = TRO(4 * t + wave); }
    SDMA_INIT(); SXDMA_INIT(); ODMA_INIT();
    SDMA(a.Wv, 0, 0); SXDMA(0, 0); SDMA(a.Wv, 1, 1); SXDMA(1, 8192); SDMA(a.Wv, 2, 2);
    f32x4 kacc[2][3];
    u32x8 wl[3][2];
    SFOR(i, 5)
      if (i < 4) VMW(26); else VMW(24);
      BAR();
      u32x4 xa[2][4];
      SXRD((i & 1) * 8192);
      SFOR(t, 3)
        if (i < 4) { TRS0(lds_addr(lds) + tro[t] + (unsigned)SBUF(i), ZFV(t, 2 * (i & 3))); TRS1(lds_addr(lds) + tro[t] + (unsigned)SBUF(i), ZFV(t, 2 * (i & 3) + 1)); }
        else tr8(false, lds_addr(lds) + tro[t] + (unsigned)SBUF(i), wl[t][0], wl[t][1]);
      SEND;
#pragma unroll
      for (int n = 0; n < 8; ++n) PINV(xa[n >> 2][n & 3]);
      BAR();
      if (i + 2 < 5) SXDMA(i + 2, (i & 1) * 8192);
      if (i + 3 < 5) SDMA(a.Wv, i + 3, i + 3); else ODMA(i - 2, i + 3);
      SFOR(n, 24)
        constexpr int kk = n / 6, t = (n % 6) >> 1, rt = n & 1;
        if (i < 4) mf_w<true>(kacc[rt][t], xa[rt][kk], 2 * ZFV(t, 2 * (i & 3) + (kk >> 1)) + (kk & 1), i == 0 && kk == 0, W);
        else { const u32x4 w_ = HALF(wl[t][kk >> 1], kk & 1); MFV(kacc[rt][t], w_, xa[rt][kk], false); }
      SEND;
      SB();
    SEND;
    u32x4 vx[2][6];
    asm volatile("s_nop 7\n s_nop 7" : "+v"(kacc[0][0]), "+v"(kacc[0][1]), "+v"(kacc[0][2]), "+v"(kacc[1][0]), "+v"(kacc[1][1]), "+v"(kacc[1][2]));
#pragma unroll
    for (int rt = 0; rt < 2; ++rt)
#pragma unroll
      for (int t = 0; t < 3; ++t)
        *(u32x2*)(lds + rt * 6144 + (2 * t + (wave >> 1)) * 1024 + xv + (wave & 1) * 8) = (u32x2){cvtpk(kacc[rt][t][0], kacc[rt][t][1]), cvtpk(kacc[rt][t][2], kacc[rt][t][3])};
    BAR();
#pragma unroll
    for (int n = 0; n < 12; ++n) { vx[n / 6][n % 6] = *(const u32x4*)(lds + n * 1024 + xv); PINV(vx[n / 6][n % 6]); }
    SFOR(i5, 5)
      constexpr int i = i5 + 5;
      if (i == 5) VMW(24); else if (i == 6) VMW(26); else if (i == 7) VMW(28); else if (i == 8) VMW(18); else VMW(6);
      BAR();
      u32x8 res[2][3];
      SFOR(t, 2)
        const unsigned ad = lds_addr(lds) + otr[t] + (unsigned)SBUF(i);
        constexpr int n = 2 * i5 + t;
        if (n < 8) TRO4(ad, 0, ZFO(n & 7, 0)); else tr4<0>(false, ad, res[t][0]);
        if (n < 6) TRO4(ad, 1, ZFO(n & 7, 1)); else tr4<1>(false, ad, res[t][1]);
        if (n < 8) TRO4(ad, 2, ZFO(n & 7, 2)); else tr4<2>(false, ad, res[t][2]);
      SEND;
      BAR();
      if (i + 3 < 10) ODMA(i - 2, i + 3);
      f32x4 ua[2][2];
      SFOR(m, 24)
        constexpr int e = m >> 2, t = (m >> 1) & 1, rt = m & 1, n = 2 * i5 + t;
        if (n >= 8 || (n >= 6 && (e >> 1) == 1)) { const u32x4 w_ = HALF(res[t][e >> 1], e & 1); MFV(ua[t][rt], vx[rt][e], w_, e == 0); }
        else mf_w<false>(ua[t][rt], vx[rt][e], 2 * ZFO(n & 7, e >> 1) + (e & 1), e == 0, W);
      SEND;
      asm volatile("s_nop 7" : "+v"(ua[0][0]), "+v"(ua[0][1]), "+v"(ua[1][0]), "+v"(ua[1][1]));
#pragma unroll
      for (int t = 0; t < 2; ++t)
        st16(rs_u, xv, u_off(0u, (unsigned)(8 * OROT(i5) + 2 * wave + t)), (u32x4){cvtpk(ua[t][0][0], ua[t][0][1]), cvtpk(ua[t][0][2], ua[t][0][3]), cvtpk(ua[t][1][0], ua[t][1][1]), cvtpk(ua[t][1][2], ua[t][1][3])});
      SB();
      if (i == 7) {
        PINV(xv); pkv = xv + (unsigned)(L_B1 + wave * 12288);
#pragma unroll
        for (int n = 0; n < 6; ++n) { *(u32x4*)(lds + pkv + n * 2048) = lo4(wl[n >> 1][n & 1]); *(u32x4*)(lds + pkv + n * 2048 + 1024) = hi4(wl[n >> 1][n & 1]); }
      }
      if (i == 8) {
        PINV(xv); pk3 = xv + (unsigned)(L_B2 + wave * 12288);
#pragma unroll
        for (int t = 0; t < 2; ++t) { *(u32x4*)(lds + pk3 + t * 2048) = lo4(res[t][1]); *(u32x4*)(lds + pk3 + t * 2048 + 1024) = hi4(res[t][1]); }
      }
      if (i == 9) {
        PINV(xv); pk4 = xv + (unsigned)(L_B0 + wave * 12288);
#pragma unroll
        for (int n = 0; n < 6; ++n) { *(u32x4*)(lds + pk4 + n * 2048) = lo4(res[n & 1][n >> 1]); *(u32x4*)(lds + pk4 + n * 2048 + 1024) = hi4(res[n & 1][n >> 1]); }
      }
      SB();
    SEND;
  }
  const int lane = lane_id(), l15 = lane & 15, lq = lane >> 4;
  constexpr int L_VA = 10240;
  unsigned pol;
  {
    const u32x2 pf = ld8(mkrs(cnt + CNT(C_POL)), lane < 32 ? 0u : 128u, 0u);
    pol = (unsigned)__builtin_amdgcn_readlane((int)pf.x, 0);
    VMW(0);
    __builtin_amdgcn_raw_buffer_store_b32(1u, mkrs(cnt + CNT(C_PZ + h)), lane == 0 ? 4 * wave : (int)0x80000000u, 0, 16);
    if (__builtin_expect((unsigned)__builtin_amdgcn_readlane((int)pf.x, 32) < 40u || pol == 0u, 0)) { wait_ge<8>(cnt + CNT(C_FILL), 40u, tmo); pol = ld_word(cnt + CNT(C_POL)); }
    pol &= 1u;
  }
  const __amdgpu_buffer_rsrc_t rs_x1 = mkrs(X1), rs_z = mkrs(ws + O_ZB), rs_ts = mkrs(ws + O_TS + half_of(ws + O_TS, 0u, 0u, pol));
  int ix = (l15 & 2) ? 0xEEEE : 0x4444; PINV(ix);
  unsigned ia = (unsigned)(L_VA + wave * 384 + ((l15 >> 2) & 1) * 128 + (l15 >> 3) * 64 + (l15 & 1) * 32 + lq * 8 + ((l15 >> 1) & 1) * 4);
  unsigned rda = (unsigned)(L_VA + (lq >> 1) * 768 + (l15 >> 3) * 384 + (lq & 1) * 64 + ((l15 >> 2) & 1) * 32 + (l15 & 3) * 8);
  unsigned zvo = (unsigned)((2 * wave + (lq >> 1)) * 16384 + h * 256 + l15 * 16 + (lq & 1) * 8);
  unsigned evo = (unsigned)((2 * wave + (lq & 1) + 8 * (lq >> 1)) * 16384 + h * 256 + l15 * 16);
  unsigned gvo = xv + (unsigned)(wave * 2048), gv2 = xv + 16384u + (unsigned)((wave >> 1) * 1024);
  u64 hmask = 0xffffffffull << (32 * (wave & 1)); PINS(hmask);
  const unsigned gm0 = lds_addr(lds) + (unsigned)(L_MEM + ((wave - h) & 3) * 2048), gm2 = lds_addr(lds) + (unsigned)(L_MEM + 8192 + (wave >> 1) * 1024);
  PINV(ia); PINV(rda); PINV(zvo); PINV(gvo); PINV(gv2); PINV(pkv); PINV(pk3); PINV(pk4);
  unsigned tsv = ~0u, tries = 0; int dly = a.d[1]; PINS(dly);
#define PLO(b_, n_) (*(const u32x4*)(lds + (b_) + (n_) * 2048))
#define PHI(b_, n_) (*(const u32x4*)(lds + (b_) + (n_) * 2048 + 1024))
#define MV(t_, f_, w_) SMFV(av[t_], f_, w_)
#define VW(t_, k_) SMFW(av[t_], A[k_], ZFV(t_, k_))
#define MO(n_, j_, w_) SMFV(az[n_], va[j_], w_)
#define OW(n_, j_) SMFW(az[n_], va[j_], ZFO(n_, j_))
#define AFR(k_) (*(const u32x4*)(lds + afb + (k_) * 1024))
  unsigned afb = xv + L_MEM; PINV(afb);
#define G_LOAD() do { asm volatile("s_mov_b32 m0, %3\n s_nop 0\n global_load_lds_dwordx4 %0, %4 sc1\n global_load_lds_dwordx4 %0, %4 offset:1024 sc1\n s_mov_b32 m0, %2\n s_mov_b64 exec, %5\n global_load_lds_dwordx4 %1, %4 sc1\n s_mov_b64 exec, -1\n s_waitcnt vmcnt(2)\n s_barrier" \
      :: "v"(gvo), "v"(gv2), "s"(gm2), "s"(gm0), "s"(xs), "s"(hmask) : "memory"); SB(); } while (0)
  u32x4 ts_desc = mkdesc(ws + O_TS + half_of(ws + O_TS, 0u, 0u, pol) + 16384 + 256 * h);
  int w3m = wave, dzs = a.d[9]; PINS(w3m); PINS(dzs); PINS(ts_desc);
  u32x4 z_desc = mkdesc(ws + O_ZB); PINS(z_desc);
#pragma unroll 1
  for (int s = 0; s < 64; ++s) {
    f32x4 av[3], az[10];
#pragma unroll
    for (int n = 0; n < 10; ++n) { az[n] = ZERO4; PINV(az[n]); }
    unsigned snp = (unsigned)(s + 1) * 0x01010101u, snv = snp, adj = 0; PINS(snp); PINV(snv);
    const unsigned so = half_of(X1, XSLOT(s), 0u, pol);
    const char* xs = X1 + so; int tss = TS_STEP(s); PINS(xs); PINS(tss);
    unsigned zs[5], tnx = (unsigned)__builtin_amdgcn_readfirstlane(wave == 3 && tsv != ~0u ? tsv + (unsigned)dly : 0u); PINS(tnx);
    { const unsigned z0 = half_of(ws + O_ZB, ZSLOT(s), 0u, pol);
#pragma unroll
      for (int i = 0; i < 5; ++i) { zs[i] = z0 + (unsigned)OROT(i) * (8u * 16384u); PINS(zs[i]); } }
    if (s == 1 && wave == 3) x1_light(rs_x1, so, tmo);
    SB();
    asm volatile("s_branch .Lzre\n"
                 ".Lzbad:\n s_waitcnt lgkmcnt(0)\n s_add_u32 %0, %0, 1\n s_add_u32 %1, %1, 1\n s_cmp_lt_u32 %0, 4\n s_cbranch_scc1 .Lznow\n s_sleep 8\n"
                 ".Lznow:\n s_cmp_lg_u32 %6, 3\n s_cbranch_scc1 .Lzre\n s_memrealtime vcc\n s_waitcnt lgkmcnt(0)\n s_add_u32 vcc_lo, vcc_lo, %7\n s_and_b32 vcc_lo, vcc_lo, -2\n v_mov_b32 %2, vcc_lo\n"
                 " s_mov_b64 exec, 1\n buffer_store_dword %2, off, %4, %5 sc1\n s_mov_b64 exec, -1\n v_mov_b32 %2, %3\n"
                 ".Lzre:" : "+s"(adj), "+s"(tries) : "v"(snv), "s"(snp), "s"(ts_desc), "s"(tss), "s"(w3m), "s"(dzs) : "vcc", "scc", "memory");
    SB();
    u32x8 ra = cat8(PLO(pkv, 0), PHI(pkv, 0)), rb = cat8(PLO(pkv, 1), PHI(pkv, 1));
    av[0] = ZERO4; av[1] = ZERO4; av[2] = ZERO4;
    asm volatile("" : "+v"(av[0]), "+v"(av[1]), "+v"(av[2]));
    SB();
    wait_rt(tnx);
    BAR();
    G_LOAD();
    u32x4 A[10], lo, hi, va[3];
    unsigned w0, w1;
    float ta, tb_;
    u64 nanm;
    DS(A[0] = AFR(0)); DS(A[2] = AFR(2)); DS(A[4] = AFR(4)); DS(A[6] = AFR(6));
    VW(0, 0); VW(1, 0); asm volatile("s_waitcnt vmcnt(0)\n s_barrier" ::: "memory"); SB();
    VW(2, 0); DS(A[8] = AFR(8));
    VW(0, 2); DS(A[9] = AFR(9));
    VW(1, 2); DS(A[1] = AFR(1));
    VW(2, 2); DS(A[3] = AFR(3));
    VW(0, 4); DS(A[5] = AFR(5));
    VW(0, 6); DS(A[7] = AFR(7));
    MV(0, A[8], ra); DS(lo = PLO(pkv, 2));
    MV(0, A[9], rb); DS(hi = PHI(pkv, 2)); ra = cat8(lo, hi);
    VW(0, 1); DS(lo = PLO(pkv, 3));
    VW(0, 3); DS(hi = PHI(pkv, 3)); rb = cat8(lo, hi);
    VW(0, 5); VW(0, 7);
    VW(1, 4); VW(1, 6);
    MV(1, A[8], ra); DS(lo = PLO(pkv, 4)); ADD1(ta, av[0][0], av[0][2]);
    VW(1, 1); DS(hi = PHI(pkv, 4)); ra = cat8(lo, hi); ADD1(tb_, av[0][1], av[0][3]);
    VW(1, 3); LGW(2); CVT1(w0, ta, tb_);
    MV(1, A[9], rb); DS(LW32(ia, 0, w0));
    VW(1, 5); DS(lo = PLO(pkv, 5)); NANM(nanm, ta, tb_);
    VW(1, 7); DS(hi = PHI(pkv, 5)); rb = cat8(lo, hi);
    VW(2, 4);
    if (__builtin_expect(nanm != 0ull, 0)) {
      unsigned m = ~0u;
#define Z_BACK() asm volatile("v_cmp_eq_u32 vcc, 0, %0\n s_cmp_lt_u32 %1, %2\n s_cselect_b64 vcc, vcc, 0\n s_cbranch_vccnz .Lzbad" : "+v"(m) : "s"(tries), "n"(CH_SPIN_CAP) : "vcc", "scc")
#pragma unroll
      for (int k = 0; k < 10; k += 2) m = sent_min(m, A[k].x, A[k + 1].x);
      Z_BACK();
#pragma unroll
      for (int k = 0; k < 10; ++k) m = sent_min(m, A[k]);
      Z_BACK();
    }
    asm volatile("" :: "s"(xs), "s"(tnx), "s"(tries), "s"(tss), "s"(snp), "s"(ts_desc), "s"(w3m), "s"(dzs), "v"(snv));
    PINS(adj);
    u32x2 g0, g1;
#define VR0(j_) DS(g0 = trrd(lds, rda + (unsigned)((j_) * 1536)))
#define VR1(j_) do { DS(g1 = trrd(lds, rda + (unsigned)((j_) * 1536 + 128))); va[j_] = (u32x4){g0.x, g0.y, g1.x, g1.y}; } while (0)
    VW(2, 6); LBAR(2);
    MV(2, A[8], ra); VR0(0); ADD1(ta, av[1][0], av[1][2]);
    VW(2, 1); VR1(0); ADD1(tb_, av[1][1], av[1][3]);
    VW(2, 3); DS(lo = PLO(pk4, 0)); CVT1(w0, ta, tb_);
    VW(2, 5); DS(LW32(ia, 1536, w0));
    MV(2, A[9], rb); DS(hi = PHI(pk4, 0)); ra = cat8(lo, hi);
    VW(2, 7); DS(lo = PLO(pk4, 1));
    OW(0, 0); DS(hi = PHI(pk4, 1)); rb = cat8(lo, hi);
    OW(1, 0); LBAR(3);
    OW(2, 0); VR0(1); ADD1(ta, av[2][0], av[2][2]);
    OW(3, 0); VR1(1); ADD1(tb_, av[2][1], av[2][3]);
    OW(4, 0); LGW(2); CVT1(w0, ta, tb_);
    MO(8, 0, ra); DS(LW32(ia, 3072, w0));
    MO(9, 0, rb); DS(lo = PLO(pk3, 0));
    OW(5, 0); DS(hi = PHI(pk3, 0)); ra = cat8(lo, hi);
    OW(6, 0); DS(lo = PLO(pk3, 1));
    OW(7, 0); LGW(3); MEMB(); __builtin_amdgcn_s_barrier(); MEMB(); SB();
    OW(0, 1); VR0(2); VR1(2);
    OW(1, 1); DS(hi = PHI(pk3, 1)); rb = cat8(lo, hi);
    OW(2, 1);
    MO(6, 1, ra); DS(lo = PLO(pk4, 2));
    OW(3, 1); DS(hi = PHI(pk4, 2)); ra = cat8(lo, hi);
#define ZSW() do { asm volatile("v_permlane32_swap_b32 %0, %1" : "+v"(w0), "+v"(w1)); SB(); } while (0)
#define ZST(i_) do { const u32x2 w_ = {w0, w1}; asm volatile("buffer_store_dwordx2 %0, %1, %2, %3 offen sc1" :: "v"(w_), "v"(zvo), "s"(z_desc), "s"(zs[i_]) : "memory"); SB(); } while (0)
    OW(0, 2);
    OW(1, 2);
    MO(7, 1, rb); DS(lo = PLO(pk4, 3));
    OW(2, 2); DS(hi = PHI(pk4, 3)); rb = cat8(lo, hi); ADD2(ta, tb_, az[0]);
    OW(3, 2); LGW(2); CVT1(w0, ta, tb_); ADD1(ta, az[1][0], az[1][2]);
    MO(8, 1, ra); DS(lo = PLO(pk4, 4)); ADDCVT(w1, ta, az[1]);
    OW(6, 2); DS(hi = PHI(pk4, 4)); ra = cat8(lo, hi); ZSW();
    OW(7, 2); ZST(0); ADD2(ta, tb_, az[2]);
    OW(4, 1); LGW(2); CVT1(w0, ta, tb_); ADD1(ta, az[3][0], az[3][2]);
    MO(9, 1, rb); DS(lo = PLO(pk4, 5)); ADDCVT(w1, ta, az[3]);
    OW(5, 1); DS(hi = PHI(pk4, 5)); rb = cat8(lo, hi); ZSW();
    OW(4, 2); ZST(1); ADD2(ta, tb_, az[6]);
    OW(5, 2); LGW(2); CVT1(w0, ta, tb_); ADD1(ta, az[7][0], az[7][2]);
    MO(8, 2, ra); LGW(0); ADDCVT(w1, ta, az[7]);
    MO(9, 2, rb);
    asm volatile("v_add_f32 v2, %2, %4\n v_permlane32_swap_b32 %0, %1\n v_add_f32 v0, %3, %5\n v_cvt_pk_bf16_f32 v0, v2, v0\n v_add_f32 v2, %6, %8\n v_add_f32 v1, %7, %9\n v_cvt_pk_bf16_f32 v1, v2, v1"
                 : "+v"(w0), "+v"(w1) : "v"(az[4][0]), "v"(az[4][1]), "v"(az[4][2]), "v"(az[4][3]), "v"(az[5][0]), "v"(az[5][1]), "v"(az[5][2]), "v"(az[5][3]) : "v0", "v1", "v2", "v3");
    { const u32x2 w_ = {w0, w1};
      asm volatile("buffer_store_dwordx2 %0, %1, %2, %3 offen sc1\n v_add_f32 v3, %6, %8\n v_permlane32_swap_b32 v0, v1\n v_add_f32 v2, %7, %9\n buffer_store_dwordx2 v[0:1], %1, %2, %4 offen sc1\n v_cvt_pk_bf16_f32 v2, v3, v2\n"
                   "v_add_f32 %10, %10, %12\n v_add_f32 v3, %11, %13\n v_cvt_pk_bf16_f32 v3, %10, v3\n s_nop 1\n v_permlane32_swap_b32 v2, v3\n buffer_store_dwordx2 v[2:3], %1, %2, %5 offen sc1"
                   :: "v"(w_), "v"(zvo), "s"(z_desc), "s"(zs[3]), "s"(zs[2]), "s"(zs[4]), "v"(az[8][0]), "v"(az[8][1]), "v"(az[8][2]), "v"(az[8][3]), "v"(az[9][0]), "v"(az[9][1]), "v"(az[9][2]), "v"(az[9][3]) : "v0", "v1", "v2", "v3", "memory"); }
    SB();
    ADAPT(dly, adj);
    if (s < 63) {
      if (s + PRE < 64) {
        const unsigned e0 = half_of(ws + O_ZB, ZSLOT((s + PRE)), 0u, pol);
        st16(rs_z, evo, e0, SENT4); st16(rs_z, evo, e0 + 16u * 16384u, SENT4); st16(rs_z, lane < 32 ? evo : 0x80000000u, e0 + 32u * 16384u, SENT4);
      }
      if (wave == 3) { tsv = ts_settle(rs_ts, tss, 0u, a.d[4]); if (tsv != ~0u && (int)(tsv - tnx) > dzs + a.d[10]) tsv += (unsigned)a.d[12]; }
    }
  }
}

#define WCK 3
#define UNAP 0
__device__ __forceinline__ void r_role(const Args& a, unsigned char* lds, const int c, const int wave) {
  char* ws = a.ws; asm volatile("" : "+s"(ws));
  unsigned* const cnt = (unsigned*)(ws + O_CNT); unsigned* const tmo = cnt + CNT(C_TMO);
  char* const X1 = ws + O_X1; char* const P = ws + O_PS;
  const int lane = lane_id(), l15 = lane & 15, lq = lane >> 4;
  const unsigned pol = wait_ge<8>(cnt + CNT(C_POL), 1u, tmo) & 1u;
  const __amdgpu_buffer_rsrc_t rs_x1 = mkrs(X1 + (c >> 5) * 16384 + (c & 31) * 256), rs_p = mkrs(P + half_of(P, 0u, 0u, pol)), rs_z = mkrs(ws + O_ZB + (size_t)c * 16384);
  const __amdgpu_buffer_rsrc_t rs_ts = mkrs(ws + O_TS + half_of(ws + O_TS, 0u, 0u, pol));
  unsigned xv = (unsigned)lane * 16u; PINV(xv);
  {
    const u32x4 ones = {~0u, ~0u, ~0u, ~0u};
    static_assert(PRE == 4, "");
#pragma unroll
    for (int i = 0; i < 8; ++i) st16(rs_z, xv + (unsigned)((i & 3) * 1024), half_of(ws + O_ZB, ZSLOT(wave), 0u, pol) + (unsigned)((i >> 2) * 4096), SENT4);
    st16(rs_p, lane < 32 ? xv : 0x80000000u, (unsigned)(PSLOT(wave) + (c >> 4) * 16384 + (c & 15) * 512), SENT4);
    st16(rs_x1, lane < 16 ? xv : 0x80000000u, half_of(X1, XSLOT((wave + 1)), 0u, pol), SENT4);
    if (c < 4) st16(rs_ts, xv, (unsigned)((c >> 1) * 16384 + (c & 1) * 4096 + wave * 1024), ones);
    if (c < 24) st16(mkrs(ws + O_KM + (size_t)63 * 32 * 3072), xv, (unsigned)(c * 4096 + wave * 1024), ones);
    if (wave == 0) {
      const int q = lane >> 2, r = (q >> 2) * 2 + (q & 1);
      const unsigned v = *(const unsigned*)(a.mem0 + r * 640 + 16 * c + 4 * (lane & 3) + (q & 2));
      __builtin_amdgcn_raw_buffer_store_b32(v == SENT ? ~0u : v, rs_x1, lane * 4, (int)half_of(X1, 0u, 0u, pol), 16);
    }
    VMW(0);
    BAR();
    if (wave != 0) return;
    if (lane == 0) __hip_atomic_fetch_add((gu32*)(cnt + CNT(C_FILL)), 1u, RLX_AGENT);
  }
  const u32x4 rs_u = mkdesc(ws + O_UO);
  const __amdgpu_buffer_rsrc_t rs_pw = mkrs(cnt + CNT(C_PW) + ((c >> 1) & 3));
  int ix = (l15 & 2) ? 0xEEEE : 0x4444; PINV(ix);
  unsigned pm = (unsigned)(lq * 256 + l15 * 8);
  int xo = (4 * lq + (l15 & 3)) * 16 + (l15 >> 2) * 4; unsigned sel = (l15 & 1) ? 0x07060302u : 0x01000504u;
  int tsa = (lane & 31) * 4;
  PINV(pm); PINV(xo); PINV(sel); PINV(tsa);
  WReg W;
#define U_LOAD(sg_, def_) SFOR(i, 16) ld_w(rs_u, xv, u_off((unsigned)(sg_), (unsigned)c) + (unsigned)((i >> 1) * 4096), (i & 1) * 2048, i, (def_) && (i & 3) == 0, W); nap4<UNAP>(); SEND
  wait_ge<8>(cnt + CNT(C_FILL), 40u, tmo);
  wait_heads<8>(cnt + CNT(C_PZ) + ((c >> 1) & 3), 0, 1u, tmo);
  unsigned wl = __builtin_amdgcn_raw_buffer_load_b32(rs_pw, tsa * 32, 0, 16), tsl = 0, wn = 0; int tso = TS_OFF(lane), two = tsa * 32; PINV(tso); PINV(two); PINV(wl);
  const u32x4 ts_desc = mkdesc(ws + O_TS + half_of(ws + O_TS, 0u, 0u, pol)), pw_desc = mkdesc(cnt + CNT(C_PW) + ((c >> 1) & 3));
  U_LOAD(0, true);
  unsigned tsv = ~0u, tpub; int dlp = a.d[2]; PINS(dlp);
  { u64 t_; RT_MARK(t_); tpub = (unsigned)t_ - 4096u; }
#pragma unroll 1
  for (int s = 0; s < 63; ++s) {
    unsigned ps[5], zs[2], xs;
#pragma unroll
    for (int j = 0; j < 5; ++j) { ps[j] = (unsigned)PSLOT(s) + (unsigned)((j >> 1) * 16384 + (j & 1) * 4096); PINS(ps[j]); }
    zs[0] = half_of(ws + O_ZB, ZSLOT(s), 0u, pol); zs[1] = zs[0] + 4096u; xs = half_of(X1, XSLOT((s + 1)), 0u, pol); PINS(zs[0]); PINS(zs[1]); PINS(xs);
    int tss = TS_STEP(s); PINS(tss);
    asm volatile("s_waitcnt vmcnt(32)" : "+v"(tsl), "+v"(wn) :: "memory");
    wl = max(wl, wn);
    tsv = s < 2 ? ~0u : ts_max(tsl);
    if (__builtin_expect(s >= 2 && (tsv == ~0u || (int)(tsv - tpub) < a.d[8]), 0)) { tsv = ts_settle(rs_ts, tss, tsv, a.d[4]); }
    const bool uok = __all(wl >= (unsigned)(s + 2)), uok2 = __all(wl >= (unsigned)(s + 3));
    f32x4 o = ZERO4; PINV(o);
    unsigned tis = tsv + (unsigned)dlp; (void)tis;
#define R_LIGHT() do { const unsigned ln = (unsigned)lane_id(); light(mkrs(ws + O_ZB), ln < 32u ? zs[0] + (unsigned)c * 16384u + ln * 256u : (unsigned)(O_PS - O_ZB) + half_of(P, 0u, 0u, pol) + ps[4] + (ln - 32u) * 128u, 0u, tmo); } while (0)
    if (__builtin_expect(tsv & 1u, 0)) { R_LIGHT(); } else tis = wait_rt(tsv + (unsigned)dlp);
    unsigned adj = 0, pk, tsx; u64 nanm;
    u32x4 z[8], p[16]; u32x2 m[8];
#define R_LOAD() do { PINV(xv); PINV(pm); \
      _Pragma("unroll") for (int i = 0; i < 8; ++i) DS(z[i] = ld16(rs_z, xv + (unsigned)((i & 3) * 1024), zs[i >> 2])); \
      _Pragma("unroll") for (int i = 0; i < 16; ++i) DS(p[i] = ld16(rs_p, xv + (unsigned)((i & 3) * 1024), ps[i >> 2])); \
      _Pragma("unroll") for (int i = 0; i < 8; ++i) DS(m[i] = ld8(rs_p, pm + (unsigned)((i >> 1) * 1024 + (i & 1) * 128), ps[4])); \
      DS(tsx = __builtin_amdgcn_raw_buffer_load_b32(rs_ts, tso, tss, 16)); } while (0)
#define R_PROD() do { \
      SFOR(i, 16) SMFW(o, p[i], i); SEND; \
      _Pragma("unroll") for (int i = 0; i < 4; ++i) { const u32x4 a_ = {m[2 * i].x, m[2 * i].y, m[2 * i + 1].x, m[2 * i + 1].y}; const u32x8 b_ = cat8(z[2 * i], z[2 * i + 1]); SMFV(o, a_, b_); } \
      float ta, tb_; unsigned q; \
      asm volatile("s_nop 7\n v_add_f32 %2, %5, %7\n v_add_f32 %3, %6, %8\n v_cvt_pk_bf16_f32 %0, %2, %3\n v_cmp_u_f32_e64 %1, %2, %3\n s_nop 0\n" \
                   "v_mov_b32_dpp %4, %0 quad_perm:[1,0,3,2] row_mask:0xf bank_mask:0xf\n v_perm_b32 %0, %0, %4, %9" \
                   : "=&v"(pk), "=&s"(nanm), "=&v"(ta), "=&v"(tb_), "=&v"(q) : "v"(o[0]), "v"(o[1]), "v"(o[2]), "v"(o[3]), "v"(sel)); } while (0)
    R_LOAD();
    R_PROD();
    if (__builtin_expect(nanm != 0ull, 0)) {
      while (spin_check(adj, tmo)) {
        if (adj > 2u) R_LIGHT();
        else { const unsigned t_ = ts_max(tsx); if (t_ != ~0u) wait_rt(t_ + (unsigned)dlp); }
        R_LOAD();
        o = ZERO4; asm volatile("s_nop 1" : "+v"(o)); SB();
        R_PROD();
        if (nanm == 0ull) break;
        if ((adj & 7u) == 0u) {
          unsigned e = ~0u;
#pragma unroll
          for (int i = 0; i < 16; ++i) e = sent_min(e, p[i]);
#pragma unroll
          for (int i = 0; i < 8; ++i) e = sent_min(sent_min(e, z[i]), m[i].x, m[i].y);
          if (!__any(e == 0u)) break;
        }
      }
    }
    __builtin_amdgcn_raw_buffer_store_b32(pk, rs_x1, xo, (int)xs, 16);
    SB();
    PINU(tsx);
    { u64 t_; RT_MARK(t_); tpub = (unsigned)t_; }
    ADAPT(dlp, adj);
    if (s + PRE + 1 < 64) st16(rs_x1, lane < 16 ? xv : 0x80000000u, half_of(X1, XSLOT((s + PRE + 1)), 0u, pol), SENT4);
    if (s < 62) {
      if (__builtin_expect(!uok, 0)) { wait_heads<4>(cnt + CNT(C_PW) + ((c >> 1) & 3), 0, (unsigned)(s + 2), tmo); }
      wait_rt(tpub + (unsigned)a.d[3]);
      asm volatile("s_nop 4\n buffer_load_dword %0, %1, %2, %3 offen sc1" : "=v"(tsl) : "v"(tso), "s"(ts_desc), "s"(TS_STEP(s + 1)) : "memory");
      asm volatile("s_nop 4\n buffer_load_dword %0, %1, %2, 0 offen sc1" : "=v"(wn) : "v"(uok2 ? (int)0x80000000u : two), "s"(pw_desc) : "memory");
      U_LOAD(s + 1, false);
    }
  }
}

__device__ __forceinline__ void w_role(const Args& a, unsigned char* lds, const int h, const int wave) {
  char* ws = a.ws; asm volatile("" : "+s"(ws));
  unsigned* const cnt = (unsigned*)(ws + O_CNT); unsigned* const tmo = cnt + CNT(C_TMO);
  const int lane = lane_id(), l15 = lane & 15, lq = lane >> 4;
  WReg W;
  {
    ODMA_INIT();
    ODMA(0, 0); ODMA(1, 1); ODMA(2, 2);
#pragma unroll
    for (int i = 0; i < 5; ++i) {
      if (i < 3) VMW(24); else if (i == 3) VMW(12); else VMW(0);
      BAR();
#pragma unroll
      for (int t = 0; t < 2; ++t) {
        const unsigned ad = lds_addr(lds) + otr[t] + (unsigned)SBUF(i);
        TRO4(ad, 0, 6 * i + 3 * t); TRO4(ad, 1, 6 * i + 3 * t + 1); TRO4(ad, 2, 6 * i + 3 * t + 2);
      }
      if (i + 3 < 5) { BAR(); ODMA(i + 3, i + 3); }
    }
  }
  const __amdgpu_buffer_rsrc_t rs_u = mkrs(ws + O_UO + h * 1024);
  const __amdgpu_buffer_rsrc_t rs_pv = mkrs(cnt + CNT(C_PV + h)), rs_pw = mkrs(cnt + CNT(C_PW + h));
  unsigned xv = (unsigned)lane * 16u; int flo = lane == 0 ? 4 * wave : (int)0x80000000u; PINV(xv); PINV(flo);
  unsigned uo[10];
#pragma unroll
  for (int i = 0; i < 10; ++i) { uo[i] = (unsigned)(8 * OROT(i >> 1) + 2 * wave + (i & 1)) << 15; PINS(uo[i]); }
  const __amdgpu_buffer_rsrc_t rs_po = mkrs(cnt + CNT(C_PO)); int poo = wave == 0 ? (lane % 24) / 6 * 128 + (lane % 6) * 4 : (int)0x80000000u; PINV(poo);
  const char* vsrc = ws + O_VF + (size_t)h * 12288 + wave * 3072;
#define V_DMA(sg_) do { m0_set(lds_addr(lds) + (unsigned)(((sg_) % 3) * 12288 + wave * 3072)); const char* p_ = vsrc + KSLOT(sg_); asm volatile("s_nop 4" : "+s"(p_)); glds_x<0>(p_, xv); glds_x<1024>(p_, xv); glds_x<2048>(p_, xv); } while (0)
  BAR();
  wait_t<2>(cnt + CNT(C_PV + h), 1, 2u, tmo);
  V_DMA(1); V_DMA(2);
  VMW(3);
  int nxt = 3;
#pragma unroll 1
  for (int s = 1; s < 64; ++s) {
    unsigned ub = u_off((unsigned)s, 0u), sv = (unsigned)s, va = lds_addr(lds) + (unsigned)((s % 3) * 12288) + xv; PINS(ub); PINV(sv); PINV(va);
    f32x4 acc[2][2]; u32x4 vx[12], pw, w; unsigned sp; int vprog; bool late, far;
    BAR();
#pragma unroll
    for (int n = 0; n < 12; ++n) DS(vx[(n >> 1) + 6 * (n & 1)] = *(const LAS u32x4*)(size_t)(va + ((n >> 1) + 6 * (n & 1)) * 1024));
    SFOR(t, 11)
      SFOR(m, 12)
        constexpr int e = m >> 1, rt = m & 1, c = (t + 1) & 1;
        if (t < 10) { mf_w<false>(acc[t & 1][rt], vx[6 * rt + e], 6 * (t % 10) + e, e == 0, W); SB(); }
        else if (m == 0) { asm volatile("s_nop 7" : "+v"(acc[1][0]), "+v"(acc[1][1])); SB(); }
        if (t > 0 && m >= 2 && m < 6) { if (t < 10) FILL(); CVT1(w[(m + 2) & 3], acc[c][(m >> 1) - 1 & 1][2 * (m & 1)], acc[c][(m >> 1) - 1 & 1][2 * (m & 1) + 1]); }
        if (t > 0 && m == 6) { if (t < 10) __builtin_amdgcn_raw_buffer_store_b128(w, rs_u, (int)xv, (int)(ub + uo[(t + 9) % 10]), 16); else st16(rs_u, xv, ub + uo[9], w); SB(); }
        if (t == 0 && m == 1) { pw = ld16(rs_pv, 0u, 0u); SB(); }
        if (t == 0 && m == 4) { sp = __builtin_amdgcn_raw_buffer_load_b32(rs_po, poo, 0, 16); SB(); }
        if (t == WCK && m == 8) { vprog = (int)min_t(pw, 1); PINS(vprog); far = wave == 0 && __any((int)sp < s + 2 - WGATE); MEMB(); SB(); }
        if (t == WCK && m == 10) { __builtin_amdgcn_raw_buffer_store_b32(sv, rs_pw, flo, 0, 16); SB(); }
        if (t == WCK + 1 && m == 0) {
          late = nxt <= s + 1;
          const int upto = min(min(vprog, s + 2), 63);
          while (nxt <= upto) { V_DMA(nxt); ++nxt; }
          SB(); }
        if (t == WCK + 1 && m == 8) { pw = ld16(rs_pv, 0u, 0u); SB(); }
      SEND;
    SEND;
    if (__builtin_expect(late || far, 0)) {
      const int upto = min(min((int)min_t(pw, 1), s + 2), 63);
      while (nxt <= upto) { V_DMA(nxt); ++nxt; }
      VMW(0); __builtin_amdgcn_raw_buffer_store_b32((unsigned)(s + 1), rs_pw, flo, 0, 16);
      if (s < 63) {
        if (far) { unsigned spins = 0; while (__any((int)__builtin_amdgcn_raw_buffer_load_b32(rs_po, poo, 0, 16) < s + 2 - WGATE) && spin_check(spins, tmo)) __builtin_amdgcn_s_sleep(24); MEMB(); }
        if (nxt <= s + 1) { wait_t<1>(cnt + CNT(C_PV + h), 1, (unsigned)(s + 1), tmo); V_DMA(nxt); ++nxt; VMW(0); }
      }
    }
  }
}

#define TSTRICT 24
#define LDS16(ad_) (*(LAS u32x4*)(size_t)(ad_))
struct TReg { u32x32 b[12]; u32x16 e; };
#define TR_SW(g_, X) switch (g_) { case 0: X("a", "{a[0:31]}", T.b[0]); break; case 1: X("a", "{a[32:63]}", T.b[1]); break; case 2: X("a", "{a[64:95]}", T.b[2]); break; case 3: X("a", "{a[96:127]}", T.b[3]); break; \
    case 4: X("a", "{a[128:159]}", T.b[4]); break; case 5: X("a", "{a[160:191]}", T.b[5]); break; case 6: X("a", "{a[192:223]}", T.b[6]); break; case 7: X("a", "{a[224:255]}", T.b[7]); break; \
    case 8: X("v", "{v[110:141]}", T.b[8]); break; case 9: X("v", "{v[142:173]}", T.b[9]); break; case 10: X("v", "{v[174:205]}", T.b[10]); break; case 11: X("v", "{v[206:237]}", T.b[11]); break; default: X("v", "{v[238:253]}", T.e); break; }
__device__ __forceinline__ void mf_t(f32x4& c, const u32x4 x, const int d, const bool first, const TReg& T) {
  const int r = d < 64 ? 4 * d : 4 * d - 146;
#define X_(rf_, cs_, v_) if (first) asm volatile("v_mfma_f32_16x16x32_bf16 %0, " rf_ "[%c2:%c3], %1, 0" : "=&v"(c) : "v"(x), "n"(r), "n"(r + 3), cs_(v_)); \
      else asm volatile("v_mfma_f32_16x16x32_bf16 %0, " rf_ "[%c2:%c3], %1, %0" : "+v"(c) : "v"(x), "n"(r), "n"(r + 3), cs_(v_))
  TR_SW(d >> 3, X_);
#undef X_
}
__device__ __forceinline__ void tr_t(const unsigned ad0, const unsigned ad1, const int off, const int d, TReg& T) {
  const int r = d < 64 ? 4 * d : 4 * d - 146;
#define Y_(md_, rf_, cs_, v_) asm volatile("ds_read_b64_tr_b16 " rf_ "[%c5:%c6], %1 offset:%3\n ds_read_b64_tr_b16 " rf_ "[%c7:%c8], %1 offset:%4\n ds_read_b64_tr_b16 " rf_ "[%c9:%c10], %2 offset:%3\n ds_read_b64_tr_b16 " rf_ "[%c11:%c12], %2 offset:%4\n s_waitcnt lgkmcnt(0)" \
    : md_ cs_(v_) : "v"(ad0), "v"(ad1), "n"(off), "n"(off + 1536), "n"(r), "n"(r + 1), "n"(r + 2), "n"(r + 3), "n"(r + 4), "n"(r + 5), "n"(r + 6), "n"(r + 7) : "memory")
#define X_(rf_, cs_, v_) if ((d & 7) == 0) Y_("=&", rf_, cs_, v_); else Y_("+", rf_, cs_, v_)
  TR_SW(d >> 3, X_);
#undef X_
#undef Y_
}
__device__ __forceinline__ void t_role(const Args& a, unsigned char* lds, const int h, const int isv, const int wave) {
  char* ws = a.ws; asm volatile("" : "+s"(ws));
  unsigned* const cnt = (unsigned*)(ws + O_CNT);
  const int lane = lane_id(), l15 = lane & 15, lq = lane >> 4;
  TReg T;
#define TBUF(j_) (24576u + (unsigned)(((j_) + 3) % 6) * 20480u)
#define TSRC(c_, r_) ((((c_) - (r_) * 80u < 64u ? (r_) * 80u + (((c_) - (r_) * 80u + 16u * (unsigned)h) & 63u) : (c_)) ^ ((r_) & 15u)) * 16u)
#define TDMA1(src_, dst_, i_) do { const unsigned c = (unsigned)((wave + 4 * (i_)) * 64 + lane_id()), r = c / 80u; glds(src_, TSRC(c, r), (dst_) + (i_) * 4096u); } while (0)
#define TROW(j_) (16 * ((j_) + 6))
#define TDMA(j_) do { _Pragma("unroll") for (int xi_ = 0; xi_ < 5; ++xi_) TDMA1(a.Bx + (size_t)TROW(j_) * 640, lds_addr(lds) + TBUF(j_) + (unsigned)wave * 1024u, xi_); } while (0)
  const __amdgpu_buffer_rsrc_t rs_fl = mkrs(cnt + CNT((isv ? C_PV : C_PT_) + h)), rs_o = mkrs(ws + O_KF + (size_t)h * 12288);
  unsigned vo[2];
  {
    unsigned ad0[5], ad1[5], mvo[6], qvo[3], xvo, xr, x5;
    { TRO_INIT();
#pragma unroll
      for (int t = 0; t < 5; ++t) { const bool isq = t == 4 || wave == 3; const unsigned tile = t == 4 ? 4u + (unsigned)(wave >> 1) : (wave == 3 ? (unsigned)t : (unsigned)(4 * wave + t));
        ad0[t] = lds_addr(lds) + 32768u + TRO(tile) + (isq ? 24576u : 0u); ad1[t] = lds_addr(lds) + 32768u + (isq ? TRO(tile + 6u) + 24576u : TRO(tile) + 12288u); PINV(ad0[t]); PINV(ad1[t]); } }
#pragma unroll
    for (int q = 0; q < 6; ++q) { const unsigned c = (unsigned)((wave + 4 * q) * 64 + lane), r = c / 24u, pc = (c - r * 24u) ^ (((r >> 1) & 1u) * 2u + ((r >> 3) & 1u) * 4u);
      mvo[q] = r * 12288u + 16u * pc; PINV(mvo[q]);
      if (q < 3) { qvo[q] = pc < 12u ? mvo[q] : mvo[q] + 32u * 12288u - 192u; PINV(qvo[q]); } }
    xvo = (unsigned)((8 * wave + (lane >> 3)) * 1280 + ((lane & 7) ^ (lane >> 3)) * 16);
    xr = lds_addr(lds) + (unsigned)(l15 * 128 + (lq ^ (l15 & 7)) * 16); x5 = xr + (unsigned)((wave & 1) * 2048);
    PINV(xvo); PINV(xr); PINV(x5);
    const u16* Wm = (isv ? a.Wv : a.Wk) + 192 * h; const u16* Wh = a.Wq + 192 * h + 96 * isv;
#define TW_DMA(i_) do { const int r_ = 128 * KROT((i_) >> 1) + 64 * ((i_) & 1); const unsigned d_ = lds_addr(lds) + (unsigned)(32768 + ((i_) % 3) * 36864 + wave * 1024); \
      _Pragma("unroll") for (int q_ = 0; q_ < 2; ++q_) glds(a.Bx + (32 + 32 * q_) * 640 + r_, xvo, lds_addr(lds) + (unsigned)(((i_) & 3) * 8192 + q_ * 4096 + wave * 1024)); \
      _Pragma("unroll") for (int q_ = 0; q_ < 6; ++q_) glds(Wm + (size_t)r_ * 6144, mvo[q_], d_ + (unsigned)(q_ * 4096)); \
      _Pragma("unroll") for (int q_ = 0; q_ < 3; ++q_) glds(Wh + (size_t)r_ * 6144, qvo[q_], d_ + (unsigned)(24576 + q_ * 4096)); SB(); } while (0)
    TW_DMA(0); TW_DMA(1); TW_DMA(2);
    f32x4 pa[4][4], p5[2];
    SFOR(i, 10)
      if (i < 8) VMW(22); else if (i == 8) VMW(16); else VMW(10);
      BAR();
      SFOR(t, 5)
        if (i % 3 < 2) tr_t(ad0[t], ad1[t], (i % 3) * 36864, 10 * i + 2 * t, T); else tr_t(ad0[t] + 73728u, ad1[t] + 73728u, 0, 10 * i + 2 * t, T);
      SEND;
      if (i < 9) BAR();
      if (i + 3 < 10) TW_DMA(i + 3);
      if (i == 7) TDMA(0);
      if (i == 8) TDMA(1);
      SFOR(kk, 2)
        u32x4 x_[4], y_[2];
#pragma unroll
        for (int rt = 0; rt < 4; ++rt) x_[rt] = LDS16((xr ^ (unsigned)(kk * 64)) + (i & 3) * 8192 + rt * 2048);
#pragma unroll
        for (int g = 0; g < 2; ++g) y_[g] = LDS16((x5 ^ (unsigned)(kk * 64)) + (i & 3) * 8192 + g * 4096);
        SFOR(n, 16) mf_t(pa[n >> 2][n & 3], x_[n >> 2], 10 * i + 2 * (n & 3) + kk, i == 0 && kk == 0, T); SEND;
        SFOR(g, 2) mf_t(p5[g], y_[g], 10 * i + 8 + kk, i == 0 && kk == 0, T); SEND;
        SB();
      SEND;
    SEND;
    const int ln = lane_id();
    vo[0] = (unsigned)ln * 16u + (wave == 3 ? (unsigned)(O_QF - O_KF) + (unsigned)(3 * isv) * 1024u : (isv ? (unsigned)(O_VF - O_KF) : 0u) + (unsigned)wave * 2048u);
    vo[1] = (unsigned)ln * 16u + (unsigned)(O_QF - O_KF) + (unsigned)(3 * isv + 2) * 1024u + (unsigned)(wave >> 1) * 8u;
    PINV(vo[0]); PINV(vo[1]);
    asm volatile("s_nop 7" : "+v"(pa[3][0]), "+v"(pa[3][1]), "+v"(pa[3][2]), "+v"(pa[3][3]), "+v"(p5[0]), "+v"(p5[1]));
#pragma unroll
    for (int g = 0; g < 2; ++g) {
#pragma unroll
      for (int n = 0; n < 4; ++n) { const f32x4 c0 = pa[2 * g + (n >> 1)][2 * (n & 1)], c1 = pa[2 * g + (n >> 1)][2 * (n & 1) + 1];
        st16(rs_o, vo[0] + (unsigned)((n & 1) * 1024), KSLOT(1 + g) + (unsigned)((n >> 1) * 6144), (u32x4){cvtpk(c0[0], c0[1]), cvtpk(c0[2], c0[3]), cvtpk(c1[0], c1[1]), cvtpk(c1[2], c1[3])}); }
      st8(rs_o, vo[1], KSLOT(1 + g) + (unsigned)((wave & 1) * 6144), cvtpk(p5[g][0], p5[g][1]), cvtpk(p5[g][2], p5[g][3]));
    }
  }
  const int lane2 = lane_id();
#define lane lane2
#define l15 (lane2 & 15)
#define lq (lane2 >> 4)
  unsigned tvo[5];
#pragma unroll
  for (int i = 0; i < 5; ++i) { const unsigned c = (unsigned)((5 * wave + i) * 64 + lane), r = c / 80u; tvo[i] = TSRC(c, r) + (unsigned)(4096 - (i - 2) * 1024); PINV(tvo[i]); }
  int flo = lane == 0 ? wave * 4 : (int)0x80000000u; PINV(flo);
  unsigned xq[4];
#pragma unroll
  for (int q = 0; q < 4; ++q) { xq[q] = lds_addr(lds) + (unsigned)TBUF(0) + (unsigned)(l15 * 1280 + ((4 * q + lq) ^ l15) * 16); PINV(xq[q]); }
#define XFRT(k_) LDS16(xq[(k_) & 3] + ((k_) >> 2) * 256)
  const unsigned fla = lds_addr(lds) + 147456u;
  unsigned flw = fla + 4u * (unsigned)lane, one = 1u; PINV(flw); PINV(one);
  if (wave == 0) *(volatile LAS unsigned*)(size_t)flw = 0u;
  u32x4 xa[4]; u32x2 cv[5]; unsigned fl;
  f32x4 acc[5];
#pragma unroll
  for (int t = 0; t < 5; ++t) { acc[t] = ZERO4; cv[t] = (u32x2){0u, 0u}; }
  VMW(0);
  __builtin_amdgcn_raw_buffer_store_b32(2u, rs_fl, flo, 0, 16);
  BAR();
  TDMA(2); TDMA(3);
  xa[0] = XFRT(0); xa[1] = XFRT(1);
#define T_CVT(t_) do { unsigned c0_, c1_; \
      asm volatile("v_cvt_pk_bf16_f32 %0, %2, %3\n v_cvt_pk_bf16_f32 %1, %4, %5" : "=&v"(c0_), "=&v"(c1_) : "v"(acc[t_][0]), "v"(acc[t_][1]), "v"(acc[t_][2]), "v"(acc[t_][3])); \
      cv[t_] = (u32x2){c0_, c1_}; SB(); } while (0)
#define T_ST(i_) do { PINV(vo[0]); if ((i_) < 2) __builtin_amdgcn_raw_buffer_store_b128((u32x4){cv[2 * (i_)].x, cv[2 * (i_)].y, cv[(2 * (i_) + 1) % 5].x, cv[(2 * (i_) + 1) % 5].y}, rs_o, (int)vo[0] + (i_) * 1024, (int)sof, 16); \
      else __builtin_amdgcn_raw_buffer_store_b64(cv[4], rs_o, (int)vo[1], (int)sof, 16); SB(); } while (0)
#define T_REST(five_) do { asm volatile("s_nop 7\n s_nop 7"); T_CVT(1); T_CVT(2); T_CVT(3); T_CVT(4); T_ST(0); T_ST(1); if (five_) T_ST(2); VMW(0); } while (0)
  const char* dsrc = (const char*)(a.Bx + (size_t)TROW(4) * 640) - 4096;
  unsigned tb = TBUF(4) - 24576u, rb = TBUF(0) - 24576u;
  unsigned sof = 3u * K_STEP + 6144u, sinc = 0u - 6144u, tgt = 4u, dstep = 20480u, fv = 0u, dq = 0u;
  const unsigned dd0 = lds_addr(lds) + 24576u + (unsigned)(5 * wave + 2) * 1024u;
#define T_MF(t_) do { mf_t(acc[t_], xa[k & 3], 10 * (k >> 1) + 2 * (t_) + (k & 1), k == 0, T); SB(); } while (0)
#define T_TILE(STRICT, FIVE) do { \
    SFOR(k, 20) \
      constexpr int k2 = k + 2, kn = k2 < 20 ? k2 : k2 - 20; \
      T_MF(0); \
      if (k != 18) { xa[k2 & 3] = XFRT(kn); SB(); } \
      if (k == 0) T_CVT(1); \
      if (k == 18) { xq[0] += dq; PINV(xq[0]); SB(); }        \
      T_MF(1); \
      if (k == 0) T_CVT(2); \
      if (k == 3) { unsigned m_ = dd0 + tb; PINS(m_); asm volatile("s_mov_b32 m0, %0" :: "s"(m_) : "memory"); SB(); } \
      if (k == 16) { fl = *(const volatile LAS unsigned*)(size_t)flw; SB(); } \
      if (k == 18) {        \
        if (__builtin_expect(fs < tgt, 0)) { do fl = *(const volatile LAS unsigned*)(size_t)flw; while ((unsigned)__builtin_amdgcn_readfirstlane(fl) < tgt); } \
        MEMB(); SB(); } \
      if (k == 9) { PINS(tb); tb += 20480u; PINS(tb); SB(); } \
      if (k == 11) { PINS(rb); rb += 20480u; PINS(rb); SB(); } \
      if (k == 12) { PINS(rb); rb = rb == 6u * 20480u ? 0u : rb; PINS(rb); SB(); } \
      if (k == 13) { PINS(sof); PINS(sinc); sof = (sof + sinc) & (NK * K_STEP - 1u); PINS(sof); SB(); } \
      if (k == 15) { PINS(dstep); dstep = j < 116 ? 20480u : 0u; PINS(dstep); SB(); } \
      if (k == 10) { fv = (unsigned)(STRICT ? (j < 1 ? 4 : j + 3) : j + 2) >> 1; PINV(fv); SB(); } \
      if (k == 19) { xq[2] += dq; PINV(xq[2]); SB(); } \
      T_MF(2); \
      if (k == 0) T_CVT(3); \
      if (k == 1 || k == 2 || (k == 3 && !FIVE)) T_ST(k >= 1 && k <= 3 ? k - 1 : 0); \
      if (k == 4) glds_o<-2048>(dsrc, tvo[0]); else if (k == 5) glds_o<-1024>(dsrc, tvo[1]); else if (k == 6) glds_o<0>(dsrc, tvo[2]); else if (k == 7) glds_o<1024>(dsrc, tvo[3]); else if (k == 8) glds_o<2048>(dsrc, tvo[4]); \
      if (k == 9) { PINS(dsrc); dsrc += dstep; PINS(dsrc); SB(); } \
      if (k == 11) { PINS(tb); tb = tb == 6u * 20480u ? 0u : tb; PINS(tb); SB(); } \
      if (k == 14) { PINS(rb); dq = rb == 0u ? 0u - 5u * 20480u : 20480u; PINS(dq); SB(); } \
      if (k == 15) { PINS(sinc); sinc = sinc == 6144u ? K_STEP - 6144u : 6144u; PINS(sinc); SB(); } \
      if (k == 12) {        \
        asm volatile("s_waitcnt vmcnt(%0)" :: "n"(STRICT ? 13 : 22) : "memory"); \
        __hip_atomic_fetch_add((LAS unsigned*)(size_t)flw, one, __ATOMIC_RELAXED, __HIP_MEMORY_SCOPE_WORKGROUP); SB(); \
      } \
      if (k == 13) { __builtin_amdgcn_raw_buffer_store_b32(fv, rs_fl, flo, 0, 16); SB(); } \
      if (k == 17) { fs = (unsigned)__builtin_amdgcn_readfirstlane(fl); PINS(fs); SB(); } \
      if (k == 18) { xa[0] = XFRT(0); SB(); xq[1] += dq; PINV(xq[1]); SB(); } \
      if (k == 19) { PINS(tgt); tgt += 4u; PINS(tgt); xq[3] += dq; PINV(xq[3]); SB(); } \
      T_MF(3); \
      if (k == 0 && !FIVE) T_CVT(4); \
      if (k == 19) T_CVT(0); \
      if (FIVE) T_MF(4); \
    SEND; } while (0)
  unsigned fs;
  static_assert(TSTRICT % 2 == 0, "");
#define T_ALL(ODD) do { \
    if (ODD) { const int j = 0; T_TILE(true, false); } \
    _Pragma("unroll 1") for (int j2 = ODD; j2 < TSTRICT; j2 += 2) { \
      { const int j = j2; T_TILE(true, true); } \
      { const int j = j2 + 1; T_TILE(true, false); } } \
    _Pragma("unroll 1") for (int j2 = TSTRICT + ODD; j2 < 121; j2 += 2) { \
      { const int j = j2; T_TILE(false, true); } \
      { const int j = j2 + 1; T_TILE(false, false); } } \
    if (ODD) { const int j = 121; T_TILE(false, true); } \
    T_REST(ODD); \
    __builtin_amdgcn_raw_buffer_store_b32(64u, rs_fl, flo, 0, 16); } while (0)
  if (wave & 1) T_ALL(1); else T_ALL(0);
}
#undef lane
#undef l15
#undef lq

__device__ __forceinline__ void o_role(const Args& a, unsigned char* lds, const int j, const int wave) {
  char* ws = a.ws; asm volatile("" : "+s"(ws));
  unsigned* const cnt = (unsigned*)(ws + O_CNT); unsigned* const tmo = cnt + CNT(C_TMO);
  const int lane = lane_id(), l15 = lane & 15, lq = lane >> 4;
  const unsigned pol = wait_ge<32>(cnt + CNT(C_POL), 1u, tmo) & 1u;
  const int xq = j / 6, c0 = 10 * xq + 5 * (j & 1), s0 = (j - 6 * xq) >> 1;
  const u32x4 rs_u = mkdesc(ws + O_UO + wave * 8192);
  const __amdgpu_buffer_rsrc_t rs_z = mkrs(ws + O_ZB + (size_t)c0 * 16384 + wave * 2048), rs_p = mkrs(ws + O_PT + wave * 16384), rs_m = mkrs(ws + O_PT + 65536 + wave * 4096), rs_out = mkrs(a.out + 640 + 16 * c0);
  unsigned xv = (unsigned)lane * 16u, xh = (unsigned)lane * 8u;
  unsigned wa = (unsigned)(wave * 10880 + lq * 272 + l15 * 16), ra[2], oo[2];
#pragma unroll
  for (int i = 0; i < 2; ++i) {
    const int n = i == 0 ? lane : 64 + l15, tok = 8 * wave + n / 10, oc = n % 10;
    ra[i] = (unsigned)((((oc >> 1) * 2 + (tok >> 4)) * 4 + 2 * (oc & 1)) * 272 + (tok & 15) * 16);
    oo[i] = i == 0 || lane < 16 ? (unsigned)((tok * 1280 + 8 * oc) * 2) : 0x80000000u;
    PINV(ra[i]); PINV(oo[i]);
  }
  PINV(xv); PINV(xh); PINV(wa);
  WReg W;
#pragma unroll 1
  for (int s = s0; s < 64; s += 3) {
    if (wave == 0) {
      const unsigned tk = __hip_atomic_load((const gu32*)(cnt + CNT(C_TOK + 4 * s + xq)), RLX_AGENT);
      wait_ge<12>(cnt + CNT(C_PS + j), (unsigned)(s < 63 ? s + 1 : 63), tmo);
      if (s >= 60) { wait_heads<4>(cnt + CNT(C_PW), 1, (unsigned)(s + 1), tmo); wait_heads<4>(cnt + CNT(C_PW) + 2, 1, (unsigned)(s + 1), tmo); }
      *(volatile LAS unsigned*)(lds + 65536) = tk;
    }
    BAR();
    const bool behind = (unsigned)__builtin_amdgcn_readfirstlane(*(volatile LAS unsigned*)(lds + 65536)) >= 32u;
    const int np = behind || s >= 60 ? 0 : a.d[14];
    SFOR(i, 20) ld_w(rs_u, xv, u_off((unsigned)s, (unsigned)(c0 + (i >> 2))) + (unsigned)(((i >> 1) & 1) * 4096), (i & 1) * 2048, i, (i & 3) == 0, W); nap_rt(np); SEND;
    u32x4 z[5][2], p1[8], pm[2][2]; u32x2 p0[8];
    const unsigned zs = half_of(ws + O_ZB, ZSLOT(s), 0u, pol), ps = (unsigned)s * 81920u;
    unsigned spins = 0;
    for (;;) {
      unsigned e = ~0u;
      PINV(xv);
#pragma unroll
      for (int n = 0; n < 10; ++n) { z[n >> 1][n & 1] = ld16(rs_z, xv + (unsigned)((n & 1) * 1024), zs + (unsigned)((n >> 1) * 16384)); if (s >= 60) e = sent_min(e, z[n >> 1][n & 1]); }
      if (!__any(e == 0u) || !spin_check(spins, tmo)) break;
      __builtin_amdgcn_s_sleep(4);
    }
    if (!behind) {
      if (wave == 0) { if (s >= 60) wait_ge<1>(cnt + CNT(C_TOK + 4 * s + xq), 32u, tmo); else wait_ge<12>(cnt + CNT(C_TOK + 4 * s + xq), 32u, tmo); }
      BAR();
    }
    PINV(xv); PINV(xh);
#pragma unroll
    for (int n = 0; n < 8; ++n) { p1[n] = ld16(rs_p, xv + (unsigned)((n & 1) * 2048 + 1024), ps + (unsigned)((n >> 1) * 4096)); p0[n] = ld8(rs_p, xh + (unsigned)((n & 1) * 2048), ps + (unsigned)((n >> 1) * 4096)); SB(); }
#pragma unroll
    for (int n = 0; n < 4; ++n) pm[n >> 1][n & 1] = ld16(rs_m, xv + (unsigned)(n * 1024), ps);
    SB();
    f32x4 acc[5][2];
    SFOR(n, 8)
      u32x4 q0 = {p0[n].x, p0[n].y, 0u, 0u}; PINV(q0);
      SFOR(t, 5) mf_w<true>(acc[t][1], p1[n], 8 * t + n, n == 0, W); SB(); SEND;
      SFOR(t, 5) mf_w<true>(acc[t][0], q0, 8 * t + n, n == 0, W); SB(); SEND;
    SEND;
#pragma unroll
    for (int n = 0; n < 4; ++n)
#pragma unroll
      for (int t = 0; t < 5; ++t) asm volatile("v_mfma_f32_16x16x32_bf16 %0, %1, %2, %0" : "+v"(acc[t][n & 1]) : "v"(z[t][n >> 1]), "v"(pm[n >> 1][n & 1]));
    asm volatile("s_nop 7" : "+v"(acc[0][1]), "+v"(acc[1][1]), "+v"(acc[2][1]), "+v"(acc[3][1]), "+v"(acc[4][1]));
#pragma unroll
    for (int n = 0; n < 10; ++n) *(f32x4*)(lds + wa + (n % 5 * 2 + n / 5) * 1088) = acc[n % 5][n / 5];
    BAR();
    if (wave == 0 && lane == 0) __hip_atomic_store((gu32*)(cnt + CNT(C_PO + xq) + j - 6 * xq), (unsigned)(s + 3), RLX_AGENT);
#pragma unroll
    for (int i = 0; i < 2; ++i) {
      f32x4 v0 = *(const f32x4*)(lds + ra[i]), v1 = *(const f32x4*)(lds + ra[i] + 272);
#pragma unroll
      for (int w = 1; w < 4; ++w) { v0 += *(const f32x4*)(lds + ra[i] + w * 10880); v1 += *(const f32x4*)(lds + ra[i] + w * 10880 + 272); }
      const u32x4 w_ = {cvtpk(v0[0], v0[1]), cvtpk(v0[2], v0[3]), cvtpk(v1[0], v1[1]), cvtpk(v1[2], v1[3])};
      __builtin_amdgcn_raw_buffer_store_b128(w_, rs_out, (int)oo[i], 32 * s * 1280 * 2, 0);
      asm volatile("s_nop 1" :: "v"(w_) : "memory");
    }
  }
}

#define XPF 200
__device__ __forceinline__ void att_role(const Args& a, unsigned char* lds, const int h, const int wave) {
  char* ws = a.ws;
  unsigned* const cnt = (unsigned*)(ws + O_CNT); unsigned* const tmo = cnt + CNT(C_TMO);
  const int lane = lane_id(), l15 = lane & 15, lq = lane >> 4, par = wave >> 1;
  const __amdgpu_buffer_rsrc_t rs_q = mkrs(ws + O_QF + (size_t)h * 12288), rs_k = mkrs(ws + O_KF + (size_t)h * 12288), rs_km = mkrs(ws + O_KM + (size_t)h * 3072), rs_pt = mkrs(ws + O_PT);
  const float scale = 0.07216878364870323f, LOG2E = 1.4426950408889634f;
  unsigned tprog = 0, vprog = 0;
  {
    const __amdgpu_buffer_rsrc_t rb = __builtin_amdgcn_make_buffer_rsrc((void*)a.Bx, 0, 2048 * 1280, 0x00020000), ra = __builtin_amdgcn_make_buffer_rsrc((void*)a.Ax, 0, 2048 * 1280, 0x00020000);
    const int w16 = (h & 7) * 2 + par, w64 = h * 2 + par;
    nap_rt(XPF);
    unsigned v = 0;
#pragma unroll
    for (int i = 0; i < 4; ++i) v ^= __builtin_amdgcn_raw_buffer_load_b32(rb, lane * 128, 40960 + (i * 16 + w16) * 8192, 0);
#pragma unroll
    for (int i = 0; i < 5; ++i) v ^= __builtin_amdgcn_raw_buffer_load_b32(ra, lane * 128, (i * 64 + w64) * 8192, 0) ^ __builtin_amdgcn_raw_buffer_load_b32(rb, lane * 128, (i * 64 + w64) * 8192, 0);
    asm volatile("" :: "v"(v));
  }
#pragma unroll 1
  for (int s = par; s < 64; s += 2) {
    if (s == 0) wait_ge<32>(cnt + CNT(C_PS + h), 1u, tmo);
    { const unsigned tneed = (unsigned)s; if (tprog < tneed) tprog = wait_t<16>(cnt + CNT(C_PT_ + h), 0, tneed, tmo); if (vprog < tneed) vprog = wait_t<16>(cnt + CNT(C_PV + h), 0, tneed, tmo); }
    f32x4 sc[2][3];
    u32x4 qf[2][6];
    float mx[2];
    {
      u32x4 kf[2][6];
#pragma unroll
      for (int i = 0; i < 12; ++i) { const unsigned so = KSLOT(s) + (unsigned)((i >> 2) * 4096), vo = (unsigned)((i & 3) * 1024 + lane * 16);
        qf[i / 6][i % 6] = ld16(rs_q, vo, so); kf[i / 6][i % 6] = ld16(rs_k, vo, so); }
      SB();
#pragma unroll
      for (int q = 0; q < 2; ++q) {
        mx[q] = -3.0e38f;
#pragma unroll
        for (int t = 0; t <= q; ++t) {
          sc[q][t] = ZERO4;
#pragma unroll
          for (int kk = 0; kk < 6; ++kk) sc[q][t] = mfma(kf[t][kk], qf[q][kk], sc[q][t]);
#pragma unroll
          for (int r = 0; r < 4; ++r) {
            const float v = rbf(rbf(sc[q][t][r]) * scale);
            sc[q][t][r] = (t < q || 4 * lq + r <= l15) ? v : -3.0e38f;
            mx[q] = fmaxf(mx[q], sc[q][t][r]);
          }
        }
        PINV(mx[q]);
      }
    }
    SB();
    if (s >= 60) wait_ge<2>(cnt + CNT(C_PS + h), (unsigned)(s == 63 ? 63 : s + 1), tmo); else wait_ge<8>(cnt + CNT(C_PS + h), (unsigned)(s + 1), tmo);
    {
      u32x4 km[6];
      unsigned spins = 0;
      for (;;) {
        unsigned m = 0u;
#pragma unroll
        for (int kk = 0; kk < 6; ++kk) { km[kk] = ld16(rs_km, (unsigned)(kk * 512 + lq * 128 + ((l15 & 1) + (l15 & 2) * 2 + ((l15 >> 1) & 2)) * 16), (unsigned)s * (32u * 3072u)); m = max(m, max(km[kk].x, km[kk].z)); }
        if (__all(m != ~0u)) break;
        __builtin_amdgcn_s_sleep(3);
        if (!spin_check(spins, tmo)) break;
      }
#pragma unroll
      for (int q = 0; q < 2; ++q) {
        sc[q][2] = ZERO4;
#pragma unroll
        for (int kk = 0; kk < 6; ++kk) sc[q][2] = mfma(km[kk], qf[q][kk], sc[q][2]);
      }
    }
    asm volatile("" : "+v"(sc[0][2]), "+v"(sc[1][2]));
#pragma unroll
    for (int q = 0; q < 2; ++q) {
#pragma unroll
      for (int r = 0; r < 4; ++r) { sc[q][2][r] = rbf(rbf(sc[q][2][r]) * scale); mx[q] = fmaxf(mx[q], sc[q][2][r]); }
      const float m = max32(max16(mx[q])), nml = -m * LOG2E;
      float sum = 0.f, sm = 0.f;
#pragma unroll
      for (int t = 0; t <= q; ++t)
#pragma unroll
        for (int r = 0; r < 4; ++r) { const float e = (t < q || 4 * lq + r <= l15) ? __builtin_amdgcn_exp2f(__builtin_fmaf(sc[q][t][r], LOG2E, nml)) : 0.f; sc[q][t][r] = e; sum += e; }
#pragma unroll
      for (int r = 0; r < 4; ++r) { sc[q][2][r] = __builtin_amdgcn_exp2f(__builtin_fmaf(sc[q][2][r], LOG2E, nml)); sm += sc[q][2][r]; }
      sum = sum32(sum16(__builtin_fmaf(sm, 0.5f, sum)));
      const float inv = 1.0f / sum;
      if (q == 0) st8(rs_pt, (unsigned)(h * 2048 + lane * 8), (unsigned)s * 81920u, cvtpk(sc[0][0][0] * inv, sc[0][0][1] * inv), cvtpk(sc[0][0][2] * inv, sc[0][0][3] * inv));
      else st16(rs_pt, (unsigned)(h * 2048 + 1024 + lane * 16), (unsigned)s * 81920u, (u32x4){cvtpk(sc[1][0][0] * inv, sc[1][0][1] * inv), cvtpk(sc[1][0][2] * inv, sc[1][0][3] * inv), cvtpk(sc[1][1][0] * inv, sc[1][1][1] * inv), cvtpk(sc[1][1][2] * inv, sc[1][1][3] * inv)});
      st8(rs_pt, lq < 2 ? (unsigned)(65536 + (h >> 2) * 2048 + q * 1024 + (h & 3) * 256 + l15 * 16 + lq * 8) : 0x80000000u, (unsigned)s * 81920u, cvtpk(sc[q][2][0] * inv, sc[q][2][1] * inv), cvtpk(sc[q][2][2] * inv, sc[q][2][3] * inv));
    }
    VMW(0);
    if (lane < 4) __hip_atomic_fetch_add((gu32*)(cnt + CNT(C_TOK + 4 * s + lane)), 1u, RLX_AGENT);
  }
}

__device__ __forceinline__ float dot2bf(unsigned x, unsigned y, float c) { return __builtin_amdgcn_fdot2_f32_bf16(__builtin_bit_cast(bf16x2_t, x), __builtin_bit_cast(bf16x2_t, y), c, false); }
__device__ __forceinline__ void a_role(const Args& a, unsigned char* lds, const int j, const int wave) {
  char* ws = a.ws; asm volatile("" : "+s"(ws));
  unsigned* const cnt = (unsigned*)(ws + O_CNT); unsigned* const tmo = cnt + CNT(C_TMO);
  const int lane = lane_id(), l15 = lane & 15, lq = lane >> 4;
  const int c0 = 40 * j - 8 * (j & 1);
  u32x4 wt[3][20];
#define A_WDMA(src_, ld_, off_) do { const int ln_ = lane_id(); _Pragma("unroll") for (int q_ = 0; q_ < 15; ++q_) { const int ins = wave + 4 * q_; const unsigned c = (unsigned)(ins * 64 + ln_), r = c / 6u, pc = c - r * 6u; \
      glds(src_, r * (unsigned)((ld_) * 2) + 16u * pc, lds_addr(lds) + (unsigned)((off_) + ins * 1024)); } } while (0)
#define A_WREAD(off_) do { unsigned o_ = (unsigned)((off_) + (8 * lq + (l15 >> 2)) * 96 + 8 * (l15 & 3)); PINV(o_); \
    _Pragma("unroll") for (int t = 0; t < 3; ++t) { _Pragma("unroll") for (int kk = 0; kk < 20; ++kk) wt[t][kk] = trrd2(lds, o_ + (unsigned)(kk * 3072 + 32 * t), 384u); SB(); } } while (0)
  A_WDMA(a.Awqkv + c0, 1920, 0); A_WDMA(a.Awqkv + 640 + c0, 1920, 61440);
  VMW(0); BAR();
  if (wave < 2) A_WREAD(wave * 61440);
  BAR();
  A_WDMA(a.Awqkv + 1280 + c0, 1920, 0);
  VMW(0); BAR();
  if (wave == 2) A_WREAD(0);
  BAR();
  constexpr int LX = 0, LR = 122880, RS = 304, NR = 128;
  unsigned dvo[10];
#pragma unroll
  for (int i = 0; i < 10; ++i) { const unsigned c = (unsigned)((wave + 4 * i) * 64 + lane), r = c / 80u; dvo[i] = (c ^ (r & 15u)) * 16u; PINV(dvo[i]); }
#define A_XDMA(src_, b_) do { _Pragma("unroll") for (int xi_ = 0; xi_ < 10; ++xi_) glds(src_, dvo[xi_], lds_addr(lds) + (unsigned)(LX + ((b_) & 1) * 40960 + wave * 1024 + xi_ * 4096)); } while (0)
  unsigned xr[4];
#pragma unroll
  for (int q = 0; q < 4; ++q) xr[q] = (unsigned)(LX + l15 * 1280 + ((4 * q + lq) ^ l15) * 16);
  unsigned dv3[14];
#pragma unroll
  for (int i = 0; i < 14; ++i) { const unsigned c = (unsigned)((13 * (wave < 3 ? wave : 0) + i) * 64 + lane), r = c / 80u; dv3[i] = (c ^ (r & 15u)) * 16u + (unsigned)(4096 - (i % 7 - 3) * 1024); PINV(dv3[i]); }
  const unsigned am0 = lds_addr(lds) + (unsigned)(LX + (13 * wave + 3) * 1024);
#define A_XD3(src_, lb_, i_) do { if ((i_) % 7 == 0) m0_set(am0 + (lb_) + (unsigned)((i_) / 7 * 7168)); \
    switch ((i_) % 7) { case 0: glds_o<-3072>(src_, dv3[i_]); break; case 1: glds_o<-2048>(src_, dv3[i_]); break; case 2: glds_o<-1024>(src_, dv3[i_]); break; case 3: glds_o<0>(src_, dv3[i_]); break; \
      case 4: glds_o<1024>(src_, dv3[i_]); break; case 5: glds_o<2048>(src_, dv3[i_]); break; default: glds_o<3072>(src_, dv3[i_]); break; } } while (0)
  A_XDMA(a.Ax, 0);
  A_XDMA(a.Ax + 32 * 640, 1);
  unsigned wo_[3];
#pragma unroll
  for (int t = 0; t < 3; ++t) { const int v = 4 * t + lq - 2 * (j & 1); wo_[t] = (unsigned)(LR + l15 * RS + wave * 96 + (v < 0 ? 40 + (v + 2) * 48 : (v >= 10 ? 40 + (v - 10) * 48 : (v >= 5 ? 8 : 0) + v * 8))); }
  const __amdgpu_buffer_rsrc_t rs_oa = mkrs(ws + O_OA);
#pragma unroll 1
  for (int b = 0; b <= 64; ++b) {
    const int s = b; (void)s;
    if (b == 0) VMW(0); else if (wave < 3) VMW(14);
    BAR();
    const char* xsrc = (const char*)(a.Ax + (size_t)(32 * (b < 62 ? b + 2 : 63)) * 640) - 4096;
    const unsigned xb = (unsigned)(b % 3) * 40960u, xb2 = (unsigned)((b + 2) % 3) * 40960u;
    if (wave < 3) {
      if (b < 64) {
        unsigned xq[4];
#pragma unroll
        for (int q = 0; q < 4; ++q) { xq[q] = xr[q] + xb; PINV(xq[q]); }
#define A_XF(rt_, k_) (*(const u32x4*)(lds + xq[(k_) & 3] + (rt_) * 20480 + ((k_) >> 2) * 256))
        f32x4 acc[2][3]; u32x4 xa[3][2];
#pragma unroll
        for (int k = 0; k < 2; ++k) { xa[k][0] = A_XF(0, k); xa[k][1] = A_XF(1, k); }
#pragma unroll
        for (int k = 0; k < 20; ++k) {
          if (k + 2 < 20) { xa[(k + 2) % 3][0] = A_XF(0, k + 2); xa[(k + 2) % 3][1] = A_XF(1, k + 2); }
#pragma unroll
          for (int t = 0; t < 3; ++t) { acc[0][t] = mfma(wt[t][k], xa[k % 3][0], k == 0 ? ZERO4 : acc[0][t]); acc[1][t] = mfma(wt[t][k], xa[k % 3][1], k == 0 ? ZERO4 : acc[1][t]);
            if (t == 1 && k < 14) { asm volatile("" : "+a"(acc[0][0]), "+a"(acc[0][1]), "+a"(acc[1][0]), "+a"(acc[1][1])); A_XD3(xsrc, xb2, k < 14 ? k : 0); SB(); } }
          if (k >= 14) IL(6, 1);
          asm volatile("" : "+a"(acc[0][0]), "+a"(acc[0][1]), "+a"(acc[0][2]), "+a"(acc[1][0]), "+a"(acc[1][1]), "+a"(acc[1][2])); SB();
        }
        const unsigned ro = (unsigned)(((32 * b) & (NR - 1)) * RS);
#pragma unroll
        for (int t = 0; t < 3; ++t)
#pragma unroll
          for (int rt = 0; rt < 2; ++rt) *(u32x2*)(lds + wo_[t] + ro + rt * 16 * RS) = (u32x2){cvtpk(acc[rt][t][0], acc[rt][t][1]), cvtpk(acc[rt][t][2], acc[rt][t][3])};
      }
    } else {
      const int row = 32 * (b > 0 ? b - 1 : 0) + (lane & 31), hh = lane >> 5;
      const unsigned ho = (unsigned)(LR + 48 * hh);
      unsigned ra[8];
#pragma unroll
      for (int w = 0; w < 8; ++w) { const int p = row - 2 * w; ra[w] = ho + (unsigned)(((p > 0 ? p : 0) & (NR - 1)) * RS); }
#define A_LD10(d_, ad_) do { const u32x4 x0 = *(const u32x4*)(lds + (ad_)), x1 = *(const u32x4*)(lds + (ad_) + 16); const u32x2 x2 = *(const u32x2*)(lds + (ad_) + 32); \
        d_[0] = x0.x; d_[1] = x0.y; d_[2] = x0.z; d_[3] = x0.w; d_[4] = x1.x; d_[5] = x1.y; d_[6] = x1.z; d_[7] = x1.w; d_[8] = x2.x; d_[9] = x2.y; } while (0)
      unsigned q[10], kv[8][10], vv[8][10];
      A_LD10(q, ra[0]);
#pragma unroll
      for (int w = 0; w < 8; ++w) A_LD10(kv[w], ra[w] + 96);
#pragma unroll
      for (int w = 0; w < 8; ++w) A_LD10(vv[w], ra[w] + 192);
      SB();
      float lg[8], mx = -3.0e38f;
#pragma unroll
      for (int w = 0; w < 8; ++w) lg[w] = 0.f;
#pragma unroll
      for (int i = 0; i < 10; ++i)
#pragma unroll
        for (int w = 0; w < 8; ++w) lg[w] = dot2bf(q[i], kv[w][i], lg[w]);
#pragma unroll
      for (int w = 0; w < 8; ++w) { lg[w] = row - 2 * w >= 0 ? rbf(rbf(lg[w]) * 0.22360679774997896f) : rbf(-1000000000.0f); mx = fmaxf(mx, lg[w]); }
      float sum = 0.f;
#pragma unroll
      for (int w = 0; w < 8; ++w) { lg[w] = __expf(lg[w] - mx); sum += lg[w]; }
      const float inv = __builtin_amdgcn_rcpf(sum);
      float ov[20];
#pragma unroll
      for (int d = 0; d < 20; ++d) ov[d] = 0.f;
#pragma unroll
      for (int w = 0; w < 8; w += 2) {
        const unsigned pw = cvtpk(lg[w] * inv, lg[w + 1] * inv);
#pragma unroll
        for (int i = 0; i < 10; ++i) {
          ov[2 * i] = dot2bf(__builtin_amdgcn_perm(vv[w + 1][i], vv[w][i], 0x05040100u), pw, ov[2 * i]);
          ov[2 * i + 1] = dot2bf(__builtin_amdgcn_perm(vv[w + 1][i], vv[w][i], 0x07060302u), pw, ov[2 * i + 1]);
        }
      }
      const unsigned oo = b > 0 ? (unsigned)((row * 640 + 40 * j + 20 * hh) * 2) : 0x80000000u;
#pragma unroll
      for (int i = 0; i < 5; ++i) st8(rs_oa, oo + 8u * i, 0u, cvtpk(ov[4 * i], ov[4 * i + 1]), cvtpk(ov[4 * i + 2], ov[4 * i + 3]));
      if (b > 0 && (b & 15) == 0) { VMW(0); if (lane == 0) __hip_atomic_fetch_add((gu32*)(cnt + CNT(C_AO + (b >> 4) - 1)), 1u, RLX_AGENT); }
    }
  }
  VMW(0);
  BAR();
  {
    const int ln = lane_id(), m15 = ln & 15, mq = ln >> 4, cg = j & 3, rg = j >> 2;
    WReg W;
    {
      unsigned wv[10];
#pragma unroll
      for (int q = 0; q < 10; ++q) { const unsigned c = (unsigned)((wave + 4 * q) * 64 + ln), r = c / 20u; wv[q] = r * 1280u + (c - r * 20u) * 16u; PINV(wv[q]); }
#define A2_WDMA(i_) do { _Pragma("unroll") for (int q_ = 0; q_ < 10; ++q_) glds(a.Awo + (size_t)(128 * (i_)) * 640 + 160 * cg, wv[q_], lds_addr(lds) + (unsigned)(((i_) & 3) * 40960 + (wave + 4 * q_) * 1024)); } while (0)
      A2_WDMA(0); A2_WDMA(1); A2_WDMA(2); A2_WDMA(3);
      const unsigned rb = lds_addr(lds) + (unsigned)((8 * mq + (m15 >> 2)) * 320);
      const unsigned ad[3] = {rb + (unsigned)(64 * wave + 16 * (m15 & 3)), rb + (unsigned)(64 * wave + 16 * (m15 & 3) + 8), rb + (unsigned)(256 + 32 * (wave >> 1) + 8 * (m15 & 3))};
      SFOR(i, 5)
        if (i < 2) VMW(30); else if (i == 2) VMW(20); else if (i == 3) VMW(10); else VMW(0);
        BAR();
        SFOR(n, 6) tr4_w(ad[n >> 1] + (unsigned)((i & 3) * 40960), (n & 1) * 20480, (n & 1) * 20480 + 1280, (n & 1) * 20480 + 10240, (n & 1) * 20480 + 11520, 6 * i + n, W); SEND;
        if (i == 0) { BAR(); A2_WDMA(4); }
      SEND;
    }
    unsigned dv[10], xq[4], x5[4];
#pragma unroll
    for (int i = 0; i < 10; ++i) { const unsigned c = (unsigned)((10 * wave + i) * 64 + ln), r = c / 80u; dv[i] = (c ^ (r & 15u)) * 16u + (unsigned)(4096 - (i % 5 - 2) * 1024); PINV(dv[i]); }
    const char* src = ws + O_OA + (size_t)(512 * rg) * 1280 - 4096;
    const unsigned dm = lds_addr(lds) + (unsigned)((10 * wave + 2) * 1024);
#define A2_X(p_, lb_, i_) do { if ((i_) % 5 == 0) m0_set(dm + (lb_) + (unsigned)((i_) / 5 * 5120)); \
      switch ((i_) % 5) { case 0: glds_x<-2048>(p_, dv[i_]); break; case 1: glds_x<-1024>(p_, dv[i_]); break; case 2: glds_x<0>(p_, dv[i_]); break; case 3: glds_x<1024>(p_, dv[i_]); break; default: glds_x<2048>(p_, dv[i_]); break; } } while (0)
    const __amdgpu_buffer_rsrc_t rs_out = mkrs(a.out + (size_t)(512 * rg) * 1280 + 160 * cg);
    unsigned o2 = (unsigned)((m15 * 1280 + 32 * wave + 8 * mq) * 2), o5 = (unsigned)(((16 * (wave & 1) + m15) * 1280 + 128 + 16 * (wave >> 1) + 4 * mq) * 2); PINV(o2); PINV(o5);
    if (wave == 0) wait_ge<8>(cnt + CNT(C_AO + rg), 16u, tmo);
    BAR();
#pragma unroll
    for (int b = 0; b < 3; ++b) { const char* p_ = src + b * 40960; asm volatile("s_nop 4" : "+s"(p_));
#pragma unroll
      for (int i = 0; i < 10; ++i) A2_X(p_, (unsigned)(b * 40960), i); }
#pragma unroll 1
    for (int b = 0; b < 16; ++b) {
      if (b == 0) VMW(20); else if (b == 1) VMW(23); else VMW(26);
      BAR();
      const char* p_ = src + (b < 13 ? b + 3 : 15) * 40960; unsigned lb = (unsigned)(((b + 3) & 3) * 40960); PINS(p_); PINS(lb);
#pragma unroll
      for (int q = 0; q < 4; ++q) { xq[q] = lds_addr(lds) + (unsigned)((b & 3) * 40960 + m15 * 1280 + ((4 * q + mq) ^ m15) * 16); x5[q] = xq[q] + (unsigned)((wave & 1) * 20480); PINV(xq[q]); PINV(x5[q]); }
      f32x4 acc[5]; u32x4 xa[3][3];
#define A2_XF(n_, k_) LDS16(((n_) == 2 ? x5[(k_) & 3] : xq[(k_) & 3] + (n_) * 20480) + ((k_) >> 2) * 256)
#pragma unroll
      for (int n = 0; n < 6; ++n) xa[n / 3][n % 3] = A2_XF(n % 3, n / 3);
      SFOR(k, 20)
        SFOR(n, 5)
          mf_w<true>(acc[n], xa[k % 3][n >> 1], 12 * (k >> 2) + 4 * (n == 4 ? 2 : (n & 1)) + (k & 3), k == 0, W); SB();
          if (n < 3 && k + 2 < 20) { xa[(k + 2) % 3][n] = A2_XF(n, k + 2); SB(); }
          if (n == 3 && k >= 2 && k < 12) { A2_X(p_, lb, (k + 8) % 10); SB(); }
        SEND;
      SEND;
      asm volatile("s_nop 7" : "+v"(acc[0]), "+v"(acc[1]), "+v"(acc[2]), "+v"(acc[3]), "+v"(acc[4]));
#pragma unroll
      for (int rt = 0; rt < 2; ++rt) { const u32x4 w = {cvtpk(acc[2 * rt][0], acc[2 * rt][1]), cvtpk(acc[2 * rt][2], acc[2 * rt][3]), cvtpk(acc[2 * rt + 1][0], acc[2 * rt + 1][1]), cvtpk(acc[2 * rt + 1][2], acc[2 * rt + 1][3])};
        __builtin_amdgcn_raw_buffer_store_b128(w, rs_out, (int)o2, (32 * b + 16 * rt) * 2560, 0); asm volatile("s_nop 1" :: "v"(w) : "memory"); }
      __builtin_amdgcn_raw_buffer_store_b64((u32x2){cvtpk(acc[4][0], acc[4][1]), cvtpk(acc[4][2], acc[4][3])}, rs_out, (int)o5, 32 * b * 2560, 0);
    }
    VMW(0);
  }
}

#define CALN 6
__device__ __forceinline__ void calibrate(char* ws, int* vote, const int wave) {
  char* cal = ws + O_CAL + wave * 16384;
  unsigned best0 = ~0u, best1 = ~0u, acc = 0;
  const int lane = lane_id();
  for (int r = 0; r < CALN; ++r) {
#pragma unroll
    for (int hf = 0; hf < 2; ++hf) {
      const unsigned long long t0 = __builtin_amdgcn_s_memtime();
      if (lane == 0) acc += __hip_atomic_fetch_add((gu32*)(cal + hf * 8192 + (acc & 0u)), 1u, RLX_AGENT);
      asm volatile("s_waitcnt vmcnt(0)" ::: "memory");
      const unsigned d = (unsigned)(__builtin_amdgcn_s_memtime() - t0);
      if (hf == 0) best0 = d < best0 ? d : best0; else best1 = d < best1 ? d : best1;
    }
  }
  int dl = (int)best1 - (int)best0 + (int)(acc & 0u);
  dl = dl > 400 ? 400 : (dl < -400 ? -400 : dl);
  if ((((unsigned)((size_t)cal >> 21) ^ xcc_side()) & 1u) != 0u) dl = -dl;
  if (lane == 0) vote[wave] = dl;
  __syncthreads();
  if (wave == 0 && lane == 0) __hip_atomic_store((gu32*)((unsigned*)(ws + O_CNT) + CNT(C_POL)), vote[0] + vote[1] + vote[2] + vote[3] < 0 ? 3u : 2u, RLX_AGENT);
  __syncthreads();
}

#define TCH 32
#define XOFF 0
#define CPF 3
__global__ __launch_bounds__(256, 1) void k_mega(Args a) {
  extern __shared__ __attribute__((aligned(16))) unsigned char shm[];
  volatile LAS int* sl = (volatile LAS int*)shm;
  if (threadIdx.x >= 64) {
    u64 pc; unsigned off, len;
    asm volatile("s_getpc_b64 %0\n.Lcpf_mega:\n s_mov_b32 %1, .Lcpf_mega-_Z6k_mega4Args\n s_mov_b32 %2, _Z6k_prepPcPKcS1_S1_S1_S1_-_Z6k_mega4Args" : "=s"(pc), "=s"(off), "=s"(len));
    const __amdgpu_buffer_rsrc_t rs = __builtin_amdgcn_make_buffer_rsrc((void*)(pc - off), 0, (int)len, 0x00020000);
    const unsigned g = ((blockIdx.x >> 3) * 192u + threadIdx.x - 64u) * 16u;
    const u32x4 v0 = __builtin_amdgcn_raw_buffer_load_b128(rs, (int)g, 0, 0), v1 = __builtin_amdgcn_raw_buffer_load_b128(rs, (int)g, 98304, 0);
    asm volatile("" :: "v"(v0), "v"(v1));
  }
  if (threadIdx.x >= 64) {
    const unsigned o = (threadIdx.x - 64u) * (unsigned)(TCH * 65536) + (blockIdx.x >> 3) * 2048u;
    const unsigned v = __builtin_amdgcn_raw_buffer_load_b32(__builtin_amdgcn_make_buffer_rsrc((void*)(a.ws + O_X1), 0, (int)(O_END - O_X1), 0x00020000), (int)o, 0, 0);
    asm volatile("" :: "v"(v));
  }
  if (threadIdx.x == 0) {
    const unsigned x0 = (unsigned)__builtin_amdgcn_s_getreg((3 << 11) | 20) & 7u;
    int slot = 0;
    for (unsigned i = 0; i < 8u; ++i) {
      const unsigned x = (x0 + i) & 7u;
      const unsigned t = __hip_atomic_fetch_add((gu32*)((unsigned*)(a.ws + O_CNT) + CNT(C_SLOT + x)), 1u, RLX_AGENT);
      if (t < 32u) { slot = (int)(x * 32u + t); break; }
    }
    *sl = slot;
  }
  __syncthreads();
  const int slot = __builtin_amdgcn_readfirstlane(*sl);
  __syncthreads();
  const int xq = (slot >> 5) & 3, kq = slot & 31, wave = __builtin_amdgcn_readfirstlane(threadIdx.x >> 6);
#define ENTER __builtin_amdgcn_s_waitcnt(0)
  const int h = xq * 8 + (kq & 7);
  if (slot < 128) {
    if (kq < 8) { ENTER; s_role(a, shm, h, wave);
    } else if (kq < 16) { ENTER; z_role(a, shm, h, wave); }
    else if (kq < 26) { ENTER; r_role(a, shm, xq * 10 + kq - 16, wave); }
    else { if (slot == 26) calibrate(a.ws, (int*)shm, wave); ENTER; if (!(XOFF & 1)) o_role(a, shm, 6 * xq + kq - 26, wave); }
  } else {
    if (kq < 16) { ENTER; t_role(a, shm, h, kq >> 3, wave); }
    else if (kq < 24) { ENTER; w_role(a, shm, h, wave); }
    else if (kq < 28) { ENTER; if (!(XOFF & 4)) a_role(a, shm, xq * 4 + kq - 24, wave); }
    else { ENTER; if (!(XOFF & 2)) att_role(a, shm, xq * 8 + (kq - 28) * 2 + (wave & 1), wave); }
  }
}

#define PFW 15
#define PFN 15
constexpr int PB0 = (PFW & 1) ? 1920 / PFN : 0, PB1 = (PFW & 2) ? 1920 / PFN : 0, PB2 = (PFW & 4) ? 1920 / PFN : 0, PB3 = (PFW & 8) ? 1920 / PFN : 0, PF_B = PB0 + PB1 + PB2 + PB3;
__global__ __launch_bounds__(256) void k_prep(char* ws, const char* w0, const char* w1, const char* w2, const char* w3, const char* w4) {
  if (blockIdx.x < 15) { ((u32x4*)(ws + O_CNT))[blockIdx.x * 256 + threadIdx.x] = (u32x4){0u, 0u, 0u, 0u}; return; }
  if (blockIdx.x < 15 + PF_B) {
    const unsigned bi = blockIdx.x - 15;
    const char* w = bi < PB0 ? w0 : (bi < PB0 + PB1 ? w1 : (bi < PB0 + PB1 + PB2 ? w2 : w3));
    const unsigned bb = bi < PB0 ? bi : (bi < PB0 + PB1 ? bi - PB0 : (bi < PB0 + PB1 + PB2 ? bi - PB0 - PB1 : bi - PB0 - PB1 - PB2));
    const unsigned g = bb * (PFN * 4096u) + threadIdx.x * 16u;
    const __amdgpu_buffer_rsrc_t rs = __builtin_amdgcn_make_buffer_rsrc((void*)w, 0, 1920 * 4096, 0x00020000);
    u32x4 v[PFN];
#pragma unroll
    for (int i = 0; i < PFN; ++i) v[i] = __builtin_amdgcn_raw_buffer_load_b128(rs, (int)g, i * 4096, 0);
    __builtin_amdgcn_sched_barrier(0);
    u32x4 s = v[0];
#pragma unroll
    for (int i = 1; i < PFN; ++i) s ^= v[i];
    asm volatile("" :: "v"(s));
    return;
  }
  {
    u64 pc; unsigned off, len;
    asm volatile("s_getpc_b64 %0\n.Lcpf_prep:\n s_mov_b32 %1, .Lcpf_prep-_Z6k_mega4Args\n s_mov_b32 %2, _Z6k_prepPcPKcS1_S1_S1_S1_-_Z6k_mega4Args" : "=s"(pc), "=s"(off), "=s"(len));
    const unsigned g = min(((blockIdx.x - 15 - PF_B) * 256u + threadIdx.x) * 16u, len - 16u);
    u32x4 v; asm volatile("global_load_dwordx4 %0, %1, %2" : "=v"(v) : "v"(g), "s"(pc - off));
  }
}
extern "C" void kernel_launch(void* const* d_in, const int* in_sizes, int n_in, void* d_out, int out_size, void* d_ws, size_t ws_size, hipStream_t stream) {
  Args a;
  a.Ax = (const u16*)d_in[0]; a.Awqkv = (const u16*)d_in[1]; a.Awo = (const u16*)d_in[2]; a.Bx = (const u16*)d_in[3]; a.mem0 = (const u16*)d_in[4];
  a.Wq = (const u16*)d_in[5]; a.Wk = (const u16*)d_in[6]; a.Wv = (const u16*)d_in[7]; a.Wo = (const u16*)d_in[8];
  a.out = (u16*)d_out;
  a.ws = (char*)(((size_t)d_ws + 2 * MB - 1) & ~(size_t)(2 * MB - 1));
  if ((size_t)(a.ws - (char*)d_ws) + O_END > ws_size) return;
  const int dd[16] = {DDV};
  for (int i = 0; i < 16; ++i) a.d[i] = dd[i];
  static_assert(CNT(C_TOTAL) * 4 == 15 * 4096, "");
  k_prep<<<15 + PF_B + ((CPF & 2) ? 48 : 0), 256, 0, stream>>>(a.ws, (const char*)a.Wq, (const char*)a.Wk, (const char*)a.Wv, (const char*)a.Wo, (const char*)a.Bx);
  (void)hipFuncSetAttribute((const void*)k_mega, hipFuncAttributeMaxDynamicSharedMemorySize, 163840);
  k_mega<<<256, 256, 163840, stream>>>(a);
}
```

```cpp
#include <hip/hip_runtime.h>
#include <hip/hip_bf16.h>
#include <stdint.h>

typedef unsigned short u16;
typedef unsigned long long u64;
typedef __attribute__((ext_vector_type(4))) float f32x4;
typedef __attribute__((address_space(1))) u64 gu64;
typedef __attribute__((address_space(1))) unsigned gu32;
typedef unsigned u32x8 __attribute__((ext_vector_type(8)));
typedef unsigned u32x4 __attribute__((ext_vector_type(4)));
typedef unsigned u32x2 __attribute__((ext_vector_type(2)));
typedef __attribute__((ext_vector_type(4))) short bf16x4;
typedef __attribute__((ext_vector_type(8))) __bf16 bfv8;
typedef __attribute__((ext_vector_type(16))) __bf16 bfv16;
typedef __bf16 bf16x2_t __attribute__((ext_vector_type(2)));
typedef float f32x2_t __attribute__((ext_vector_type(2)));
typedef unsigned v2u_t __attribute__((ext_vector_type(2)));
#define RLX_AGENT __ATOMIC_RELAXED, __HIP_MEMORY_SCOPE_AGENT
#define LAS __attribute__((address_space(3)))

__device__ __forceinline__ float bf2f(u16 v) { return __uint_as_float(((unsigned)v) << 16); }
__device__ __forceinline__ u16 f2bf(float f) { return __builtin_bit_cast(u16, (__bf16)f); }
__device__ __forceinline__ float rbf(float f) { return bf2f(f2bf(f)); }
__device__ __forceinline__ unsigned cvtpk(float lo, float hi) { f32x2_t v = {lo, hi}; return __builtin_bit_cast(unsigned, __builtin_convertvector(v, bf16x2_t)); }
__device__ __forceinline__ __amdgpu_buffer_rsrc_t mkrs(const void* p) { return __builtin_amdgcn_make_buffer_rsrc((void*)p, 0, 0x7fffffff, 0x00020000); }
__device__ __forceinline__ u32x4 ld16(__amdgpu_buffer_rsrc_t rs, unsigned voff, unsigned soff) { return __builtin_amdgcn_raw_buffer_load_b128(rs, (int)voff, (int)soff, 16); }
#define OLA 0
__device__ __forceinline__ u32x4 ld16c(__amdgpu_buffer_rsrc_t rs, unsigned voff, unsigned soff) { return __builtin_amdgcn_raw_buffer_load_b128(rs, (int)voff, (int)soff, OLA); }
__device__ __forceinline__ u32x2 ld8(__amdgpu_buffer_rsrc_t rs, unsigned voff, unsigned soff) { return __builtin_amdgcn_raw_buffer_load_b64(rs, (int)voff, (int)soff, 16); }
__device__ __forceinline__ void st16(__amdgpu_buffer_rsrc_t rs, unsigned voff, unsigned soff, u32x4 w) { __builtin_amdgcn_raw_buffer_store_b128(w, rs, (int)voff, (int)soff, 16); asm volatile("s_nop 1" :: "v"(w) : "memory"); }
__device__ __forceinline__ void st8(__amdgpu_buffer_rsrc_t rs, unsigned voff, unsigned soff, unsigned lo, unsigned hi) { u32x2 w = {lo, hi}; __builtin_amdgcn_raw_buffer_store_b64(w, rs, (int)voff, (int)soff, 16); }
__device__ __forceinline__ bool full16(u32x4 v) { return ((v.x & v.y) != ~0u) & ((v.z & v.w) != ~0u); }
__device__ __forceinline__ float max16(float x) { v2u_t r = __builtin_amdgcn_permlane16_swap(__float_as_uint(x), __float_as_uint(x), false, false); return fmaxf(__uint_as_float(r[0]), __uint_as_float(r[1])); }
__device__ __forceinline__ float max32(float x) { v2u_t r = __builtin_amdgcn_permlane32_swap(__float_as_uint(x), __float_as_uint(x), false, false); return fmaxf(__uint_as_float(r[0]), __uint_as_float(r[1])); }
__device__ __forceinline__ float sum16(float x) { v2u_t r = __builtin_amdgcn_permlane16_swap(__float_as_uint(x), __float_as_uint(x), false, false); return __uint_as_float(r[0]) + __uint_as_float(r[1]); }
__device__ __forceinline__ float sum32(float x) { v2u_t r = __builtin_amdgcn_permlane32_swap(__float_as_uint(x), __float_as_uint(x), false, false); return __uint_as_float(r[0]) + __uint_as_float(r[1]); }
__device__ __forceinline__ float tred32(float a, float b) { v2u_t r = __builtin_amdgcn_permlane32_swap(__float_as_uint(a), __float_as_uint(b), false, false); return __uint_as_float(r[0]) + __uint_as_float(r[1]); }
__device__ __forceinline__ float tred16(float a, float b) { v2u_t r = __builtin_amdgcn_permlane16_swap(__float_as_uint(a), __float_as_uint(b), false, false); return __uint_as_float(r[0]) + __uint_as_float(r[1]); }
__device__ __forceinline__ unsigned ror8(unsigned x) { return (unsigned)__builtin_amdgcn_mov_dpp((int)x, 0x128, 0xf, 0xf, true); }
template <int N> __device__ __forceinline__ void nap() { if constexpr (N > 0) { if constexpr (N > 127) { __builtin_amdgcn_s_sleep(127); nap<N - 127>(); } else __builtin_amdgcn_s_sleep(N); } }
__device__ __forceinline__ unsigned half_of(const char* base, unsigned off, unsigned side, unsigned pol) {
  return off + ((((unsigned)(((size_t)base + off) >> 21) ^ side ^ pol) & 1u) << 13);
}
__device__ __forceinline__ unsigned side_map(const void* base, unsigned L, unsigned side, unsigned pol) { return half_of((const char*)base, (L >> 13) << 14, side, pol) + (L & 8191u); }
constexpr unsigned CH_SPIN_CAP = 1u << 18;
__device__ __forceinline__ bool spin_check(unsigned& spins, unsigned* tmop) {
  asm volatile("" ::: "memory");
  ++spins;
  if ((spins & 63u) == 0u) {
    gu32* tmo = (gu32*)tmop;
    if (__builtin_amdgcn_readfirstlane(__hip_atomic_load(tmo, RLX_AGENT))) return false;
    if (spins > CH_SPIN_CAP) { __hip_atomic_store(tmo, 1u, RLX_AGENT); return false; }
  }
  return true;
}
__device__ __forceinline__ unsigned xcc_side() { return ((unsigned)__builtin_amdgcn_s_getreg((3 << 11) | 20) >> 2) & 1u; }
__device__ __forceinline__ f32x4 mfma(u32x4 a, u32x4 b, f32x4 c) { return __builtin_amdgcn_mfma_f32_16x16x32_bf16(__builtin_bit_cast(bfv8, a), __builtin_bit_cast(bfv8, b), c, 0, 0, 0); }
__device__ __forceinline__ f32x4 smf(u32x4 a, u32x8 b, f32x4 c, int ix) { return __builtin_amdgcn_smfmac_f32_16x16x64_bf16(__builtin_bit_cast(bfv8, a), __builtin_bit_cast(bfv16, b), c, ix, 0, 0); }
__device__ __forceinline__ u32x4 lo4(u32x8 v) { return __builtin_shufflevector(v, v, 0, 1, 2, 3); }
__device__ __forceinline__ u32x4 hi4(u32x8 v) { return __builtin_shufflevector(v, v, 4, 5, 6, 7); }
__device__ __forceinline__ u32x8 cat8(u32x4 a, u32x4 b) { return __builtin_shufflevector(a, b, 0, 1, 2, 3, 4, 5, 6, 7); }
#define BAR() do { asm volatile("s_waitcnt lgkmcnt(0)" ::: "memory"); __builtin_amdgcn_s_barrier(); asm volatile("" ::: "memory"); } while (0)
#define SB() __builtin_amdgcn_sched_barrier(0)
#define VMW(n) asm volatile("s_waitcnt vmcnt(" #n ")" ::: "memory")
#define ZERO4 ((f32x4){0.f, 0.f, 0.f, 0.f})
__device__ __forceinline__ u32x2 trrd(const unsigned char* lds, unsigned off) { return __builtin_bit_cast(u32x2, __builtin_amdgcn_ds_read_tr16_b64_v4i16((LAS bf16x4*)(lds + off))); }
__device__ __forceinline__ u32x4 trrd2(const unsigned char* lds, unsigned off, unsigned d) { const u32x2 lo = trrd(lds, off), hi = trrd(lds, off + d); return (u32x4){lo.x, lo.y, hi.x, hi.y}; }


#include <stdlib.h>
#define SAG 16
#define DDV 84,84,10,0,30,0,127,99,105,115,25,30,20,12,1,0
#define XLN 4
#define XLS 2
struct Args {
  const u16* mem0; const u16* Wq; const u16* Wk; const u16* Wv; const u16* Wo; const u16* Bx; const u16* Ax; const u16* Awqkv; const u16* Awo;
  u16* out; char* ws;
  int d[16];
};
constexpr size_t MB = 1u << 20;
#define WOFF 0
#define NK 16
#define NU 16
#define NZ 16
#define NP 16
constexpr unsigned X1_STEP = 32768, Z_STEP = 1u << 20, K_STEP = 1u << 19;
constexpr size_t O_PROF = 0, O_CNT = (WOFF + 1) * MB, O_CAL = O_CNT + 65536, O_TS = O_CAL + 65536, O_X1 = (WOFF + 2) * MB, O_PS = O_X1 + 1 * MB, O_KM = O_PS + 1 * MB, O_PT = O_KM + 2 * MB, O_OA = O_PT + NZ * 81920,
                 O_KF = O_PT + 4 * MB, O_QF = O_KF + NK * (size_t)K_STEP, O_VF = O_QF + NK * (size_t)K_STEP, O_ZB = O_VF + NK * (size_t)K_STEP, O_UO = O_ZB + NZ * MB, O_END = O_UO + NU * 40 * (size_t)32768;
static_assert(O_ZB % (2 * MB) == 0 && NP <= 16 && NZ <= 16 && O_OA + 2048 * 1280 <= O_KF, "");
#define MSLOT(s_) (((s_) == 63 ? (unsigned)NZ : (unsigned)(s_) & (NZ - 1u)) * 98304u)
#define TSLOT(s_) (((unsigned)(s_) & (NZ - 1u)) * 81920u)
#define KSLOT(s_) (((unsigned)(s_) & (NK - 1u)) * K_STEP)
#define ZSLOT(s_) (((unsigned)(s_) & (NZ - 1u)) * Z_STEP)
#define XSLOT(s_) (((unsigned)(s_) & (NP - 1u)) * X1_STEP)
#define CNT(i) (32 * (i))
__device__ __forceinline__ unsigned u_off(const unsigned s, const unsigned c) { return ((s & (NU - 1u)) * 40u + c) << 15; }
constexpr int PRE = 4;
#define WGATE 11
static_assert(WGATE + 3 <= NK && WGATE <= NU && WGATE + PRE + 1 <= NZ && NP >= PRE + 4, "");
constexpr unsigned SENT = 0xFF81FF81u;
#define SENT4 ((u32x4){SENT, SENT, SENT, SENT})
#define PSLOT(s_) (((s_) & (NP - 1)) * 65536)
constexpr int C_SLOT = 0, C_TMO = 8, C_POL = 9, C_FILL = 10, C_PO = 12, C_PT_ = 32, C_PV = 64, C_PS = 96, C_PW = 128, C_PZ = 160, C_TOK = 192, C_AO = 464, C_TOTAL = 480;
__device__ __forceinline__ float nopk(float x) { asm("" : "+v"(x)); return x; }
__device__ __forceinline__ float fmx(float a, float b) { float big = __builtin_inff(); asm("" : "+s"(big)); return __builtin_amdgcn_fmed3f(a, b, big); }
__device__ __forceinline__ float mx16(float x) { v2u_t r = __builtin_amdgcn_permlane16_swap(__float_as_uint(x), __float_as_uint(x), false, false); return fmx(__uint_as_float(r[0]), __uint_as_float(r[1])); }
__device__ __forceinline__ float mx32(float x) { v2u_t r = __builtin_amdgcn_permlane32_swap(__float_as_uint(x), __float_as_uint(x), false, false); return fmx(__uint_as_float(r[0]), __uint_as_float(r[1])); }
__device__ __forceinline__ int lane_id() { int l = (int)__builtin_amdgcn_mbcnt_hi(~0u, __builtin_amdgcn_mbcnt_lo(~0u, 0u)); asm volatile("" : "+v"(l)); return l; }
__device__ __forceinline__ unsigned sent_min(unsigned m, unsigned x, unsigned y) { unsigned r = min(min(m, x ^ SENT), y ^ SENT); asm("" : "+v"(r)); return r; }
__device__ __forceinline__ unsigned sent_min(unsigned m, u32x4 v) { return sent_min(sent_min(m, v.x, v.y), v.z, v.w); }
#define RT_MARK(t_) asm volatile("s_memrealtime %0\n s_waitcnt lgkmcnt(0)" : "=s"(t_) :: "memory")
__device__ __forceinline__ unsigned wait_rt(unsigned target) {
  unsigned t;
  target = (unsigned)__builtin_amdgcn_readfirstlane(target);
  asm volatile("s_branch .Lwb%=\n.Lwa%=:\n s_sleep 1\n.Lwb%=:\n s_memrealtime vcc\n s_waitcnt lgkmcnt(0)\n s_sub_i32 %0, %1, vcc_lo\n s_add_i32 vcc_hi, %0, -9\n s_cmp_lt_u32 vcc_hi, 392\n s_cbranch_scc1 .Lwa%=\n"
               " s_add_i32 %0, %0, -1\n s_cmp_lt_u32 %0, 8\n s_cbranch_scc1 .Lwb%=\n s_mov_b32 %0, vcc_lo" : "=&s"(t) : "s"(target) : "vcc", "scc", "memory");
  return t;
}
__device__ __forceinline__ unsigned ts_max(unsigned v) {
  if ((unsigned)__ballot(v == ~0u)) return ~0u;
  const unsigned base = (unsigned)__builtin_amdgcn_readfirstlane(v) - 4096u;
  unsigned d = v == ~0u ? 0u : v - base;
  d = max(d, (unsigned)__builtin_amdgcn_mov_dpp((int)d, 0x121, 0xf, 0xf, true)); d = max(d, (unsigned)__builtin_amdgcn_mov_dpp((int)d, 0x122, 0xf, 0xf, true));
  d = max(d, (unsigned)__builtin_amdgcn_mov_dpp((int)d, 0x124, 0xf, 0xf, true)); d = max(d, (unsigned)__builtin_amdgcn_mov_dpp((int)d, 0x128, 0xf, 0xf, true));
  return (base + max(max((unsigned)__builtin_amdgcn_readlane(d, 0), (unsigned)__builtin_amdgcn_readlane(d, 16)), max((unsigned)__builtin_amdgcn_readlane(d, 32), (unsigned)__builtin_amdgcn_readlane(d, 48)))) & ~1u;
}
#define TS_OFF(l_) (((l_) & 31) * 256 + ((l_) >> 5) * 16384)
#define TS_STEP(s_) (((s_) & 1) * 128 + ((s_) >> 1) * 4)
__device__ __forceinline__ unsigned ts_settle(const __amdgpu_buffer_rsrc_t rs_ts, const int tss, unsigned t, const int lead) {
  for (int n = 0; n < 48; ++n) {
    unsigned now;
    if (t == ~0u) { __builtin_amdgcn_s_sleep(4); u64 t_; RT_MARK(t_); now = (unsigned)t_; } else now = wait_rt(t - (unsigned)lead);
    t = ts_max(__builtin_amdgcn_raw_buffer_load_b32(rs_ts, TS_OFF(lane_id()), tss, 16));
    if (t != ~0u && (int)(t - now) <= lead + 4) break;
  }
  return t;
}
#define ADAPT(d_, failed_) do { if (s > 3) d_ += (failed_) ? a.d[5] : -(int)((s & a.d[6]) == 0); } while (0)
__device__ __forceinline__ void nap_rt(int n) { for (int i = 0; i < n; ++i) __builtin_amdgcn_s_sleep(1); }
#define PINV(x) asm volatile("" : "+v"(x))
#define PINS(x) asm volatile("" : "+s"(x))
#define PINU(x) asm volatile("" :: "v"(x))
#define IL(nm, nv) do { _Pragma("unroll") for (int il_ = 0; il_ < (nm); ++il_) { __builtin_amdgcn_sched_group_barrier(0x008, 1, 0); __builtin_amdgcn_sched_group_barrier(0x7f6, nv, 0); } } while (0)
constexpr int L_MEM = 0, L_QI = 10240;
constexpr int L_B0 = 16384, L_B1 = 65536, L_B2 = 114688;
constexpr int L_QA = L_B1, L_XV = L_B1 + 9216, L_ST = L_B1 + 10240, L_FLG = L_B1 + 10752, L_XR = L_B1 + 12288;

#define TR8_ASM "ds_read_b64_tr_b16 %0, %8\n ds_read_b64_tr_b16 %1, %8 offset:1536\n ds_read_b64_tr_b16 %2, %8 offset:12288\n ds_read_b64_tr_b16 %3, %8 offset:13824\n" \
               "ds_read_b64_tr_b16 %4, %8 offset:24576\n ds_read_b64_tr_b16 %5, %8 offset:26112\n ds_read_b64_tr_b16 %6, %8 offset:36864\n ds_read_b64_tr_b16 %7, %8 offset:38400\n s_waitcnt lgkmcnt(0)"
__device__ __forceinline__ void tr8(const bool agpr, const unsigned ad, u32x8& f0, u32x8& f1) {
  u32x2 a, b, c, d, e, f, g, h;
  if (agpr) asm volatile(TR8_ASM : "=&a"(a), "=&a"(b), "=&a"(c), "=&a"(d), "=&a"(e), "=&a"(f), "=&a"(g), "=&a"(h) : "v"(ad) : "memory");
  else asm volatile(TR8_ASM : "=&v"(a), "=&v"(b), "=&v"(c), "=&v"(d), "=&v"(e), "=&v"(f), "=&v"(g), "=&v"(h) : "v"(ad) : "memory");
  f0 = (u32x8){a.x, a.y, b.x, b.y, c.x, c.y, d.x, d.y}; f1 = (u32x8){e.x, e.y, f.x, f.y, g.x, g.y, h.x, h.y};
}
__device__ __forceinline__ void tr8d(const bool agpr, const unsigned ad, u32x4& f0, u32x4& f1, u32x4& f2, u32x4& f3) {
  u32x2 a, b, c, d, e, f, g, h;
  if (agpr) asm volatile(TR8_ASM : "=&a"(a), "=&a"(b), "=&a"(c), "=&a"(d), "=&a"(e), "=&a"(f), "=&a"(g), "=&a"(h) : "v"(ad) : "memory");
  else asm volatile(TR8_ASM : "=&v"(a), "=&v"(b), "=&v"(c), "=&v"(d), "=&v"(e), "=&v"(f), "=&v"(g), "=&v"(h) : "v"(ad) : "memory");
  f0 = (u32x4){a.x, a.y, b.x, b.y}; f1 = (u32x4){c.x, c.y, d.x, d.y}; f2 = (u32x4){e.x, e.y, f.x, f.y}; f3 = (u32x4){g.x, g.y, h.x, h.y};
}
#define TR4_ASM "ds_read_b64_tr_b16 %0, %4 offset:%5\n ds_read_b64_tr_b16 %1, %4 offset:%6\n ds_read_b64_tr_b16 %2, %4 offset:%7\n ds_read_b64_tr_b16 %3, %4 offset:%8\n s_waitcnt lgkmcnt(0)"
template <int J> __device__ __forceinline__ void tr4(const bool agpr, const unsigned ad, u32x8& f) {
  u32x2 a, b, c, d;
  if (agpr) asm volatile(TR4_ASM : "=&a"(a), "=&a"(b), "=&a"(c), "=&a"(d) : "v"(ad), "n"(16384 * J), "n"(16384 * J + 4096), "n"(16384 * J + 8192), "n"(16384 * J + 12288) : "memory");
  else asm volatile(TR4_ASM : "=&v"(a), "=&v"(b), "=&v"(c), "=&v"(d) : "v"(ad), "n"(16384 * J), "n"(16384 * J + 4096), "n"(16384 * J + 8192), "n"(16384 * J + 12288) : "memory");
  f = (u32x8){a.x, a.y, b.x, b.y, c.x, c.y, d.x, d.y};
}
template <int J> __device__ __forceinline__ void tr4d(const bool agpr, const unsigned ad, u32x4& f0, u32x4& f1) {
  u32x2 a, b, c, d;
  if (agpr) asm volatile(TR4_ASM : "=&a"(a), "=&a"(b), "=&a"(c), "=&a"(d) : "v"(ad), "n"(16384 * J), "n"(16384 * J + 4096), "n"(16384 * J + 8192), "n"(16384 * J + 12288) : "memory");
  else asm volatile(TR4_ASM : "=&v"(a), "=&v"(b), "=&v"(c), "=&v"(d) : "v"(ad), "n"(16384 * J), "n"(16384 * J + 4096), "n"(16384 * J + 8192), "n"(16384 * J + 12288) : "memory");
  f0 = (u32x4){a.x, a.y, b.x, b.y}; f1 = (u32x4){c.x, c.y, d.x, d.y};
}
__device__ __forceinline__ void glds(const void* sbase, unsigned voff, unsigned ldsdst) {
  unsigned keep;
  asm volatile("s_mov_b32 %0, m0\n s_mov_b32 m0, %3\n s_nop 0\n global_load_lds_dwordx4 %1, %2\n s_mov_b32 m0, %0" : "=&s"(keep) : "v"(voff), "s"(sbase), "s"(ldsdst) : "memory");
}
__device__ __forceinline__ void m0_set(unsigned ldsdst) { asm volatile("s_mov_b32 m0, %0\n s_nop 0" :: "s"(ldsdst) : "memory"); }
template <int OFF> __device__ __forceinline__ void glds_o(const void* sbase, unsigned voff) { asm volatile("global_load_lds_dwordx4 %0, %1 offset:%2" :: "v"(voff), "s"(sbase), "n"(OFF) : "memory"); }
template <int OFF> __device__ __forceinline__ void glds_x(const void* sbase, unsigned voff) { asm volatile("global_load_lds_dwordx4 %0, %1 offset:%2 sc1" :: "v"(voff), "s"(sbase), "n"(OFF) : "memory"); }
__device__ __forceinline__ unsigned lds_addr(const unsigned char* p) { return (unsigned)(size_t)(const LAS unsigned char*)p; }
__device__ __forceinline__ unsigned ld_word(const unsigned* p) { return (unsigned)__builtin_amdgcn_readfirstlane(__hip_atomic_load((const gu32*)p, RLX_AGENT)); }
template <int SLEEP>
__device__ __forceinline__ unsigned wait_ge(const unsigned* p, unsigned target, unsigned* tmo) {
  unsigned spins = 0, v;
  while ((v = ld_word(p)) < target) { __builtin_amdgcn_s_sleep(SLEEP); if (!spin_check(spins, tmo)) break; }
  asm volatile("" ::: "memory");
  return v;
}
__device__ __forceinline__ unsigned min_t(const u32x4 w, const int kv) {
  unsigned v = min(min(w.x, w.y), w.z); if (!kv) v = min(v, w.w);
  return (unsigned)__builtin_amdgcn_readfirstlane(v);
}
template <int SLEEP>
__device__ __forceinline__ unsigned wait_t(const unsigned* p, const int first, unsigned target, unsigned* tmo) {
  unsigned spins = 0, v;
  for (;;) {
    v = min_t(ld16(mkrs(p), 0u, 0u), first);
    if (v >= target) break;
    __builtin_amdgcn_s_sleep(SLEEP); if (!spin_check(spins, tmo)) break;
  }
  asm volatile("" ::: "memory");
  return v;
}
template <int SLEEP>
__device__ __forceinline__ void wait_heads(const unsigned* p, const int second, unsigned target, unsigned* tmo) {
  unsigned spins = 0; const int ln = lane_id();
  while (!__all(__builtin_amdgcn_raw_buffer_load_b32(mkrs(p), (ln & 31) * 128 + (ln >> 5) * second * 4, 0, 16) >= target)) { __builtin_amdgcn_s_sleep(SLEEP); if (!spin_check(spins, tmo)) break; }
  asm volatile("" ::: "memory");
}
#define WNAP 12
template <int N> __device__ __forceinline__ void nap4() { if constexpr (N > 16) { asm volatile("s_nop 15"); nap4<N - 16>(); } else if constexpr (N > 0) asm volatile("s_nop %0" :: "n"(N - 1)); }
#define DPPM(d_, s_, ctl_) asm volatile("v_max_f32_dpp %0, %1, %1 " ctl_ " row_mask:0xf bank_mask:0xf" : "=v"(d_) : "v"(s_))
#define XOR1 "quad_perm:[1,0,3,2]"
#define XOR2 "quad_perm:[2,3,0,1]"
__device__ __forceinline__ void light(const __amdgpu_buffer_rsrc_t rs, unsigned vo, const unsigned so, unsigned* tmo, const int slow = 0) {
  unsigned spins = 0;
  for (;;) {
    const bool empty = __builtin_amdgcn_raw_buffer_load_b32(rs, (int)vo, (int)so, 16) == SENT;
    if (!__any(empty) || !spin_check(spins, tmo)) break;
    vo = empty ? vo : 0x80000000u;
    if (slow) __builtin_amdgcn_s_sleep(16); else __builtin_amdgcn_s_sleep(XLS);
  }
}
__device__ __forceinline__ void x1_light(const __amdgpu_buffer_rsrc_t rs_x1, const unsigned so, unsigned* tmo) {
  const unsigned ln = (unsigned)lane_id();
  light(rs_x1, ln < 40u ? (ln >> 5) * 16384u + (ln & 31u) * 256u + 252u : 0x80000000u, so, tmo);
}
#define DS(x) do { x; SB(); } while (0)
template <int V> struct IC { static constexpr int v = V; };
template <int I, int N, class F> __device__ __forceinline__ void sfor(F&& f) { if constexpr (I < N) { f(IC<I>{}); sfor<I + 1, N>(f); } }
#define SFOR(i_, n_) sfor<0, n_>([&](auto i_##_c) __attribute__((always_inline)) { constexpr int i_ = decltype(i_##_c)::v;
#define SEND })
typedef unsigned u32x32 __attribute__((ext_vector_type(32)));
typedef unsigned u32x16 __attribute__((ext_vector_type(16)));
struct WReg { u32x32 b[11]; u32x16 e; };
#define WR_SW(f_, X) switch ((f_) >> 2) { case 0: X("a", "{a[0:31]}", W.b[0]); break; case 1: X("a", "{a[32:63]}", W.b[1]); break; case 2: X("a", "{a[64:95]}", W.b[2]); break; case 3: X("a", "{a[96:127]}", W.b[3]); break; \
    case 4: X("a", "{a[128:159]}", W.b[4]); break; case 5: X("a", "{a[160:191]}", W.b[5]); break; case 6: X("a", "{a[192:223]}", W.b[6]); break; case 7: X("a", "{a[224:255]}", W.b[7]); break; \
    case 8: X("v", "{v[142:173]}", W.b[8]); break; case 9: X("v", "{v[174:205]}", W.b[9]); break; case 10: X("v", "{v[206:237]}", W.b[10]); break; default: X("v", "{v[238:253]}", W.e); break; }
__device__ __forceinline__ void smf_w(f32x4& c, const u32x4 a, const int f, const int ix, const WReg& W) {
  const int r = f < 32 ? 8 * f : 8 * f - 114;
#define X_(rf_, cs_, v_) asm volatile("v_smfmac_f32_16x16x64_bf16 %0, %1, " rf_ "[%c3:%c4], %2" : "+v"(c) : "v"(a), "v"(ix), "n"(r), "n"(r + 7), cs_(v_))
  WR_SW(f, X_);
#undef X_
}
template <bool WA> __device__ __forceinline__ void mf_w(f32x4& c, const u32x4 x, const int d, const bool first, const WReg& W) {
  const int r = d < 64 ? 4 * d : 4 * d - 114;
#define X_(rf_, cs_, v_) if (WA) { if (first) asm volatile("v_mfma_f32_16x16x32_bf16 %0, " rf_ "[%c2:%c3], %1, 0" : "=&v"(c) : "v"(x), "n"(r), "n"(r + 3), cs_(v_)); \
      else asm volatile("v_mfma_f32_16x16x32_bf16 %0, " rf_ "[%c2:%c3], %1, %0" : "+v"(c) : "v"(x), "n"(r), "n"(r + 3), cs_(v_)); } \
    else { if (first) asm volatile("v_mfma_f32_16x16x32_bf16 %0, %1, " rf_ "[%c2:%c3], 0" : "=&v"(c) : "v"(x), "n"(r), "n"(r + 3), cs_(v_)); \
      else asm volatile("v_mfma_f32_16x16x32_bf16 %0, %1, " rf_ "[%c2:%c3], %0" : "+v"(c) : "v"(x), "n"(r), "n"(r + 3), cs_(v_)); }
  WR_SW(d >> 1, X_);
#undef X_
}
__device__ __forceinline__ void ld_w(const u32x4 rs, const unsigned vo, const unsigned so, const int off, const int f, const bool def, WReg& W) {
  const int r = f < 32 ? 8 * f : 8 * f - 114;
#define Y_(md_, rf_, cs_, v_) asm volatile("s_nop 4\n buffer_load_dwordx4 " rf_ "[%c6:%c7], %1, %2, %3 offen offset:%4 sc1\n buffer_load_dwordx4 " rf_ "[%c8:%c9], %1, %2, %3 offen offset:%5 sc1" \
    : md_ cs_(v_) : "v"(vo), "s"(rs), "s"(so), "n"(off), "n"(off + 1024), "n"(r), "n"(r + 3), "n"(r + 4), "n"(r + 7) : "memory")
#define X_(rf_, cs_, v_) if (def) Y_("=&", rf_, cs_, v_); else Y_("+", rf_, cs_, v_)
  WR_SW(f, X_);
#undef X_
#undef Y_
}
__device__ __forceinline__ u32x4 mkdesc(const void* p) { const size_t b = (size_t)p; return (u32x4){(unsigned)b, (unsigned)(b >> 32) & 0xffffu, 0x7fffffffu, 0x00020000u}; }
__device__ __forceinline__ void tr4_w(const unsigned ad, const int o0, const int o1, const int o2, const int o3, const int f, WReg& W) {
  const int r = f < 32 ? 8 * f : 8 * f - 114;
#define Y_(md_, rf_, cs_, v_) asm volatile("ds_read_b64_tr_b16 " rf_ "[%c6:%c7], %1 offset:%2\n ds_read_b64_tr_b16 " rf_ "[%c8:%c9], %1 offset:%3\n ds_read_b64_tr_b16 " rf_ "[%c10:%c11], %1 offset:%4\n ds_read_b64_tr_b16 " rf_ "[%c12:%c13], %1 offset:%5\n s_waitcnt lgkmcnt(0)" \
    : md_ cs_(v_) : "v"(ad), "n"(o0), "n"(o1), "n"(o2), "n"(o3), "n"(r), "n"(r + 1), "n"(r + 2), "n"(r + 3), "n"(r + 4), "n"(r + 5), "n"(r + 6), "n"(r + 7) : "memory")
#define X_(rf_, cs_, v_) if ((f & 3) == 0) Y_("=&", rf_, cs_, v_); else Y_("+", rf_, cs_, v_)
  WR_SW(f, X_);
#undef X_
#undef Y_
}
#define TRS0(ad_, f_) tr4_w(ad_, 0, 1536, 12288, 13824, f_, W)
#define TRS1(ad_, f_) tr4_w(ad_, 24576, 26112, 36864, 38400, f_, W)
#define TRO4(ad_, j_, f_) tr4_w(ad_, 16384 * (j_), 16384 * (j_) + 4096, 16384 * (j_) + 8192, 16384 * (j_) + 12288, f_, W)
#define SMFV(c_, a_, b_) do { asm volatile("v_smfmac_f32_16x16x64_bf16 %0, %1, %2, %3" : "+v"(c_) : "v"(a_), "v"(b_), "v"(ix)); SB(); } while (0)
#define SMFW(c_, a_, f_) do { smf_w(c_, a_, f_, ix, W); SB(); } while (0)
#define ADD1(d_, x_, y_) do { asm volatile("v_add_f32 %0, %1, %2" : "=v"(d_) : "v"(x_), "v"(y_)); SB(); } while (0)
#define ADD2(d0_, d1_, c_) do { asm volatile("v_add_f32 %0, %2, %3\n v_add_f32 %1, %4, %5" : "=&v"(d0_), "=v"(d1_) : "v"(c_[0]), "v"(c_[2]), "v"(c_[1]), "v"(c_[3])); SB(); } while (0)
#define CVT1(d_, x_, y_) do { asm volatile("v_cvt_pk_bf16_f32 %0, %1, %2" : "=v"(d_) : "v"(x_), "v"(y_)); SB(); } while (0)
#define EPI3(d_, c_) do { float e_; asm volatile("v_add_f32 %1, %2, %3\n v_add_f32 %0, %4, %5\n v_cvt_pk_bf16_f32 %0, %1, %0" : "=&v"(d_), "=&v"(e_) : "v"(c_[0]), "v"(c_[2]), "v"(c_[1]), "v"(c_[3])); SB(); } while (0)
#define EPI4(d_, m_, c_) do { float e_, f_; asm volatile("v_add_f32 %1, %4, %5\n v_add_f32 %2, %6, %7\n v_cvt_pk_bf16_f32 %0, %1, %2\n v_cmp_u_f32_e64 %3, %1, %2" : "=&v"(d_), "=&v"(e_), "=&v"(f_), "=s"(m_) : "v"(c_[0]), "v"(c_[2]), "v"(c_[1]), "v"(c_[3])); SB(); } while (0)
#define CVTN(d_, m_, x_, y_) do { NANM(m_, x_, y_); CVT1(d_, x_, y_); } while (0)
#define ADDCVT(d_, x_, c_) do { asm volatile("v_add_f32 %0, %2, %3\n v_cvt_pk_bf16_f32 %0, %1, %0" : "=&v"(d_) : "v"(x_), "v"(c_[1]), "v"(c_[3])); SB(); } while (0)
#define LGW(n_) do { __builtin_amdgcn_s_waitcnt(0xC07F | ((n_) << 8)); SB(); } while (0)
#define LBAR(n_) do { asm volatile("s_waitcnt lgkmcnt(" #n_ ")" ::: "memory"); __builtin_amdgcn_s_barrier(); asm volatile("" ::: "memory"); SB(); } while (0)
#define FILL() do { __builtin_amdgcn_s_setprio(0); SB(); } while (0)
#define NANM(m_, x_, y_) do { asm volatile("v_cmp_u_f32_e64 %0, %1, %2" : "=s"(m_) : "v"(x_), "v"(y_)); SB(); } while (0)
#define LW32(ad_, off_, v_) (*(unsigned*)(lds + (ad_) + (off_)) = (v_))
#define MEMB() asm volatile("" ::: "memory")
#define LG0() __builtin_amdgcn_s_waitcnt(0xC07F)
#define FLAG(i_) do { MEMB(); *(volatile LAS unsigned char*)(LAS unsigned char*)(lds + fla + 4 * (i_)) = (unsigned char)snv; SB(); } while (0)
#define FLRD(i_) (*(const volatile LAS unsigned*)(LAS unsigned char*)(lds + flr + 4 * (i_)))
#define SBUF(i_) (((i_) % 3) == 0 ? L_B0 : (((i_) % 3) == 1 ? L_B1 : L_B2))
#define SDMA_INIT() unsigned svo[12]; const int sq0 = 3 * ((h >> 2) & 3); \
    _Pragma("unroll") for (int q_ = 0; q_ < 12; ++q_) { const int qr_ = q_ + sq0, ins = wave + 4 * (qr_ < 12 ? qr_ : qr_ - 12); const unsigned c = (unsigned)(ins * 64 + lane), r = c / 24u, pc = c - r * 24u, sw = ((r >> 1) & 1u) * 2u + ((r >> 3) & 1u) * 4u; \
      svo[q_] = r * 12288u + 16u * (pc ^ sw); PINV(svo[q_]); }
#define KROT(kr_) ((kr_) < 4 ? (((kr_) + h) & 3) : 4)
#define SDMA(W_m, kr_, i_) do { const char* W_ = (const char*)((W_m) + (size_t)(128 * KROT(kr_)) * 6144 + 192 * h); \
    _Pragma("unroll") for (int q_ = 0; q_ < 12; ++q_) { const int qr_ = q_ + sq0; glds(W_, svo[q_], lds_addr(lds) + (unsigned)(SBUF(i_) + (wave + 4 * (qr_ < 12 ? qr_ : qr_ - 12)) * 1024)); } SB(); } while (0)
#define SDMAQK(i_) SDMA(((i_) & 1) ? a.Wk : a.Wq, (i_) >> 1, i_)
#define TRO(tile_) (trow * 384u + (((unsigned)(2 * (tile_) + ((l15 & 3) >> 1))) ^ tswz) * 16u + (unsigned)(l15 & 1) * 8u)
#define TRO_INIT() const unsigned trow = (unsigned)(8 * lq + (l15 >> 2)), tswz = ((trow >> 1) & 1u) * 2u + ((trow >> 3) & 1u) * 4u
#define SXDMA_INIT() unsigned xvo = (unsigned)((4 * wave + lq) * 1280 + (l15 ^ ((4 * wave + lq) & 15)) * 16); PINV(xvo)
#define SXDMA(kr_, xb_) do { const char* X_ = (const char*)a.Bx + 256 * KROT(kr_); \
    glds(X_, xvo, lds_addr(lds) + (unsigned)((xb_) + wave * 1024)); glds(X_ + 16 * 1280, xvo, lds_addr(lds) + (unsigned)((xb_) + 4096 + wave * 1024)); SB(); } while (0)
#define SXRD(xb_) _Pragma("unroll") for (int kk = 0; kk < 4; ++kk) _Pragma("unroll") for (int rt = 0; rt < 2; ++rt) xa[rt][kk] = *(const u32x4*)(lds + (xb_) + rt * 4096 + l15 * 256 + ((4 * kk + lq) ^ l15) * 16)
#define MFV(c_, a_, b_, first_) do { if (first_) asm volatile("v_mfma_f32_16x16x32_bf16 %0, %1, %2, 0" : "=&v"(c_) : "v"(a_), "v"(b_)); else asm volatile("v_mfma_f32_16x16x32_bf16 %0, %1, %2, %0" : "+v"(c_) : "v"(a_), "v"(b_)); } while (0)
#define HALF(f_, hi_) ((hi_) ? hi4(f_) : lo4(f_))
#define ODMA_INIT() unsigned ovo = (unsigned)((4 * wave + lq) * 1280 + (l15 ^ (((4 * wave + lq) & 7) << 1)) * 16); PINV(ovo); \
    const unsigned orow = (unsigned)(4 * lq + (l15 >> 2)); \
    unsigned otr[2]; _Pragma("unroll") for (int t = 0; t < 2; ++t) otr[t] = orow * 256u + (((unsigned)(2 * (2 * wave + t) + ((l15 & 3) >> 1))) ^ ((orow & 7u) << 1)) * 16u + (unsigned)(l15 & 1) * 8u
#define OROT(cr_) (((cr_) + h) % 5)
#define ODMA(cr_, i_) do { const char* W_ = (const char*)(a.Wo + (size_t)(192 * h) * 640) + 256 * OROT(cr_); \
    _Pragma("unroll") for (int q_ = 0; q_ < 12; ++q_) glds(W_ + q_ * (16 * 1280), ovo, lds_addr(lds) + (unsigned)(SBUF(i_) + (wave + 4 * q_) * 1024)); SB(); } while (0)

#define KXL 8
#define SAL 2
#define SOF 0
#define MNAP 0
#define SF0(m_, t_, k_) (6 * (2 * ((k_) >> 1) + (m_)) + 2 * (t_) + ((k_) & 1))
#define SFR(m_, t_, k_) (SF0(m_, t_, k_) - (SF0(m_, t_, k_) > 39) - (SF0(m_, t_, k_) > 41))
constexpr int L_KX = L_B1 + 28672;
template <int wave>
__device__ __forceinline__ void s_loop(const Args& a, unsigned char* lds, const int h, const unsigned pol, const WReg& W) {
  constexpr int KIND = wave < 2 ? 0 : wave - 1;
  char* ws = a.ws; asm volatile("" : "+s"(ws));
  unsigned* const cnt = (unsigned*)(ws + O_CNT); unsigned* const tmo = cnt + CNT(C_TMO);
  const int lane = lane_id(), l15 = lane & 15, lq = lane >> 4;
  char* const X1 = ws + O_X1; char* const P = ws + O_PS;
  u32x8 kxf[3];
  if (KIND == 0) {
#pragma unroll
    for (int t = 0; t < 3; ++t) kxf[t] = cat8(*(const u32x4*)(lds + L_KX + wave * 6144 + t * 2048 + lane * 16), *(const u32x4*)(lds + L_KX + wave * 6144 + t * 2048 + 1024 + lane * 16));
  }
  const __amdgpu_buffer_rsrc_t rs_x1 = mkrs(X1), rs_p = mkrs(P), rs_kf = mkrs(ws + O_KF);
  unsigned tprog = 0;
#define KX_LOAD(sg_) do { PINV(xv); const unsigned kb_ = KSLOT(sg_) + (unsigned)(h * 12288 + wave * 6144); \
    _Pragma("unroll") for (int t_ = 0; t_ < 3; ++t_) kxf[t_] = cat8(ld16(rs_kf, xv + (unsigned)((t_ & 1) * 2048), kb_ + (unsigned)((t_ >> 1) * 4096)), ld16(rs_kf, xv + (unsigned)((t_ & 1) * 2048 + 1024), kb_ + (unsigned)((t_ >> 1) * 4096))); } while (0)
  unsigned trx = (unsigned)(L_XR + wave * 4096 + lane * 16); PINV(trx);
  unsigned trq = (unsigned)(L_B2 + wave * 12288 + lane * 16); PINV(trq);
  unsigned trk = (unsigned)(L_B0 + wave * 12288 + lane * 16); PINV(trk);
  int ix = (l15 & 2) ? 0xEEEE : 0x4444; PINV(ix);
  const float CS = 0.07216878364870323f * 1.4426950408889634f;
  unsigned xv = (unsigned)lane * 16u;
  unsigned ib = (unsigned)(L_QI + (16 * wave + l15) * 16 + lq * 4);
  unsigned ia = (unsigned)(L_QA + wave * 384 + ((l15 >> 2) & 1) * 128 + (l15 >> 3) * 64 + (l15 & 1) * 32 + lq * 8 + ((l15 >> 1) & 1) * 4);
  unsigned rda = (unsigned)(L_QA + (lq >> 1) * 768 + (l15 >> 3) * 384 + (lq & 1) * 64 + ((l15 >> 2) & 1) * 32 + (l15 & 3) * 8);
  unsigned qk_rd = (unsigned)(L_QI + (4 * lq + (l15 >> 2)) * 16 + (l15 & 1) * 8);
  unsigned flr = (unsigned)L_FLG;
  unsigned xva = KIND == 0 ? (unsigned)(L_XV + lq * 256 + (l15 >> 1) * 16 + wave * 8 + (l15 & 1) * 4) : (unsigned)(L_XV + lane * 16);
  PINV(xv); PINV(ib); PINV(ia);
  if (KIND != 2) { PINV(rda); PINV(xva); } if (KIND == 1) PINV(qk_rd); if (KIND != 0) PINV(flr);
  const bool hi8 = l15 >= 8;
  const __amdgpu_buffer_rsrc_t rs_ts = mkrs(ws + O_TS + half_of(ws + O_TS, 0u, 0u, pol));
  int tso = lane == 0 ? 256 * h : (int)0x80000000u; if (KIND != 0) PINV(tso);
  int tsa = TS_OFF(lane); if (KIND == 2) PINV(tsa);
  const __amdgpu_buffer_rsrc_t rs_ps = mkrs(cnt + CNT(C_PS) - 32 * h);
  unsigned po = side_map(P, (unsigned)(h * 512 + ((l15 >> 2) & 1) * 256 + (4 * lq + 2 * (l15 & 1) + (l15 >> 3)) * 16 + ((l15 >> 1) & 1) * 8), 0u, pol);
  unsigned pm = !(l15 & 1) ? side_map(P, (unsigned)(16384 + h * 128 + (4 * lq + 2 * ((l15 >> 2) & 1) + (l15 >> 3)) * 8 + ((l15 >> 1) & 1) * 4), 0u, pol) : 0x80000000u;
  u32x4 p_desc = mkdesc(P); if (KIND == 1) PINS(p_desc);
  unsigned pfo = lane < 32 ? side_map(P, (unsigned)(h * 512 + lane * 16), 0u, pol) : (lane < 40 ? side_map(P, (unsigned)(16384 + h * 128 + (lane - 32) * 16), 0u, pol) : 0x80000000u);
  if (KIND == 1) { PINV(po); PINV(pm); }
  if (KIND == 1) PINV(pfo);
  unsigned dur = 130, tnx = 0, tpr = 0, adj = 0; int dly = a.d[0]; PINS(dly);
#define MQ(t_, f_, w_) SMFV(aq[t_], f_, w_)
#define MK(t_, f_, w_) SMFV(ak[t_], f_, w_)
#define QW(t_, k_) SMFW(aq[t_], A[k_], SFR(0, t_, k_))
#define KW(t_, k_) SMFW(ak[t_], A[k_], SFR(1, t_, k_))
#define RLO(tb_, t_, k2_) (*(const u32x4*)(lds + (tb_) + (t_) * 4096 + (k2_) * 2048))
#define RHI(tb_, t_, k2_) (*(const u32x4*)(lds + (tb_) + (t_) * 4096 + (k2_) * 2048 + 1024))
#define RFR(tb_, t_, k2_) cat8(RLO(tb_, t_, k2_), RHI(tb_, t_, k2_))
#define XLO(j_) (*(const u32x4*)(lds + trx + (j_) * 2048))
#define XHI(j_) (*(const u32x4*)(lds + trx + (j_) * 2048 + 1024))
#define ALD(k_) DS(A[k_] = ld16(rs_x1, xv + (unsigned)(((k_) & 1) * 1024), sx[(k_) >> 1]))
#define APACE 10
#define ALP(k_) do { ALD(k_); nap4<APACE>(); SB(); } while (0)
#define A_LOAD() do { PINV(xv); ALP(0); ALP(8); ALP(1); ALP(9); ALP(2); ALP(7); ALP(3); ALP(4); ALP(5); ALD(6); SB(); } while (0)
  u32x4 ts_desc = mkdesc(ws + O_TS + half_of(ws + O_TS, 0u, 0u, pol) + 256 * h);
  unsigned fad = lds_addr(lds) + (unsigned)L_FLG; PINV(fad); PINS(ts_desc);
  if (KIND == 2) LW32(flr, 24, dur);
#pragma unroll 1
  for (int s = 0; s < 64; ++s) {
    int pss = PSLOT(s), tss = TS_STEP(s), tsn = TS_STEP(s + 1); PINS(pss); PINS(tss); if (KIND == 2) PINS(tsn);
    unsigned snp = (unsigned)(s + 1) * 0x01010101u, snv = snp, tries = 0; PINS(snp); PINV(snv);
    f32x4 sc = ZERO4; if (KIND != 2) PINV(sc);
    unsigned sx[5];
    { const unsigned so = half_of(X1, XSLOT(s), 0u, pol);
#pragma unroll
      for (int j = 0; j < 5; ++j) { sx[j] = so + (j < 4 ? (unsigned)((j + h) & 3) * 2048u : 16384u); PINS(sx[j]); }
      if (KIND == 2 && s == 1) x1_light(rs_x1, so, tmo); }
    SB();
    asm volatile("s_branch .Lsre%c6\n"
                 ".Lsbad%c6:\n s_add_u32 %0, %0, 1\n s_cmp_lt_u32 %0, 4\n s_cbranch_scc1 .Lsnow%c6\n s_sleep 8\n"
                 ".Lsnow%c6:\n s_memrealtime vcc\n ds_write_b32 %5, %1 offset:28\n ds_read_b32 %1, %5 offset:24\n s_waitcnt lgkmcnt(0)\n v_add_u32 %1, vcc_lo, %1\n v_and_b32 %1, -2, %1\n"
                 " s_mov_b64 exec, 1\n buffer_store_dword %1, off, %3, %4 sc1\n s_mov_b64 exec, -1\n v_mov_b32 %1, %2\n"
                 ".Lsre%c6:" : "+s"(tries) : "v"(snv), "s"(snp), "s"(ts_desc), "s"(tss), "v"(fad), "n"(wave) : "vcc", "scc", "memory");
    SB();
    u32x8 ra = RFR(trq, 0, 0), rb = RFR(trq, 1, 0), rc = RFR(trq, 2, 0);
    f32x4 aq[3] = {ZERO4, ZERO4, ZERO4}, ak[3] = {ZERO4, ZERO4, ZERO4};
    asm volatile("s_cmp_lg_u32 %6, 0\n s_cbranch_scc1 .Lsgo%c7" : "+v"(aq[0]), "+v"(aq[1]), "+v"(aq[2]), "+v"(ak[0]), "+v"(ak[1]), "+v"(ak[2]) : "s"(tries), "n"(wave) : "scc", "memory");
    SB();
    unsigned tis = 0, tsl = 0; bool calm = false;
    asm volatile(".p2align %c0\n .fill %c1, 4, 0xbf800000" :: "n"(SAL), "n"(SOF));
    if (KIND == 2) tis = wait_rt(tnx);
    BAR();
    asm volatile(".Lsgo%c0:" :: "n"(wave) : "memory");
    SB();
    u32x4 A[10], lo, hi;
    A_LOAD();
    unsigned w0;
    float ta, tb_;
    u64 nanm;
    QW(0, 0); QW(1, 0); QW(2, 0); KW(0, 0);
    MQ(0, A[8], ra); DS(lo = RLO(trq, 0, 1));
    KW(1, 0); DS(hi = RHI(trq, 0, 1)); ra = cat8(lo, hi);
    MQ(1, A[8], rb); DS(lo = RLO(trq, 1, 1));
    KW(2, 0); DS(hi = RHI(trq, 1, 1)); rb = cat8(lo, hi);
    QW(0, 1);
    MQ(2, A[8], rc); DS(lo = RLO(trq, 2, 1));
    QW(1, 1); DS(hi = RHI(trq, 2, 1)); rc = cat8(lo, hi);
    QW(2, 1);
    MQ(0, A[9], ra); DS(lo = RLO(trk, 0, 0));
    KW(0, 1); DS(hi = RHI(trk, 0, 0)); ra = cat8(lo, hi);
    MQ(1, A[9], rb); DS(lo = XLO(0));
    KW(1, 1); DS(hi = XHI(0)); rb = cat8(lo, hi);
    QW(0, 2);
    MQ(2, A[9], rc); DS(lo = XLO(1));
    QW(1, 2); DS(hi = XHI(1)); rc = cat8(lo, hi);
    QW(2, 2);
    MK(0, A[8], ra); DS(lo = RLO(trk, 1, 0));
    QW(0, 7); DS(hi = RHI(trk, 1, 0)); ra = cat8(lo, hi);
    MQ(1, A[7], rb); DS(lo = RLO(trk, 0, 1));
    KW(0, 2); DS(hi = RHI(trk, 0, 1)); rb = cat8(lo, hi);
    QW(0, 3);
    MQ(2, A[7], rc); DS(lo = RLO(trk, 2, 0));
    QW(1, 3); DS(hi = RHI(trk, 2, 0)); rc = cat8(lo, hi);
    QW(2, 3);
    QW(0, 4);
    MK(1, A[8], ra); DS(lo = RLO(trk, 1, 1));
    QW(1, 4); DS(hi = RHI(trk, 1, 1)); ra = cat8(lo, hi);
    QW(2, 4);
    QW(0, 5);
    MK(0, A[9], rb); DS(lo = RLO(trk, 2, 1));
    QW(1, 5); DS(hi = RHI(trk, 2, 1)); rb = cat8(lo, hi);
    QW(2, 5);
    QW(0, 6); QW(1, 6); QW(2, 6); LG0(); SB();
    KW(0, 3); FILL(); ADD1(ta, aq[0][0], aq[0][2]);
    KW(0, 4); FILL(); ADD1(tb_, aq[0][1], aq[0][3]);
    KW(0, 5); FILL(); CVTN(w0, nanm, ta, tb_);
    KW(0, 6); DS(LW32(ia, 0, w0)); ADD1(ta, aq[1][0], aq[1][2]);
    KW(0, 7); ADD1(tb_, aq[1][1], aq[1][3]);
    KW(1, 2);
    if (__builtin_expect(nanm != 0ull, 0)) {
      unsigned m = ~0u;
#define S_BACK() asm volatile("v_cmp_eq_u32 vcc, 0, %0\n s_cmp_lt_u32 %1, %3\n s_cselect_b64 vcc, vcc, 0\n s_cbranch_vccnz .Lsbad%c2" : "+v"(m) : "s"(tries), "n"(wave), "n"(CH_SPIN_CAP) : "vcc", "scc")
#pragma unroll
      for (int k = 0; k < 10; k += 2) m = sent_min(m, A[k].x, A[k + 1].x);
      S_BACK();
#pragma unroll
      for (int k = 0; k < 10; ++k) m = sent_min(m, A[k]);
      S_BACK();
    }
    asm volatile("" :: "s"(sx[0]), "s"(sx[1]), "s"(sx[2]), "s"(sx[3]), "s"(sx[4]), "s"(tries), "s"(tss), "s"(snp), "s"(ts_desc), "v"(fad), "v"(snv));
    CVT1(w0, ta, tb_);
    KW(1, 3); DS(LW32(ia, 1536, w0)); ADD1(ta, aq[2][0], aq[2][2]);
    KW(1, 4); FILL(); ADD1(tb_, aq[2][1], aq[2][3]);
    KW(1, 5); FILL(); CVT1(w0, ta, tb_);
    KW(1, 6); DS(LW32(ia, 3072, w0)); ADD1(ta, ak[0][0], ak[0][2]);
    KW(1, 7); FILL(); ADD1(tb_, ak[0][1], ak[0][3]);
    MK(1, A[9], ra); CVT1(w0, ta, tb_);
    KW(2, 1); DS(LW32(ib, 0, w0));
    u32x2 g[6];
#define QAR(i_) do { if (KIND != 2) DS(g[i_] = trrd(lds, rda + (unsigned)(((i_) >> 1) * 1536 + ((i_) & 1) * 128))); else if ((i_) >= 1 && (i_) <= 3) FILL(); } while (0)
#define QA(t_) ((u32x4){g[2 * (t_)].x, g[2 * (t_)].y, g[2 * (t_) + 1].x, g[2 * (t_) + 1].y})
    KW(2, 2); LBAR(1); QAR(0);
    KW(2, 3); QAR(1); ADD1(ta, ak[1][0], ak[1][2]);
    KW(2, 4); QAR(2); ADD1(tb_, ak[1][1], ak[1][3]);
    KW(2, 5); QAR(3); CVT1(w0, ta, tb_);
    KW(2, 6); DS(LW32(ib, 1024, w0));
    KW(2, 7); QAR(4); QAR(5);
    if (KIND == 2) DS(tsl = __builtin_amdgcn_raw_buffer_load_b32(rs_ts, tsa, tss, 16));
    MK(2, A[8], rc);
    MK(2, A[9], rb);
    if (KIND == 0) {
      float la, lb;
      { const u32x4 q_ = QA(0); SMFV(sc, q_, kxf[0]); } LBAR(2);
      { const u32x4 q_ = QA(1); SMFV(sc, q_, kxf[1]); }
      { const u32x4 q_ = QA(2); SMFV(sc, q_, kxf[2]); }
      EPI3(w0, ak[2]); DS(LW32(ib, 2048, w0));
      asm volatile("s_nop 3\n v_add_f32 %0, %2, %3\n v_add_f32 %1, %4, %5" : "=&v"(la), "=v"(lb) : "v"(sc[0]), "v"(sc[2]), "v"(sc[1]), "v"(sc[3])); SB();
      DS(LW32(xva, 0, __float_as_uint(la)); LW32(xva, 128, __float_as_uint(lb)));
      LBAR(1); LBAR(0);
    } else if (KIND == 2) {
      LBAR(0);
      asm volatile("s_nop 5"); SB();
      EPI3(w0, ak[2]); DS(LW32(ib, 2048, w0));
      LBAR(0); LBAR(0);
      tsl = ts_max(tsl);
      calm = s > 1 && tsl != ~0u && (int)(tsl - tpr) <= a.d[10];
      if (calm && s < 63) __builtin_amdgcn_raw_buffer_store_b32((tsl + (unsigned)dly + dur) & ~1u, rs_ts, tso, tsn, 16);
    } else {
      u32x2 kb[12];
#define KBR(i_) DS(kb[i_] = trrd(lds, qk_rd + (unsigned)((i_) * 256)))
#define ML(t_) do { const u32x4 a_ = QA(t_); \
        const u32x8 b_ = {kb[4 * (t_)].x, kb[4 * (t_)].y, kb[4 * (t_) + 1].x, kb[4 * (t_) + 1].y, kb[4 * (t_) + 2].x, kb[4 * (t_) + 2].y, kb[4 * (t_) + 3].x, kb[4 * (t_) + 3].y}; SMFV(sc, a_, b_); } while (0)
      LBAR(2);
      KBR(0); KBR(1); KBR(2); KBR(3); KBR(4); KBR(5);
      EPI3(w0, ak[2]); DS(LW32(ib, 2048, w0));
      KBR(6); KBR(7);
      LBAR(2);
      KBR(8); KBR(9); KBR(10); KBR(11);
      ML(0);
      asm volatile("s_barrier" ::: "memory"); SB();
      f32x4 t; DS(t = *(const f32x4*)(lds + xva));
      ML(1); ML(2);
      float tl, nl, x, m, u, e, d;
      asm volatile("v_max3_f32 %4, %0, %1, %2\n v_max_f32 %4, %4, %3\n v_mul_f32 %5, %15, %4\n v_fma_f32 %0, %16, %0, %5\n v_fma_f32 %1, %16, %1, %5\n v_fma_f32 %2, %16, %2, %5\n v_fma_f32 %3, %16, %3, %5\n s_nop 0\n"
                   "v_add_f32_dpp %6, %11, %13 quad_perm:[0,1,2,3] row_mask:0xf bank_mask:0x3\n v_add_f32_dpp %6, %12, %14 quad_perm:[0,1,2,3] row_mask:0xf bank_mask:0xc\n v_max_f32 %7, %6, %4\n"
                   "v_exp_f32 %0, %0\n v_exp_f32 %1, %1\n v_max_f32_dpp %7, %7, %7 quad_perm:[1,0,3,2] row_mask:0xf bank_mask:0xf\n"
                   "v_exp_f32 %2, %2\n v_exp_f32 %3, %3\n v_max_f32_dpp %7, %7, %7 quad_perm:[2,3,0,1] row_mask:0xf bank_mask:0xf\n"
                   "v_add_f32 %8, %0, %1\n v_add_f32 %10, %2, %3\n v_max_f32_dpp %7, %7, %7 row_half_mirror row_mask:0xf bank_mask:0xf\n"
                   "v_mul_f32 %7, %15, %7\n v_sub_f32 %4, %7, %5\n v_fma_f32 %9, %16, %6, %7\n v_exp_f32 %4, %4\n v_exp_f32 %9, %9\n v_add_f32 %8, %8, %10\n v_fma_f32 %10, %4, %8, %9\n"
                   "v_mul_f32 %0, %0, %4\n v_mul_f32 %1, %1, %4\n v_add_f32_dpp %10, %10, %10 quad_perm:[1,0,3,2] row_mask:0xf bank_mask:0xf\n"
                   "v_mul_f32 %2, %2, %4\n v_mul_f32 %3, %3, %4\n v_add_f32_dpp %10, %10, %10 quad_perm:[2,3,0,1] row_mask:0xf bank_mask:0xf\n"
                   "v_mov_b32_dpp %6, %9 quad_perm:[1,0,3,2] row_mask:0xf bank_mask:0xf\n s_nop 0\n v_add_f32_dpp %10, %10, %10 row_half_mirror row_mask:0xf bank_mask:0xf\n v_rcp_f32 %10, %10\n s_nop 0\n"
                   "v_mul_f32 %0, %0, %10\n v_mul_f32 %1, %1, %10\n v_cvt_pk_bf16_f32 v0, %0, %1\n v_mul_f32 %2, %2, %10\n v_mul_f32 %3, %3, %10\n v_cvt_pk_bf16_f32 v1, %2, %3\n"
                   "buffer_store_dwordx2 v[0:1], %17, %19, %20 offen sc1\n v_mul_f32 %9, %9, %10\n v_mul_f32 %6, %6, %10\n v_cvt_pk_bf16_f32 %9, %9, %6\n buffer_store_dword %9, %18, %19, %20 offen sc1"
                   : "+v"(t[0]), "+v"(t[1]), "+v"(t[2]), "+v"(t[3]), "=&v"(tl), "=&v"(nl), "=&v"(x), "=&v"(m), "=&v"(u), "=&v"(e), "=&v"(d)
                   : "v"(sc[0]), "v"(sc[1]), "v"(sc[2]), "v"(sc[3]), "s"(-CS), "s"(CS), "v"(po), "v"(pm), "s"(p_desc), "s"(pss) : "v0", "v1", "memory");
      SB();
      { u64 t_; RT_MARK(t_); LW32(flr, 20, (unsigned)t_); }
    }
    BAR();
    if (KIND == 0) {
      nap_rt(KXL - 1);
      if (s < 63) {
        const unsigned tneed = (unsigned)(s + 1);
        if (tprog < tneed) tprog = wait_t<4>(cnt + CNT(C_PT_ + h), 1, tneed, tmo);
        KX_LOAD(s + 1);
      }
    }
    if (KIND == 0) tprog = min_t(ld16(mkrs(cnt + CNT(C_PT_ + h)), 0u, 0u), 1);
    if (KIND == 2 && s < 63) {
      const unsigned f7 = FLRD(7), f5 = FLRD(5);
      const unsigned tb2 = (unsigned)__builtin_amdgcn_readfirstlane(f5);
      adj = __builtin_amdgcn_readfirstlane(f7) == snp;
      if (__builtin_expect(!calm || adj, 0)) {
        if (s < 2) __builtin_amdgcn_raw_buffer_store_b32(((unsigned)tb2 + (unsigned)a.d[11]) & ~1u, rs_ts, tso, tss, 16);
        tsl = ts_settle(rs_ts, tss, tsl, a.d[4]) + (unsigned)a.d[12];
        if (s > 0) __builtin_amdgcn_raw_buffer_store_b32((tsl + (unsigned)dly + dur) & ~1u, rs_ts, tso, tsn, 16);
      }
      tnx = tsl + (unsigned)dly; tpr = tnx + dur;
      dur = min(dur + 1u, (unsigned)tb2 - tis); PINS(dur);
      LW32(flr, 24, dur + (unsigned)a.d[13]);
      ADAPT(dly, adj);
    }
    if (KIND == 1) {
      const int ln = lane_id();
      const __amdgpu_buffer_rsrc_t rs_km = mkrs(ws + O_KM);
      int s2 = s; PINS(s2);
      if (s + PRE < 63) st16(rs_p, pfo, (unsigned)PSLOT(s + PRE), SENT4);
#pragma unroll
      for (int kk = 0; kk < 6; kk += 2) {
        const u32x4 v = trrd2(lds, qk_rd + kk * 512, 256u), v2 = trrd2(lds, qk_rd + (kk + 1) * 512, 256u);
        st16(rs_km, (unsigned)((kk + ((ln >> 3) & 1)) * 512 + (ln >> 4) * 128 + (ln & 7) * 16), MSLOT(s2) + (unsigned)h * 3072u, (u32x4){min(hi8 ? v2.x : v.x, 0xfffffffeu), hi8 ? v2.y : v.y, min(hi8 ? v2.z : v.z, 0xfffffffeu), hi8 ? v2.w : v.w});
      }
      if (s >= 60) VMW(0);
      __builtin_amdgcn_raw_buffer_store_b32((unsigned)(s >= 60 ? s + 1 : s), rs_ps, tso, 0, 16);
    }
  }
}
__device__ __forceinline__ void s_role(const Args& a, unsigned char* lds, const int h, const int wave) {
  char* ws = a.ws; asm volatile("" : "+s"(ws));
  unsigned* const cnt = (unsigned*)(ws + O_CNT); unsigned* const tmo = cnt + CNT(C_TMO);
  const int lane = lane_id(), l15 = lane & 15, lq = lane >> 4;
  WReg W; u32x8 wx[3];
  unsigned tro[3];
  { TRO_INIT();
#pragma unroll
    for (int t = 0; t < 3; ++t) tro[t] = TRO(4 * t + wave); }
  SDMA_INIT();
  SXDMA_INIT();
  SXDMA(0, 0); SDMAQK(0); SDMAQK(1); SXDMA(1, 8192); SDMAQK(2);
  f32x4 pac[2][2][3];
#define kacc pac[1]
#define qacc pac[0]
  SFOR(i, 10)
    if (i < 2 || i == 3 || i == 5 || i == 7) VMW(26); else if (i < 8) VMW(24); else if (i == 8) VMW(12); else VMW(0);
    BAR();
    u32x8 res[3][2];
    SFOR(t, 3)
      const unsigned ad = lds_addr(lds) + tro[t] + (unsigned)SBUF(i);
      if (i < 8) { TRS0(ad, SFR(i & 1, t, i & 6)); if (i == 6 && t > 0) tr8(false, ad, res[t][0], wx[t]); else TRS1(ad, SFR(i & 1, t, (i & 6) + 1)); }
      else tr8(false, ad, res[t][0], res[t][1]);
    SEND;
    BAR();
    if (i == 2 || i == 4 || i == 6) SXDMA((i >> 1) + 1, (((i >> 1) + 1) & 1) * 8192);
    if (i + 3 < 10) SDMAQK(i + 3);
    if (i >= 8) {
#pragma unroll
      for (int t = 0; t < 3; ++t)
#pragma unroll
        for (int k = 0; k < 2; ++k) {
          unsigned char* dst = lds + SBUF(i) + wave * 12288 + t * 4096 + k * 2048 + lane * 16;
          *(u32x4*)dst = lo4(res[t][k]); *(u32x4*)(dst + 1024) = hi4(res[t][k]);
        }
    }
    if (i == 7) {
#pragma unroll
      for (int t = 1; t < 3; ++t) { unsigned char* dst = lds + L_XR + wave * 4096 + lane * 16 + (t - 1) * 2048; *(u32x4*)dst = lo4(wx[t]); *(u32x4*)(dst + 1024) = hi4(wx[t]); }
    }
    SB();
    SFOR(kk, 4)
      u32x4 xa[2];
#pragma unroll
      for (int rt = 0; rt < 2; ++rt) xa[rt] = *(const u32x4*)(lds + ((i >> 1) & 1) * 8192 + rt * 4096 + l15 * 256 + ((4 * kk + lq) ^ l15) * 16);
      SFOR(n, 6)
        constexpr int t = n >> 1, rt = n & 1;
        if (i >= 8) { const u32x4 w_ = HALF(res[t][kk >> 1], kk & 1); MFV(pac[i & 1][rt][t], w_, xa[rt], false); }
        else if (i == 6 && t > 0 && kk >= 2) { const u32x4 w_ = HALF(wx[t], kk & 1); MFV(pac[0][rt][t], w_, xa[rt], false); }
        else mf_w<true>(pac[i & 1][rt][t], xa[rt], 2 * SFR(i & 1, t, (i & 6) + (kk >> 1)) + (kk & 1), i < 2 && kk == 0, W);
      SEND;
    SEND;
    SB();
  SEND;
  unsigned pol;
  {
    const u32x2 pf = ld8(mkrs(cnt + CNT(C_POL)), lane < 32 ? 0u : 128u, 0u);
    asm volatile("s_nop 7\n s_nop 7" : "+v"(kacc[0][0]), "+v"(kacc[0][1]), "+v"(kacc[0][2]), "+v"(kacc[1][0]), "+v"(kacc[1][1]), "+v"(kacc[1][2]));
#pragma unroll
    for (int rt = 0; rt < 2; ++rt)
#pragma unroll
      for (int t = 0; t < 3; ++t) { const int ix = 4 * t + wave; const unsigned o_ = (unsigned)(rt * 6144 + (ix >> 1) * 1024 + lane * 16 + (ix & 1) * 8);
        const unsigned k0 = cvtpk(kacc[rt][t][0], kacc[rt][t][1]), k1 = cvtpk(kacc[rt][t][2], kacc[rt][t][3]);
        *(u32x2*)(lds + L_KX + o_) = (u32x2){k0, k1};
        st8(mkrs(ws + O_KF), o_, (unsigned)h * 12288u, k0, k1);
        st8(mkrs(ws + O_QF), o_, (unsigned)h * 12288u, cvtpk(qacc[rt][t][0], qacc[rt][t][1]), cvtpk(qacc[rt][t][2], qacc[rt][t][3])); }
    if (wave == 2) { if (lane < 8) *(unsigned*)(lds + L_FLG + 4 * lane) = 0u; }
    BAR();
    pol = (unsigned)__builtin_amdgcn_readlane((int)pf.x, 0);
    if (__builtin_expect((unsigned)__builtin_amdgcn_readlane((int)pf.x, 32) < 40u || pol == 0u, 0)) { wait_ge<8>(cnt + CNT(C_FILL), 40u, tmo); pol = ld_word(cnt + CNT(C_POL)); }
    pol &= 1u;
  }
  if (wave == 0) s_loop<0>(a, lds, h, pol, W); else if (wave == 1) s_loop<1>(a, lds, h, pol, W); else if (wave == 2) s_loop<2>(a, lds, h, pol, W); else s_loop<3>(a, lds, h, pol, W);
}
#undef kacc
#undef qacc

__device__ __forceinline__ void z_role(const Args& a, unsigned char* lds, const int h, const int wave) {
  char* ws = a.ws; asm volatile("" : "+s"(ws));
  unsigned* const cnt = (unsigned*)(ws + O_CNT); unsigned* const tmo = cnt + CNT(C_TMO);
  char* const X1 = ws + O_X1;
#define ZFV(t_, k_) (8 * (t_) + (k_))
#define ZFO(n_, j_) ((n_) < 6 ? 24 + 3 * (n_) + (j_) : 30 + 2 * (n_) + ((j_) >> 1))
  WReg W;
  const __amdgpu_buffer_rsrc_t rs_u = mkrs(ws + O_UO + h * 1024);
  unsigned xv = (unsigned)lane_id() * 16u; PINV(xv);
  unsigned pkv, pk3, pk4;
  {
    const int lane = lane_id(), l15 = lane & 15, lq = lane >> 4;
    unsigned tro[3];
    { TRO_INIT();
#pragma unroll
      for (int t = 0; t < 3; ++t) tro[t] = TRO(4 * t + wave); }
    SDMA_INIT(); SXDMA_INIT(); ODMA_INIT();
    SDMA(a.Wv, 0, 0); SXDMA(0, 0); SDMA(a.Wv, 1, 1); SXDMA(1, 8192); SDMA(a.Wv, 2, 2);
    f32x4 kacc[2][3];
    u32x8 wl[3][2];
    SFOR(i, 5)
      if (i < 4) VMW(26); else VMW(24);
      BAR();
      u32x4 xa[2][4];
      SXRD((i & 1) * 8192);
      SFOR(t, 3)
        if (i < 4) { TRS0(lds_addr(lds) + tro[t] + (unsigned)SBUF(i), ZFV(t, 2 * (i & 3))); TRS1(lds_addr(lds) + tro[t] + (unsigned)SBUF(i), ZFV(t, 2 * (i & 3) + 1)); }
        else tr8(false, lds_addr(lds) + tro[t] + (unsigned)SBUF(i), wl[t][0], wl[t][1]);
      SEND;
#pragma unroll
      for (int n = 0; n < 8; ++n) PINV(xa[n >> 2][n & 3]);
      BAR();
      if (i + 2 < 5) SXDMA(i + 2, (i & 1) * 8192);
      if (i + 3 < 5) SDMA(a.Wv, i + 3, i + 3); else ODMA(i - 2, i + 3);
      SFOR(n, 24)
        constexpr int kk = n / 6, t = (n % 6) >> 1, rt = n & 1;
        if (i < 4) mf_w<true>(kacc[rt][t], xa[rt][kk], 2 * ZFV(t, 2 * (i & 3) + (kk >> 1)) + (kk & 1), i == 0 && kk == 0, W);
        else { const u32x4 w_ = HALF(wl[t][kk >> 1], kk & 1); MFV(kacc[rt][t], w_, xa[rt][kk], false); }
      SEND;
      SB();
    SEND;
    u32x4 vx[2][6];
    asm volatile("s_nop 7\n s_nop 7" : "+v"(kacc[0][0]), "+v"(kacc[0][1]), "+v"(kacc[0][2]), "+v"(kacc[1][0]), "+v"(kacc[1][1]), "+v"(kacc[1][2]));
#pragma unroll
    for (int rt = 0; rt < 2; ++rt)
#pragma unroll
      for (int t = 0; t < 3; ++t)
        *(u32x2*)(lds + rt * 6144 + (2 * t + (wave >> 1)) * 1024 + xv + (wave & 1) * 8) = (u32x2){cvtpk(kacc[rt][t][0], kacc[rt][t][1]), cvtpk(kacc[rt][t][2], kacc[rt][t][3])};
    BAR();
#pragma unroll
    for (int n = 0; n < 12; ++n) { vx[n / 6][n % 6] = *(const u32x4*)(lds + n * 1024 + xv); PINV(vx[n / 6][n % 6]); }
    SFOR(i5, 5)
      constexpr int i = i5 + 5;
      if (i == 5) VMW(24); else if (i == 6) VMW(26); else if (i == 7) VMW(28); else if (i == 8) VMW(18); else VMW(6);
      BAR();
      u32x8 res[2][3];
      SFOR(t, 2)
        const unsigned ad = lds_addr(lds) + otr[t] + (unsigned)SBUF(i);
        constexpr int n = 2 * i5 + t;
        if (n < 8) TRO4(ad, 0, ZFO(n & 7, 0)); else tr4<0>(false, ad, res[t][0]);
        if (n < 6) TRO4(ad, 1, ZFO(n & 7, 1)); else tr4<1>(false, ad, res[t][1]);
        if (n < 8) TRO4(ad, 2, ZFO(n & 7, 2)); else tr4<2>(false, ad, res[t][2]);
      SEND;
      BAR();
      if (i + 3 < 10) ODMA(i - 2, i + 3);
      f32x4 ua[2][2];
      SFOR(m, 24)
        constexpr int e = m >> 2, t = (m >> 1) & 1, rt = m & 1, n = 2 * i5 + t;
        if (n >= 8 || (n >= 6 && (e >> 1) == 1)) { const u32x4 w_ = HALF(res[t][e >> 1], e & 1); MFV(ua[t][rt], vx[rt][e], w_, e == 0); }
        else mf_w<false>(ua[t][rt], vx[rt][e], 2 * ZFO(n & 7, e >> 1) + (e & 1), e == 0, W);
      SEND;
      asm volatile("s_nop 7" : "+v"(ua[0][0]), "+v"(ua[0][1]), "+v"(ua[1][0]), "+v"(ua[1][1]));
#pragma unroll
      for (int t = 0; t < 2; ++t)
        st16(rs_u, xv, u_off(0u, (unsigned)(8 * OROT(i5) + 2 * wave + t)), (u32x4){cvtpk(ua[t][0][0], ua[t][0][1]), cvtpk(ua[t][0][2], ua[t][0][3]), cvtpk(ua[t][1][0], ua[t][1][1]), cvtpk(ua[t][1][2], ua[t][1][3])});
      SB();
      if (i == 7) {
        PINV(xv); pkv = xv + (unsigned)(L_B1 + wave * 12288);
#pragma unroll
        for (int n = 0; n < 6; ++n) { *(u32x4*)(lds + pkv + n * 2048) = lo4(wl[n >> 1][n & 1]); *(u32x4*)(lds + pkv + n * 2048 + 1024) = hi4(wl[n >> 1][n & 1]); }
      }
      if (i == 8) {
        PINV(xv); pk3 = xv + (unsigned)(L_B2 + wave * 12288);
#pragma unroll
        for (int t = 0; t < 2; ++t) { *(u32x4*)(lds + pk3 + t * 2048) = lo4(res[t][1]); *(u32x4*)(lds + pk3 + t * 2048 + 1024) = hi4(res[t][1]); }
      }
      if (i == 9) {
        PINV(xv); pk4 = xv + (unsigned)(L_B0 + wave * 12288);
#pragma unroll
        for (int n = 0; n < 6; ++n) { *(u32x4*)(lds + pk4 + n * 2048) = lo4(res[n & 1][n >> 1]); *(u32x4*)(lds + pk4 + n * 2048 + 1024) = hi4(res[n & 1][n >> 1]); }
      }
      SB();
    SEND;
  }
  const int lane = lane_id(), l15 = lane & 15, lq = lane >> 4;
  constexpr int L_VA = 10240;
  unsigned pol;
  {
    const u32x2 pf = ld8(mkrs(cnt + CNT(C_POL)), lane < 32 ? 0u : 128u, 0u);
    pol = (unsigned)__builtin_amdgcn_readlane((int)pf.x, 0);
    VMW(0);
    __builtin_amdgcn_raw_buffer_store_b32(1u, mkrs(cnt + CNT(C_PZ + h)), lane == 0 ? 4 * wave : (int)0x80000000u, 0, 16);
    if (__builtin_expect((unsigned)__builtin_amdgcn_readlane((int)pf.x, 32) < 40u || pol == 0u, 0)) { wait_ge<8>(cnt + CNT(C_FILL), 40u, tmo); pol = ld_word(cnt + CNT(C_POL)); }
    pol &= 1u;
  }
  const __amdgpu_buffer_rsrc_t rs_x1 = mkrs(X1), rs_z = mkrs(ws + O_ZB), rs_ts = mkrs(ws + O_TS + half_of(ws + O_TS, 0u, 0u, pol));
  int ix = (l15 & 2) ? 0xEEEE : 0x4444; PINV(ix);
  unsigned ia = (unsigned)(L_VA + wave * 384 + ((l15 >> 2) & 1) * 128 + (l15 >> 3) * 64 + (l15 & 1) * 32 + lq * 8 + ((l15 >> 1) & 1) * 4);
  unsigned rda = (unsigned)(L_VA + (lq >> 1) * 768 + (l15 >> 3) * 384 + (lq & 1) * 64 + ((l15 >> 2) & 1) * 32 + (l15 & 3) * 8);
  unsigned zvo = (unsigned)((2 * wave + (lq >> 1)) * 16384 + h * 256 + l15 * 16 + (lq & 1) * 8);
  unsigned evo = (unsigned)((2 * wave + (lq & 1) + 8 * (lq >> 1)) * 16384 + h * 256 + l15 * 16);
  unsigned gvo = xv + (unsigned)(wave * 2048), gv2 = xv + 16384u + (unsigned)((wave >> 1) * 1024);
  u64 hmask = 0xffffffffull << (32 * (wave & 1)); PINS(hmask);
  const unsigned gm0 = lds_addr(lds) + (unsigned)(L_MEM + ((wave - h) & 3) * 2048), gm2 = lds_addr(lds) + (unsigned)(L_MEM + 8192 + (wave >> 1) * 1024);
  PINV(ia); PINV(rda); PINV(zvo); PINV(gvo); PINV(gv2); PINV(pkv); PINV(pk3); PINV(pk4);
  unsigned tsv = ~0u, tries = 0; int dly = a.d[1]; PINS(dly);
#define PLO(b_, n_) (*(const u32x4*)(lds + (b_) + (n_) * 2048))
#define PHI(b_, n_) (*(const u32x4*)(lds + (b_) + (n_) * 2048 + 1024))
#define MV(t_, f_, w_) SMFV(av[t_], f_, w_)
#define VW(t_, k_) SMFW(av[t_], A[k_], ZFV(t_, k_))
#define MO(n_, j_, w_) SMFV(az[n_], va[j_], w_)
#define OW(n_, j_) SMFW(az[n_], va[j_], ZFO(n_, j_))
#define AFR(k_) (*(const u32x4*)(lds + afb + (k_) * 1024))
  unsigned afb = xv + L_MEM; PINV(afb);
#define G_LOAD() do { asm volatile("s_mov_b32 m0, %3\n s_nop 0\n global_load_lds_dwordx4 %0, %4 sc1\n global_load_lds_dwordx4 %0, %4 offset:1024 sc1\n s_mov_b32 m0, %2\n s_mov_b64 exec, %5\n global_load_lds_dwordx4 %1, %4 sc1\n s_mov_b64 exec, -1\n s_waitcnt vmcnt(2)\n s_barrier" \
      :: "v"(gvo), "v"(gv2), "s"(gm2), "s"(gm0), "s"(xs), "s"(hmask) : "memory"); SB(); } while (0)
  u32x4 ts_desc = mkdesc(ws + O_TS + half_of(ws + O_TS, 0u, 0u, pol) + 16384 + 256 * h);
  int w3m = wave, dzs = a.d[9]; PINS(w3m); PINS(dzs); PINS(ts_desc);
  u32x4 z_desc = mkdesc(ws + O_ZB); PINS(z_desc);
#pragma unroll 1
  for (int s = 0; s < 64; ++s) {
    f32x4 av[3], az[10];
#pragma unroll
    for (int n = 0; n < 10; ++n) { az[n] = ZERO4; PINV(az[n]); }
    unsigned snp = (unsigned)(s + 1) * 0x01010101u, snv = snp, adj = 0; PINS(snp); PINV(snv);
    const unsigned so = half_of(X1, XSLOT(s), 0u, pol);
    const char* xs = X1 + so; int tss = TS_STEP(s); PINS(xs); PINS(tss);
    unsigned zs[5], tnx = (unsigned)__builtin_amdgcn_readfirstlane(wave == 3 && tsv != ~0u ? tsv + (unsigned)dly : 0u); PINS(tnx);
    { const unsigned z0 = half_of(ws + O_ZB, ZSLOT(s), 0u, pol);
#pragma unroll
      for (int i = 0; i < 5; ++i) { zs[i] = z0 + (unsigned)OROT(i) * (8u * 16384u); PINS(zs[i]); } }
    if (s == 1 && wave == 3) x1_light(rs_x1, so, tmo);
    SB();
    asm volatile("s_branch .Lzre\n"
                 ".Lzbad:\n s_waitcnt lgkmcnt(0)\n s_add_u32 %0, %0, 1\n s_add_u32 %1, %1, 1\n s_cmp_lt_u32 %0, 4\n s_cbranch_scc1 .Lznow\n s_sleep 8\n"
                 ".Lznow:\n s_cmp_lg_u32 %6, 3\n s_cbranch_scc1 .Lzre\n s_memrealtime vcc\n s_waitcnt lgkmcnt(0)\n s_add_u32 vcc_lo, vcc_lo, %7\n s_and_b32 vcc_lo, vcc_lo, -2\n v_mov_b32 %2, vcc_lo\n"
                 " s_mov_b64 exec, 1\n buffer_store_dword %2, off, %4, %5 sc1\n s_mov_b64 exec, -1\n v_mov_b32 %2, %3\n"
                 ".Lzre:" : "+s"(adj), "+s"(tries) : "v"(snv), "s"(snp), "s"(ts_desc), "s"(tss), "s"(w3m), "s"(dzs) : "vcc", "scc", "memory");
    SB();
    u32x8 ra = cat8(PLO(pkv, 0), PHI(pkv, 0)), rb = cat8(PLO(pkv, 1), PHI(pkv, 1));
    av[0] = ZERO4; av[1] = ZERO4; av[2] = ZERO4;
    asm volatile("" : "+v"(av[0]), "+v"(av[1]), "+v"(av[2]));
    SB();
    wait_rt(tnx);
    BAR();
    G_LOAD();
    u32x4 A[10], lo, hi, va[3];
    unsigned w0, w1;
    float ta, tb_;
    u64 nanm;
    DS(A[0] = AFR(0)); DS(A[2] = AFR(2)); DS(A[4] = AFR(4)); DS(A[6] = AFR(6));
    VW(0, 0); VW(1, 0); asm volatile("s_waitcnt vmcnt(0)\n s_barrier" ::: "memory"); SB();
    VW(2, 0); DS(A[8] = AFR(8));
    VW(0, 2); DS(A[9] = AFR(9));
    VW(1, 2); DS(A[1] = AFR(1));
    VW(2, 2); DS(A[3] = AFR(3));
    VW(0, 4); DS(A[5] = AFR(5));
    VW(0, 6); DS(A[7] = AFR(7));
    MV(0, A[8], ra); DS(lo = PLO(pkv, 2));
    MV(0, A[9], rb); DS(hi = PHI(pkv, 2)); ra = cat8(lo, hi);
    VW(0, 1); DS(lo = PLO(pkv, 3));
    VW(0, 3); DS(hi = PHI(pkv, 3)); rb = cat8(lo, hi);
    VW(0, 5); VW(0, 7);
    VW(1, 4); VW(1, 6);
    MV(1, A[8], ra); DS(lo = PLO(pkv, 4)); ADD1(ta, av[0][0], av[0][2]);
    VW(1, 1); DS(hi = PHI(pkv, 4)); ra = cat8(lo, hi); ADD1(tb_, av[0][1], av[0][3]);
    VW(1, 3); LGW(2); CVT1(w0, ta, tb_);
    MV(1, A[9], rb); DS(LW32(ia, 0, w0));
    VW(1, 5); DS(lo = PLO(pkv, 5)); NANM(nanm, ta, tb_);
    VW(1, 7); DS(hi = PHI(pkv, 5)); rb = cat8(lo, hi);
    VW(2, 4);
    if (__builtin_expect(nanm != 0ull, 0)) {
      unsigned m = ~0u;
#define Z_BACK() asm volatile("v_cmp_eq_u32 vcc, 0, %0\n s_cmp_lt_u32 %1, %2\n s_cselect_b64 vcc, vcc, 0\n s_cbranch_vccnz .Lzbad" : "+v"(m) : "s"(tries), "n"(CH_SPIN_CAP) : "vcc", "scc")
#pragma unroll
      for (int k = 0; k < 10; k += 2) m = sent_min(m, A[k].x, A[k + 1].x);
      Z_BACK();
#pragma unroll
      for (int k = 0; k < 10; ++k) m = sent_min(m, A[k]);
      Z_BACK();
    }
    asm volatile("" :: "s"(xs), "s"(tnx), "s"(tries), "s"(tss), "s"(snp), "s"(ts_desc), "s"(w3m), "s"(dzs), "v"(snv));
    PINS(adj);
    u32x2 g0, g1;
#define VR0(j_) DS(g0 = trrd(lds, rda + (unsigned)((j_) * 1536)))
#define VR1(j_) do { DS(g1 = trrd(lds, rda + (unsigned)((j_) * 1536 + 128))); va[j_] = (u32x4){g0.x, g0.y, g1.x, g1.y}; } while (0)
    VW(2, 6); LBAR(2);
    MV(2, A[8], ra); VR0(0); ADD1(ta, av[1][0], av[1][2]);
    VW(2, 1); VR1(0); ADD1(tb_, av[1][1], av[1][3]);
    VW(2, 3); DS(lo = PLO(pk4, 0)); CVT1(w0, ta, tb_);
    VW(2, 5); DS(LW32(ia, 1536, w0));
    MV(2, A[9], rb); DS(hi = PHI(pk4, 0)); ra = cat8(lo, hi);
    VW(2, 7); DS(lo = PLO(pk4, 1));
    OW(0, 0); DS(hi = PHI(pk4, 1)); rb = cat8(lo, hi);
    OW(1, 0); LBAR(3);
    OW(2, 0); VR0(1); ADD1(ta, av[2][0], av[2][2]);
    OW(3, 0); VR1(1); ADD1(tb_, av[2][1], av[2][3]);
    OW(4, 0); LGW(2); CVT1(w0, ta, tb_);
    MO(8, 0, ra); DS(LW32(ia, 3072, w0));
    MO(9, 0, rb); DS(lo = PLO(pk3, 0));
    OW(5, 0); DS(hi = PHI(pk3, 0)); ra = cat8(lo, hi);
    OW(6, 0); DS(lo = PLO(pk3, 1));
    OW(7, 0); LGW(3); MEMB(); __builtin_amdgcn_s_barrier(); MEMB(); SB();
    OW(0, 1); VR0(2); VR1(2);
    OW(1, 1); DS(hi = PHI(pk3, 1)); rb = cat8(lo, hi);
    OW(2, 1);
    MO(6, 1, ra); DS(lo = PLO(pk4, 2));
    OW(3, 1); DS(hi = PHI(pk4, 2)); ra = cat8(lo, hi);
#define ZSW() do { asm volatile("v_permlane32_swap_b32 %0, %1" : "+v"(w0), "+v"(w1)); SB(); } while (0)
#define ZST(i_) do { const u32x2 w_ = {w0, w1}; asm volatile("buffer_store_dwordx2 %0, %1, %2, %3 offen sc1" :: "v"(w_), "v"(zvo), "s"(z_desc), "s"(zs[i_]) : "memory"); SB(); } while (0)
    OW(0, 2);
    OW(1, 2);
    MO(7, 1, rb); DS(lo = PLO(pk4, 3));
    OW(2, 2); DS(hi = PHI(pk4, 3)); rb = cat8(lo, hi); ADD2(ta, tb_, az[0]);
    OW(3, 2); LGW(2); CVT1(w0, ta, tb_); ADD1(ta, az[1][0], az[1][2]);
    MO(8, 1, ra); DS(lo = PLO(pk4, 4)); ADDCVT(w1, ta, az[1]);
    OW(6, 2); DS(hi = PHI(pk4, 4)); ra = cat8(lo, hi); ZSW();
    OW(7, 2); ZST(0); ADD2(ta, tb_, az[2]);
    OW(4, 1); LGW(2); CVT1(w0, ta, tb_); ADD1(ta, az[3][0], az[3][2]);
    MO(9, 1, rb); DS(lo = PLO(pk4, 5)); ADDCVT(w1, ta, az[3]);
    OW(5, 1); DS(hi = PHI(pk4, 5)); rb = cat8(lo, hi); ZSW();
    OW(4, 2); ZST(1); ADD2(ta, tb_, az[6]);
    OW(5, 2); LGW(2); CVT1(w0, ta, tb_); ADD1(ta, az[7][0], az[7][2]);
    MO(8, 2, ra); LGW(0); ADDCVT(w1, ta, az[7]);
    MO(9, 2, rb);
    asm volatile("v_add_f32 v2, %2, %4\n v_permlane32_swap_b32 %0, %1\n v_add_f32 v0, %3, %5\n v_cvt_pk_bf16_f32 v0, v2, v0\n v_add_f32 v2, %6, %8\n v_add_f32 v1, %7, %9\n v_cvt_pk_bf16_f32 v1, v2, v1"
                 : "+v"(w0), "+v"(w1) : "v"(az[4][0]), "v"(az[4][1]), "v"(az[4][2]), "v"(az[4][3]), "v"(az[5][0]), "v"(az[5][1]), "v"(az[5][2]), "v"(az[5][3]) : "v0", "v1", "v2", "v3");
    { const u32x2 w_ = {w0, w1};
      asm volatile("buffer_store_dwordx2 %0, %1, %2, %3 offen sc1\n v_add_f32 v3, %6, %8\n v_permlane32_swap_b32 v0, v1\n v_add_f32 v2, %7, %9\n buffer_store_dwordx2 v[0:1], %1, %2, %4 offen sc1\n v_cvt_pk_bf16_f32 v2, v3, v2\n"
                   "v_add_f32 %10, %10, %12\n v_add_f32 v3, %11, %13\n v_cvt_pk_bf16_f32 v3, %10, v3\n s_nop 1\n v_permlane32_swap_b32 v2, v3\n buffer_store_dwordx2 v[2:3], %1, %2, %5 offen sc1"
                   :: "v"(w_), "v"(zvo), "s"(z_desc), "s"(zs[3]), "s"(zs[2]), "s"(zs[4]), "v"(az[8][0]), "v"(az[8][1]), "v"(az[8][2]), "v"(az[8][3]), "v"(az[9][0]), "v"(az[9][1]), "v"(az[9][2]), "v"(az[9][3]) : "v0", "v1", "v2", "v3", "memory"); }
    SB();
    ADAPT(dly, adj);
    if (s < 63) {
      if (s + PRE < 64) {
        const unsigned e0 = half_of(ws + O_ZB, ZSLOT((s + PRE)), 0u, pol);
        st16(rs_z, evo, e0, SENT4); st16(rs_z, evo, e0 + 16u * 16384u, SENT4); st16(rs_z, lane < 32 ? evo : 0x80000000u, e0 + 32u * 16384u, SENT4);
      }
      if (wave == 3) { tsv = ts_settle(rs_ts, tss, 0u, a.d[4]); if (tsv != ~0u && (int)(tsv - tnx) > dzs + a.d[10]) tsv += (unsigned)a.d[12]; }
    }
  }
}

#define WCK 3
#define UNAP 0
__device__ __forceinline__ void r_role(const Args& a, unsigned char* lds, const int c, const int wave) {
  char* ws = a.ws; asm volatile("" : "+s"(ws));
  unsigned* const cnt = (unsigned*)(ws + O_CNT); unsigned* const tmo = cnt + CNT(C_TMO);
  char* const X1 = ws + O_X1; char* const P = ws + O_PS;
  const int lane = lane_id(), l15 = lane & 15, lq = lane >> 4;
  const unsigned pol = wait_ge<8>(cnt + CNT(C_POL), 1u, tmo) & 1u;
  const __amdgpu_buffer_rsrc_t rs_x1 = mkrs(X1 + (c >> 5) * 16384 + (c & 31) * 256), rs_p = mkrs(P + half_of(P, 0u, 0u, pol)), rs_z = mkrs(ws + O_ZB + (size_t)c * 16384);
  const __amdgpu_buffer_rsrc_t rs_ts = mkrs(ws + O_TS + half_of(ws + O_TS, 0u, 0u, pol));
  unsigned xv = (unsigned)lane * 16u; PINV(xv);
  {
    const u32x4 ones = {~0u, ~0u, ~0u, ~0u};
    static_assert(PRE == 4, "");
#pragma unroll
    for (int i = 0; i < 8; ++i) st16(rs_z, xv + (unsigned)((i & 3) * 1024), half_of(ws + O_ZB, ZSLOT(wave), 0u, pol) + (unsigned)((i >> 2) * 4096), SENT4);
    st16(rs_p, lane < 32 ? xv : 0x80000000u, (unsigned)(PSLOT(wave) + (c >> 4) * 16384 + (c & 15) * 512), SENT4);
    st16(rs_x1, lane < 16 ? xv : 0x80000000u, half_of(X1, XSLOT((wave + 1)), 0u, pol), SENT4);
    if (c < 4) st16(rs_ts, xv, (unsigned)((c >> 1) * 16384 + (c & 1) * 4096 + wave * 1024), ones);
    if (c < 24) st16(mkrs(ws + O_KM + MSLOT(63)), xv, (unsigned)(c * 4096 + wave * 1024), ones);
    if (wave == 0) {
      const int q = lane >> 2, r = (q >> 2) * 2 + (q & 1);
      const unsigned v = *(const unsigned*)(a.mem0 + r * 640 + 16 * c + 4 * (lane & 3) + (q & 2));
      __builtin_amdgcn_raw_buffer_store_b32(v == SENT ? ~0u : v, rs_x1, lane * 4, (int)half_of(X1, 0u, 0u, pol), 16);
    }
    VMW(0);
    BAR();
    if (wave != 0) return;
    if (lane == 0) __hip_atomic_fetch_add((gu32*)(cnt + CNT(C_FILL)), 1u, RLX_AGENT);
  }
  const u32x4 rs_u = mkdesc(ws + O_UO);
  const __amdgpu_buffer_rsrc_t rs_pw = mkrs(cnt + CNT(C_PW) + ((c >> 1) & 3));
  int ix = (l15 & 2) ? 0xEEEE : 0x4444; PINV(ix);
  unsigned pm = (unsigned)(lq * 256 + l15 * 8);
  int xo = (4 * lq + (l15 & 3)) * 16 + (l15 >> 2) * 4; unsigned sel = (l15 & 1) ? 0x07060302u : 0x01000504u;
  int tsa = (lane & 31) * 4;
  PINV(pm); PINV(xo); PINV(sel); PINV(tsa);
  WReg W;
#define U_LOAD(sg_, def_) SFOR(i, 16) ld_w(rs_u, xv, u_off((unsigned)(sg_), (unsigned)c) + (unsigned)((i >> 1) * 4096), (i & 1) * 2048, i, (def_) && (i & 3) == 0, W); nap4<UNAP>(); SEND
  wait_ge<8>(cnt + CNT(C_FILL), 40u, tmo);
  wait_heads<8>(cnt + CNT(C_PZ) + ((c >> 1) & 3), 0, 1u, tmo);
  unsigned wl = __builtin_amdgcn_raw_buffer_load_b32(rs_pw, tsa * 32, 0, 16), tsl = 0, wn = 0; int tso = TS_OFF(lane), two = tsa * 32; PINV(tso); PINV(two); PINV(wl);
  const u32x4 ts_desc = mkdesc(ws + O_TS + half_of(ws + O_TS, 0u, 0u, pol)), pw_desc = mkdesc(cnt + CNT(C_PW) + ((c >> 1) & 3));
  U_LOAD(0, true);
  unsigned tsv = ~0u, tpub; int dlp = a.d[2]; PINS(dlp);
  { u64 t_; RT_MARK(t_); tpub = (unsigned)t_ - 4096u; }
#pragma unroll 1
  for (int s = 0; s < 63; ++s) {
    unsigned ps[5], zs[2], xs;
#pragma unroll
    for (int j = 0; j < 5; ++j) { ps[j] = (unsigned)PSLOT(s) + (unsigned)((j >> 1) * 16384 + (j & 1) * 4096); PINS(ps[j]); }
    zs[0] = half_of(ws + O_ZB, ZSLOT(s), 0u, pol); zs[1] = zs[0] + 4096u; xs = half_of(X1, XSLOT((s + 1)), 0u, pol); PINS(zs[0]); PINS(zs[1]); PINS(xs);
    int tss = TS_STEP(s); PINS(tss);
    asm volatile("s_waitcnt vmcnt(32)" : "+v"(tsl), "+v"(wn) :: "memory");
    wl = max(wl, wn);
    tsv = s < 2 ? ~0u : ts_max(tsl);
    if (__builtin_expect(s >= 2 && (tsv == ~0u || (int)(tsv - tpub) < a.d[8]), 0)) { tsv = ts_settle(rs_ts, tss, tsv, a.d[4]); }
    const bool uok = __all(wl >= (unsigned)(s + 2)), uok2 = __all(wl >= (unsigned)(s + 3));
    f32x4 o = ZERO4; PINV(o);
    unsigned tis = tsv + (unsigned)dlp; (void)tis;
#define R_LIGHT() do { const unsigned ln = (unsigned)lane_id(); light(mkrs(ws + O_ZB), ln < 32u ? zs[0] + (unsigned)c * 16384u + ln * 256u : (unsigned)(O_PS - O_ZB) + half_of(P, 0u, 0u, pol) + ps[4] + (ln - 32u) * 128u, 0u, tmo); } while (0)
    if (__builtin_expect(tsv & 1u, 0)) { R_LIGHT(); } else tis = wait_rt(tsv + (unsigned)dlp);
    unsigned adj = 0, pk, tsx; u64 nanm;
    u32x4 z[8], p[16]; u32x2 m[8];
#define R_LOAD() do { PINV(xv); PINV(pm); \
      _Pragma("unroll") for (int i = 0; i < 8; ++i) DS(z[i] = ld16(rs_z, xv + (unsigned)((i & 3) * 1024), zs[i >> 2])); \
      _Pragma("unroll") for (int i = 0; i < 16; ++i) DS(p[i] = ld16(rs_p, xv + (unsigned)((i & 3) * 1024), ps[i >> 2])); \
      _Pragma("unroll") for (int i = 0; i < 8; ++i) DS(m[i] = ld8(rs_p, pm + (unsigned)((i >> 1) * 1024 + (i & 1) * 128), ps[4])); \
      DS(tsx = __builtin_amdgcn_raw_buffer_load_b32(rs_ts, tso, tss, 16)); } while (0)
#define R_PROD() do { \
      SFOR(i, 16) SMFW(o, p[i], i); SEND; \
      _Pragma("unroll") for (int i = 0; i < 4; ++i) { const u32x4 a_ = {m[2 * i].x, m[2 * i].y, m[2 * i + 1].x, m[2 * i + 1].y}; const u32x8 b_ = cat8(z[2 * i], z[2 * i + 1]); SMFV(o, a_, b_); } \
      float ta, tb_; unsigned q; \
      asm volatile("s_nop 7\n v_add_f32 %2, %5, %7\n v_add_f32 %3, %6, %8\n v_cvt_pk_bf16_f32 %0, %2, %3\n v_cmp_u_f32_e64 %1, %2, %3\n s_nop 0\n" \
                   "v_mov_b32_dpp %4, %0 quad_perm:[1,0,3,2] row_mask:0xf bank_mask:0xf\n v_perm_b32 %0, %0, %4, %9" \
                   : "=&v"(pk), "=&s"(nanm), "=&v"(ta), "=&v"(tb_), "=&v"(q) : "v"(o[0]), "v"(o[1]), "v"(o[2]), "v"(o[3]), "v"(sel)); } while (0)
    R_LOAD();
    R_PROD();
    if (__builtin_expect(nanm != 0ull, 0)) {
      while (spin_check(adj, tmo)) {
        if (adj > 2u) R_LIGHT();
        else { const unsigned t_ = ts_max(tsx); if (t_ != ~0u) wait_rt(t_ + (unsigned)dlp); }
        R_LOAD();
        o = ZERO4; asm volatile("s_nop 1" : "+v"(o)); SB();
        R_PROD();
        if (nanm == 0ull) break;
        if ((adj & 7u) == 0u) {
          unsigned e = ~0u;
#pragma unroll
          for (int i = 0; i < 16; ++i) e = sent_min(e, p[i]);
#pragma unroll
          for (int i = 0; i < 8; ++i) e = sent_min(sent_min(e, z[i]), m[i].x, m[i].y);
          if (!__any(e == 0u)) break;
        }
      }
    }
    __builtin_amdgcn_raw_buffer_store_b32(pk, rs_x1, xo, (int)xs, 16);
    SB();
    PINU(tsx);
    { u64 t_; RT_MARK(t_); tpub = (unsigned)t_; }
    ADAPT(dlp, adj);
    if (s + PRE + 1 < 64) st16(rs_x1, lane < 16 ? xv : 0x80000000u, half_of(X1, XSLOT((s + PRE + 1)), 0u, pol), SENT4);
    if (s < 62) {
      if (__builtin_expect(!uok, 0)) { wait_heads<4>(cnt + CNT(C_PW) + ((c >> 1) & 3), 0, (unsigned)(s + 2), tmo); }
      wait_rt(tpub + (unsigned)a.d[3]);
      asm volatile("s_nop 4\n buffer_load_dword %0, %1, %2, %3 offen sc1" : "=v"(tsl) : "v"(tso), "s"(ts_desc), "s"(TS_STEP(s + 1)) : "memory");
      asm volatile("s_nop 4\n buffer_load_dword %0, %1, %2, 0 offen sc1" : "=v"(wn) : "v"(uok2 ? (int)0x80000000u : two), "s"(pw_desc) : "memory");
      U_LOAD(s + 1, false);
    }
  }
}

__device__ __forceinline__ void w_role(const Args& a, unsigned char* lds, const int h, const int wave) {
  char* ws = a.ws; asm volatile("" : "+s"(ws));
  unsigned* const cnt = (unsigned*)(ws + O_CNT); unsigned* const tmo = cnt + CNT(C_TMO);
  const int lane = lane_id(), l15 = lane & 15, lq = lane >> 4;
  WReg W;
  {
    ODMA_INIT();
    ODMA(0, 0); ODMA(1, 1); ODMA(2, 2);
#pragma unroll
    for (int i = 0; i < 5; ++i) {
      if (i < 3) VMW(24); else if (i == 3) VMW(12); else VMW(0);
      BAR();
#pragma unroll
      for (int t = 0; t < 2; ++t) {
        const unsigned ad = lds_addr(lds) + otr[t] + (unsigned)SBUF(i);
        TRO4(ad, 0, 6 * i + 3 * t); TRO4(ad, 1, 6 * i + 3 * t + 1); TRO4(ad, 2, 6 * i + 3 * t + 2);
      }
      if (i + 3 < 5) { BAR(); ODMA(i + 3, i + 3); }
    }
  }
  const __amdgpu_buffer_rsrc_t rs_u = mkrs(ws + O_UO + h * 1024);
  const __amdgpu_buffer_rsrc_t rs_pv = mkrs(cnt + CNT(C_PV + h)), rs_pw = mkrs(cnt + CNT(C_PW + h));
  unsigned xv = (unsigned)lane * 16u; int flo = lane == 0 ? 4 * wave : (int)0x80000000u; PINV(xv); PINV(flo);
  unsigned uo[10];
#pragma unroll
  for (int i = 0; i < 10; ++i) { uo[i] = (unsigned)(8 * OROT(i >> 1) + 2 * wave + (i & 1)) << 15; PINS(uo[i]); }
  const __amdgpu_buffer_rsrc_t rs_po = mkrs(cnt + CNT(C_PO)); int poo = wave == 0 ? (lane % 24) / 6 * 128 + (lane % 6) * 4 : (int)0x80000000u; PINV(poo);
  const char* vsrc = ws + O_VF + (size_t)h * 12288 + wave * 3072;
#define V_DMA(sg_) do { m0_set(lds_addr(lds) + (unsigned)(((sg_) % 3) * 12288 + wave * 3072)); const char* p_ = vsrc + KSLOT(sg_); asm volatile("s_nop 4" : "+s"(p_)); glds_x<0>(p_, xv); glds_x<1024>(p_, xv); glds_x<2048>(p_, xv); } while (0)
  BAR();
  wait_t<2>(cnt + CNT(C_PV + h), 1, 2u, tmo);
  V_DMA(1); V_DMA(2);
  VMW(3);
  int nxt = 3;
#pragma unroll 1
  for (int s = 1; s < 64; ++s) {
    unsigned ub = u_off((unsigned)s, 0u), sv = (unsigned)s, va = lds_addr(lds) + (unsigned)((s % 3) * 12288) + xv; PINS(ub); PINV(sv); PINV(va);
    f32x4 acc[2][2]; u32x4 vx[12], pw, w; unsigned sp; int vprog; bool late, far;
    BAR();
#pragma unroll
    for (int n = 0; n < 12; ++n) DS(vx[(n >> 1) + 6 * (n & 1)] = *(const LAS u32x4*)(size_t)(va + ((n >> 1) + 6 * (n & 1)) * 1024));
    SFOR(t, 11)
      SFOR(m, 12)
        constexpr int e = m >> 1, rt = m & 1, c = (t + 1) & 1;
        if (t < 10) { mf_w<false>(acc[t & 1][rt], vx[6 * rt + e], 6 * (t % 10) + e, e == 0, W); SB(); }
        else if (m == 0) { asm volatile("s_nop 7" : "+v"(acc[1][0]), "+v"(acc[1][1])); SB(); }
        if (t > 0 && m >= 2 && m < 6) { if (t < 10) FILL(); CVT1(w[(m + 2) & 3], acc[c][(m >> 1) - 1 & 1][2 * (m & 1)], acc[c][(m >> 1) - 1 & 1][2 * (m & 1) + 1]); }
        if (t > 0 && m == 6) { if (t < 10) __builtin_amdgcn_raw_buffer_store_b128(w, rs_u, (int)xv, (int)(ub + uo[(t + 9) % 10]), 16); else st16(rs_u, xv, ub + uo[9], w); SB(); }
        if (t == 0 && m == 1) { pw = ld16(rs_pv, 0u, 0u); SB(); }
        if (t == 0 && m == 4) { sp = __builtin_amdgcn_raw_buffer_load_b32(rs_po, poo, 0, 16); SB(); }
        if (t == WCK && m == 8) { vprog = (int)min_t(pw, 1); PINS(vprog); far = wave == 0 && __any((int)sp < s + 2 - WGATE); MEMB(); SB(); }
        if (t == WCK && m == 10) { __builtin_amdgcn_raw_buffer_store_b32(sv, rs_pw, flo, 0, 16); SB(); }
        if (t == WCK + 1 && m == 0) {
          late = nxt <= s + 1;
          const int upto = min(min(vprog, s + 2), 63);
          while (nxt <= upto) { V_DMA(nxt); ++nxt; }
          SB(); }
        if (t == WCK + 1 && m == 8) { pw = ld16(rs_pv, 0u, 0u); SB(); }
      SEND;
    SEND;
    if (__builtin_expect(late || far, 0)) {
      const int upto = min(min((int)min_t(pw, 1), s + 2), 63);
      while (nxt <= upto) { V_DMA(nxt); ++nxt; }
      VMW(0); __builtin_amdgcn_raw_buffer_store_b32((unsigned)(s + 1), rs_pw, flo, 0, 16);
      if (s < 63) {
        if (far) { unsigned spins = 0; while (__any((int)__builtin_amdgcn_raw_buffer_load_b32(rs_po, poo, 0, 16) < s + 2 - WGATE) && spin_check(spins, tmo)) __builtin_amdgcn_s_sleep(24); MEMB(); }
        if (nxt <= s + 1) { wait_t<1>(cnt + CNT(C_PV + h), 1, (unsigned)(s + 1), tmo); V_DMA(nxt); ++nxt; VMW(0); }
      }
    }
  }
}

#define TSTRICT 24
#define LDS16(ad_) (*(LAS u32x4*)(size_t)(ad_))
struct TReg { u32x32 b[12]; u32x16 e; };
#define TR_SW(g_, X) switch (g_) { case 0: X("a", "{a[0:31]}", T.b[0]); break; case 1: X("a", "{a[32:63]}", T.b[1]); break; case 2: X("a", "{a[64:95]}", T.b[2]); break; case 3: X("a", "{a[96:127]}", T.b[3]); break; \
    case 4: X("a", "{a[128:159]}", T.b[4]); break; case 5: X("a", "{a[160:191]}", T.b[5]); break; case 6: X("a", "{a[192:223]}", T.b[6]); break; case 7: X("a", "{a[224:255]}", T.b[7]); break; \
    case 8: X("v", "{v[110:141]}", T.b[8]); break; case 9: X("v", "{v[142:173]}", T.b[9]); break; case 10: X("v", "{v[174:205]}", T.b[10]); break; case 11: X("v", "{v[206:237]}", T.b[11]); break; default: X("v", "{v[238:253]}", T.e); break; }
__device__ __forceinline__ void mf_t(f32x4& c, const u32x4 x, const int d, const bool first, const TReg& T) {
  const int r = d < 64 ? 4 * d : 4 * d - 146;
#define X_(rf_, cs_, v_) if (first) asm volatile("v_mfma_f32_16x16x32_bf16 %0, " rf_ "[%c2:%c3], %1, 0" : "=&v"(c) : "v"(x), "n"(r), "n"(r + 3), cs_(v_)); \
      else asm volatile("v_mfma_f32_16x16x32_bf16 %0, " rf_ "[%c2:%c3], %1, %0" : "+v"(c) : "v"(x), "n"(r), "n"(r + 3), cs_(v_))
  TR_SW(d >> 3, X_);
#undef X_
}
__device__ __forceinline__ void tr_t(const unsigned ad0, const unsigned ad1, const int off, const int d, TReg& T) {
  const int r = d < 64 ? 4 * d : 4 * d - 146;
#define Y_(md_, rf_, cs_, v_) asm volatile("ds_read_b64_tr_b16 " rf_ "[%c5:%c6], %1 offset:%3\n ds_read_b64_tr_b16 " rf_ "[%c7:%c8], %1 offset:%4\n ds_read_b64_tr_b16 " rf_ "[%c9:%c10], %2 offset:%3\n ds_read_b64_tr_b16 " rf_ "[%c11:%c12], %2 offset:%4\n s_waitcnt lgkmcnt(0)" \
    : md_ cs_(v_) : "v"(ad0), "v"(ad1), "n"(off), "n"(off + 1536), "n"(r), "n"(r + 1), "n"(r + 2), "n"(r + 3), "n"(r + 4), "n"(r + 5), "n"(r + 6), "n"(r + 7) : "memory")
#define X_(rf_, cs_, v_) if ((d & 7) == 0) Y_("=&", rf_, cs_, v_); else Y_("+", rf_, cs_, v_)
  TR_SW(d >> 3, X_);
#undef X_
#undef Y_
}
__device__ __forceinline__ void t_role(const Args& a, unsigned char* lds, const int h, const int isv, const int wave) {
  char* ws = a.ws; asm volatile("" : "+s"(ws));
  unsigned* const cnt = (unsigned*)(ws + O_CNT); unsigned* const tmo = cnt + CNT(C_TMO);
  const int lane = lane_id(), l15 = lane & 15, lq = lane >> 4;
  TReg T;
#define TBUF(j_) (24576u + (unsigned)(((j_) + 3) % 6) * 20480u)
#define TSRC(c_, r_) ((((c_) - (r_) * 80u < 64u ? (r_) * 80u + (((c_) - (r_) * 80u + 16u * (unsigned)h) & 63u) : (c_)) ^ ((r_) & 15u)) * 16u)
#define TDMA1(src_, dst_, i_) do { const unsigned c = (unsigned)((wave + 4 * (i_)) * 64 + lane_id()), r = c / 80u; glds(src_, TSRC(c, r), (dst_) + (i_) * 4096u); } while (0)
#define TROW(j_) (16 * ((j_) + 6))
#define TDMA(j_) do { _Pragma("unroll") for (int xi_ = 0; xi_ < 5; ++xi_) TDMA1(a.Bx + (size_t)TROW(j_) * 640, lds_addr(lds) + TBUF(j_) + (unsigned)wave * 1024u, xi_); } while (0)
  const __amdgpu_buffer_rsrc_t rs_fl = mkrs(cnt + CNT((isv ? C_PV : C_PT_) + h)), rs_o = mkrs(ws + O_KF + (size_t)h * 12288);
  unsigned vo[2];
  {
    unsigned ad0[5], ad1[5], mvo[6], qvo[3], xvo, xr, x5;
    { TRO_INIT();
#pragma unroll
      for (int t = 0; t < 5; ++t) { const bool isq = t == 4 || wave == 3; const unsigned tile = t == 4 ? 4u + (unsigned)(wave >> 1) : (wave == 3 ? (unsigned)t : (unsigned)(4 * wave + t));
        ad0[t] = lds_addr(lds) + 32768u + TRO(tile) + (isq ? 24576u : 0u); ad1[t] = lds_addr(lds) + 32768u + (isq ? TRO(tile + 6u) + 24576u : TRO(tile) + 12288u); PINV(ad0[t]); PINV(ad1[t]); } }
#pragma unroll
    for (int q = 0; q < 6; ++q) { const unsigned c = (unsigned)((wave + 4 * q) * 64 + lane), r = c / 24u, pc = (c - r * 24u) ^ (((r >> 1) & 1u) * 2u + ((r >> 3) & 1u) * 4u);
      mvo[q] = r * 12288u + 16u * pc; PINV(mvo[q]);
      if (q < 3) { qvo[q] = pc < 12u ? mvo[q] : mvo[q] + 32u * 12288u - 192u; PINV(qvo[q]); } }
    xvo = (unsigned)((8 * wave + (lane >> 3)) * 1280 + ((lane & 7) ^ (lane >> 3)) * 16);
    xr = lds_addr(lds) + (unsigned)(l15 * 128 + (lq ^ (l15 & 7)) * 16); x5 = xr + (unsigned)((wave & 1) * 2048);
    PINV(xvo); PINV(xr); PINV(x5);
    const u16* Wm = (isv ? a.Wv : a.Wk) + 192 * h; const u16* Wh = a.Wq + 192 * h + 96 * isv;
#define TW_DMA(i_) do { const int r_ = 128 * KROT((i_) >> 1) + 64 * ((i_) & 1); const unsigned d_ = lds_addr(lds) + (unsigned)(32768 + ((i_) % 3) * 36864 + wave * 1024); \
      _Pragma("unroll") for (int q_ = 0; q_ < 2; ++q_) glds(a.Bx + (32 + 32 * q_) * 640 + r_, xvo, lds_addr(lds) + (unsigned)(((i_) & 3) * 8192 + q_ * 4096 + wave * 1024)); \
      _Pragma("unroll") for (int q_ = 0; q_ < 6; ++q_) glds(Wm + (size_t)r_ * 6144, mvo[q_], d_ + (unsigned)(q_ * 4096)); \
      _Pragma("unroll") for (int q_ = 0; q_ < 3; ++q_) glds(Wh + (size_t)r_ * 6144, qvo[q_], d_ + (unsigned)(24576 + q_ * 4096)); SB(); } while (0)
    TW_DMA(0); TW_DMA(1); TW_DMA(2);
    f32x4 pa[4][4], p5[2];
    SFOR(i, 10)
      if (i < 8) VMW(22); else if (i == 8) VMW(16); else VMW(10);
      BAR();
      SFOR(t, 5)
        if (i % 3 < 2) tr_t(ad0[t], ad1[t], (i % 3) * 36864, 10 * i + 2 * t, T); else tr_t(ad0[t] + 73728u, ad1[t] + 73728u, 0, 10 * i + 2 * t, T);
      SEND;
      if (i < 9) BAR();
      if (i + 3 < 10) TW_DMA(i + 3);
      if (i == 7) TDMA(0);
      if (i == 8) TDMA(1);
      SFOR(kk, 2)
        u32x4 x_[4], y_[2];
#pragma unroll
        for (int rt = 0; rt < 4; ++rt) x_[rt] = LDS16((xr ^ (unsigned)(kk * 64)) + (i & 3) * 8192 + rt * 2048);
#pragma unroll
        for (int g = 0; g < 2; ++g) y_[g] = LDS16((x5 ^ (unsigned)(kk * 64)) + (i & 3) * 8192 + g * 4096);
        SFOR(n, 16) mf_t(pa[n >> 2][n & 3], x_[n >> 2], 10 * i + 2 * (n & 3) + kk, i == 0 && kk == 0, T); SEND;
        SFOR(g, 2) mf_t(p5[g], y_[g], 10 * i + 8 + kk, i == 0 && kk == 0, T); SEND;
        SB();
      SEND;
    SEND;
    const int ln = lane_id();
    vo[0] = (unsigned)ln * 16u + (wave == 3 ? (unsigned)(O_QF - O_KF) + (unsigned)(3 * isv) * 1024u : (isv ? (unsigned)(O_VF - O_KF) : 0u) + (unsigned)wave * 2048u);
    vo[1] = (unsigned)ln * 16u + (unsigned)(O_QF - O_KF) + (unsigned)(3 * isv + 2) * 1024u + (unsigned)(wave >> 1) * 8u;
    PINV(vo[0]); PINV(vo[1]);
    asm volatile("s_nop 7" : "+v"(pa[3][0]), "+v"(pa[3][1]), "+v"(pa[3][2]), "+v"(pa[3][3]), "+v"(p5[0]), "+v"(p5[1]));
#pragma unroll
    for (int g = 0; g < 2; ++g) {
#pragma unroll
      for (int n = 0; n < 4; ++n) { const f32x4 c0 = pa[2 * g + (n >> 1)][2 * (n & 1)], c1 = pa[2 * g + (n >> 1)][2 * (n & 1) + 1];
        st16(rs_o, vo[0] + (unsigned)((n & 1) * 1024), KSLOT(1 + g) + (unsigned)((n >> 1) * 6144), (u32x4){cvtpk(c0[0], c0[1]), cvtpk(c0[2], c0[3]), cvtpk(c1[0], c1[1]), cvtpk(c1[2], c1[3])}); }
      st8(rs_o, vo[1], KSLOT(1 + g) + (unsigned)((wave & 1) * 6144), cvtpk(p5[g][0], p5[g][1]), cvtpk(p5[g][2], p5[g][3]));
    }
  }
  const int lane2 = lane_id();
#define lane lane2
#define l15 (lane2 & 15)
#define lq (lane2 >> 4)
  unsigned tvo[5];
#pragma unroll
  for (int i = 0; i < 5; ++i) { const unsigned c = (unsigned)((5 * wave + i) * 64 + lane), r = c / 80u; tvo[i] = TSRC(c, r) + (unsigned)(4096 - (i - 2) * 1024); PINV(tvo[i]); }
  int flo = lane == 0 ? wave * 4 : (int)0x80000000u; PINV(flo);
  unsigned xq[4];
#pragma unroll
  for (int q = 0; q < 4; ++q) { xq[q] = lds_addr(lds) + (unsigned)TBUF(0) + (unsigned)(l15 * 1280 + ((4 * q + lq) ^ l15) * 16); PINV(xq[q]); }
#define XFRT(k_) LDS16(xq[(k_) & 3] + ((k_) >> 2) * 256)
  const unsigned fla = lds_addr(lds) + 147456u;
  unsigned flw = fla + 4u * (unsigned)lane, one = 1u; PINV(flw); PINV(one);
  if (wave == 0) *(volatile LAS unsigned*)(size_t)flw = 0u;
  unsigned gw = flw + 256u * (unsigned)(wave + 1), go = (unsigned)((lane % 24) / 6 * 128 + (lane % 6) * 4), gm = fla + 256u * (unsigned)(wave + 1), gl; PINV(gw); PINV(go); PINS(gm);
  const unsigned* gsrc = cnt + CNT(C_PO); PINS(gsrc);
  *(volatile LAS unsigned*)(size_t)gw = 0u;
  u64 gb; int gt;
  u32x4 xa[4]; u32x2 cv[5]; unsigned fl;
  f32x4 acc[5];
#pragma unroll
  for (int t = 0; t < 5; ++t) { acc[t] = ZERO4; cv[t] = (u32x2){0u, 0u}; }
  VMW(0);
  __builtin_amdgcn_raw_buffer_store_b32(2u, rs_fl, flo, 0, 16);
  BAR();
  TDMA(2); TDMA(3);
  xa[0] = XFRT(0); xa[1] = XFRT(1);
#define T_CVT(t_) do { unsigned c0_, c1_; \
      asm volatile("v_cvt_pk_bf16_f32 %0, %2, %3\n v_cvt_pk_bf16_f32 %1, %4, %5" : "=&v"(c0_), "=&v"(c1_) : "v"(acc[t_][0]), "v"(acc[t_][1]), "v"(acc[t_][2]), "v"(acc[t_][3])); \
      cv[t_] = (u32x2){c0_, c1_}; SB(); } while (0)
#define T_ST(i_) do { PINV(vo[0]); if ((i_) < 2) __builtin_amdgcn_raw_buffer_store_b128((u32x4){cv[2 * (i_)].x, cv[2 * (i_)].y, cv[(2 * (i_) + 1) % 5].x, cv[(2 * (i_) + 1) % 5].y}, rs_o, (int)vo[0] + (i_) * 1024, (int)sof, 16); \
      else __builtin_amdgcn_raw_buffer_store_b64(cv[4], rs_o, (int)vo[1], (int)sof, 16); SB(); } while (0)
#define T_REST(five_) do { asm volatile("s_nop 7\n s_nop 7"); T_CVT(1); T_CVT(2); T_CVT(3); T_CVT(4); T_ST(0); T_ST(1); if (five_) T_ST(2); VMW(0); } while (0)
  const char* dsrc = (const char*)(a.Bx + (size_t)TROW(4) * 640) - 4096;
  unsigned tb = TBUF(4) - 24576u, rb = TBUF(0) - 24576u;
  unsigned sof = 3u * K_STEP + 6144u, sinc = 0u - 6144u, tgt = 4u, dstep = 20480u, fv = 0u, dq = 0u;
  const unsigned dd0 = lds_addr(lds) + 24576u + (unsigned)(5 * wave + 2) * 1024u;
#define T_MF(t_) do { mf_t(acc[t_], xa[k & 3], 10 * (k >> 1) + 2 * (t_) + (k & 1), k == 0, T); SB(); } while (0)
#define T_TILE(STRICT, FIVE) do { \
    SFOR(k, 20) \
      constexpr int k2 = k + 2, kn = k2 < 20 ? k2 : k2 - 20; \
      T_MF(0); \
      if (k != 18) { xa[k2 & 3] = XFRT(kn); SB(); } \
      if (k == 0) T_CVT(1); \
      if (k == 18) { xq[0] += dq; PINV(xq[0]); SB(); }        \
      T_MF(1); \
      if (k == 0) T_CVT(2); \
      if (k == 2 && FIVE) { asm volatile("s_mov_b32 m0, %0" :: "s"(gm) : "memory"); SB(); } \
      if (k == 14 && FIVE) { gl = *(const volatile LAS unsigned*)(size_t)gw; SB(); } \
      if (k == 17 && FIVE) {        \
        if (__builtin_expect(gb != 0ull, 0)) { unsigned spins = 0; while (__any((int)__hip_atomic_load((const gu32*)gsrc + (go >> 2), RLX_AGENT) < gt) && spin_check(spins, tmo)) __builtin_amdgcn_s_sleep(24); } \
        MEMB(); SB(); } \
      if (k == 3) { unsigned m_ = dd0 + tb; PINS(m_); asm volatile("s_mov_b32 m0, %0" :: "s"(m_) : "memory"); SB(); } \
      if (k == 16) { fl = *(const volatile LAS unsigned*)(size_t)flw; SB(); } \
      if (k == 18) {        \
        if (__builtin_expect(fs < tgt, 0)) { do fl = *(const volatile LAS unsigned*)(size_t)flw; while ((unsigned)__builtin_amdgcn_readfirstlane(fl) < tgt); } \
        MEMB(); SB(); } \
      if (k == 9) { PINS(tb); tb += 20480u; PINS(tb); SB(); } \
      if (k == 11) { PINS(rb); rb += 20480u; PINS(rb); SB(); } \
      if (k == 12) { PINS(rb); rb = rb == 6u * 20480u ? 0u : rb; PINS(rb); SB(); } \
      if (k == 13) { PINS(sof); PINS(sinc); sof = (sof + sinc) & (NK * K_STEP - 1u); PINS(sof); SB(); } \
      if (k == 15) { PINS(dstep); dstep = j < 116 ? 20480u : 0u; PINS(dstep); SB(); } \
      if (k == 10) { fv = (unsigned)(STRICT ? (j < 1 ? 4 : j + 3) : j + 2) >> 1; PINV(fv); SB(); } \
      if (k == 19) { xq[2] += dq; PINV(xq[2]); SB(); } \
      T_MF(2); \
      if (k == 0) T_CVT(3); \
      if (k == 1 || k == 2 || (k == 3 && !FIVE)) T_ST(k >= 1 && k <= 3 ? k - 1 : 0); \
      if (k == 4) glds_o<-2048>(dsrc, tvo[0]); else if (k == 5) glds_o<-1024>(dsrc, tvo[1]); else if (k == 6) glds_o<0>(dsrc, tvo[2]); else if (k == 7) glds_o<1024>(dsrc, tvo[3]); else if (k == 8) glds_o<2048>(dsrc, tvo[4]); \
      if (k == 9) { PINS(dsrc); dsrc += dstep; PINS(dsrc); SB(); } \
      if (k == 11) { PINS(tb); tb = tb == 6u * 20480u ? 0u : tb; PINS(tb); SB(); } \
      if (k == 14) { PINS(rb); dq = rb == 0u ? 0u - 5u * 20480u : 20480u; PINS(dq); SB(); } \
      if (k == 15) { PINS(sinc); sinc = sinc == 6144u ? K_STEP - 6144u : 6144u; PINS(sinc); SB(); } \
      if (k == 12) {        \
        asm volatile("s_waitcnt vmcnt(%0)" :: "n"(STRICT ? 14 : 23) : "memory"); \
        __hip_atomic_fetch_add((LAS unsigned*)(size_t)flw, one, __ATOMIC_RELAXED, __HIP_MEMORY_SCOPE_WORKGROUP); SB(); \
      } \
      if (k == 13) { __builtin_amdgcn_raw_buffer_store_b32(fv, rs_fl, flo, 0, 16); SB(); } \
      if (k == 17) { fs = (unsigned)__builtin_amdgcn_readfirstlane(fl); PINS(fs); SB(); } \
      if (k == 18) { xa[0] = XFRT(0); SB(); xq[1] += dq; PINV(xq[1]); SB(); } \
      if (k == 19) { PINS(tgt); tgt += 4u; PINS(tgt); xq[3] += dq; PINV(xq[3]); SB(); } \
      T_MF(3); \
      if (k == 0 && !FIVE) T_CVT(4); \
      if (k == 2 && FIVE) { asm volatile("global_load_lds_dword %0, %1 sc1" :: "v"(go), "s"(gsrc) : "memory"); SB(); } \
      if (k == 14 && FIVE) { gt = (j >> 1) + 5 - NK; PINS(gt); SB(); } \
      if (k == 15 && FIVE) { asm volatile("v_cmp_gt_i32_e64 %0, %1, %2" : "=s"(gb) : "s"(gt), "v"(gl)); SB(); } \
      if (k == 19) T_CVT(0); \
      if (FIVE) T_MF(4); \
    SEND; } while (0)
  unsigned fs;
  static_assert(TSTRICT % 2 == 0, "");
#define T_ALL(ODD) do { \
    if (ODD) { const int j = 0; T_TILE(true, false); } \
    _Pragma("unroll 1") for (int j2 = ODD; j2 < TSTRICT; j2 += 2) { \
      { const int j = j2; T_TILE(true, true); } \
      { const int j = j2 + 1; T_TILE(true, false); } } \
    _Pragma("unroll 1") for (int j2 = TSTRICT + ODD; j2 < 121; j2 += 2) { \
      { const int j = j2; T_TILE(false, true); } \
      { const int j = j2 + 1; T_TILE(false, false); } } \
    if (ODD) { const int j = 121; T_TILE(false, true); } \
    T_REST(ODD); \
    __builtin_amdgcn_raw_buffer_store_b32(64u, rs_fl, flo, 0, 16); } while (0)
  if (wave & 1) T_ALL(1); else T_ALL(0);
}
#undef lane
#undef l15
#undef lq

__device__ __forceinline__ void o_role(const Args& a, unsigned char* lds, const int j, const int wave) {
  char* ws = a.ws; asm volatile("" : "+s"(ws));
  unsigned* const cnt = (unsigned*)(ws + O_CNT); unsigned* const tmo = cnt + CNT(C_TMO);
  const int lane = lane_id(), l15 = lane & 15, lq = lane >> 4;
  const unsigned pol = wait_ge<32>(cnt + CNT(C_POL), 1u, tmo) & 1u;
  const int xq = j / 6, c0 = 10 * xq + 5 * (j & 1), s0 = (j - 6 * xq) >> 1;
  const u32x4 rs_u = mkdesc(ws + O_UO + wave * 8192);
  const __amdgpu_buffer_rsrc_t rs_z = mkrs(ws + O_ZB + (size_t)c0 * 16384 + wave * 2048), rs_p = mkrs(ws + O_PT + wave * 16384), rs_m = mkrs(ws + O_PT + 65536 + wave * 4096), rs_out = mkrs(a.out + 640 + 16 * c0);
  unsigned xv = (unsigned)lane * 16u, xh = (unsigned)lane * 8u;
  unsigned wa = (unsigned)(wave * 10880 + lq * 272 + l15 * 16), ra[2], oo[2];
#pragma unroll
  for (int i = 0; i < 2; ++i) {
    const int n = i == 0 ? lane : 64 + l15, tok = 8 * wave + n / 10, oc = n % 10;
    ra[i] = (unsigned)((((oc >> 1) * 2 + (tok >> 4)) * 4 + 2 * (oc & 1)) * 272 + (tok & 15) * 16);
    oo[i] = i == 0 || lane < 16 ? (unsigned)((tok * 1280 + 8 * oc) * 2) : 0x80000000u;
    PINV(ra[i]); PINV(oo[i]);
  }
  PINV(xv); PINV(xh); PINV(wa);
  WReg W;
#pragma unroll 1
  for (int s = s0; s < 64; s += 3) {
    if (wave == 0) {
      const unsigned tk = __hip_atomic_load((const gu32*)(cnt + CNT(C_TOK + 4 * s + xq)), RLX_AGENT);
      wait_ge<12>(cnt + CNT(C_PS + j), (unsigned)(s < 63 ? s + 1 : 63), tmo);
      if (s >= 60) { wait_heads<4>(cnt + CNT(C_PW), 1, (unsigned)(s + 1), tmo); wait_heads<4>(cnt + CNT(C_PW) + 2, 1, (unsigned)(s + 1), tmo); }
      *(volatile LAS unsigned*)(lds + 65536) = tk;
    }
    BAR();
    const bool behind = (unsigned)__builtin_amdgcn_readfirstlane(*(volatile LAS unsigned*)(lds + 65536)) >= 32u;
    const int np = behind || s >= 60 ? 0 : a.d[14];
    SFOR(i, 20) ld_w(rs_u, xv, u_off((unsigned)s, (unsigned)(c0 + (i >> 2))) + (unsigned)(((i >> 1) & 1) * 4096), (i & 1) * 2048, i, (i & 3) == 0, W); nap_rt(np); SEND;
    u32x4 z[5][2], p1[8], pm[2][2]; u32x2 p0[8];
    const unsigned zs = half_of(ws + O_ZB, ZSLOT(s), 0u, pol), ps = TSLOT(s);
    unsigned spins = 0;
    for (;;) {
      unsigned e = ~0u;
      PINV(xv);
#pragma unroll
      for (int n = 0; n < 10; ++n) { z[n >> 1][n & 1] = ld16(rs_z, xv + (unsigned)((n & 1) * 1024), zs + (unsigned)((n >> 1) * 16384)); if (s >= 60) e = sent_min(e, z[n >> 1][n & 1]); }
      if (!__any(e == 0u) || !spin_check(spins, tmo)) break;
      __builtin_amdgcn_s_sleep(4);
    }
    if (!behind) {
      if (wave == 0) { if (s >= 60) wait_ge<1>(cnt + CNT(C_TOK + 4 * s + xq), 32u, tmo); else wait_ge<12>(cnt + CNT(C_TOK + 4 * s + xq), 32u, tmo); }
      BAR();
    }
    PINV(xv); PINV(xh);
#pragma unroll
    for (int n = 0; n < 8; ++n) { p1[n] = ld16(rs_p, xv + (unsigned)((n & 1) * 2048 + 1024), ps + (unsigned)((n >> 1) * 4096)); p0[n] = ld8(rs_p, xh + (unsigned)((n & 1) * 2048), ps + (unsigned)((n >> 1) * 4096)); SB(); }
#pragma unroll
    for (int n = 0; n < 4; ++n) pm[n >> 1][n & 1] = ld16(rs_m, xv + (unsigned)(n * 1024), ps);
    SB();
    f32x4 acc[5][2];
    SFOR(n, 8)
      u32x4 q0 = {p0[n].x, p0[n].y, 0u, 0u}; PINV(q0);
      SFOR(t, 5) mf_w<true>(acc[t][1], p1[n], 8 * t + n, n == 0, W); SB(); SEND;
      SFOR(t, 5) mf_w<true>(acc[t][0], q0, 8 * t + n, n == 0, W); SB(); SEND;
    SEND;
#pragma unroll
    for (int n = 0; n < 4; ++n)
#pragma unroll
      for (int t = 0; t < 5; ++t) asm volatile("v_mfma_f32_16x16x32_bf16 %0, %1, %2, %0" : "+v"(acc[t][n & 1]) : "v"(z[t][n >> 1]), "v"(pm[n >> 1][n & 1]));
    asm volatile("s_nop 7" : "+v"(acc[0][1]), "+v"(acc[1][1]), "+v"(acc[2][1]), "+v"(acc[3][1]), "+v"(acc[4][1]));
#pragma unroll
    for (int n = 0; n < 10; ++n) *(f32x4*)(lds + wa + (n % 5 * 2 + n / 5) * 1088) = acc[n % 5][n / 5];
    BAR();
    if (wave == 0 && lane == 0) __hip_atomic_store((gu32*)(cnt + CNT(C_PO + xq) + j - 6 * xq), (unsigned)(s + 3), RLX_AGENT);
#pragma unroll
    for (int i = 0; i < 2; ++i) {
      f32x4 v0 = *(const f32x4*)(lds + ra[i]), v1 = *(const f32x4*)(lds + ra[i] + 272);
#pragma unroll
      for (int w = 1; w < 4; ++w) { v0 += *(const f32x4*)(lds + ra[i] + w * 10880); v1 += *(const f32x4*)(lds + ra[i] + w * 10880 + 272); }
      const u32x4 w_ = {cvtpk(v0[0], v0[1]), cvtpk(v0[2], v0[3]), cvtpk(v1[0], v1[1]), cvtpk(v1[2], v1[3])};
      __builtin_amdgcn_raw_buffer_store_b128(w_, rs_out, (int)oo[i], 32 * s * 1280 * 2, 0);
      asm volatile("s_nop 1" :: "v"(w_) : "memory");
    }
  }
}

#define XPF 200
__device__ __forceinline__ void att_role(const Args& a, unsigned char* lds, const int h, const int wave) {
  char* ws = a.ws;
  unsigned* const cnt = (unsigned*)(ws + O_CNT); unsigned* const tmo = cnt + CNT(C_TMO);
  const int lane = lane_id(), l15 = lane & 15, lq = lane >> 4, par = wave >> 1;
  const __amdgpu_buffer_rsrc_t rs_q = mkrs(ws + O_QF + (size_t)h * 12288), rs_k = mkrs(ws + O_KF + (size_t)h * 12288), rs_km = mkrs(ws + O_KM + (size_t)h * 3072), rs_pt = mkrs(ws + O_PT);
  const float scale = 0.07216878364870323f, LOG2E = 1.4426950408889634f;
  unsigned tprog = 0, vprog = 0;
  {
    const __amdgpu_buffer_rsrc_t rb = __builtin_amdgcn_make_buffer_rsrc((void*)a.Bx, 0, 2048 * 1280, 0x00020000), ra = __builtin_amdgcn_make_buffer_rsrc((void*)a.Ax, 0, 2048 * 1280, 0x00020000);
    const int w16 = (h & 7) * 2 + par, w64 = h * 2 + par;
    nap_rt(XPF);
    unsigned v = 0;
#pragma unroll
    for (int i = 0; i < 4; ++i) v ^= __builtin_amdgcn_raw_buffer_load_b32(rb, lane * 128, 40960 + (i * 16 + w16) * 8192, 0);
#pragma unroll
    for (int i = 0; i < 5; ++i) v ^= __builtin_amdgcn_raw_buffer_load_b32(ra, lane * 128, (i * 64 + w64) * 8192, 0) ^ __builtin_amdgcn_raw_buffer_load_b32(rb, lane * 128, (i * 64 + w64) * 8192, 0);
    asm volatile("" :: "v"(v));
  }
#pragma unroll 1
  for (int s = par; s < 64; s += 2) {
    if (s == 0) wait_ge<32>(cnt + CNT(C_PS + h), 1u, tmo);
    { const unsigned tneed = (unsigned)s; if (tprog < tneed) tprog = wait_t<16>(cnt + CNT(C_PT_ + h), 0, tneed, tmo); if (vprog < tneed) vprog = wait_t<16>(cnt + CNT(C_PV + h), 0, tneed, tmo); }
    f32x4 sc[2][3];
    u32x4 qf[2][6];
    float mx[2];
    {
      u32x4 kf[2][6];
#pragma unroll
      for (int i = 0; i < 12; ++i) { const unsigned so = KSLOT(s) + (unsigned)((i >> 2) * 4096), vo = (unsigned)((i & 3) * 1024 + lane * 16);
        qf[i / 6][i % 6] = ld16(rs_q, vo, so); kf[i / 6][i % 6] = ld16(rs_k, vo, so); }
      SB();
#pragma unroll
      for (int q = 0; q < 2; ++q) {
        mx[q] = -3.0e38f;
#pragma unroll
        for (int t = 0; t <= q; ++t) {
          sc[q][t] = ZERO4;
#pragma unroll
          for (int kk = 0; kk < 6; ++kk) sc[q][t] = mfma(kf[t][kk], qf[q][kk], sc[q][t]);
#pragma unroll
          for (int r = 0; r < 4; ++r) {
            const float v = rbf(rbf(sc[q][t][r]) * scale);
            sc[q][t][r] = (t < q || 4 * lq + r <= l15) ? v : -3.0e38f;
            mx[q] = fmaxf(mx[q], sc[q][t][r]);
          }
        }
        PINV(mx[q]);
      }
    }
    SB();
    if (s >= 60) wait_ge<2>(cnt + CNT(C_PS + h), (unsigned)(s == 63 ? 63 : s + 1), tmo); else wait_ge<8>(cnt + CNT(C_PS + h), (unsigned)(s + 1), tmo);
    {
      u32x4 km[6];
      unsigned spins = 0;
      for (;;) {
        unsigned m = 0u;
#pragma unroll
        for (int kk = 0; kk < 6; ++kk) { km[kk] = ld16(rs_km, (unsigned)(kk * 512 + lq * 128 + ((l15 & 1) + (l15 & 2) * 2 + ((l15 >> 1) & 2)) * 16), MSLOT(s)); m = max(m, max(km[kk].x, km[kk].z)); }
        if (__all(m != ~0u)) break;
        __builtin_amdgcn_s_sleep(3);
        if (!spin_check(spins, tmo)) break;
      }
#pragma unroll
      for (int q = 0; q < 2; ++q) {
        sc[q][2] = ZERO4;
#pragma unroll
        for (int kk = 0; kk < 6; ++kk) sc[q][2] = mfma(km[kk], qf[q][kk], sc[q][2]);
      }
    }
    asm volatile("" : "+v"(sc[0][2]), "+v"(sc[1][2]));
#pragma unroll
    for (int q = 0; q < 2; ++q) {
#pragma unroll
      for (int r = 0; r < 4; ++r) { sc[q][2][r] = rbf(rbf(sc[q][2][r]) * scale); mx[q] = fmaxf(mx[q], sc[q][2][r]); }
      const float m = max32(max16(mx[q])), nml = -m * LOG2E;
      float sum = 0.f, sm = 0.f;
#pragma unroll
      for (int t = 0; t <= q; ++t)
#pragma unroll
        for (int r = 0; r < 4; ++r) { const float e = (t < q || 4 * lq + r <= l15) ? __builtin_amdgcn_exp2f(__builtin_fmaf(sc[q][t][r], LOG2E, nml)) : 0.f; sc[q][t][r] = e; sum += e; }
#pragma unroll
      for (int r = 0; r < 4; ++r) { sc[q][2][r] = __builtin_amdgcn_exp2f(__builtin_fmaf(sc[q][2][r], LOG2E, nml)); sm += sc[q][2][r]; }
      sum = sum32(sum16(__builtin_fmaf(sm, 0.5f, sum)));
      const float inv = 1.0f / sum;
      if (q == 0) st8(rs_pt, (unsigned)(h * 2048 + lane * 8), TSLOT(s), cvtpk(sc[0][0][0] * inv, sc[0][0][1] * inv), cvtpk(sc[0][0][2] * inv, sc[0][0][3] * inv));
      else st16(rs_pt, (unsigned)(h * 2048 + 1024 + lane * 16), TSLOT(s), (u32x4){cvtpk(sc[1][0][0] * inv, sc[1][0][1] * inv), cvtpk(sc[1][0][2] * inv, sc[1][0][3] * inv), cvtpk(sc[1][1][0] * inv, sc[1][1][1] * inv), cvtpk(sc[1][1][2] * inv, sc[1][1][3] * inv)});
      st8(rs_pt, lq < 2 ? (unsigned)(65536 + (h >> 2) * 2048 + q * 1024 + (h & 3) * 256 + l15 * 16 + lq * 8) : 0x80000000u, TSLOT(s), cvtpk(sc[q][2][0] * inv, sc[q][2][1] * inv), cvtpk(sc[q][2][2] * inv, sc[q][2][3] * inv));
    }
    VMW(0);
    if (lane < 4) __hip_atomic_fetch_add((gu32*)(cnt + CNT(C_TOK + 4 * s + lane)), 1u, RLX_AGENT);
  }
}

__device__ __forceinline__ float dot2bf(unsigned x, unsigned y, float c) { return __builtin_amdgcn_fdot2_f32_bf16(__builtin_bit_cast(bf16x2_t, x), __builtin_bit_cast(bf16x2_t, y), c, false); }
__device__ __forceinline__ void a_role(const Args& a, unsigned char* lds, const int j, const int wave) {
  char* ws = a.ws; asm volatile("" : "+s"(ws));
  unsigned* const cnt = (unsigned*)(ws + O_CNT); unsigned* const tmo = cnt + CNT(C_TMO);
  const int lane = lane_id(), l15 = lane & 15, lq = lane >> 4;
  const int c0 = 40 * j - 8 * (j & 1);
  u32x4 wt[3][20];
#define A_WDMA(src_, ld_, off_) do { const int ln_ = lane_id(); _Pragma("unroll") for (int q_ = 0; q_ < 15; ++q_) { const int ins = wave + 4 * q_; const unsigned c = (unsigned)(ins * 64 + ln_), r = c / 6u, pc = c - r * 6u; \
      glds(src_, r * (unsigned)((ld_) * 2) + 16u * pc, lds_addr(lds) + (unsigned)((off_) + ins * 1024)); } } while (0)
#define A_WREAD(off_) do { unsigned o_ = (unsigned)((off_) + (8 * lq + (l15 >> 2)) * 96 + 8 * (l15 & 3)); PINV(o_); \
    _Pragma("unroll") for (int t = 0; t < 3; ++t) { _Pragma("unroll") for (int kk = 0; kk < 20; ++kk) wt[t][kk] = trrd2(lds, o_ + (unsigned)(kk * 3072 + 32 * t), 384u); SB(); } } while (0)
  A_WDMA(a.Awqkv + c0, 1920, 0); A_WDMA(a.Awqkv + 640 + c0, 1920, 61440);
  VMW(0); BAR();
  if (wave < 2) A_WREAD(wave * 61440);
  BAR();
  A_WDMA(a.Awqkv + 1280 + c0, 1920, 0);
  VMW(0); BAR();
  if (wave == 2) A_WREAD(0);
  BAR();
  constexpr int LX = 0, LR = 122880, RS = 304, NR = 128;
  unsigned dvo[10];
#pragma unroll
  for (int i = 0; i < 10; ++i) { const unsigned c = (unsigned)((wave + 4 * i) * 64 + lane), r = c / 80u; dvo[i] = (c ^ (r & 15u)) * 16u; PINV(dvo[i]); }
#define A_XDMA(src_, b_) do { _Pragma("unroll") for (int xi_ = 0; xi_ < 10; ++xi_) glds(src_, dvo[xi_], lds_addr(lds) + (unsigned)(LX + ((b_) & 1) * 40960 + wave * 1024 + xi_ * 4096)); } while (0)
  unsigned xr[4];
#pragma unroll
  for (int q = 0; q < 4; ++q) xr[q] = (unsigned)(LX + l15 * 1280 + ((4 * q + lq) ^ l15) * 16);
  unsigned dv3[14];
#pragma unroll
  for (int i = 0; i < 14; ++i) { const unsigned c = (unsigned)((13 * (wave < 3 ? wave : 0) + i) * 64 + lane), r = c / 80u; dv3[i] = (c ^ (r & 15u)) * 16u + (unsigned)(4096 - (i % 7 - 3) * 1024); PINV(dv3[i]); }
  const unsigned am0 = lds_addr(lds) + (unsigned)(LX + (13 * wave + 3) * 1024);
#define A_XD3(src_, lb_, i_) do { if ((i_) % 7 == 0) m0_set(am0 + (lb_) + (unsigned)((i_) / 7 * 7168)); \
    switch ((i_) % 7) { case 0: glds_o<-3072>(src_, dv3[i_]); break; case 1: glds_o<-2048>(src_, dv3[i_]); break; case 2: glds_o<-1024>(src_, dv3[i_]); break; case 3: glds_o<0>(src_, dv3[i_]); break; \
      case 4: glds_o<1024>(src_, dv3[i_]); break; case 5: glds_o<2048>(src_, dv3[i_]); break; default: glds_o<3072>(src_, dv3[i_]); break; } } while (0)
  A_XDMA(a.Ax, 0);
  A_XDMA(a.Ax + 32 * 640, 1);
  unsigned wo_[3];
#pragma unroll
  for (int t = 0; t < 3; ++t) { const int v = 4 * t + lq - 2 * (j & 1); wo_[t] = (unsigned)(LR + l15 * RS + wave * 96 + (v < 0 ? 40 + (v + 2) * 48 : (v >= 10 ? 40 + (v - 10) * 48 : (v >= 5 ? 8 : 0) + v * 8))); }
  const __amdgpu_buffer_rsrc_t rs_oa = mkrs(ws + O_OA);
#pragma unroll 1
  for (int b = 0; b <= 64; ++b) {
    const int s = b; (void)s;
    if (b == 0) VMW(0); else if (wave < 3) VMW(14);
    BAR();
    const char* xsrc = (const char*)(a.Ax + (size_t)(32 * (b < 62 ? b + 2 : 63)) * 640) - 4096;
    const unsigned xb = (unsigned)(b % 3) * 40960u, xb2 = (unsigned)((b + 2) % 3) * 40960u;
    if (wave < 3) {
      if (b < 64) {
        unsigned xq[4];
#pragma unroll
        for (int q = 0; q < 4; ++q) { xq[q] = xr[q] + xb; PINV(xq[q]); }
#define A_XF(rt_, k_) (*(const u32x4*)(lds + xq[(k_) & 3] + (rt_) * 20480 + ((k_) >> 2) * 256))
        f32x4 acc[2][3]; u32x4 xa[3][2];
#pragma unroll
        for (int k = 0; k < 2; ++k) { xa[k][0] = A_XF(0, k); xa[k][1] = A_XF(1, k); }
#pragma unroll
        for (int k = 0; k < 20; ++k) {
          if (k + 2 < 20) { xa[(k + 2) % 3][0] = A_XF(0, k + 2); xa[(k + 2) % 3][1] = A_XF(1, k + 2); }
#pragma unroll
          for (int t = 0; t < 3; ++t) { acc[0][t] = mfma(wt[t][k], xa[k % 3][0], k == 0 ? ZERO4 : acc[0][t]); acc[1][t] = mfma(wt[t][k], xa[k % 3][1], k == 0 ? ZERO4 : acc[1][t]);
            if (t == 1 && k < 14) { asm volatile("" : "+a"(acc[0][0]), "+a"(acc[0][1]), "+a"(acc[1][0]), "+a"(acc[1][1])); A_XD3(xsrc, xb2, k < 14 ? k : 0); SB(); } }
          if (k >= 14) IL(6, 1);
          asm volatile("" : "+a"(acc[0][0]), "+a"(acc[0][1]), "+a"(acc[0][2]), "+a"(acc[1][0]), "+a"(acc[1][1]), "+a"(acc[1][2])); SB();
        }
        const unsigned ro = (unsigned)(((32 * b) & (NR - 1)) * RS);
#pragma unroll
        for (int t = 0; t < 3; ++t)
#pragma unroll
          for (int rt = 0; rt < 2; ++rt) *(u32x2*)(lds + wo_[t] + ro + rt * 16 * RS) = (u32x2){cvtpk(acc[rt][t][0], acc[rt][t][1]), cvtpk(acc[rt][t][2], acc[rt][t][3])};
      }
    } else {
      const int row = 32 * (b > 0 ? b - 1 : 0) + (lane & 31), hh = lane >> 5;
      const unsigned ho = (unsigned)(LR + 48 * hh);
      unsigned ra[8];
#pragma unroll
      for (int w = 0; w < 8; ++w) { const int p = row - 2 * w; ra[w] = ho + (unsigned)(((p > 0 ? p : 0) & (NR - 1)) * RS); }
#define A_LD10(d_, ad_) do { const u32x4 x0 = *(const u32x4*)(lds + (ad_)), x1 = *(const u32x4*)(lds + (ad_) + 16); const u32x2 x2 = *(const u32x2*)(lds + (ad_) + 32); \
        d_[0] = x0.x; d_[1] = x0.y; d_[2] = x0.z; d_[3] = x0.w; d_[4] = x1.x; d_[5] = x1.y; d_[6] = x1.z; d_[7] = x1.w; d_[8] = x2.x; d_[9] = x2.y; } while (0)
      unsigned q[10], kv[8][10], vv[8][10];
      A_LD10(q, ra[0]);
#pragma unroll
      for (int w = 0; w < 8; ++w) A_LD10(kv[w], ra[w] + 96);
#pragma unroll
      for (int w = 0; w < 8; ++w) A_LD10(vv[w], ra[w] + 192);
      SB();
      float lg[8], mx = -3.0e38f;
#pragma unroll
      for (int w = 0; w < 8; ++w) lg[w] = 0.f;
#pragma unroll
      for (int i = 0; i < 10; ++i)
#pragma unroll
        for (int w = 0; w < 8; ++w) lg[w] = dot2bf(q[i], kv[w][i], lg[w]);
#pragma unroll
      for (int w = 0; w < 8; ++w) { lg[w] = row - 2 * w >= 0 ? rbf(rbf(lg[w]) * 0.22360679774997896f) : rbf(-1000000000.0f); mx = fmaxf(mx, lg[w]); }
      float sum = 0.f;
#pragma unroll
      for (int w = 0; w < 8; ++w) { lg[w] = __expf(lg[w] - mx); sum += lg[w]; }
      const float inv = __builtin_amdgcn_rcpf(sum);
      float ov[20];
#pragma unroll
      for (int d = 0; d < 20; ++d) ov[d] = 0.f;
#pragma unroll
      for (int w = 0; w < 8; w += 2) {
        const unsigned pw = cvtpk(lg[w] * inv, lg[w + 1] * inv);
#pragma unroll
        for (int i = 0; i < 10; ++i) {
          ov[2 * i] = dot2bf(__builtin_amdgcn_perm(vv[w + 1][i], vv[w][i], 0x05040100u), pw, ov[2 * i]);
          ov[2 * i + 1] = dot2bf(__builtin_amdgcn_perm(vv[w + 1][i], vv[w][i], 0x07060302u), pw, ov[2 * i + 1]);
        }
      }
      const unsigned oo = b > 0 ? (unsigned)((row * 640 + 40 * j + 20 * hh) * 2) : 0x80000000u;
#pragma unroll
      for (int i = 0; i < 5; ++i) st8(rs_oa, oo + 8u * i, 0u, cvtpk(ov[4 * i], ov[4 * i + 1]), cvtpk(ov[4 * i + 2], ov[4 * i + 3]));
      if (b > 0 && (b & 15) == 0) { VMW(0); if (lane == 0) __hip_atomic_fetch_add((gu32*)(cnt + CNT(C_AO + (b >> 4) - 1)), 1u, RLX_AGENT); }
    }
  }
  VMW(0);
  BAR();
  {
    const int ln = lane_id(), m15 = ln & 15, mq = ln >> 4, cg = j & 3, rg = j >> 2;
    WReg W;
    {
      unsigned wv[10];
#pragma unroll
      for (int q = 0; q < 10; ++q) { const unsigned c = (unsigned)((wave + 4 * q) * 64 + ln), r = c / 20u; wv[q] = r * 1280u + (c - r * 20u) * 16u; PINV(wv[q]); }
#define A2_WDMA(i_) do { _Pragma("unroll") for (int q_ = 0; q_ < 10; ++q_) glds(a.Awo + (size_t)(128 * (i_)) * 640 + 160 * cg, wv[q_], lds_addr(lds) + (unsigned)(((i_) & 3) * 40960 + (wave + 4 * q_) * 1024)); } while (0)
      A2_WDMA(0); A2_WDMA(1); A2_WDMA(2); A2_WDMA(3);
      const unsigned rb = lds_addr(lds) + (unsigned)((8 * mq + (m15 >> 2)) * 320);
      const unsigned ad[3] = {rb + (unsigned)(64 * wave + 16 * (m15 & 3)), rb + (unsigned)(64 * wave + 16 * (m15 & 3) + 8), rb + (unsigned)(256 + 32 * (wave >> 1) + 8 * (m15 & 3))};
      SFOR(i, 5)
        if (i < 2) VMW(30); else if (i == 2) VMW(20); else if (i == 3) VMW(10); else VMW(0);
        BAR();
        SFOR(n, 6) tr4_w(ad[n >> 1] + (unsigned)((i & 3) * 40960), (n & 1) * 20480, (n & 1) * 20480 + 1280, (n & 1) * 20480 + 10240, (n & 1) * 20480 + 11520, 6 * i + n, W); SEND;
        if (i == 0) { BAR(); A2_WDMA(4); }
      SEND;
    }
    unsigned dv[10], xq[4], x5[4];
#pragma unroll
    for (int i = 0; i < 10; ++i) { const unsigned c = (unsigned)((10 * wave + i) * 64 + ln), r = c / 80u; dv[i] = (c ^ (r & 15u)) * 16u + (unsigned)(4096 - (i % 5 - 2) * 1024); PINV(dv[i]); }
    const char* src = ws + O_OA + (size_t)(512 * rg) * 1280 - 4096;
    const unsigned dm = lds_addr(lds) + (unsigned)((10 * wave + 2) * 1024);
#define A2_X(p_, lb_, i_) do { if ((i_) % 5 == 0) m0_set(dm + (lb_) + (unsigned)((i_) / 5 * 5120)); \
      switch ((i_) % 5) { case 0: glds_x<-2048>(p_, dv[i_]); break; case 1: glds_x<-1024>(p_, dv[i_]); break; case 2: glds_x<0>(p_, dv[i_]); break; case 3: glds_x<1024>(p_, dv[i_]); break; default: glds_x<2048>(p_, dv[i_]); break; } } while (0)
    const __amdgpu_buffer_rsrc_t rs_out = mkrs(a.out + (size_t)(512 * rg) * 1280 + 160 * cg);
    unsigned o2 = (unsigned)((m15 * 1280 + 32 * wave + 8 * mq) * 2), o5 = (unsigned)(((16 * (wave & 1) + m15) * 1280 + 128 + 16 * (wave >> 1) + 4 * mq) * 2); PINV(o2); PINV(o5);
    if (wave == 0) wait_ge<8>(cnt + CNT(C_AO + rg), 16u, tmo);
    BAR();
#pragma unroll
    for (int b = 0; b < 3; ++b) { const char* p_ = src + b * 40960; asm volatile("s_nop 4" : "+s"(p_));
#pragma unroll
      for (int i = 0; i < 10; ++i) A2_X(p_, (unsigned)(b * 40960), i); }
#pragma unroll 1
    for (int b = 0; b < 16; ++b) {
      if (b == 0) VMW(20); else if (b == 1) VMW(23); else VMW(26);
      BAR();
      const char* p_ = src + (b < 13 ? b + 3 : 15) * 40960; unsigned lb = (unsigned)(((b + 3) & 3) * 40960); PINS(p_); PINS(lb);
#pragma unroll
      for (int q = 0; q < 4; ++q) { xq[q] = lds_addr(lds) + (unsigned)((b & 3) * 40960 + m15 * 1280 + ((4 * q + mq) ^ m15) * 16); x5[q] = xq[q] + (unsigned)((wave & 1) * 20480); PINV(xq[q]); PINV(x5[q]); }
      f32x4 acc[5]; u32x4 xa[3][3];
#define A2_XF(n_, k_) LDS16(((n_) == 2 ? x5[(k_) & 3] : xq[(k_) & 3] + (n_) * 20480) + ((k_) >> 2) * 256)
#pragma unroll
      for (int n = 0; n < 6; ++n) xa[n / 3][n % 3] = A2_XF(n % 3, n / 3);
      SFOR(k, 20)
        SFOR(n, 5)
          mf_w<true>(acc[n], xa[k % 3][n >> 1], 12 * (k >> 2) + 4 * (n == 4 ? 2 : (n & 1)) + (k & 3), k == 0, W); SB();
          if (n < 3 && k + 2 < 20) { xa[(k + 2) % 3][n] = A2_XF(n, k + 2); SB(); }
          if (n == 3 && k >= 2 && k < 12) { A2_X(p_, lb, (k + 8) % 10); SB(); }
        SEND;
      SEND;
      asm volatile("s_nop 7" : "+v"(acc[0]), "+v"(acc[1]), "+v"(acc[2]), "+v"(acc[3]), "+v"(acc[4]));
#pragma unroll
      for (int rt = 0; rt < 2; ++rt) { const u32x4 w = {cvtpk(acc[2 * rt][0], acc[2 * rt][1]), cvtpk(acc[2 * rt][2], acc[2 * rt][3]), cvtpk(acc[2 * rt + 1][0], acc[2 * rt + 1][1]), cvtpk(acc[2 * rt + 1][2], acc[2 * rt + 1][3])};
        __builtin_amdgcn_raw_buffer_store_b128(w, rs_out, (int)o2, (32 * b + 16 * rt) * 2560, 0); asm volatile("s_nop 1" :: "v"(w) : "memory"); }
      __builtin_amdgcn_raw_buffer_store_b64((u32x2){cvtpk(acc[4][0], acc[4][1]), cvtpk(acc[4][2], acc[4][3])}, rs_out, (int)o5, 32 * b * 2560, 0);
    }
    VMW(0);
  }
}

#define CALN 6
__device__ __forceinline__ void calibrate(char* ws, int* vote, const int wave) {
  char* cal = ws + O_CAL + wave * 16384;
  unsigned best0 = ~0u, best1 = ~0u, acc = 0;
  const int lane = lane_id();
  for (int r = 0; r < CALN; ++r) {
#pragma unroll
    for (int hf = 0; hf < 2; ++hf) {
      const unsigned long long t0 = __builtin_amdgcn_s_memtime();
      if (lane == 0) acc += __hip_atomic_fetch_add((gu32*)(cal + hf * 8192 + (acc & 0u)), 1u, RLX_AGENT);
      asm volatile("s_waitcnt vmcnt(0)" ::: "memory");
      const unsigned d = (unsigned)(__builtin_amdgcn_s_memtime() - t0);
      if (hf == 0) best0 = d < best0 ? d : best0; else best1 = d < best1 ? d : best1;
    }
  }
  int dl = (int)best1 - (int)best0 + (int)(acc & 0u);
  dl = dl > 400 ? 400 : (dl < -400 ? -400 : dl);
  if ((((unsigned)((size_t)cal >> 21) ^ xcc_side()) & 1u) != 0u) dl = -dl;
  if (lane == 0) vote[wave] = dl;
  __syncthreads();
  if (wave == 0 && lane == 0) __hip_atomic_store((gu32*)((unsigned*)(ws + O_CNT) + CNT(C_POL)), vote[0] + vote[1] + vote[2] + vote[3] < 0 ? 3u : 2u, RLX_AGENT);
  __syncthreads();
}

#define TCH 32
#define XOFF 0
#define CPF 3
__global__ __launch_bounds__(256, 1) void k_mega(Args a) {
  extern __shared__ __attribute__((aligned(16))) unsigned char shm[];
  volatile LAS int* sl = (volatile LAS int*)shm;
  if (threadIdx.x >= 64) {
    u64 pc; unsigned off, len;
    asm volatile("s_getpc_b64 %0\n.Lcpf_mega:\n s_mov_b32 %1, .Lcpf_mega-_Z6k_mega4Args\n s_mov_b32 %2, _Z6k_prepPcPKcS1_S1_S1_S1_-_Z6k_mega4Args" : "=s"(pc), "=s"(off), "=s"(len));
    const __amdgpu_buffer_rsrc_t rs = __builtin_amdgcn_make_buffer_rsrc((void*)(pc - off), 0, (int)len, 0x00020000);
    const unsigned g = ((blockIdx.x >> 3) * 192u + threadIdx.x - 64u) * 16u;
    const u32x4 v0 = __builtin_amdgcn_raw_buffer_load_b128(rs, (int)g, 0, 0), v1 = __builtin_amdgcn_raw_buffer_load_b128(rs, (int)g, 98304, 0);
    asm volatile("" :: "v"(v0), "v"(v1));
  }
  if (threadIdx.x >= 64) {
    const unsigned o = (threadIdx.x - 64u) * (unsigned)(TCH * 65536) + (blockIdx.x >> 3) * 2048u;
    const unsigned v = __builtin_amdgcn_raw_buffer_load_b32(__builtin_amdgcn_make_buffer_rsrc((void*)(a.ws + O_X1), 0, (int)(O_END - O_X1), 0x00020000), (int)o, 0, 0);
    asm volatile("" :: "v"(v));
  }
  if (threadIdx.x == 0) {
    const unsigned x0 = (unsigned)__builtin_amdgcn_s_getreg((3 << 11) | 20) & 7u;
    int slot = 0;
    for (unsigned i = 0; i < 8u; ++i) {
      const unsigned x = (x0 + i) & 7u;
      const unsigned t = __hip_atomic_fetch_add((gu32*)((unsigned*)(a.ws + O_CNT) + CNT(C_SLOT + x)), 1u, RLX_AGENT);
      if (t < 32u) { slot = (int)(x * 32u + t); break; }
    }
    *sl = slot;
  }
  __syncthreads();
  const int slot = __builtin_amdgcn_readfirstlane(*sl);
  __syncthreads();
  const int xq = (slot >> 5) & 3, kq = slot & 31, wave = __builtin_amdgcn_readfirstlane(threadIdx.x >> 6);
#define ENTER __builtin_amdgcn_s_waitcnt(0)
  const int h = xq * 8 + (kq & 7);
  if (slot < 128) {
    if (kq < 8) { ENTER; s_role(a, shm, h, wave);
    } else if (kq < 16) { ENTER; z_role(a, shm, h, wave); }
    else if (kq < 26) { ENTER; r_role(a, shm, xq * 10 + kq - 16, wave); }
    else { if (slot == 26) calibrate(a.ws, (int*)shm, wave); ENTER; if (!(XOFF & 1)) o_role(a, shm, 6 * xq + kq - 26, wave); }
  } else {
    if (kq < 16) { ENTER; t_role(a, shm, h, kq >> 3, wave); }
    else if (kq < 24) { ENTER; w_role(a, shm, h, wave); }
    else if (kq < 28) { ENTER; if (!(XOFF & 4)) a_role(a, shm, xq * 4 + kq - 24, wave); }
    else { ENTER; if (!(XOFF & 2)) att_role(a, shm, xq * 8 + (kq - 28) * 2 + (wave & 1), wave); }
  }
}

#define PFW 3
#define PFN 15
constexpr int PB0 = (PFW & 1) ? 1920 / PFN : 0, PB1 = (PFW & 2) ? 1920 / PFN : 0, PB2 = (PFW & 4) ? 1920 / PFN : 0, PB3 = (PFW & 8) ? 1920 / PFN : 0, PF_B = PB0 + PB1 + PB2 + PB3;
__global__ __launch_bounds__(256) void k_prep(char* ws, const char* w0, const char* w1, const char* w2, const char* w3, const char* w4) {
  if (blockIdx.x < 15) { ((u32x4*)(ws + O_CNT))[blockIdx.x * 256 + threadIdx.x] = (u32x4){0u, 0u, 0u, 0u}; return; }
  if (blockIdx.x < 15 + PF_B) {
    const unsigned bi = blockIdx.x - 15;
    const char* w = bi < PB0 ? w0 : (bi < PB0 + PB1 ? w1 : (bi < PB0 + PB1 + PB2 ? w2 : w3));
    const unsigned bb = bi < PB0 ? bi : (bi < PB0 + PB1 ? bi - PB0 : (bi < PB0 + PB1 + PB2 ? bi - PB0 - PB1 : bi - PB0 - PB1 - PB2));
    const unsigned g = bb * (PFN * 4096u) + threadIdx.x * 16u;
    const __amdgpu_buffer_rsrc_t rs = __builtin_amdgcn_make_buffer_rsrc((void*)w, 0, 1920 * 4096, 0x00020000);
    u32x4 v[PFN];
#pragma unroll
    for (int i = 0; i < PFN; ++i) v[i] = __builtin_amdgcn_raw_buffer_load_b128(rs, (int)g, i * 4096, 0);
    __builtin_amdgcn_sched_barrier(0);
    u32x4 s = v[0];
#pragma unroll
    for (int i = 1; i < PFN; ++i) s ^= v[i];
    asm volatile("" :: "v"(s));
    return;
  }
  {
    u64 pc; unsigned off, len;
    asm volatile("s_getpc_b64 %0\n.Lcpf_prep:\n s_mov_b32 %1, .Lcpf_prep-_Z6k_mega4Args\n s_mov_b32 %2, _Z6k_prepPcPKcS1_S1_S1_S1_-_Z6k_mega4Args" : "=s"(pc), "=s"(off), "=s"(len));
    const unsigned g = min(((blockIdx.x - 15 - PF_B) * 256u + threadIdx.x) * 16u, len - 16u);
    u32x4 v; asm volatile("global_load_dwordx4 %0, %1, %2" : "=v"(v) : "v"(g), "s"(pc - off));
  }
}
extern "C" void kernel_launch(void* const* d_in, const int* in_sizes, int n_in, void* d_out, int out_size, void* d_ws, size_t ws_size, hipStream_t stream) {
  Args a;
  a.Ax = (const u16*)d_in[0]; a.Awqkv = (const u16*)d_in[1]; a.Awo = (const u16*)d_in[2]; a.Bx = (const u16*)d_in[3]; a.mem0 = (const u16*)d_in[4];
  a.Wq = (const u16*)d_in[5]; a.Wk = (const u16*)d_in[6]; a.Wv = (const u16*)d_in[7]; a.Wo = (const u16*)d_in[8];
  a.out = (u16*)d_out;
  a.ws = (char*)(((size_t)d_ws + 2 * MB - 1) & ~(size_t)(2 * MB - 1));
  if ((size_t)(a.ws - (char*)d_ws) + O_END > ws_size) return;
  const int dd[16] = {DDV};
  for (int i = 0; i < 16; ++i) a.d[i] = dd[i];
  static_assert(CNT(C_TOTAL) * 4 == 15 * 4096, "");
  k_prep<<<15 + PF_B + ((CPF & 2) ? 48 : 0), 256, 0, stream>>>(a.ws, (const char*)a.Wq, (const char*)a.Wk, (const char*)a.Wv, (const char*)a.Wo, (const char*)a.Bx);
  (void)hipFuncSetAttribute((const void*)k_mega, hipFuncAttributeMaxDynamicSharedMemorySize, 163840);
  k_mega<<<256, 256, 163840, stream>>>(a);
}
```

```cpp
#include <hip/hip_runtime.h>
#include <hip/hip_bf16.h>
#include <stdint.h>

typedef unsigned short u16;
typedef unsigned long long u64;
typedef __attribute__((ext_vector_type(4))) float f32x4;
typedef __attribute__((address_space(1))) u64 gu64;
typedef __attribute__((address_space(1))) unsigned gu32;
typedef unsigned u32x8 __attribute__((ext_vector_type(8)));
typedef unsigned u32x4 __attribute__((ext_vector_type(4)));
typedef unsigned u32x2 __attribute__((ext_vector_type(2)));
typedef __attribute__((ext_vector_type(4))) short bf16x4;
typedef __attribute__((ext_vector_type(8))) __bf16 bfv8;
typedef __attribute__((ext_vector_type(16))) __bf16 bfv16;
typedef __bf16 bf16x2_t __attribute__((ext_vector_type(2)));
typedef float f32x2_t __attribute__((ext_vector_type(2)));
typedef unsigned v2u_t __attribute__((ext_vector_type(2)));
#define RLX_AGENT __ATOMIC_RELAXED, __HIP_MEMORY_SCOPE_AGENT
#define LAS __attribute__((address_space(3)))

__device__ __forceinline__ float bf2f(u16 v) { return __uint_as_float(((unsigned)v) << 16); }
__device__ __forceinline__ u16 f2bf(float f) { return __builtin_bit_cast(u16, (__bf16)f); }
__device__ __forceinline__ float rbf(float f) { return bf2f(f2bf(f)); }
__device__ __forceinline__ unsigned cvtpk(float lo, float hi) { f32x2_t v = {lo, hi}; return __builtin_bit_cast(unsigned, __builtin_convertvector(v, bf16x2_t)); }
__device__ __forceinline__ __amdgpu_buffer_rsrc_t mkrs(const void* p) { return __builtin_amdgcn_make_buffer_rsrc((void*)p, 0, 0x7fffffff, 0x00020000); }
__device__ __forceinline__ u32x4 ld16(__amdgpu_buffer_rsrc_t rs, unsigned voff, unsigned soff) { return __builtin_amdgcn_raw_buffer_load_b128(rs, (int)voff, (int)soff, 16); }
#define OLA 0
__device__ __forceinline__ u32x4 ld16c(__amdgpu_buffer_rsrc_t rs, unsigned voff, unsigned soff) { return __builtin_amdgcn_raw_buffer_load_b128(rs, (int)voff, (int)soff, OLA); }
__device__ __forceinline__ u32x2 ld8(__amdgpu_buffer_rsrc_t rs, unsigned voff, unsigned soff) { return __builtin_amdgcn_raw_buffer_load_b64(rs, (int)voff, (int)soff, 16); }
__device__ __forceinline__ void st16(__amdgpu_buffer_rsrc_t rs, unsigned voff, unsigned soff, u32x4 w) { __builtin_amdgcn_raw_buffer_store_b128(w, rs, (int)voff, (int)soff, 16); asm volatile("s_nop 1" :: "v"(w) : "memory"); }
__device__ __forceinline__ void st8(__amdgpu_buffer_rsrc_t rs, unsigned voff, unsigned soff, unsigned lo, unsigned hi) { u32x2 w = {lo, hi}; __builtin_amdgcn_raw_buffer_store_b64(w, rs, (int)voff, (int)soff, 16); }
__device__ __forceinline__ bool full16(u32x4 v) { return ((v.x & v.y) != ~0u) & ((v.z & v.w) != ~0u); }
__device__ __forceinline__ float max16(float x) { v2u_t r = __builtin_amdgcn_permlane16_swap(__float_as_uint(x), __float_as_uint(x), false, false); return fmaxf(__uint_as_float(r[0]), __uint_as_float(r[1])); }
__device__ __forceinline__ float max32(float x) { v2u_t r = __builtin_amdgcn_permlane32_swap(__float_as_uint(x), __float_as_uint(x), false, false); return fmaxf(__uint_as_float(r[0]), __uint_as_float(r[1])); }
__device__ __forceinline__ float sum16(float x) { v2u_t r = __builtin_amdgcn_permlane16_swap(__float_as_uint(x), __float_as_uint(x), false, false); return __uint_as_float(r[0]) + __uint_as_float(r[1]); }
__device__ __forceinline__ float sum32(float x) { v2u_t r = __builtin_amdgcn_permlane32_swap(__float_as_uint(x), __float_as_uint(x), false, false); return __uint_as_float(r[0]) + __uint_as_float(r[1]); }
__device__ __forceinline__ float tred32(float a, float b) { v2u_t r = __builtin_amdgcn_permlane32_swap(__float_as_uint(a), __float_as_uint(b), false, false); return __uint_as_float(r[0]) + __uint_as_float(r[1]); }
__device__ __forceinline__ float tred16(float a, float b) { v2u_t r = __builtin_amdgcn_permlane16_swap(__float_as_uint(a), __float_as_uint(b), false, false); return __uint_as_float(r[0]) + __uint_as_float(r[1]); }
__device__ __forceinline__ unsigned ror8(unsigned x) { return (unsigned)__builtin_amdgcn_mov_dpp((int)x, 0x128, 0xf, 0xf, true); }
template <int N> __device__ __forceinline__ void nap() { if constexpr (N > 0) { if constexpr (N > 127) { __builtin_amdgcn_s_sleep(127); nap<N - 127>(); } else __builtin_amdgcn_s_sleep(N); } }
__device__ __forceinline__ unsigned half_of(const char* base, unsigned off, unsigned side, unsigned pol) {
  return off + ((((unsigned)(((size_t)base + off) >> 21) ^ side ^ pol) & 1u) << 13);
}
__device__ __forceinline__ unsigned side_map(const void* base, unsigned L, unsigned side, unsigned pol) { return half_of((const char*)base, (L >> 13) << 14, side, pol) + (L & 8191u); }
constexpr unsigned CH_SPIN_CAP = 1u << 18;
__device__ __forceinline__ bool spin_check(unsigned& spins, unsigned* tmop) {
  asm volatile("" ::: "memory");
  ++spins;
  if ((spins & 63u) == 0u) {
    gu32* tmo = (gu32*)tmop;
    if (__builtin_amdgcn_readfirstlane(__hip_atomic_load(tmo, RLX_AGENT))) return false;
    if (spins > CH_SPIN_CAP) { __hip_atomic_store(tmo, 1u, RLX_AGENT); return false; }
  }
  return true;
}
__device__ __forceinline__ unsigned xcc_side() { return ((unsigned)__builtin_amdgcn_s_getreg((3 << 11) | 20) >> 2) & 1u; }
__device__ __forceinline__ f32x4 mfma(u32x4 a, u32x4 b, f32x4 c) { return __builtin_amdgcn_mfma_f32_16x16x32_bf16(__builtin_bit_cast(bfv8, a), __builtin_bit_cast(bfv8, b), c, 0, 0, 0); }
__device__ __forceinline__ f32x4 smf(u32x4 a, u32x8 b, f32x4 c, int ix) { return __builtin_amdgcn_smfmac_f32_16x16x64_bf16(__builtin_bit_cast(bfv8, a), __builtin_bit_cast(bfv16, b), c, ix, 0, 0); }
__device__ __forceinline__ u32x4 lo4(u32x8 v) { return __builtin_shufflevector(v, v, 0, 1, 2, 3); }
__device__ __forceinline__ u32x4 hi4(u32x8 v) { return __builtin_shufflevector(v, v, 4, 5, 6, 7); }
__device__ __forceinline__ u32x8 cat8(u32x4 a, u32x4 b) { return __builtin_shufflevector(a, b, 0, 1, 2, 3, 4, 5, 6, 7); }
#define BAR() do { asm volatile("s_waitcnt lgkmcnt(0)" ::: "memory"); __builtin_amdgcn_s_barrier(); asm volatile("" ::: "memory"); } while (0)
#define SB() __builtin_amdgcn_sched_barrier(0)
#define VMW(n) asm volatile("s_waitcnt vmcnt(" #n ")" ::: "memory")
#define ZERO4 ((f32x4){0.f, 0.f, 0.f, 0.f})
__device__ __forceinline__ u32x2 trrd(const unsigned char* lds, unsigned off) { return __builtin_bit_cast(u32x2, __builtin_amdgcn_ds_read_tr16_b64_v4i16((LAS bf16x4*)(lds + off))); }
__device__ __forceinline__ u32x4 trrd2(const unsigned char* lds, unsigned off, unsigned d) { const u32x2 lo = trrd(lds, off), hi = trrd(lds, off + d); return (u32x4){lo.x, lo.y, hi.x, hi.y}; }


#include <stdlib.h>
#define SAG 16
#define DDV 84,84,10,0,30,0,127,99,105,115,25,30,20,12,0,330
#define XLN 4
#define XLS 2
struct Args {
  const u16* mem0; const u16* Wq; const u16* Wk; const u16* Wv; const u16* Wo; const u16* Bx; const u16* Ax; const u16* Awqkv; const u16* Awo;
  u16* out; char* ws;
  int d[16];
};
constexpr size_t MB = 1u << 20;
#define WOFF 0
#define NK 16
#define NU 16
#define NZ 16
#define NP 16
#define KSIDE 1
constexpr unsigned X1_STEP = 32768, Z_STEP = 1u << 20, K_STEP = 1u << 20;
constexpr size_t O_PROF = 0, O_CNT = (WOFF + 1) * MB, O_CAL = O_CNT + 65536, O_TS = O_CAL + 65536, O_X1 = (WOFF + 2) * MB, O_PS = O_X1 + 1 * MB, O_KM = O_PS + 1 * MB, O_PT = O_KM + 2 * MB, O_OA = O_PT + NZ * 81920,
                 O_KF = O_PT + 4 * MB, O_QF = O_KF + NK * (size_t)K_STEP, O_VF = O_QF + NK * (size_t)K_STEP, O_ZB = KSIDE == 1 ? O_KF : O_VF + NK * MB, O_UO = O_VF + (KSIDE == 1 ? NK : NK + NZ) * MB, O_END = O_UO + NU * 40 * (size_t)32768;
static_assert(O_ZB % (2 * MB) == 0 && NP <= 16 && NZ <= 16 && NZ <= NK && NK % 4 == 0 && O_OA + 2048 * 1280 <= O_KF, "");
#define MSLOT(s_) (((unsigned)(s_) & (NZ - 1u)) * 98304u)
#define TSLOT(s_) (((unsigned)(s_) & (NZ - 1u)) * 81920u)
__device__ __forceinline__ unsigned k_fix(const unsigned o, const unsigned key) { return o ^ ((o >> 8) & 0x2000u) ^ key; }
#define K_KEY() ((((unsigned)((size_t)(ws + O_KF) >> 21) ^ (KSIDE == 2 ? (unsigned)h : KSIDE ^ pol)) & 1u) << 13)
#define KSLOT(s_) k_fix(((unsigned)(s_) & (NK - 1u)) * K_STEP, kkey)
#define ZSLOT(s_) (((unsigned)(s_) & (NZ - 1u)) * Z_STEP)
#define XSLOT(s_) (((unsigned)(s_) & (NP - 1u)) * X1_STEP)
#define CNT(i) (32 * (i))
__device__ __forceinline__ unsigned u_off(const unsigned s, const unsigned c) { return ((s & (NU - 1u)) * 40u + c) << 15; }
constexpr int PRE = 4;
#define WGATE 11
static_assert(WGATE + 3 <= NK && WGATE <= NU && WGATE + PRE + 1 <= NZ && NP >= PRE + 4, "");
constexpr unsigned NFILL = 40 + 64;
constexpr unsigned SENT = 0xFF81FF81u;
#define SENT4 ((u32x4){SENT, SENT, SENT, SENT})
#define PSLOT(s_) (((s_) & (NP - 1)) * 65536)
constexpr int C_SLOT = 0, C_TMO = 8, C_POL = 9, C_FILL = 10, C_PO = 12, C_PT_ = 32, C_PV = 64, C_PS = 96, C_PW = 128, C_PZ = 160, C_TOK = 192, C_AO = 464, C_TOTAL = 480;
__device__ __forceinline__ float nopk(float x) { asm("" : "+v"(x)); return x; }
__device__ __forceinline__ float fmx(float a, float b) { float big = __builtin_inff(); asm("" : "+s"(big)); return __builtin_amdgcn_fmed3f(a, b, big); }
__device__ __forceinline__ float mx16(float x) { v2u_t r = __builtin_amdgcn_permlane16_swap(__float_as_uint(x), __float_as_uint(x), false, false); return fmx(__uint_as_float(r[0]), __uint_as_float(r[1])); }
__device__ __forceinline__ float mx32(float x) { v2u_t r = __builtin_amdgcn_permlane32_swap(__float_as_uint(x), __float_as_uint(x), false, false); return fmx(__uint_as_float(r[0]), __uint_as_float(r[1])); }
__device__ __forceinline__ int lane_id() { int l; asm volatile("v_mbcnt_lo_u32_b32 %0, -1, 0\n v_mbcnt_hi_u32_b32 %0, -1, %0" : "=v"(l)); return l; }
__device__ __forceinline__ unsigned sent_min(unsigned m, unsigned x, unsigned y) { unsigned r = min(min(m, x ^ SENT), y ^ SENT); asm("" : "+v"(r)); return r; }
__device__ __forceinline__ unsigned sent_min(unsigned m, u32x4 v) { return sent_min(sent_min(m, v.x, v.y), v.z, v.w); }
#define RT_MARK(t_) asm volatile("s_memrealtime %0\n s_waitcnt lgkmcnt(0)" : "=s"(t_) :: "memory")
__device__ __forceinline__ unsigned wait_rt(unsigned target) {
  unsigned t;
  target = (unsigned)__builtin_amdgcn_readfirstlane(target);
  asm volatile("s_branch .Lwb%=\n.Lwa%=:\n s_sleep 1\n.Lwb%=:\n s_memrealtime vcc\n s_waitcnt lgkmcnt(0)\n s_sub_i32 %0, %1, vcc_lo\n s_add_i32 vcc_hi, %0, -9\n s_cmp_lt_u32 vcc_hi, 392\n s_cbranch_scc1 .Lwa%=\n"
               " s_add_i32 %0, %0, -1\n s_cmp_lt_u32 %0, 8\n s_cbranch_scc1 .Lwb%=\n s_mov_b32 %0, vcc_lo" : "=&s"(t) : "s"(target) : "vcc", "scc", "memory");
  return t;
}
__device__ __forceinline__ unsigned ts_max(unsigned v) {
  if ((unsigned)__ballot(v == ~0u)) return ~0u;
  const unsigned base = (unsigned)__builtin_amdgcn_readfirstlane(v) - 4096u;
  unsigned d = v == ~0u ? 0u : v - base;
  d = max(d, (unsigned)__builtin_amdgcn_mov_dpp((int)d, 0x121, 0xf, 0xf, true)); d = max(d, (unsigned)__builtin_amdgcn_mov_dpp((int)d, 0x122, 0xf, 0xf, true));
  d = max(d, (unsigned)__builtin_amdgcn_mov_dpp((int)d, 0x124, 0xf, 0xf, true)); d = max(d, (unsigned)__builtin_amdgcn_mov_dpp((int)d, 0x128, 0xf, 0xf, true));
  return (base + max(max((unsigned)__builtin_amdgcn_readlane(d, 0), (unsigned)__builtin_amdgcn_readlane(d, 16)), max((unsigned)__builtin_amdgcn_readlane(d, 32), (unsigned)__builtin_amdgcn_readlane(d, 48)))) & ~1u;
}
#define TS_OFF(l_) (((l_) & 31) * 256 + ((l_) >> 5) * 16384)
#define TS_STEP(s_) (((s_) & 1) * 128 + ((s_) >> 1) * 4)
__device__ __forceinline__ unsigned ts_settle(const __amdgpu_buffer_rsrc_t rs_ts, const int tss, unsigned t, const int lead) {
  for (int n = 0; n < 48; ++n) {
    unsigned now;
    if (t == ~0u) { __builtin_amdgcn_s_sleep(4); u64 t_; RT_MARK(t_); now = (unsigned)t_; } else now = wait_rt(t - (unsigned)lead);
    t = ts_max(__builtin_amdgcn_raw_buffer_load_b32(rs_ts, TS_OFF(lane_id()), tss, 16));
    if (t != ~0u && (int)(t - now) <= lead + 4) break;
  }
  return t;
}
#define ADAPT(d_, failed_) do { if (s > 3) d_ += (failed_) ? a.d[5] : -(int)((s & a.d[6]) == 0); } while (0)
__device__ __forceinline__ void nap_rt(int n) { for (int i = 0; i < n; ++i) __builtin_amdgcn_s_sleep(1); }
#define PINV(x) asm volatile("" : "+v"(x))
#define PINS(x) asm volatile("" : "+s"(x))
#define PINU(x) asm volatile("" :: "v"(x))
#define IL(nm, nv) do { _Pragma("unroll") for (int il_ = 0; il_ < (nm); ++il_) { __builtin_amdgcn_sched_group_barrier(0x008, 1, 0); __builtin_amdgcn_sched_group_barrier(0x7f6, nv, 0); } } while (0)
constexpr int L_MEM = 0, L_QI = 10240;
constexpr int L_B0 = 16384, L_B1 = 65536, L_B2 = 114688;
constexpr int L_QA = L_B1, L_XV = L_B1 + 9216, L_ST = L_B1 + 10240, L_FLG = L_B1 + 10752, L_XR = L_B1 + 12288;

#define TR8_ASM "ds_read_b64_tr_b16 %0, %8\n ds_read_b64_tr_b16 %1, %8 offset:1536\n ds_read_b64_tr_b16 %2, %8 offset:12288\n ds_read_b64_tr_b16 %3, %8 offset:13824\n" \
               "ds_read_b64_tr_b16 %4, %8 offset:24576\n ds_read_b64_tr_b16 %5, %8 offset:26112\n ds_read_b64_tr_b16 %6, %8 offset:36864\n ds_read_b64_tr_b16 %7, %8 offset:38400\n s_waitcnt lgkmcnt(0)"
__device__ __forceinline__ void tr8(const bool agpr, const unsigned ad, u32x8& f0, u32x8& f1) {
  u32x2 a, b, c, d, e, f, g, h;
  if (agpr) asm volatile(TR8_ASM : "=&a"(a), "=&a"(b), "=&a"(c), "=&a"(d), "=&a"(e), "=&a"(f), "=&a"(g), "=&a"(h) : "v"(ad) : "memory");
  else asm volatile(TR8_ASM : "=&v"(a), "=&v"(b), "=&v"(c), "=&v"(d), "=&v"(e), "=&v"(f), "=&v"(g), "=&v"(h) : "v"(ad) : "memory");
  f0 = (u32x8){a.x, a.y, b.x, b.y, c.x, c.y, d.x, d.y}; f1 = (u32x8){e.x, e.y, f.x, f.y, g.x, g.y, h.x, h.y};
}
__device__ __forceinline__ void tr8d(const bool agpr, const unsigned ad, u32x4& f0, u32x4& f1, u32x4& f2, u32x4& f3) {
  u32x2 a, b, c, d, e, f, g, h;
  if (agpr) asm volatile(TR8_ASM : "=&a"(a), "=&a"(b), "=&a"(c), "=&a"(d), "=&a"(e), "=&a"(f), "=&a"(g), "=&a"(h) : "v"(ad) : "memory");
  else asm volatile(TR8_ASM : "=&v"(a), "=&v"(b), "=&v"(c), "=&v"(d), "=&v"(e), "=&v"(f), "=&v"(g), "=&v"(h) : "v"(ad) : "memory");
  f0 = (u32x4){a.x, a.y, b.x, b.y}; f1 = (u32x4){c.x, c.y, d.x, d.y}; f2 = (u32x4){e.x, e.y, f.x, f.y}; f3 = (u32x4){g.x, g.y, h.x, h.y};
}
#define TR4_ASM "ds_read_b64_tr_b16 %0, %4 offset:%5\n ds_read_b64_tr_b16 %1, %4 offset:%6\n ds_read_b64_tr_b16 %2, %4 offset:%7\n ds_read_b64_tr_b16 %3, %4 offset:%8\n s_waitcnt lgkmcnt(0)"
template <int J> __device__ __forceinline__ void tr4(const bool agpr, const unsigned ad, u32x8& f) {
  u32x2 a, b, c, d;
  if (agpr) asm volatile(TR4_ASM : "=&a"(a), "=&a"(b), "=&a"(c), "=&a"(d) : "v"(ad), "n"(16384 * J), "n"(16384 * J + 4096), "n"(16384 * J + 8192), "n"(16384 * J + 12288) : "memory");
  else asm volatile(TR4_ASM : "=&v"(a), "=&v"(b), "=&v"(c), "=&v"(d) : "v"(ad), "n"(16384 * J), "n"(16384 * J + 4096), "n"(16384 * J + 8192), "n"(16384 * J + 12288) : "memory");
  f = (u32x8){a.x, a.y, b.x, b.y, c.x, c.y, d.x, d.y};
}
template <int J> __device__ __forceinline__ void tr4d(const bool agpr, const unsigned ad, u32x4& f0, u32x4& f1) {
  u32x2 a, b, c, d;
  if (agpr) asm volatile(TR4_ASM : "=&a"(a), "=&a"(b), "=&a"(c), "=&a"(d) : "v"(ad), "n"(16384 * J), "n"(16384 * J + 4096), "n"(16384 * J + 8192), "n"(16384 * J + 12288) : "memory");
  else asm volatile(TR4_ASM : "=&v"(a), "=&v"(b), "=&v"(c), "=&v"(d) : "v"(ad), "n"(16384 * J), "n"(16384 * J + 4096), "n"(16384 * J + 8192), "n"(16384 * J + 12288) : "memory");
  f0 = (u32x4){a.x, a.y, b.x, b.y}; f1 = (u32x4){c.x, c.y, d.x, d.y};
}
__device__ __forceinline__ void glds(const void* sbase, unsigned voff, unsigned ldsdst) {
  unsigned keep;
  asm volatile("s_mov_b32 %0, m0\n s_mov_b32 m0, %3\n s_nop 0\n global_load_lds_dwordx4 %1, %2\n s_mov_b32 m0, %0" : "=&s"(keep) : "v"(voff), "s"(sbase), "s"(ldsdst) : "memory");
}
__device__ __forceinline__ void m0_set(unsigned ldsdst) { asm volatile("s_mov_b32 m0, %0\n s_nop 0" :: "s"(ldsdst) : "memory"); }
template <int OFF> __device__ __forceinline__ void glds_o(const void* sbase, unsigned voff) { asm volatile("global_load_lds_dwordx4 %0, %1 offset:%2" :: "v"(voff), "s"(sbase), "n"(OFF) : "memory"); }
template <int OFF> __device__ __forceinline__ void glds_x(const void* sbase, unsigned voff) { asm volatile("global_load_lds_dwordx4 %0, %1 offset:%2 sc1" :: "v"(voff), "s"(sbase), "n"(OFF) : "memory"); }
__device__ __forceinline__ unsigned lds_addr(const unsigned char* p) { return (unsigned)(size_t)(const LAS unsigned char*)p; }
__device__ __forceinline__ unsigned ld_word(const unsigned* p) { return (unsigned)__builtin_amdgcn_readfirstlane(__hip_atomic_load((const gu32*)p, RLX_AGENT)); }
template <int SLEEP>
__device__ __forceinline__ unsigned wait_ge(const unsigned* p, unsigned target, unsigned* tmo) {
  unsigned spins = 0, v;
  while ((v = ld_word(p)) < target) { __builtin_amdgcn_s_sleep(SLEEP); if (!spin_check(spins, tmo)) break; }
  asm volatile("" ::: "memory");
  return v;
}
__device__ __forceinline__ unsigned min_t(const u32x4 w, const int kv) {
  unsigned v = min(min(w.x, w.y), w.z); if (!kv) v = min(v, w.w);
  return (unsigned)__builtin_amdgcn_readfirstlane(v);
}
template <int SLEEP>
__device__ __forceinline__ unsigned wait_t(const unsigned* p, const int first, unsigned target, unsigned* tmo) {
  unsigned spins = 0, v;
  for (;;) {
    v = min_t(ld16(mkrs(p), 0u, 0u), first);
    if (v >= target) break;
    __builtin_amdgcn_s_sleep(SLEEP); if (!spin_check(spins, tmo)) break;
  }
  asm volatile("" ::: "memory");
  return v;
}
template <int SLEEP>
__device__ __forceinline__ void wait_heads(const unsigned* p, const int second, unsigned target, unsigned* tmo) {
  unsigned spins = 0; const int ln = lane_id();
  while (!__all(__builtin_amdgcn_raw_buffer_load_b32(mkrs(p), (ln & 31) * 128 + (ln >> 5) * second * 4, 0, 16) >= target)) { __builtin_amdgcn_s_sleep(SLEEP); if (!spin_check(spins, tmo)) break; }
  asm volatile("" ::: "memory");
}
#define WNAP 12
template <int N> __device__ __forceinline__ void nap4() { if constexpr (N > 16) { asm volatile("s_nop 15"); nap4<N - 16>(); } else if constexpr (N > 0) asm volatile("s_nop %0" :: "n"(N - 1)); }
#define DPPM(d_, s_, ctl_) asm volatile("v_max_f32_dpp %0, %1, %1 " ctl_ " row_mask:0xf bank_mask:0xf" : "=v"(d_) : "v"(s_))
#define XOR1 "quad_perm:[1,0,3,2]"
#define XOR2 "quad_perm:[2,3,0,1]"
__device__ __forceinline__ void light(const __amdgpu_buffer_rsrc_t rs, unsigned vo, const unsigned so, unsigned* tmo, const int slow = 0) {
  unsigned spins = 0;
  for (;;) {
    const bool empty = __builtin_amdgcn_raw_buffer_load_b32(rs, (int)vo, (int)so, 16) == SENT;
    if (!__any(empty) || !spin_check(spins, tmo)) break;
    vo = empty ? vo : 0x80000000u;
    if (slow) __builtin_amdgcn_s_sleep(16); else __builtin_amdgcn_s_sleep(XLS);
  }
}
__device__ __forceinline__ void x1_light(const __amdgpu_buffer_rsrc_t rs_x1, const unsigned so, unsigned* tmo) {
  const unsigned ln = (unsigned)lane_id();
  light(rs_x1, ln < 40u ? (ln >> 5) * 16384u + (ln & 31u) * 256u + 252u : 0x80000000u, so, tmo);
}
__device__ __forceinline__ unsigned touch(const void* p, const int bytes, const int piece) { return __builtin_amdgcn_raw_buffer_load_b32(__builtin_amdgcn_make_buffer_rsrc((void*)p, 0, bytes, 0x00020000), lane_id() * 128, piece * 8192, 0); }
__device__ __forceinline__ void touch_rows(const Args& a, const int w) {
  const void* p = w % 3 == 0 ? (const void*)a.Bx : (w % 3 == 1 ? (const void*)a.Ax : (const void*)(a.ws + O_OA));
  unsigned v = 0;
#pragma unroll
  for (int i = 0; i < 5; ++i) v ^= touch(p, 2048 * 1280, w / 3 + 64 * i);
  asm volatile("" :: "v"(v));
}
#define DS(x) do { x; SB(); } while (0)
template <int V> struct IC { static constexpr int v = V; };
template <int I, int N, class F> __device__ __forceinline__ void sfor(F&& f) { if constexpr (I < N) { f(IC<I>{}); sfor<I + 1, N>(f); } }
#define SFOR(i_, n_) sfor<0, n_>([&](auto i_##_c) __attribute__((always_inline)) { constexpr int i_ = decltype(i_##_c)::v;
#define SEND })
typedef unsigned u32x32 __attribute__((ext_vector_type(32)));
typedef unsigned u32x16 __attribute__((ext_vector_type(16)));
struct WReg { u32x32 b[11]; u32x16 e; };
#define WR_SW(f_, X) switch ((f_) >> 2) { case 0: X("a", "{a[0:31]}", W.b[0]); break; case 1: X("a", "{a[32:63]}", W.b[1]); break; case 2: X("a", "{a[64:95]}", W.b[2]); break; case 3: X("a", "{a[96:127]}", W.b[3]); break; \
    case 4: X("a", "{a[128:159]}", W.b[4]); break; case 5: X("a", "{a[160:191]}", W.b[5]); break; case 6: X("a", "{a[192:223]}", W.b[6]); break; case 7: X("a", "{a[224:255]}", W.b[7]); break; \
    case 8: X("v", "{v[142:173]}", W.b[8]); break; case 9: X("v", "{v[174:205]}", W.b[9]); break; case 10: X("v", "{v[206:237]}", W.b[10]); break; default: X("v", "{v[238:253]}", W.e); break; }
__device__ __forceinline__ void smf_w(f32x4& c, const u32x4 a, const int f, const int ix, const WReg& W) {
  const int r = f < 32 ? 8 * f : 8 * f - 114;
#define X_(rf_, cs_, v_) asm volatile("v_smfmac_f32_16x16x64_bf16 %0, %1, " rf_ "[%c3:%c4], %2" : "+v"(c) : "v"(a), "v"(ix), "n"(r), "n"(r + 7), cs_(v_))
  WR_SW(f, X_);
#undef X_
}
template <bool WA> __device__ __forceinline__ void mf_w(f32x4& c, const u32x4 x, const int d, const bool first, const WReg& W) {
  const int r = d < 64 ? 4 * d : 4 * d - 114;
#define X_(rf_, cs_, v_) if (WA) { if (first) asm volatile("v_mfma_f32_16x16x32_bf16 %0, " rf_ "[%c2:%c3], %1, 0" : "=&v"(c) : "v"(x), "n"(r), "n"(r + 3), cs_(v_)); \
      else asm volatile("v_mfma_f32_16x16x32_bf16 %0, " rf_ "[%c2:%c3], %1, %0" : "+v"(c) : "v"(x), "n"(r), "n"(r + 3), cs_(v_)); } \
    else { if (first) asm volatile("v_mfma_f32_16x16x32_bf16 %0, %1, " rf_ "[%c2:%c3], 0" : "=&v"(c) : "v"(x), "n"(r), "n"(r + 3), cs_(v_)); \
      else asm volatile("v_mfma_f32_16x16x32_bf16 %0, %1, " rf_ "[%c2:%c3], %0" : "+v"(c) : "v"(x), "n"(r), "n"(r + 3), cs_(v_)); }
  WR_SW(d >> 1, X_);
#undef X_
}
__device__ __forceinline__ void ld_w(const u32x4 rs, const unsigned vo, const unsigned so, const int off, const int f, const bool def, WReg& W) {
  const int r = f < 32 ? 8 * f : 8 * f - 114;
#define Y_(md_, rf_, cs_, v_) asm volatile("s_nop 4\n buffer_load_dwordx4 " rf_ "[%c6:%c7], %1, %2, %3 offen offset:%4 sc1\n buffer_load_dwordx4 " rf_ "[%c8:%c9], %1, %2, %3 offen offset:%5 sc1" \
    : md_ cs_(v_) : "v"(vo), "s"(rs), "s"(so), "n"(off), "n"(off + 1024), "n"(r), "n"(r + 3), "n"(r + 4), "n"(r + 7) : "memory")
#define X_(rf_, cs_, v_) if (def) Y_("=&", rf_, cs_, v_); else Y_("+", rf_, cs_, v_)
  WR_SW(f, X_);
#undef X_
#undef Y_
}
__device__ __forceinline__ u32x4 mkdesc(const void* p) { const size_t b = (size_t)p; return (u32x4){(unsigned)b, (unsigned)(b >> 32) & 0xffffu, 0x7fffffffu, 0x00020000u}; }
__device__ __forceinline__ void tr4_w(const unsigned ad, const int o0, const int o1, const int o2, const int o3, const int f, WReg& W) {
  const int r = f < 32 ? 8 * f : 8 * f - 114;
#define Y_(md_, rf_, cs_, v_) asm volatile("ds_read_b64_tr_b16 " rf_ "[%c6:%c7], %1 offset:%2\n ds_read_b64_tr_b16 " rf_ "[%c8:%c9], %1 offset:%3\n ds_read_b64_tr_b16 " rf_ "[%c10:%c11], %1 offset:%4\n ds_read_b64_tr_b16 " rf_ "[%c12:%c13], %1 offset:%5\n s_waitcnt lgkmcnt(0)" \
    : md_ cs_(v_) : "v"(ad), "n"(o0), "n"(o1), "n"(o2), "n"(o3), "n"(r), "n"(r + 1), "n"(r + 2), "n"(r + 3), "n"(r + 4), "n"(r + 5), "n"(r + 6), "n"(r + 7) : "memory")
#define X_(rf_, cs_, v_) if ((f & 3) == 0) Y_("=&", rf_, cs_, v_); else Y_("+", rf_, cs_, v_)
  WR_SW(f, X_);
#undef X_
#undef Y_
}
#define TRS0(ad_, f_) tr4_w(ad_, 0, 1536, 12288, 13824, f_, W)
#define TRS1(ad_, f_) tr4_w(ad_, 24576, 26112, 36864, 38400, f_, W)
#define TRO4(ad_, j_, f_) tr4_w(ad_, 16384 * (j_), 16384 * (j_) + 4096, 16384 * (j_) + 8192, 16384 * (j_) + 12288, f_, W)
#define SMFV(c_, a_, b_) do { asm volatile("v_smfmac_f32_16x16x64_bf16 %0, %1, %2, %3" : "+v"(c_) : "v"(a_), "v"(b_), "v"(ix)); SB(); } while (0)
#define SMFW(c_, a_, f_) do { smf_w(c_, a_, f_, ix, W); SB(); } while (0)
#define ADD1(d_, x_, y_) do { asm volatile("v_add_f32 %0, %1, %2" : "=v"(d_) : "v"(x_), "v"(y_)); SB(); } while (0)
#define ADD2(d0_, d1_, c_) do { asm volatile("v_add_f32 %0, %2, %3\n v_add_f32 %1, %4, %5" : "=&v"(d0_), "=v"(d1_) : "v"(c_[0]), "v"(c_[2]), "v"(c_[1]), "v"(c_[3])); SB(); } while (0)
#define CVT1(d_, x_, y_) do { asm volatile("v_cvt_pk_bf16_f32 %0, %1, %2" : "=v"(d_) : "v"(x_), "v"(y_)); SB(); } while (0)
#define EPI3(d_, c_) do { float e_; asm volatile("v_add_f32 %1, %2, %3\n v_add_f32 %0, %4, %5\n v_cvt_pk_bf16_f32 %0, %1, %0" : "=&v"(d_), "=&v"(e_) : "v"(c_[0]), "v"(c_[2]), "v"(c_[1]), "v"(c_[3])); SB(); } while (0)
#define EPI4(d_, m_, c_) do { float e_, f_; asm volatile("v_add_f32 %1, %4, %5\n v_add_f32 %2, %6, %7\n v_cvt_pk_bf16_f32 %0, %1, %2\n v_cmp_u_f32_e64 %3, %1, %2" : "=&v"(d_), "=&v"(e_), "=&v"(f_), "=s"(m_) : "v"(c_[0]), "v"(c_[2]), "v"(c_[1]), "v"(c_[3])); SB(); } while (0)
#define CVTN(d_, m_, x_, y_) do { NANM(m_, x_, y_); CVT1(d_, x_, y_); } while (0)
#define ADDCVT(d_, x_, c_) do { asm volatile("v_add_f32 %0, %2, %3\n v_cvt_pk_bf16_f32 %0, %1, %0" : "=&v"(d_) : "v"(x_), "v"(c_[1]), "v"(c_[3])); SB(); } while (0)
#define LGW(n_) do { __builtin_amdgcn_s_waitcnt(0xC07F | ((n_) << 8)); SB(); } while (0)
#define LBAR(n_) do { asm volatile("s_waitcnt lgkmcnt(" #n_ ")" ::: "memory"); __builtin_amdgcn_s_barrier(); asm volatile("" ::: "memory"); SB(); } while (0)
#define FILL() do { __builtin_amdgcn_s_setprio(0); SB(); } while (0)
#define NANM(m_, x_, y_) do { asm volatile("v_cmp_u_f32_e64 %0, %1, %2" : "=s"(m_) : "v"(x_), "v"(y_)); SB(); } while (0)
#define LW32(ad_, off_, v_) (*(unsigned*)(lds + (ad_) + (off_)) = (v_))
#define MEMB() asm volatile("" ::: "memory")
#define LG0() __builtin_amdgcn_s_waitcnt(0xC07F)
#define FLAG(i_) do { MEMB(); *(volatile LAS unsigned char*)(LAS unsigned char*)(lds + fla + 4 * (i_)) = (unsigned char)snv; SB(); } while (0)
#define FLRD(i_) (*(const volatile LAS unsigned*)(LAS unsigned char*)(lds + flr + 4 * (i_)))
#define SBUF(i_) (((i_) % 3) == 0 ? L_B0 : (((i_) % 3) == 1 ? L_B1 : L_B2))
#define SDMA_INIT() unsigned svo[12]; const int sq0 = 3 * ((h >> 2) & 3); \
    _Pragma("unroll") for (int q_ = 0; q_ < 12; ++q_) { const int qr_ = q_ + sq0, ins = wave + 4 * (qr_ < 12 ? qr_ : qr_ - 12); const unsigned c = (unsigned)(ins * 64 + lane), r = c / 24u, pc = c - r * 24u, sw = ((r >> 1) & 1u) * 2u + ((r >> 3) & 1u) * 4u; \
      svo[q_] = r * 12288u + 16u * (pc ^ sw); PINV(svo[q_]); }
#define KROT(kr_) ((kr_) < 4 ? (((kr_) + h) & 3) : 4)
#define SDMA(W_m, kr_, i_) do { const char* W_ = (const char*)((W_m) + (size_t)(128 * KROT(kr_)) * 6144 + 192 * h); \
    _Pragma("unroll") for (int q_ = 0; q_ < 12; ++q_) { const int qr_ = q_ + sq0; glds(W_, svo[q_], lds_addr(lds) + (unsigned)(SBUF(i_) + (wave + 4 * (qr_ < 12 ? qr_ : qr_ - 12)) * 1024)); } SB(); } while (0)
#define SDMAQK(i_) SDMA(((i_) & 1) ? a.Wk : a.Wq, (i_) >> 1, i_)
#define TRO(tile_) (trow * 384u + (((unsigned)(2 * (tile_) + ((l15 & 3) >> 1))) ^ tswz) * 16u + (unsigned)(l15 & 1) * 8u)
#define TRO_INIT() const unsigned trow = (unsigned)(8 * lq + (l15 >> 2)), tswz = ((trow >> 1) & 1u) * 2u + ((trow >> 3) & 1u) * 4u
#define SXDMA_INIT() unsigned xvo = (unsigned)((4 * wave + lq) * 1280 + (l15 ^ ((4 * wave + lq) & 15)) * 16); PINV(xvo)
#define SXDMA(kr_, xb_) do { const char* X_ = (const char*)a.Bx + 256 * KROT(kr_); \
    glds(X_, xvo, lds_addr(lds) + (unsigned)((xb_) + wave * 1024)); glds(X_ + 16 * 1280, xvo, lds_addr(lds) + (unsigned)((xb_) + 4096 + wave * 1024)); SB(); } while (0)
#define SXRD(xb_) _Pragma("unroll") for (int kk = 0; kk < 4; ++kk) _Pragma("unroll") for (int rt = 0; rt < 2; ++rt) xa[rt][kk] = *(const u32x4*)(lds + (xb_) + rt * 4096 + l15 * 256 + ((4 * kk + lq) ^ l15) * 16)
#define MFV(c_, a_, b_, first_) do { if (first_) asm volatile("v_mfma_f32_16x16x32_bf16 %0, %1, %2, 0" : "=&v"(c_) : "v"(a_), "v"(b_)); else asm volatile("v_mfma_f32_16x16x32_bf16 %0, %1, %2, %0" : "+v"(c_) : "v"(a_), "v"(b_)); } while (0)
#define HALF(f_, hi_) ((hi_) ? hi4(f_) : lo4(f_))
#define ODMA_INIT() unsigned ovo = (unsigned)((4 * wave + lq) * 1280 + (l15 ^ (((4 * wave + lq) & 7) << 1)) * 16); PINV(ovo); \
    const unsigned orow = (unsigned)(4 * lq + (l15 >> 2)); \
    unsigned otr[2]; _Pragma("unroll") for (int t = 0; t < 2; ++t) otr[t] = orow * 256u + (((unsigned)(2 * (2 * wave + t) + ((l15 & 3) >> 1))) ^ ((orow & 7u) << 1)) * 16u + (unsigned)(l15 & 1) * 8u
#define OROT(cr_) (((cr_) + h) % 5)
#define ODMA(cr_, i_) do { const char* W_ = (const char*)(a.Wo + (size_t)(192 * h) * 640) + 256 * OROT(cr_); \
    _Pragma("unroll") for (int q_ = 0; q_ < 12; ++q_) glds(W_ + q_ * (16 * 1280), ovo, lds_addr(lds) + (unsigned)(SBUF(i_) + (wave + 4 * q_) * 1024)); SB(); } while (0)

#define KXL 8
#define SAL 2
#define SOF 0
#define MNAP 0
#define SF0(m_, t_, k_) (6 * (2 * ((k_) >> 1) + (m_)) + 2 * (t_) + ((k_) & 1))
#define SFR(m_, t_, k_) (SF0(m_, t_, k_) - (SF0(m_, t_, k_) > 39) - (SF0(m_, t_, k_) > 41))
constexpr int L_KX = L_B1 + 28672;
template <int wave>
__device__ __forceinline__ void s_loop(const Args& a, unsigned char* lds, const int h, const unsigned pol, const WReg& W) {
  constexpr int KIND = wave < 2 ? 0 : wave - 1;
  char* ws = a.ws; asm volatile("" : "+s"(ws));
  unsigned* const cnt = (unsigned*)(ws + O_CNT); unsigned* const tmo = cnt + CNT(C_TMO);
  const int lane = lane_id(), l15 = lane & 15, lq = lane >> 4;
  char* const X1 = ws + O_X1; char* const P = ws + O_PS;
  u32x8 kxf[3];
  if (KIND == 0) {
#pragma unroll
    for (int t = 0; t < 3; ++t) kxf[t] = cat8(*(const u32x4*)(lds + L_KX + wave * 6144 + t * 2048 + lane * 16), *(const u32x4*)(lds + L_KX + wave * 6144 + t * 2048 + 1024 + lane * 16));
  }
  const __amdgpu_buffer_rsrc_t rs_x1 = mkrs(X1), rs_p = mkrs(P), rs_kf = mkrs(ws + O_KF);
  unsigned tprog = 0;
  const unsigned kkey = K_KEY();
#define KX_LOAD(sg_) do { PINV(xv); const unsigned kb_ = KSLOT(sg_) + (unsigned)(h * 32768 + wave * 16384); \
    _Pragma("unroll") for (int t_ = 0; t_ < 3; ++t_) kxf[t_] = cat8(ld16(rs_kf, xv + (unsigned)((t_ & 1) * 2048), kb_ + (unsigned)((t_ >> 1) * 4096)), ld16(rs_kf, xv + (unsigned)((t_ & 1) * 2048 + 1024), kb_ + (unsigned)((t_ >> 1) * 4096))); } while (0)
  unsigned trx = (unsigned)(L_XR + wave * 4096 + lane * 16); PINV(trx);
  unsigned trq = (unsigned)(L_B2 + wave * 12288 + lane * 16); PINV(trq);
  unsigned trk = (unsigned)(L_B0 + wave * 12288 + lane * 16); PINV(trk);
  int ix = (l15 & 2) ? 0xEEEE : 0x4444; PINV(ix);
  const float CS = 0.07216878364870323f * 1.4426950408889634f;
  unsigned xv = (unsigned)lane * 16u;
  unsigned ib = (unsigned)(L_QI + (16 * wave + l15) * 16 + lq * 4);
  unsigned ia = (unsigned)(L_QA + wave * 384 + ((l15 >> 2) & 1) * 128 + (l15 >> 3) * 64 + (l15 & 1) * 32 + lq * 8 + ((l15 >> 1) & 1) * 4);
  unsigned rda = (unsigned)(L_QA + (lq >> 1) * 768 + (l15 >> 3) * 384 + (lq & 1) * 64 + ((l15 >> 2) & 1) * 32 + (l15 & 3) * 8);
  unsigned qk_rd = (unsigned)(L_QI + (4 * lq + (l15 >> 2)) * 16 + (l15 & 1) * 8);
  unsigned flr = (unsigned)L_FLG;
  unsigned xva = KIND == 0 ? (unsigned)(L_XV + lq * 256 + (l15 >> 1) * 16 + wave * 8 + (l15 & 1) * 4) : (unsigned)(L_XV + lane * 16);
  PINV(xv); PINV(ib); PINV(ia);
  if (KIND != 2) { PINV(rda); PINV(xva); } if (KIND == 1) PINV(qk_rd); if (KIND != 0) PINV(flr);
  const bool hi8 = l15 >= 8;
  const __amdgpu_buffer_rsrc_t rs_ts = mkrs(ws + O_TS + half_of(ws + O_TS, 0u, 0u, pol));
  int tso = lane == 0 ? 256 * h : (int)0x80000000u; if (KIND != 0) PINV(tso);
  int tsa = TS_OFF(lane); if (KIND == 2) PINV(tsa);
  const __amdgpu_buffer_rsrc_t rs_ps = mkrs(cnt + CNT(C_PS) - 32 * h);
  unsigned po = side_map(P, (unsigned)(h * 512 + ((l15 >> 2) & 1) * 256 + (4 * lq + 2 * (l15 & 1) + (l15 >> 3)) * 16 + ((l15 >> 1) & 1) * 8), 0u, pol);
  unsigned pm = !(l15 & 1) ? side_map(P, (unsigned)(16384 + h * 128 + (4 * lq + 2 * ((l15 >> 2) & 1) + (l15 >> 3)) * 8 + ((l15 >> 1) & 1) * 4), 0u, pol) : 0x80000000u;
  u32x4 p_desc = mkdesc(P); if (KIND == 1) PINS(p_desc);
  unsigned pfo = lane < 32 ? side_map(P, (unsigned)(h * 512 + lane * 16), 0u, pol) : (lane < 40 ? side_map(P, (unsigned)(16384 + h * 128 + (lane - 32) * 16), 0u, pol) : 0x80000000u);
  if (KIND == 1) { PINV(po); PINV(pm); }
  if (KIND == 1) PINV(pfo);
  unsigned dur = 130, tnx = 0, tpr = 0, adj = 0; int dly = a.d[0]; PINS(dly);
#define MQ(t_, f_, w_) SMFV(aq[t_], f_, w_)
#define MK(t_, f_, w_) SMFV(ak[t_], f_, w_)
#define QW(t_, k_) SMFW(aq[t_], A[k_], SFR(0, t_, k_))
#define KW(t_, k_) SMFW(ak[t_], A[k_], SFR(1, t_, k_))
#define RLO(tb_, t_, k2_) (*(const u32x4*)(lds + (tb_) + (t_) * 4096 + (k2_) * 2048))
#define RHI(tb_, t_, k2_) (*(const u32x4*)(lds + (tb_) + (t_) * 4096 + (k2_) * 2048 + 1024))
#define RFR(tb_, t_, k2_) cat8(RLO(tb_, t_, k2_), RHI(tb_, t_, k2_))
#define XLO(j_) (*(const u32x4*)(lds + trx + (j_) * 2048))
#define XHI(j_) (*(const u32x4*)(lds + trx + (j_) * 2048 + 1024))
#define ALD(k_) DS(A[k_] = ld16(rs_x1, xv + (unsigned)(((k_) & 1) * 1024), sx[(k_) >> 1]))
#define APACE 10
#define ALP(k_) do { ALD(k_); nap4<APACE>(); SB(); } while (0)
#define A_LOAD() do { PINV(xv); ALP(0); ALP(8); ALP(1); ALP(9); ALP(2); ALP(7); ALP(3); ALP(4); ALP(5); ALD(6); SB(); } while (0)
  u32x4 ts_desc = mkdesc(ws + O_TS + half_of(ws + O_TS, 0u, 0u, pol) + 256 * h);
  unsigned fad = lds_addr(lds) + (unsigned)L_FLG; PINV(fad); PINS(ts_desc);
  if (KIND == 2) LW32(flr, 24, dur);
#pragma unroll 1
  for (int s = 0; s < 64; ++s) {
    int pss = PSLOT(s), tss = TS_STEP(s), tsn = TS_STEP(s + 1); PINS(pss); PINS(tss); if (KIND == 2) PINS(tsn);
    unsigned snp = (unsigned)(s + 1) * 0x01010101u, snv = snp, tries = 0; PINS(snp); PINV(snv);
    f32x4 sc = ZERO4; if (KIND != 2) PINV(sc);
    unsigned sx[5];
    { const unsigned so = half_of(X1, XSLOT(s), 0u, pol);
#pragma unroll
      for (int j = 0; j < 5; ++j) { sx[j] = so + (j < 4 ? (unsigned)((j + h) & 3) * 2048u : 16384u); PINS(sx[j]); }
      if (KIND == 2 && s == 1) x1_light(rs_x1, so, tmo); }
    SB();
    asm volatile("s_branch .Lsre%c6\n"
                 ".Lsbad%c6:\n s_add_u32 %0, %0, 1\n s_cmp_lt_u32 %0, 4\n s_cbranch_scc1 .Lsnow%c6\n s_sleep 8\n"
                 ".Lsnow%c6:\n s_memrealtime vcc\n ds_write_b32 %5, %1 offset:28\n ds_read_b32 %1, %5 offset:24\n s_waitcnt lgkmcnt(0)\n v_add_u32 %1, vcc_lo, %1\n v_and_b32 %1, -2, %1\n"
                 " s_mov_b64 exec, 1\n buffer_store_dword %1, off, %3, %4 sc1\n s_mov_b64 exec, -1\n v_mov_b32 %1, %2\n"
                 ".Lsre%c6:" : "+s"(tries) : "v"(snv), "s"(snp), "s"(ts_desc), "s"(tss), "v"(fad), "n"(wave) : "vcc", "scc", "memory");
    SB();
    u32x8 ra = RFR(trq, 0, 0), rb = RFR(trq, 1, 0), rc = RFR(trq, 2, 0);
    f32x4 aq[3] = {ZERO4, ZERO4, ZERO4}, ak[3] = {ZERO4, ZERO4, ZERO4};
    asm volatile("s_cmp_lg_u32 %6, 0\n s_cbranch_scc1 .Lsgo%c7" : "+v"(aq[0]), "+v"(aq[1]), "+v"(aq[2]), "+v"(ak[0]), "+v"(ak[1]), "+v"(ak[2]) : "s"(tries), "n"(wave) : "scc", "memory");
    SB();
    unsigned tis = 0, tsl = 0; bool calm = false;
    asm volatile(".p2align %c0\n .fill %c1, 4, 0xbf800000" :: "n"(SAL), "n"(SOF));
    if (KIND == 2) tis = wait_rt(tnx);
    BAR();
    asm volatile(".Lsgo%c0:" :: "n"(wave) : "memory");
    SB();
    u32x4 A[10], lo, hi;
    A_LOAD();
    unsigned w0;
    float ta, tb_;
    u64 nanm;
    QW(0, 0); QW(1, 0); QW(2, 0); KW(0, 0);
    MQ(0, A[8], ra); DS(lo = RLO(trq, 0, 1));
    KW(1, 0); DS(hi = RHI(trq, 0, 1)); ra = cat8(lo, hi);
    MQ(1, A[8], rb); DS(lo = RLO(trq, 1, 1));
    KW(2, 0); DS(hi = RHI(trq, 1, 1)); rb = cat8(lo, hi);
    QW(0, 1);
    MQ(2, A[8], rc); DS(lo = RLO(trq, 2, 1));
    QW(1, 1); DS(hi = RHI(trq, 2, 1)); rc = cat8(lo, hi);
    QW(2, 1);
    MQ(0, A[9], ra); DS(lo = RLO(trk, 0, 0));
    KW(0, 1); DS(hi = RHI(trk, 0, 0)); ra = cat8(lo, hi);
    MQ(1, A[9], rb); DS(lo = XLO(0));
    KW(1, 1); DS(hi = XHI(0)); rb = cat8(lo, hi);
    QW(0, 2);
    MQ(2, A[9], rc); DS(lo = XLO(1));
    QW(1, 2); DS(hi = XHI(1)); rc = cat8(lo, hi);
    QW(2, 2);
    MK(0, A[8], ra); DS(lo = RLO(trk, 1, 0));
    QW(0, 7); DS(hi = RHI(trk, 1, 0)); ra = cat8(lo, hi);
    MQ(1, A[7], rb); DS(lo = RLO(trk, 0, 1));
    KW(0, 2); DS(hi = RHI(trk, 0, 1)); rb = cat8(lo, hi);
    QW(0, 3);
    MQ(2, A[7], rc); DS(lo = RLO(trk, 2, 0));
    QW(1, 3); DS(hi = RHI(trk, 2, 0)); rc = cat8(lo, hi);
    QW(2, 3);
    QW(0, 4);
    MK(1, A[8], ra); DS(lo = RLO(trk, 1, 1));
    QW(1, 4); DS(hi = RHI(trk, 1, 1)); ra = cat8(lo, hi);
    QW(2, 4);
    QW(0, 5);
    MK(0, A[9], rb); DS(lo = RLO(trk, 2, 1));
    QW(1, 5); DS(hi = RHI(trk, 2, 1)); rb = cat8(lo, hi);
    QW(2, 5);
    QW(0, 6); QW(1, 6); QW(2, 6); LG0(); SB();
    KW(0, 3); FILL(); ADD1(ta, aq[0][0], aq[0][2]);
    KW(0, 4); FILL(); ADD1(tb_, aq[0][1], aq[0][3]);
    KW(0, 5); FILL(); CVTN(w0, nanm, ta, tb_);
    KW(0, 6); DS(LW32(ia, 0, w0)); ADD1(ta, aq[1][0], aq[1][2]);
    KW(0, 7); ADD1(tb_, aq[1][1], aq[1][3]);
    KW(1, 2);
    if (__builtin_expect(nanm != 0ull, 0)) {
      unsigned m = ~0u;
#define S_BACK() asm volatile("v_cmp_eq_u32 vcc, 0, %0\n s_cmp_lt_u32 %1, %3\n s_cselect_b64 vcc, vcc, 0\n s_cbranch_vccnz .Lsbad%c2" : "+v"(m) : "s"(tries), "n"(wave), "n"(CH_SPIN_CAP) : "vcc", "scc")
#pragma unroll
      for (int k = 0; k < 10; k += 2) m = sent_min(m, A[k].x, A[k + 1].x);
      S_BACK();
#pragma unroll
      for (int k = 0; k < 10; ++k) m = sent_min(m, A[k]);
      S_BACK();
    }
    asm volatile("" :: "s"(sx[0]), "s"(sx[1]), "s"(sx[2]), "s"(sx[3]), "s"(sx[4]), "s"(tries), "s"(tss), "s"(snp), "s"(ts_desc), "v"(fad), "v"(snv));
    CVT1(w0, ta, tb_);
    KW(1, 3); DS(LW32(ia, 1536, w0)); ADD1(ta, aq[2][0], aq[2][2]);
    KW(1, 4); FILL(); ADD1(tb_, aq[2][1], aq[2][3]);
    KW(1, 5); FILL(); CVT1(w0, ta, tb_);
    KW(1, 6); DS(LW32(ia, 3072, w0)); ADD1(ta, ak[0][0], ak[0][2]);
    KW(1, 7); FILL(); ADD1(tb_, ak[0][1], ak[0][3]);
    MK(1, A[9], ra); CVT1(w0, ta, tb_);
    KW(2, 1); DS(LW32(ib, 0, w0));
    u32x2 g[6];
#define QAR(i_) do { if (KIND != 2) DS(g[i_] = trrd(lds, rda + (unsigned)(((i_) >> 1) * 1536 + ((i_) & 1) * 128))); else if ((i_) >= 1 && (i_) <= 3) FILL(); } while (0)
#define QA(t_) ((u32x4){g[2 * (t_)].x, g[2 * (t_)].y, g[2 * (t_) + 1].x, g[2 * (t_) + 1].y})
    KW(2, 2); LBAR(1); QAR(0);
    KW(2, 3); QAR(1); ADD1(ta, ak[1][0], ak[1][2]);
    KW(2, 4); QAR(2); ADD1(tb_, ak[1][1], ak[1][3]);
    KW(2, 5); QAR(3); CVT1(w0, ta, tb_);
    KW(2, 6); DS(LW32(ib, 1024, w0));
    KW(2, 7); QAR(4); QAR(5);
    if (KIND == 2) DS(tsl = __builtin_amdgcn_raw_buffer_load_b32(rs_ts, tsa, tss, 16));
    MK(2, A[8], rc);
    MK(2, A[9], rb);
    if (KIND == 0) {
      float la, lb;
      { const u32x4 q_ = QA(0); SMFV(sc, q_, kxf[0]); } LBAR(2);
      { const u32x4 q_ = QA(1); SMFV(sc, q_, kxf[1]); }
      { const u32x4 q_ = QA(2); SMFV(sc, q_, kxf[2]); }
      EPI3(w0, ak[2]); DS(LW32(ib, 2048, w0));
      asm volatile("s_nop 3\n v_add_f32 %0, %2, %3\n v_add_f32 %1, %4, %5" : "=&v"(la), "=v"(lb) : "v"(sc[0]), "v"(sc[2]), "v"(sc[1]), "v"(sc[3])); SB();
      DS(LW32(xva, 0, __float_as_uint(la)); LW32(xva, 128, __float_as_uint(lb)));
      LBAR(1); LBAR(0);
    } else if (KIND == 2) {
      LBAR(0);
      asm volatile("s_nop 5"); SB();
      EPI3(w0, ak[2]); DS(LW32(ib, 2048, w0));
      LBAR(0); LBAR(0);
      tsl = ts_max(tsl);
      calm = s > 1 && tsl != ~0u && (int)(tsl - tpr) <= a.d[10];
      if (calm && s < 63) __builtin_amdgcn_raw_buffer_store_b32((tsl + (unsigned)dly + dur) & ~1u, rs_ts, tso, tsn, 16);
    } else {
      u32x2 kb[12];
#define KBR(i_) DS(kb[i_] = trrd(lds, qk_rd + (unsigned)((i_) * 256)))
#define ML(t_) do { const u32x4 a_ = QA(t_); \
        const u32x8 b_ = {kb[4 * (t_)].x, kb[4 * (t_)].y, kb[4 * (t_) + 1].x, kb[4 * (t_) + 1].y, kb[4 * (t_) + 2].x, kb[4 * (t_) + 2].y, kb[4 * (t_) + 3].x, kb[4 * (t_) + 3].y}; SMFV(sc, a_, b_); } while (0)
      LBAR(2);
      KBR(0); KBR(1); KBR(2); KBR(3); KBR(4); KBR(5);
      EPI3(w0, ak[2]); DS(LW32(ib, 2048, w0));
      KBR(6); KBR(7);
      LBAR(2);
      KBR(8); KBR(9); KBR(10); KBR(11);
      ML(0);
      asm volatile("s_barrier" ::: "memory"); SB();
      f32x4 t; DS(t = *(const f32x4*)(lds + xva));
      ML(1); ML(2);
      float tl, nl, x, m, u, e, d;
      asm volatile("v_max3_f32 %4, %0, %1, %2\n v_max_f32 %4, %4, %3\n v_mul_f32 %5, %15, %4\n v_fma_f32 %0, %16, %0, %5\n v_fma_f32 %1, %16, %1, %5\n v_fma_f32 %2, %16, %2, %5\n v_fma_f32 %3, %16, %3, %5\n s_nop 0\n"
                   "v_add_f32_dpp %6, %11, %13 quad_perm:[0,1,2,3] row_mask:0xf bank_mask:0x3\n v_add_f32_dpp %6, %12, %14 quad_perm:[0,1,2,3] row_mask:0xf bank_mask:0xc\n v_max_f32 %7, %6, %4\n"
                   "v_exp_f32 %0, %0\n v_exp_f32 %1, %1\n v_max_f32_dpp %7, %7, %7 quad_perm:[1,0,3,2] row_mask:0xf bank_mask:0xf\n"
                   "v_exp_f32 %2, %2\n v_exp_f32 %3, %3\n v_max_f32_dpp %7, %7, %7 quad_perm:[2,3,0,1] row_mask:0xf bank_mask:0xf\n"
                   "v_add_f32 %8, %0, %1\n v_add_f32 %10, %2, %3\n v_max_f32_dpp %7, %7, %7 row_half_mirror row_mask:0xf bank_mask:0xf\n"
                   "v_mul_f32 %7, %15, %7\n v_sub_f32 %4, %7, %5\n v_fma_f32 %9, %16, %6, %7\n v_exp_f32 %4, %4\n v_exp_f32 %9, %9\n v_add_f32 %8, %8, %10\n v_fma_f32 %10, %4, %8, %9\n"
                   "v_mul_f32 %0, %0, %4\n v_mul_f32 %1, %1, %4\n v_add_f32_dpp %10, %10, %10 quad_perm:[1,0,3,2] row_mask:0xf bank_mask:0xf\n"
                   "v_mul_f32 %2, %2, %4\n v_mul_f32 %3, %3, %4\n v_add_f32_dpp %10, %10, %10 quad_perm:[2,3,0,1] row_mask:0xf bank_mask:0xf\n"
                   "v_mov_b32_dpp %6, %9 quad_perm:[1,0,3,2] row_mask:0xf bank_mask:0xf\n s_nop 0\n v_add_f32_dpp %10, %10, %10 row_half_mirror row_mask:0xf bank_mask:0xf\n v_rcp_f32 %10, %10\n s_nop 0\n"
                   "v_mul_f32 %0, %0, %10\n v_mul_f32 %1, %1, %10\n v_cvt_pk_bf16_f32 v0, %0, %1\n v_mul_f32 %2, %2, %10\n v_mul_f32 %3, %3, %10\n v_cvt_pk_bf16_f32 v1, %2, %3\n"
                   "buffer_store_dwordx2 v[0:1], %17, %19, %20 offen sc1\n v_mul_f32 %9, %9, %10\n v_mul_f32 %6, %6, %10\n v_cvt_pk_bf16_f32 %9, %9, %6\n buffer_store_dword %9, %18, %19, %20 offen sc1"
                   : "+v"(t[0]), "+v"(t[1]), "+v"(t[2]), "+v"(t[3]), "=&v"(tl), "=&v"(nl), "=&v"(x), "=&v"(m), "=&v"(u), "=&v"(e), "=&v"(d)
                   : "v"(sc[0]), "v"(sc[1]), "v"(sc[2]), "v"(sc[3]), "s"(-CS), "s"(CS), "v"(po), "v"(pm), "s"(p_desc), "s"(pss) : "v0", "v1", "memory");
      SB();
      { u64 t_; RT_MARK(t_); LW32(flr, 20, (unsigned)t_); }
    }
    BAR();
    if (KIND == 0) {
      nap_rt(KXL - 1);
      if (s < 63) {
        const unsigned tneed = (unsigned)(s + 1);
        if (tprog < tneed) tprog = wait_t<4>(cnt + CNT(C_PT_ + h), 1, tneed, tmo);
        KX_LOAD(s + 1);
      }
    }
    if (KIND == 0) tprog = min_t(ld16(mkrs(cnt + CNT(C_PT_ + h)), 0u, 0u), 1);
    if (KIND == 2 && s < 63) {
      const unsigned f7 = FLRD(7), f5 = FLRD(5);
      const unsigned tb2 = (unsigned)__builtin_amdgcn_readfirstlane(f5);
      adj = __builtin_amdgcn_readfirstlane(f7) == snp;
      if (__builtin_expect(!calm || adj, 0)) {
        if (s < 2) __builtin_amdgcn_raw_buffer_store_b32(((unsigned)tb2 + (unsigned)a.d[11]) & ~1u, rs_ts, tso, tss, 16);
        tsl = ts_settle(rs_ts, tss, tsl, a.d[4]) + (unsigned)a.d[12];
        if (s > 0) __builtin_amdgcn_raw_buffer_store_b32((tsl + (unsigned)dly + dur) & ~1u, rs_ts, tso, tsn, 16);
      }
      tnx = tsl + (unsigned)dly; tpr = tnx + dur;
      dur = min(dur + 1u, (unsigned)tb2 - tis); PINS(dur);
      LW32(flr, 24, dur + (unsigned)a.d[13]);
      ADAPT(dly, adj);
    }
    if (KIND == 1) {
      const int ln = lane_id();
      const __amdgpu_buffer_rsrc_t rs_km = mkrs(ws + O_KM);
      if (s + PRE < 63) st16(rs_p, pfo, (unsigned)PSLOT(s + PRE), SENT4);
#pragma unroll
      for (int kk = 0; kk < 6; kk += 2) {
        const u32x4 v = trrd2(lds, qk_rd + kk * 512, 256u), v2 = trrd2(lds, qk_rd + (kk + 1) * 512, 256u);
        st16(rs_km, (unsigned)((kk + ((ln >> 3) & 1)) * 512 + (ln >> 4) * 128 + (ln & 7) * 16), MSLOT(s) + (unsigned)h * 3072u, hi8 ? v2 : v);
      }
      __builtin_amdgcn_raw_buffer_store_b32((unsigned)(s + 1), rs_ps, tso, 0, 16);
    }
  }
}
__device__ __forceinline__ void s_role(const Args& a, unsigned char* lds, const int h, const int wave) {
  char* ws = a.ws; asm volatile("" : "+s"(ws));
  unsigned* const cnt = (unsigned*)(ws + O_CNT); unsigned* const tmo = cnt + CNT(C_TMO);
  const int lane = lane_id(), l15 = lane & 15, lq = lane >> 4;
  WReg W; u32x8 wx[3];
  unsigned tro[3];
  { TRO_INIT();
#pragma unroll
    for (int t = 0; t < 3; ++t) tro[t] = TRO(4 * t + wave); }
  SDMA_INIT();
  SXDMA_INIT();
  SXDMA(0, 0); SDMAQK(0); SDMAQK(1); SXDMA(1, 8192); SDMAQK(2);
  f32x4 pac[2][2][3];
#define kacc pac[1]
#define qacc pac[0]
  SFOR(i, 10)
    if (i < 2 || i == 3 || i == 5 || i == 7) VMW(26); else if (i < 8) VMW(24); else if (i == 8) VMW(12); else VMW(0);
    BAR();
    u32x8 res[3][2];
    SFOR(t, 3)
      const unsigned ad = lds_addr(lds) + tro[t] + (unsigned)SBUF(i);
      if (i < 8) { TRS0(ad, SFR(i & 1, t, i & 6)); if (i == 6 && t > 0) tr8(false, ad, res[t][0], wx[t]); else TRS1(ad, SFR(i & 1, t, (i & 6) + 1)); }
      else tr8(false, ad, res[t][0], res[t][1]);
    SEND;
    BAR();
    if (i == 2 || i == 4 || i == 6) SXDMA((i >> 1) + 1, (((i >> 1) + 1) & 1) * 8192);
    if (i + 3 < 10) SDMAQK(i + 3);
    if (i >= 8) {
#pragma unroll
      for (int t = 0; t < 3; ++t)
#pragma unroll
        for (int k = 0; k < 2; ++k) {
          unsigned char* dst = lds + SBUF(i) + wave * 12288 + t * 4096 + k * 2048 + lane * 16;
          *(u32x4*)dst = lo4(res[t][k]); *(u32x4*)(dst + 1024) = hi4(res[t][k]);
        }
    }
    if (i == 7) {
#pragma unroll
      for (int t = 1; t < 3; ++t) { unsigned char* dst = lds + L_XR + wave * 4096 + lane * 16 + (t - 1) * 2048; *(u32x4*)dst = lo4(wx[t]); *(u32x4*)(dst + 1024) = hi4(wx[t]); }
    }
    SB();
    SFOR(kk, 4)
      u32x4 xa[2];
#pragma unroll
      for (int rt = 0; rt < 2; ++rt) xa[rt] = *(const u32x4*)(lds + ((i >> 1) & 1) * 8192 + rt * 4096 + l15 * 256 + ((4 * kk + lq) ^ l15) * 16);
      SFOR(n, 6)
        constexpr int t = n >> 1, rt = n & 1;
        if (i >= 8) { const u32x4 w_ = HALF(res[t][kk >> 1], kk & 1); MFV(pac[i & 1][rt][t], w_, xa[rt], false); }
        else if (i == 6 && t > 0 && kk >= 2) { const u32x4 w_ = HALF(wx[t], kk & 1); MFV(pac[0][rt][t], w_, xa[rt], false); }
        else mf_w<true>(pac[i & 1][rt][t], xa[rt], 2 * SFR(i & 1, t, (i & 6) + (kk >> 1)) + (kk & 1), i < 2 && kk == 0, W);
      SEND;
    SEND;
    SB();
  SEND;
  unsigned pol;
  {
    const u32x2 pf = ld8(mkrs(cnt + CNT(C_POL)), lane < 32 ? 0u : 128u, 0u);
    asm volatile("s_nop 7\n s_nop 7" : "+v"(kacc[0][0]), "+v"(kacc[0][1]), "+v"(kacc[0][2]), "+v"(kacc[1][0]), "+v"(kacc[1][1]), "+v"(kacc[1][2]));
    pol = (unsigned)__builtin_amdgcn_readlane((int)pf.x, 0);
    if (__builtin_expect((unsigned)__builtin_amdgcn_readlane((int)pf.x, 32) < NFILL || pol == 0u, 0)) { wait_ge<8>(cnt + CNT(C_FILL), NFILL, tmo); pol = ld_word(cnt + CNT(C_POL)); }
    pol &= 1u;
    const unsigned kkey = K_KEY();
#pragma unroll
    for (int rt = 0; rt < 2; ++rt)
#pragma unroll
      for (int t = 0; t < 3; ++t) { const int ix = 4 * t + wave; const unsigned o_ = (unsigned)(rt * 6144 + (ix >> 1) * 1024 + lane * 16 + (ix & 1) * 8);
        const unsigned k0 = cvtpk(kacc[rt][t][0], kacc[rt][t][1]), k1 = cvtpk(kacc[rt][t][2], kacc[rt][t][3]);
        *(u32x2*)(lds + L_KX + o_) = (u32x2){k0, k1};
        st8(mkrs(ws + O_KF), o_, KSLOT(0) + (unsigned)(h * 32768 + rt * 10240), k0, k1);
        st8(mkrs(ws + O_QF), o_, KSLOT(0) + (unsigned)(h * 32768 + rt * 10240), cvtpk(qacc[rt][t][0], qacc[rt][t][1]), cvtpk(qacc[rt][t][2], qacc[rt][t][3])); }
    if (wave == 2) { if (lane < 8) *(unsigned*)(lds + L_FLG + 4 * lane) = 0u; }
    BAR();
  }
  if (wave == 0) s_loop<0>(a, lds, h, pol, W); else if (wave == 1) s_loop<1>(a, lds, h, pol, W); else if (wave == 2) s_loop<2>(a, lds, h, pol, W); else s_loop<3>(a, lds, h, pol, W);
}
#undef kacc
#undef qacc

__device__ __forceinline__ void z_role(const Args& a, unsigned char* lds, const int h, const int wave) {
  char* ws = a.ws; asm volatile("" : "+s"(ws));
  unsigned* const cnt = (unsigned*)(ws + O_CNT); unsigned* const tmo = cnt + CNT(C_TMO);
  char* const X1 = ws + O_X1;
#define ZFV(t_, k_) (8 * (t_) + (k_))
#define ZFO(n_, j_) ((n_) < 6 ? 24 + 3 * (n_) + (j_) : 30 + 2 * (n_) + ((j_) >> 1))
  WReg W;
  const __amdgpu_buffer_rsrc_t rs_u = mkrs(ws + O_UO + h * 1024);
  unsigned xv = (unsigned)lane_id() * 16u; PINV(xv);
  unsigned pkv, pk3, pk4;
  {
    const int lane = lane_id(), l15 = lane & 15, lq = lane >> 4;
    unsigned tro[3];
    { TRO_INIT();
#pragma unroll
      for (int t = 0; t < 3; ++t) tro[t] = TRO(4 * t + wave); }
    SDMA_INIT(); SXDMA_INIT(); ODMA_INIT();
    SDMA(a.Wv, 0, 0); SXDMA(0, 0); SDMA(a.Wv, 1, 1); SXDMA(1, 8192); SDMA(a.Wv, 2, 2);
    f32x4 kacc[2][3];
    u32x8 wl[3][2];
    SFOR(i, 5)
      if (i < 4) VMW(26); else VMW(24);
      BAR();
      u32x4 xa[2][4];
      SXRD((i & 1) * 8192);
      SFOR(t, 3)
        if (i < 4) { TRS0(lds_addr(lds) + tro[t] + (unsigned)SBUF(i), ZFV(t, 2 * (i & 3))); TRS1(lds_addr(lds) + tro[t] + (unsigned)SBUF(i), ZFV(t, 2 * (i & 3) + 1)); }
        else tr8(false, lds_addr(lds) + tro[t] + (unsigned)SBUF(i), wl[t][0], wl[t][1]);
      SEND;
#pragma unroll
      for (int n = 0; n < 8; ++n) PINV(xa[n >> 2][n & 3]);
      BAR();
      if (i + 2 < 5) SXDMA(i + 2, (i & 1) * 8192);
      if (i + 3 < 5) SDMA(a.Wv, i + 3, i + 3); else ODMA(i - 2, i + 3);
      SFOR(n, 24)
        constexpr int kk = n / 6, t = (n % 6) >> 1, rt = n & 1;
        if (i < 4) mf_w<true>(kacc[rt][t], xa[rt][kk], 2 * ZFV(t, 2 * (i & 3) + (kk >> 1)) + (kk & 1), i == 0 && kk == 0, W);
        else { const u32x4 w_ = HALF(wl[t][kk >> 1], kk & 1); MFV(kacc[rt][t], w_, xa[rt][kk], false); }
      SEND;
      SB();
    SEND;
    u32x4 vx[2][6];
    asm volatile("s_nop 7\n s_nop 7" : "+v"(kacc[0][0]), "+v"(kacc[0][1]), "+v"(kacc[0][2]), "+v"(kacc[1][0]), "+v"(kacc[1][1]), "+v"(kacc[1][2]));
#pragma unroll
    for (int rt = 0; rt < 2; ++rt)
#pragma unroll
      for (int t = 0; t < 3; ++t)
        *(u32x2*)(lds + rt * 6144 + (2 * t + (wave >> 1)) * 1024 + xv + (wave & 1) * 8) = (u32x2){cvtpk(kacc[rt][t][0], kacc[rt][t][1]), cvtpk(kacc[rt][t][2], kacc[rt][t][3])};
    BAR();
#pragma unroll
    for (int n = 0; n < 12; ++n) { vx[n / 6][n % 6] = *(const u32x4*)(lds + n * 1024 + xv); PINV(vx[n / 6][n % 6]); }
    SFOR(i5, 5)
      constexpr int i = i5 + 5;
      if (i == 5) VMW(24); else if (i == 6) VMW(26); else if (i == 7) VMW(28); else if (i == 8) VMW(18); else VMW(6);
      BAR();
      u32x8 res[2][3];
      SFOR(t, 2)
        const unsigned ad = lds_addr(lds) + otr[t] + (unsigned)SBUF(i);
        constexpr int n = 2 * i5 + t;
        if (n < 8) TRO4(ad, 0, ZFO(n & 7, 0)); else tr4<0>(false, ad, res[t][0]);
        if (n < 6) TRO4(ad, 1, ZFO(n & 7, 1)); else tr4<1>(false, ad, res[t][1]);
        if (n < 8) TRO4(ad, 2, ZFO(n & 7, 2)); else tr4<2>(false, ad, res[t][2]);
      SEND;
      BAR();
      if (i + 3 < 10) ODMA(i - 2, i + 3);
      f32x4 ua[2][2];
      SFOR(m, 24)
        constexpr int e = m >> 2, t = (m >> 1) & 1, rt = m & 1, n = 2 * i5 + t;
        if (n >= 8 || (n >= 6 && (e >> 1) == 1)) { const u32x4 w_ = HALF(res[t][e >> 1], e & 1); MFV(ua[t][rt], vx[rt][e], w_, e == 0); }
        else mf_w<false>(ua[t][rt], vx[rt][e], 2 * ZFO(n & 7, e >> 1) + (e & 1), e == 0, W);
      SEND;
      asm volatile("s_nop 7" : "+v"(ua[0][0]), "+v"(ua[0][1]), "+v"(ua[1][0]), "+v"(ua[1][1]));
#pragma unroll
      for (int t = 0; t < 2; ++t)
        st16(rs_u, xv, u_off(0u, (unsigned)(8 * OROT(i5) + 2 * wave + t)), (u32x4){cvtpk(ua[t][0][0], ua[t][0][1]), cvtpk(ua[t][0][2], ua[t][0][3]), cvtpk(ua[t][1][0], ua[t][1][1]), cvtpk(ua[t][1][2], ua[t][1][3])});
      SB();
      if (i == 7) {
        PINV(xv); pkv = xv + (unsigned)(L_B1 + wave * 12288);
#pragma unroll
        for (int n = 0; n < 6; ++n) { *(u32x4*)(lds + pkv + n * 2048) = lo4(wl[n >> 1][n & 1]); *(u32x4*)(lds + pkv + n * 2048 + 1024) = hi4(wl[n >> 1][n & 1]); }
      }
      if (i == 8) {
        PINV(xv); pk3 = xv + (unsigned)(L_B2 + wave * 12288);
#pragma unroll
        for (int t = 0; t < 2; ++t) { *(u32x4*)(lds + pk3 + t * 2048) = lo4(res[t][1]); *(u32x4*)(lds + pk3 + t * 2048 + 1024) = hi4(res[t][1]); }
      }
      if (i == 9) {
        PINV(xv); pk4 = xv + (unsigned)(L_B0 + wave * 12288);
#pragma unroll
        for (int n = 0; n < 6; ++n) { *(u32x4*)(lds + pk4 + n * 2048) = lo4(res[n & 1][n >> 1]); *(u32x4*)(lds + pk4 + n * 2048 + 1024) = hi4(res[n & 1][n >> 1]); }
      }
      SB();
    SEND;
  }
  const int lane = lane_id(), l15 = lane & 15, lq = lane >> 4;
  constexpr int L_VA = 10240;
  unsigned pol;
  {
    const u32x2 pf = ld8(mkrs(cnt + CNT(C_POL)), lane < 32 ? 0u : 128u, 0u);
    pol = (unsigned)__builtin_amdgcn_readlane((int)pf.x, 0);
    VMW(0);
    __builtin_amdgcn_raw_buffer_store_b32(1u, mkrs(cnt + CNT(C_PZ + h)), lane == 0 ? 4 * wave : (int)0x80000000u, 0, 16);
    if (__builtin_expect((unsigned)__builtin_amdgcn_readlane((int)pf.x, 32) < NFILL || pol == 0u, 0)) { wait_ge<8>(cnt + CNT(C_FILL), NFILL, tmo); pol = ld_word(cnt + CNT(C_POL)); }
    pol &= 1u;
  }
  const __amdgpu_buffer_rsrc_t rs_x1 = mkrs(X1), rs_z = mkrs(ws + O_ZB), rs_ts = mkrs(ws + O_TS + half_of(ws + O_TS, 0u, 0u, pol));
  int ix = (l15 & 2) ? 0xEEEE : 0x4444; PINV(ix);
  unsigned ia = (unsigned)(L_VA + wave * 384 + ((l15 >> 2) & 1) * 128 + (l15 >> 3) * 64 + (l15 & 1) * 32 + lq * 8 + ((l15 >> 1) & 1) * 4);
  unsigned rda = (unsigned)(L_VA + (lq >> 1) * 768 + (l15 >> 3) * 384 + (lq & 1) * 64 + ((l15 >> 2) & 1) * 32 + (l15 & 3) * 8);
  unsigned zvo = (unsigned)((2 * wave + (lq >> 1)) * 16384 + h * 256 + l15 * 16 + (lq & 1) * 8);
  unsigned evo = (unsigned)((2 * wave + (lq & 1) + 8 * (lq >> 1)) * 16384 + h * 256 + l15 * 16);
  unsigned gvo = xv + (unsigned)(wave * 2048), gv2 = xv + 16384u + (unsigned)((wave >> 1) * 1024);
  u64 hmask = 0xffffffffull << (32 * (wave & 1)); PINS(hmask);
  const unsigned gm0 = lds_addr(lds) + (unsigned)(L_MEM + ((wave - h) & 3) * 2048), gm2 = lds_addr(lds) + (unsigned)(L_MEM + 8192 + (wave >> 1) * 1024);
  PINV(ia); PINV(rda); PINV(zvo); PINV(gvo); PINV(gv2); PINV(pkv); PINV(pk3); PINV(pk4);
  unsigned tsv = ~0u, tries = 0; int dly = a.d[1]; PINS(dly);
#define PLO(b_, n_) (*(const u32x4*)(lds + (b_) + (n_) * 2048))
#define PHI(b_, n_) (*(const u32x4*)(lds + (b_) + (n_) * 2048 + 1024))
#define MV(t_, f_, w_) SMFV(av[t_], f_, w_)
#define VW(t_, k_) SMFW(av[t_], A[k_], ZFV(t_, k_))
#define MO(n_, j_, w_) SMFV(az[n_], va[j_], w_)
#define OW(n_, j_) SMFW(az[n_], va[j_], ZFO(n_, j_))
#define AFR(k_) (*(const u32x4*)(lds + afb + (k_) * 1024))
  unsigned afb = xv + L_MEM; PINV(afb);
#define G_LOAD() do { asm volatile("s_mov_b32 m0, %3\n s_nop 0\n global_load_lds_dwordx4 %0, %4 sc1\n global_load_lds_dwordx4 %0, %4 offset:1024 sc1\n s_mov_b32 m0, %2\n s_mov_b64 exec, %5\n global_load_lds_dwordx4 %1, %4 sc1\n s_mov_b64 exec, -1\n s_waitcnt vmcnt(2)\n s_barrier" \
      :: "v"(gvo), "v"(gv2), "s"(gm2), "s"(gm0), "s"(xs), "s"(hmask) : "memory"); SB(); } while (0)
  u32x4 ts_desc = mkdesc(ws + O_TS + half_of(ws + O_TS, 0u, 0u, pol) + 16384 + 256 * h);
  int w3m = wave, dzs = a.d[9]; PINS(w3m); PINS(dzs); PINS(ts_desc);
  u32x4 z_desc = mkdesc(ws + O_ZB); PINS(z_desc);
#pragma unroll 1
  for (int s = 0; s < 64; ++s) {
    f32x4 av[3], az[10];
#pragma unroll
    for (int n = 0; n < 10; ++n) { az[n] = ZERO4; PINV(az[n]); }
    unsigned snp = (unsigned)(s + 1) * 0x01010101u, snv = snp, adj = 0; PINS(snp); PINV(snv);
    const unsigned so = half_of(X1, XSLOT(s), 0u, pol);
    const char* xs = X1 + so; int tss = TS_STEP(s); PINS(xs); PINS(tss);
    unsigned zs[5], tnx = (unsigned)__builtin_amdgcn_readfirstlane(wave == 3 && tsv != ~0u ? tsv + (unsigned)dly : 0u); PINS(tnx);
    { const unsigned z0 = half_of(ws + O_ZB, ZSLOT(s), 0u, pol);
#pragma unroll
      for (int i = 0; i < 5; ++i) { zs[i] = z0 + (unsigned)OROT(i) * (8u * 16384u); PINS(zs[i]); } }
    if (s == 1 && wave == 3) x1_light(rs_x1, so, tmo);
    SB();
    asm volatile("s_branch .Lzre\n"
                 ".Lzbad:\n s_waitcnt lgkmcnt(0)\n s_add_u32 %0, %0, 1\n s_add_u32 %1, %1, 1\n s_cmp_lt_u32 %0, 4\n s_cbranch_scc1 .Lznow\n s_sleep 8\n"
                 ".Lznow:\n s_cmp_lg_u32 %6, 3\n s_cbranch_scc1 .Lzre\n s_memrealtime vcc\n s_waitcnt lgkmcnt(0)\n s_add_u32 vcc_lo, vcc_lo, %7\n s_and_b32 vcc_lo, vcc_lo, -2\n v_mov_b32 %2, vcc_lo\n"
                 " s_mov_b64 exec, 1\n buffer_store_dword %2, off, %4, %5 sc1\n s_mov_b64 exec, -1\n v_mov_b32 %2, %3\n"
                 ".Lzre:" : "+s"(adj), "+s"(tries) : "v"(snv), "s"(snp), "s"(ts_desc), "s"(tss), "s"(w3m), "s"(dzs) : "vcc", "scc", "memory");
    SB();
    u32x8 ra = cat8(PLO(pkv, 0), PHI(pkv, 0)), rb = cat8(PLO(pkv, 1), PHI(pkv, 1));
    av[0] = ZERO4; av[1] = ZERO4; av[2] = ZERO4;
    asm volatile("" : "+v"(av[0]), "+v"(av[1]), "+v"(av[2]));
    SB();
    wait_rt(tnx);
    BAR();
    G_LOAD();
    u32x4 A[10], lo, hi, va[3];
    unsigned w0, w1;
    float ta, tb_;
    u64 nanm;
    DS(A[0] = AFR(0)); DS(A[2] = AFR(2)); DS(A[4] = AFR(4)); DS(A[6] = AFR(6));
    VW(0, 0); VW(1, 0); asm volatile("s_waitcnt vmcnt(0)\n s_barrier" ::: "memory"); SB();
    VW(2, 0); DS(A[8] = AFR(8));
    VW(0, 2); DS(A[9] = AFR(9));
    VW(1, 2); DS(A[1] = AFR(1));
    VW(2, 2); DS(A[3] = AFR(3));
    VW(0, 4); DS(A[5] = AFR(5));
    VW(0, 6); DS(A[7] = AFR(7));
    MV(0, A[8], ra); DS(lo = PLO(pkv, 2));
    MV(0, A[9], rb); DS(hi = PHI(pkv, 2)); ra = cat8(lo, hi);
    VW(0, 1); DS(lo = PLO(pkv, 3));
    VW(0, 3); DS(hi = PHI(pkv, 3)); rb = cat8(lo, hi);
    VW(0, 5); VW(0, 7);
    VW(1, 4); VW(1, 6);
    MV(1, A[8], ra); DS(lo = PLO(pkv, 4)); ADD1(ta, av[0][0], av[0][2]);
    VW(1, 1); DS(hi = PHI(pkv, 4)); ra = cat8(lo, hi); ADD1(tb_, av[0][1], av[0][3]);
    VW(1, 3); LGW(2); CVT1(w0, ta, tb_);
    MV(1, A[9], rb); DS(LW32(ia, 0, w0));
    VW(1, 5); DS(lo = PLO(pkv, 5)); NANM(nanm, ta, tb_);
    VW(1, 7); DS(hi = PHI(pkv, 5)); rb = cat8(lo, hi);
    VW(2, 4);
    if (__builtin_expect(nanm != 0ull, 0)) {
      unsigned m = ~0u;
#define Z_BACK() asm volatile("v_cmp_eq_u32 vcc, 0, %0\n s_cmp_lt_u32 %1, %2\n s_cselect_b64 vcc, vcc, 0\n s_cbranch_vccnz .Lzbad" : "+v"(m) : "s"(tries), "n"(CH_SPIN_CAP) : "vcc", "scc")
#pragma unroll
      for (int k = 0; k < 10; k += 2) m = sent_min(m, A[k].x, A[k + 1].x);
      Z_BACK();
#pragma unroll
      for (int k = 0; k < 10; ++k) m = sent_min(m, A[k]);
      Z_BACK();
    }
    asm volatile("" :: "s"(xs), "s"(tnx), "s"(tries), "s"(tss), "s"(snp), "s"(ts_desc), "s"(w3m), "s"(dzs), "v"(snv));
    PINS(adj);
    u32x2 g0, g1;
#define VR0(j_) DS(g0 = trrd(lds, rda + (unsigned)((j_) * 1536)))
#define VR1(j_) do { DS(g1 = trrd(lds, rda + (unsigned)((j_) * 1536 + 128))); va[j_] = (u32x4){g0.x, g0.y, g1.x, g1.y}; } while (0)
    VW(2, 6); LBAR(2);
    MV(2, A[8], ra); VR0(0); ADD1(ta, av[1][0], av[1][2]);
    VW(2, 1); VR1(0); ADD1(tb_, av[1][1], av[1][3]);
    VW(2, 3); DS(lo = PLO(pk4, 0)); CVT1(w0, ta, tb_);
    VW(2, 5); DS(LW32(ia, 1536, w0));
    MV(2, A[9], rb); DS(hi = PHI(pk4, 0)); ra = cat8(lo, hi);
    VW(2, 7); DS(lo = PLO(pk4, 1));
    OW(0, 0); DS(hi = PHI(pk4, 1)); rb = cat8(lo, hi);
    OW(1, 0); LBAR(3);
    OW(2, 0); VR0(1); ADD1(ta, av[2][0], av[2][2]);
    OW(3, 0); VR1(1); ADD1(tb_, av[2][1], av[2][3]);
    OW(4, 0); LGW(2); CVT1(w0, ta, tb_);
    MO(8, 0, ra); DS(LW32(ia, 3072, w0));
    MO(9, 0, rb); DS(lo = PLO(pk3, 0));
    OW(5, 0); DS(hi = PHI(pk3, 0)); ra = cat8(lo, hi);
    OW(6, 0); DS(lo = PLO(pk3, 1));
    OW(7, 0); LGW(3); MEMB(); __builtin_amdgcn_s_barrier(); MEMB(); SB();
    OW(0, 1); VR0(2); VR1(2);
    OW(1, 1); DS(hi = PHI(pk3, 1)); rb = cat8(lo, hi);
    OW(2, 1);
    MO(6, 1, ra); DS(lo = PLO(pk4, 2));
    OW(3, 1); DS(hi = PHI(pk4, 2)); ra = cat8(lo, hi);
#define ZSW() do { asm volatile("v_permlane32_swap_b32 %0, %1" : "+v"(w0), "+v"(w1)); SB(); } while (0)
#define ZST(i_) do { const u32x2 w_ = {w0, w1}; asm volatile("buffer_store_dwordx2 %0, %1, %2, %3 offen sc1" :: "v"(w_), "v"(zvo), "s"(z_desc), "s"(zs[i_]) : "memory"); SB(); } while (0)
    OW(0, 2);
    OW(1, 2);
    MO(7, 1, rb); DS(lo = PLO(pk4, 3));
    OW(2, 2); DS(hi = PHI(pk4, 3)); rb = cat8(lo, hi); ADD2(ta, tb_, az[0]);
    OW(3, 2); LGW(2); CVT1(w0, ta, tb_); ADD1(ta, az[1][0], az[1][2]);
    MO(8, 1, ra); DS(lo = PLO(pk4, 4)); ADDCVT(w1, ta, az[1]);
    OW(6, 2); DS(hi = PHI(pk4, 4)); ra = cat8(lo, hi); ZSW();
    OW(7, 2); ZST(0); ADD2(ta, tb_, az[2]);
    OW(4, 1); LGW(2); CVT1(w0, ta, tb_); ADD1(ta, az[3][0], az[3][2]);
    MO(9, 1, rb); DS(lo = PLO(pk4, 5)); ADDCVT(w1, ta, az[3]);
    OW(5, 1); DS(hi = PHI(pk4, 5)); rb = cat8(lo, hi); ZSW();
    OW(4, 2); ZST(1); ADD2(ta, tb_, az[6]);
    OW(5, 2); LGW(2); CVT1(w0, ta, tb_); ADD1(ta, az[7][0], az[7][2]);
    MO(8, 2, ra); LGW(0); ADDCVT(w1, ta, az[7]);
    MO(9, 2, rb);
    asm volatile("v_add_f32 v2, %2, %4\n v_permlane32_swap_b32 %0, %1\n v_add_f32 v0, %3, %5\n v_cvt_pk_bf16_f32 v0, v2, v0\n v_add_f32 v2, %6, %8\n v_add_f32 v1, %7, %9\n v_cvt_pk_bf16_f32 v1, v2, v1"
                 : "+v"(w0), "+v"(w1) : "v"(az[4][0]), "v"(az[4][1]), "v"(az[4][2]), "v"(az[4][3]), "v"(az[5][0]), "v"(az[5][1]), "v"(az[5][2]), "v"(az[5][3]) : "v0", "v1", "v2", "v3");
    { const u32x2 w_ = {w0, w1};
      asm volatile("buffer_store_dwordx2 %0, %1, %2, %3 offen sc1\n v_add_f32 v3, %6, %8\n v_permlane32_swap_b32 v0, v1\n v_add_f32 v2, %7, %9\n buffer_store_dwordx2 v[0:1], %1, %2, %4 offen sc1\n v_cvt_pk_bf16_f32 v2, v3, v2\n"
                   "v_add_f32 %10, %10, %12\n v_add_f32 v3, %11, %13\n v_cvt_pk_bf16_f32 v3, %10, v3\n s_nop 1\n v_permlane32_swap_b32 v2, v3\n buffer_store_dwordx2 v[2:3], %1, %2, %5 offen sc1"
                   :: "v"(w_), "v"(zvo), "s"(z_desc), "s"(zs[3]), "s"(zs[2]), "s"(zs[4]), "v"(az[8][0]), "v"(az[8][1]), "v"(az[8][2]), "v"(az[8][3]), "v"(az[9][0]), "v"(az[9][1]), "v"(az[9][2]), "v"(az[9][3]) : "v0", "v1", "v2", "v3", "memory"); }
    SB();
    ADAPT(dly, adj);
    if (s < 63) {
      if (s + PRE < 64) {
        const unsigned e0 = half_of(ws + O_ZB, ZSLOT((s + PRE)), 0u, pol);
        st16(rs_z, evo, e0, SENT4); st16(rs_z, evo, e0 + 16u * 16384u, SENT4); st16(rs_z, lane < 32 ? evo : 0x80000000u, e0 + 32u * 16384u, SENT4);
      }
      if (wave == 3) { tsv = ts_settle(rs_ts, tss, 0u, a.d[4]); if (tsv != ~0u && (int)(tsv - tnx) > dzs + a.d[10]) tsv += (unsigned)a.d[12]; }
    }
  }
}

#define WCK 3
#define UNAP 0
__device__ __forceinline__ void r_role(const Args& a, unsigned char* lds, const int c, const int wave) {
  char* ws = a.ws; asm volatile("" : "+s"(ws));
  unsigned* const cnt = (unsigned*)(ws + O_CNT); unsigned* const tmo = cnt + CNT(C_TMO);
  char* const X1 = ws + O_X1; char* const P = ws + O_PS;
  const int lane = lane_id(), l15 = lane & 15, lq = lane >> 4;
  const unsigned pol = wait_ge<8>(cnt + CNT(C_POL), 1u, tmo) & 1u;
  const __amdgpu_buffer_rsrc_t rs_x1 = mkrs(X1 + (c >> 5) * 16384 + (c & 31) * 256), rs_p = mkrs(P + half_of(P, 0u, 0u, pol)), rs_z = mkrs(ws + O_ZB + (size_t)c * 16384);
  const __amdgpu_buffer_rsrc_t rs_ts = mkrs(ws + O_TS + half_of(ws + O_TS, 0u, 0u, pol));
  unsigned xv = (unsigned)lane * 16u; PINV(xv);
  {
    const u32x4 ones = {~0u, ~0u, ~0u, ~0u};
    static_assert(PRE == 4, "");
#pragma unroll
    for (int i = 0; i < 8; ++i) st16(rs_z, xv + (unsigned)((i & 3) * 1024), half_of(ws + O_ZB, ZSLOT(wave), 0u, pol) + (unsigned)((i >> 2) * 4096), SENT4);
    st16(rs_p, lane < 32 ? xv : 0x80000000u, (unsigned)(PSLOT(wave) + (c >> 4) * 16384 + (c & 15) * 512), SENT4);
    st16(rs_x1, lane < 16 ? xv : 0x80000000u, half_of(X1, XSLOT((wave + 1)), 0u, pol), SENT4);
    if (c < 4) st16(rs_ts, xv, (unsigned)((c >> 1) * 16384 + (c & 1) * 4096 + wave * 1024), ones);
    if (wave == 0) {
      const int q = lane >> 2, r = (q >> 2) * 2 + (q & 1);
      const unsigned v = *(const unsigned*)(a.mem0 + r * 640 + 16 * c + 4 * (lane & 3) + (q & 2));
      __builtin_amdgcn_raw_buffer_store_b32(v == SENT ? ~0u : v, rs_x1, lane * 4, (int)half_of(X1, 0u, 0u, pol), 16);
    }
    VMW(0);
    BAR();
    if (wave != 0) return;
    if (lane == 0) __hip_atomic_fetch_add((gu32*)(cnt + CNT(C_FILL)), 1u, RLX_AGENT);
  }
  const u32x4 rs_u = mkdesc(ws + O_UO);
  const __amdgpu_buffer_rsrc_t rs_pw = mkrs(cnt + CNT(C_PW) + ((c >> 1) & 3));
  int ix = (l15 & 2) ? 0xEEEE : 0x4444; PINV(ix);
  unsigned pm = (unsigned)(lq * 256 + l15 * 8);
  int xo = (4 * lq + (l15 & 3)) * 16 + (l15 >> 2) * 4; unsigned sel = (l15 & 1) ? 0x07060302u : 0x01000504u;
  int tsa = (lane & 31) * 4;
  PINV(pm); PINV(xo); PINV(sel); PINV(tsa);
  WReg W;
#define U_LOAD(sg_, def_) SFOR(i, 16) ld_w(rs_u, xv, u_off((unsigned)(sg_), (unsigned)c) + (unsigned)((i >> 1) * 4096), (i & 1) * 2048, i, (def_) && (i & 3) == 0, W); nap4<UNAP>(); SEND
  wait_ge<8>(cnt + CNT(C_FILL), NFILL, tmo);
  wait_heads<8>(cnt + CNT(C_PZ) + ((c >> 1) & 3), 0, 1u, tmo);
  unsigned wl = __builtin_amdgcn_raw_buffer_load_b32(rs_pw, tsa * 32, 0, 16), tsl = 0, wn = 0; int tso = TS_OFF(lane), two = tsa * 32; PINV(tso); PINV(two); PINV(wl);
  const u32x4 ts_desc = mkdesc(ws + O_TS + half_of(ws + O_TS, 0u, 0u, pol)), pw_desc = mkdesc(cnt + CNT(C_PW) + ((c >> 1) & 3));
  U_LOAD(0, true);
  unsigned tsv = ~0u, tpub; int dlp = a.d[2]; PINS(dlp);
  { u64 t_; RT_MARK(t_); tpub = (unsigned)t_ - 4096u; }
#pragma unroll 1
  for (int s = 0; s < 63; ++s) {
    unsigned ps[5], zs[2], xs;
#pragma unroll
    for (int j = 0; j < 5; ++j) { ps[j] = (unsigned)PSLOT(s) + (unsigned)((j >> 1) * 16384 + (j & 1) * 4096); PINS(ps[j]); }
    zs[0] = half_of(ws + O_ZB, ZSLOT(s), 0u, pol); zs[1] = zs[0] + 4096u; xs = half_of(X1, XSLOT((s + 1)), 0u, pol); PINS(zs[0]); PINS(zs[1]); PINS(xs);
    int tss = TS_STEP(s); PINS(tss);
    asm volatile("s_waitcnt vmcnt(32)" : "+v"(tsl), "+v"(wn) :: "memory");
    wl = max(wl, wn);
    tsv = s < 2 ? ~0u : ts_max(tsl);
    if (__builtin_expect(s >= 2 && (tsv == ~0u || (int)(tsv - tpub) < a.d[8]), 0)) { tsv = ts_settle(rs_ts, tss, tsv, a.d[4]); }
    const bool uok = __all(wl >= (unsigned)(s + 2)), uok2 = __all(wl >= (unsigned)(s + 3));
    f32x4 o = ZERO4; PINV(o);
    unsigned tis = tsv + (unsigned)dlp; (void)tis;
#define R_LIGHT() do { const unsigned ln = (unsigned)lane_id(); light(mkrs(ws + O_ZB), ln < 32u ? zs[0] + (unsigned)c * 16384u + ln * 256u : (unsigned)(O_PS - O_ZB) + half_of(P, 0u, 0u, pol) + ps[4] + (ln - 32u) * 128u, 0u, tmo); } while (0)
    if (__builtin_expect(tsv & 1u, 0)) { R_LIGHT(); } else tis = wait_rt(tsv + (unsigned)dlp);
    unsigned adj = 0, pk, tsx; u64 nanm;
    u32x4 z[8], p[16]; u32x2 m[8];
#define R_LOAD() do { PINV(xv); PINV(pm); \
      _Pragma("unroll") for (int i = 0; i < 8; ++i) DS(z[i] = ld16(rs_z, xv + (unsigned)((i & 3) * 1024), zs[i >> 2])); \
      _Pragma("unroll") for (int i = 0; i < 16; ++i) DS(p[i] = ld16(rs_p, xv + (unsigned)((i & 3) * 1024), ps[i >> 2])); \
      _Pragma("unroll") for (int i = 0; i < 8; ++i) DS(m[i] = ld8(rs_p, pm + (unsigned)((i >> 1) * 1024 + (i & 1) * 128), ps[4])); \
      DS(tsx = __builtin_amdgcn_raw_buffer_load_b32(rs_ts, tso, tss, 16)); } while (0)
#define R_PROD() do { \
      SFOR(i, 16) SMFW(o, p[i], i); SEND; \
      _Pragma("unroll") for (int i = 0; i < 4; ++i) { const u32x4 a_ = {m[2 * i].x, m[2 * i].y, m[2 * i + 1].x, m[2 * i + 1].y}; const u32x8 b_ = cat8(z[2 * i], z[2 * i + 1]); SMFV(o, a_, b_); } \
      float ta, tb_; unsigned q; \
      asm volatile("s_nop 7\n v_add_f32 %2, %5, %7\n v_add_f32 %3, %6, %8\n v_cvt_pk_bf16_f32 %0, %2, %3\n v_cmp_u_f32_e64 %1, %2, %3\n s_nop 0\n" \
                   "v_mov_b32_dpp %4, %0 quad_perm:[1,0,3,2] row_mask:0xf bank_mask:0xf\n v_perm_b32 %0, %0, %4, %9" \
                   : "=&v"(pk), "=&s"(nanm), "=&v"(ta), "=&v"(tb_), "=&v"(q) : "v"(o[0]), "v"(o[1]), "v"(o[2]), "v"(o[3]), "v"(sel)); } while (0)
    R_LOAD();
    R_PROD();
    if (__builtin_expect(nanm != 0ull, 0)) {
      while (spin_check(adj, tmo)) {
        if (adj > 2u) R_LIGHT();
        else { const unsigned t_ = ts_max(tsx); if (t_ != ~0u) wait_rt(t_ + (unsigned)dlp); }
        R_LOAD();
        o = ZERO4; asm volatile("s_nop 1" : "+v"(o)); SB();
        R_PROD();
        if (nanm == 0ull) break;
        if ((adj & 7u) == 0u) {
          unsigned e = ~0u;
#pragma unroll
          for (int i = 0; i < 16; ++i) e = sent_min(e, p[i]);
#pragma unroll
          for (int i = 0; i < 8; ++i) e = sent_min(sent_min(e, z[i]), m[i].x, m[i].y);
          if (!__any(e == 0u)) break;
        }
      }
    }
    __builtin_amdgcn_raw_buffer_store_b32(pk, rs_x1, xo, (int)xs, 16);
    SB();
    PINU(tsx);
    { u64 t_; RT_MARK(t_); tpub = (unsigned)t_; }
    ADAPT(dlp, adj);
    if (s + PRE + 1 < 64) st16(rs_x1, lane < 16 ? xv : 0x80000000u, half_of(X1, XSLOT((s + PRE + 1)), 0u, pol), SENT4);
    {
      if (__builtin_expect(!uok, 0)) { wait_heads<4>(cnt + CNT(C_PW) + ((c >> 1) & 3), 0, (unsigned)(s + 2), tmo); }
      wait_rt(tpub + (unsigned)a.d[3]);
      asm volatile("s_nop 4\n buffer_load_dword %0, %1, %2, %3 offen sc1" : "=v"(tsl) : "v"(tso), "s"(ts_desc), "s"(TS_STEP(s + 1)) : "memory");
      asm volatile("s_nop 4\n buffer_load_dword %0, %1, %2, 0 offen sc1" : "=v"(wn) : "v"(uok2 ? (int)0x80000000u : two), "s"(pw_desc) : "memory");
      U_LOAD(s + 1, false);
    }
  }
  VMW(0);
}

__device__ __forceinline__ void w_role(const Args& a, unsigned char* lds, const int h, const int wave) {
  char* ws = a.ws; asm volatile("" : "+s"(ws));
  unsigned* const cnt = (unsigned*)(ws + O_CNT); unsigned* const tmo = cnt + CNT(C_TMO);
  const int lane = lane_id(), l15 = lane & 15, lq = lane >> 4;
  WReg W;
  {
    ODMA_INIT();
    ODMA(0, 0); ODMA(1, 1); ODMA(2, 2);
#pragma unroll
    for (int i = 0; i < 5; ++i) {
      if (i < 3) VMW(24); else if (i == 3) VMW(12); else VMW(0);
      BAR();
#pragma unroll
      for (int t = 0; t < 2; ++t) {
        const unsigned ad = lds_addr(lds) + otr[t] + (unsigned)SBUF(i);
        TRO4(ad, 0, 6 * i + 3 * t); TRO4(ad, 1, 6 * i + 3 * t + 1); TRO4(ad, 2, 6 * i + 3 * t + 2);
      }
      if (i + 3 < 5) { BAR(); ODMA(i + 3, i + 3); }
    }
  }
  const __amdgpu_buffer_rsrc_t rs_u = mkrs(ws + O_UO + h * 1024);
  const __amdgpu_buffer_rsrc_t rs_pv = mkrs(cnt + CNT(C_PV + h)), rs_pw = mkrs(cnt + CNT(C_PW + h));
  unsigned xv = (unsigned)lane * 16u; int flo = lane == 0 ? 4 * wave : (int)0x80000000u; PINV(xv); PINV(flo);
  unsigned uo[10];
#pragma unroll
  for (int i = 0; i < 10; ++i) { uo[i] = (unsigned)(8 * OROT(i >> 1) + 2 * wave + (i & 1)) << 15; PINS(uo[i]); }
  const __amdgpu_buffer_rsrc_t rs_po = mkrs(cnt + CNT(C_PO)); int poo = wave == 0 ? (lane % 24) / 6 * 128 + (lane % 6) * 4 : (int)0x80000000u; PINV(poo);
  const unsigned pol = wait_ge<8>(cnt + CNT(C_POL), 1u, tmo) & 1u, kkey = K_KEY();
  const char* vsrc = ws + O_VF + (size_t)h * 32768 + (wave >> 1) * 16384 + (wave & 1) * 3072;
#define V_DMA(sg_) do { m0_set(lds_addr(lds) + (unsigned)(((sg_) % 3) * 12288 + wave * 3072)); const char* p_ = vsrc + KSLOT(sg_); asm volatile("s_nop 4" : "+s"(p_)); glds_x<0>(p_, xv); glds_x<1024>(p_, xv); glds_x<2048>(p_, xv); } while (0)
  touch_rows(a, 4 * h + wave);
  BAR();
  wait_t<2>(cnt + CNT(C_PV + h), 1, 2u, tmo);
  V_DMA(1); V_DMA(2);
  VMW(3);
  int nxt = 3;
#pragma unroll 1
  for (int s = 1; s < 64; ++s) {
    unsigned ub = u_off((unsigned)s, 0u), sv = (unsigned)s, va = lds_addr(lds) + (unsigned)((s % 3) * 12288) + xv; PINS(ub); PINV(sv); PINV(va);
    f32x4 acc[2][2]; u32x4 vx[12], pw, w; unsigned sp; int vprog; bool late, far;
    BAR();
#pragma unroll
    for (int n = 0; n < 12; ++n) DS(vx[(n >> 1) + 6 * (n & 1)] = *(const LAS u32x4*)(size_t)(va + ((n >> 1) + 6 * (n & 1)) * 1024));
    SFOR(t, 11)
      SFOR(m, 12)
        constexpr int e = m >> 1, rt = m & 1, c = (t + 1) & 1;
        if (t < 10) { mf_w<false>(acc[t & 1][rt], vx[6 * rt + e], 6 * (t % 10) + e, e == 0, W); SB(); }
        else if (m == 0) { asm volatile("s_nop 7" : "+v"(acc[1][0]), "+v"(acc[1][1])); SB(); }
        if (t > 0 && m >= 2 && m < 6) { if (t < 10) FILL(); CVT1(w[(m + 2) & 3], acc[c][(m >> 1) - 1 & 1][2 * (m & 1)], acc[c][(m >> 1) - 1 & 1][2 * (m & 1) + 1]); }
        if (t > 0 && m == 6) { if (t < 10) __builtin_amdgcn_raw_buffer_store_b128(w, rs_u, (int)xv, (int)(ub + uo[(t + 9) % 10]), 16); else st16(rs_u, xv, ub + uo[9], w); SB(); }
        if (t == 0 && m == 1) { pw = ld16(rs_pv, 0u, 0u); SB(); }
        if (t == 0 && m == 4) { sp = __builtin_amdgcn_raw_buffer_load_b32(rs_po, poo, 0, 16); SB(); }
        if (t == WCK && m == 8) { vprog = (int)min_t(pw, 1); PINS(vprog); far = wave == 0 && __any((int)sp < s + 2 - WGATE); MEMB(); SB(); }
        if (t == WCK && m == 10) { __builtin_amdgcn_raw_buffer_store_b32(sv, rs_pw, flo, 0, 16); SB(); }
        if (t == WCK + 1 && m == 0) {
          late = nxt <= s + 1;
          const int upto = min(min(vprog, s + 2), 63);
          while (nxt <= upto) { V_DMA(nxt); ++nxt; }
          SB(); }
        if (t == WCK + 1 && m == 8) { pw = ld16(rs_pv, 0u, 0u); SB(); }
      SEND;
    SEND;
    if (__builtin_expect(late || far, 0)) {
      const int upto = min(min((int)min_t(pw, 1), s + 2), 63);
      while (nxt <= upto) { V_DMA(nxt); ++nxt; }
      VMW(0); __builtin_amdgcn_raw_buffer_store_b32((unsigned)(s + 1), rs_pw, flo, 0, 16);
      if (s < 63) {
        if (far) { unsigned spins = 0; while (__any((int)__builtin_amdgcn_raw_buffer_load_b32(rs_po, poo, 0, 16) < s + 2 - WGATE) && spin_check(spins, tmo)) __builtin_amdgcn_s_sleep(24); MEMB(); }
        if (nxt <= s + 1) { wait_t<1>(cnt + CNT(C_PV + h), 1, (unsigned)(s + 1), tmo); V_DMA(nxt); ++nxt; VMW(0); }
      }
    }
  }
}

#define TSTRICT 24
#define LDS16(ad_) (*(LAS u32x4*)(size_t)(ad_))
struct TReg { u32x32 b[12]; u32x16 e; };
#define TR_SW(g_, X) switch (g_) { case 0: X("a", "{a[0:31]}", T.b[0]); break; case 1: X("a", "{a[32:63]}", T.b[1]); break; case 2: X("a", "{a[64:95]}", T.b[2]); break; case 3: X("a", "{a[96:127]}", T.b[3]); break; \
    case 4: X("a", "{a[128:159]}", T.b[4]); break; case 5: X("a", "{a[160:191]}", T.b[5]); break; case 6: X("a", "{a[192:223]}", T.b[6]); break; case 7: X("a", "{a[224:255]}", T.b[7]); break; \
    case 8: X("v", "{v[110:141]}", T.b[8]); break; case 9: X("v", "{v[142:173]}", T.b[9]); break; case 10: X("v", "{v[174:205]}", T.b[10]); break; case 11: X("v", "{v[206:237]}", T.b[11]); break; default: X("v", "{v[238:253]}", T.e); break; }
__device__ __forceinline__ void mf_t(f32x4& c, const u32x4 x, const int d, const bool first, const TReg& T) {
  const int r = d < 64 ? 4 * d : 4 * d - 146;
#define X_(rf_, cs_, v_) if (first) asm volatile("v_mfma_f32_16x16x32_bf16 %0, " rf_ "[%c2:%c3], %1, 0" : "=&v"(c) : "v"(x), "n"(r), "n"(r + 3), cs_(v_)); \
      else asm volatile("v_mfma_f32_16x16x32_bf16 %0, " rf_ "[%c2:%c3], %1, %0" : "+v"(c) : "v"(x), "n"(r), "n"(r + 3), cs_(v_))
  TR_SW(d >> 3, X_);
#undef X_
}
__device__ __forceinline__ void tr_t(const unsigned ad0, const unsigned ad1, const int off, const int d, TReg& T) {
  const int r = d < 64 ? 4 * d : 4 * d - 146;
#define Y_(md_, rf_, cs_, v_) asm volatile("ds_read_b64_tr_b16 " rf_ "[%c5:%c6], %1 offset:%3\n ds_read_b64_tr_b16 " rf_ "[%c7:%c8], %1 offset:%4\n ds_read_b64_tr_b16 " rf_ "[%c9:%c10], %2 offset:%3\n ds_read_b64_tr_b16 " rf_ "[%c11:%c12], %2 offset:%4\n s_waitcnt lgkmcnt(0)" \
    : md_ cs_(v_) : "v"(ad0), "v"(ad1), "n"(off), "n"(off + 1536), "n"(r), "n"(r + 1), "n"(r + 2), "n"(r + 3), "n"(r + 4), "n"(r + 5), "n"(r + 6), "n"(r + 7) : "memory")
#define X_(rf_, cs_, v_) if ((d & 7) == 0) Y_("=&", rf_, cs_, v_); else Y_("+", rf_, cs_, v_)
  TR_SW(d >> 3, X_);
#undef X_
#undef Y_
}
__device__ __forceinline__ void t_role(const Args& a, unsigned char* lds, const int h, const int isv, const int wave) {
  char* ws = a.ws; asm volatile("" : "+s"(ws));
  unsigned* const cnt = (unsigned*)(ws + O_CNT); unsigned* const tmo = cnt + CNT(C_TMO);
  const int lane = lane_id(), l15 = lane & 15, lq = lane >> 4;
  TReg T;
#define TBUF(j_) (24576u + (unsigned)(((j_) + 3) % 6) * 20480u)
#define TSRC(c_, r_) ((((c_) - (r_) * 80u < 64u ? (r_) * 80u + (((c_) - (r_) * 80u + 16u * (unsigned)h) & 63u) : (c_)) ^ ((r_) & 15u)) * 16u)
#define TDMA1(src_, dst_, i_) do { const unsigned c = (unsigned)((wave + 4 * (i_)) * 64 + lane_id()), r = c / 80u; glds(src_, TSRC(c, r), (dst_) + (i_) * 4096u); } while (0)
#define TROW(j_) (16 * ((j_) + 6))
#define TDMA(j_) do { _Pragma("unroll") for (int xi_ = 0; xi_ < 5; ++xi_) TDMA1(a.Bx + (size_t)TROW(j_) * 640, lds_addr(lds) + TBUF(j_) + (unsigned)wave * 1024u, xi_); } while (0)
  const __amdgpu_buffer_rsrc_t rs_fl = mkrs(cnt + CNT((isv ? C_PV : C_PT_) + h)), rs_o = mkrs(ws + O_KF + (size_t)h * 32768);
  unsigned vo[2], kkey;
  {
    unsigned ad0[5], ad1[5], mvo[6], qvo[3], xvo, xr, x5;
    { TRO_INIT();
#pragma unroll
      for (int t = 0; t < 5; ++t) { const bool isq = t == 4 || wave == 3; const unsigned tile = t == 4 ? 4u + (unsigned)(wave >> 1) : (wave == 3 ? (unsigned)t : (unsigned)(4 * wave + t));
        ad0[t] = lds_addr(lds) + 32768u + TRO(tile) + (isq ? 24576u : 0u); ad1[t] = lds_addr(lds) + 32768u + (isq ? TRO(tile + 6u) + 24576u : TRO(tile) + 12288u); PINV(ad0[t]); PINV(ad1[t]); } }
#pragma unroll
    for (int q = 0; q < 6; ++q) { const unsigned c = (unsigned)((wave + 4 * q) * 64 + lane), r = c / 24u, pc = (c - r * 24u) ^ (((r >> 1) & 1u) * 2u + ((r >> 3) & 1u) * 4u);
      mvo[q] = r * 12288u + 16u * pc; PINV(mvo[q]);
      if (q < 3) { qvo[q] = pc < 12u ? mvo[q] : mvo[q] + 32u * 12288u - 192u; PINV(qvo[q]); } }
    xvo = (unsigned)((8 * wave + (lane >> 3)) * 1280 + ((lane & 7) ^ (lane >> 3)) * 16);
    xr = lds_addr(lds) + (unsigned)(l15 * 128 + (lq ^ (l15 & 7)) * 16); x5 = xr + (unsigned)((wave & 1) * 2048);
    PINV(xvo); PINV(xr); PINV(x5);
    const u16* Wm = (isv ? a.Wv : a.Wk) + 192 * h; const u16* Wh = a.Wq + 192 * h + 96 * isv;
#define TW_DMA(i_) do { const int r_ = 128 * KROT((i_) >> 1) + 64 * ((i_) & 1); const unsigned d_ = lds_addr(lds) + (unsigned)(32768 + ((i_) % 3) * 36864 + wave * 1024); \
      _Pragma("unroll") for (int q_ = 0; q_ < 2; ++q_) glds(a.Bx + (32 + 32 * q_) * 640 + r_, xvo, lds_addr(lds) + (unsigned)(((i_) & 3) * 8192 + q_ * 4096 + wave * 1024)); \
      _Pragma("unroll") for (int q_ = 0; q_ < 6; ++q_) glds(Wm + (size_t)r_ * 6144, mvo[q_], d_ + (unsigned)(q_ * 4096)); \
      _Pragma("unroll") for (int q_ = 0; q_ < 3; ++q_) glds(Wh + (size_t)r_ * 6144, qvo[q_], d_ + (unsigned)(24576 + q_ * 4096)); SB(); } while (0)
    TW_DMA(0); TW_DMA(1); TW_DMA(2);
    f32x4 pa[4][4], p5[2];
    SFOR(i, 10)
      if (i < 8) VMW(22); else if (i == 8) VMW(16); else VMW(10);
      BAR();
      SFOR(t, 5)
        if (i % 3 < 2) tr_t(ad0[t], ad1[t], (i % 3) * 36864, 10 * i + 2 * t, T); else tr_t(ad0[t] + 73728u, ad1[t] + 73728u, 0, 10 * i + 2 * t, T);
      SEND;
      if (i < 9) BAR();
      if (i + 3 < 10) TW_DMA(i + 3);
      if (i == 7) TDMA(0);
      if (i == 8) TDMA(1);
      SFOR(kk, 2)
        u32x4 x_[4], y_[2];
#pragma unroll
        for (int rt = 0; rt < 4; ++rt) x_[rt] = LDS16((xr ^ (unsigned)(kk * 64)) + (i & 3) * 8192 + rt * 2048);
#pragma unroll
        for (int g = 0; g < 2; ++g) y_[g] = LDS16((x5 ^ (unsigned)(kk * 64)) + (i & 3) * 8192 + g * 4096);
        SFOR(n, 16) mf_t(pa[n >> 2][n & 3], x_[n >> 2], 10 * i + 2 * (n & 3) + kk, i == 0 && kk == 0, T); SEND;
        SFOR(g, 2) mf_t(p5[g], y_[g], 10 * i + 8 + kk, i == 0 && kk == 0, T); SEND;
        SB();
      SEND;
    SEND;
    { const unsigned pol = wait_ge<8>(cnt + CNT(C_POL), 1u, tmo) & 1u; kkey = K_KEY(); PINS(kkey); }
    const int ln = lane_id();
    vo[0] = (unsigned)ln * 16u + (wave == 3 ? (unsigned)(O_QF - O_KF) + (unsigned)(3 * isv) * 1024u : (isv ? (unsigned)(O_VF - O_KF) : 0u) + (unsigned)wave * 2048u);
    vo[1] = (unsigned)ln * 16u + (unsigned)(O_QF - O_KF) + (unsigned)(3 * isv + 2) * 1024u + (unsigned)(wave >> 1) * 8u;
    PINV(vo[0]); PINV(vo[1]);
    asm volatile("s_nop 7" : "+v"(pa[3][0]), "+v"(pa[3][1]), "+v"(pa[3][2]), "+v"(pa[3][3]), "+v"(p5[0]), "+v"(p5[1]));
#pragma unroll
    for (int g = 0; g < 2; ++g) {
#pragma unroll
      for (int n = 0; n < 4; ++n) { const f32x4 c0 = pa[2 * g + (n >> 1)][2 * (n & 1)], c1 = pa[2 * g + (n >> 1)][2 * (n & 1) + 1];
        st16(rs_o, vo[0] + (unsigned)((n & 1) * 1024), KSLOT(1 + g) + (unsigned)((n >> 1) * 16384), (u32x4){cvtpk(c0[0], c0[1]), cvtpk(c0[2], c0[3]), cvtpk(c1[0], c1[1]), cvtpk(c1[2], c1[3])}); }
      st8(rs_o, vo[1], KSLOT(1 + g) + (unsigned)((wave & 1) * 16384), cvtpk(p5[g][0], p5[g][1]), cvtpk(p5[g][2], p5[g][3]));
    }
  }
  const int lane2 = lane_id();
#define lane lane2
#define l15 (lane2 & 15)
#define lq (lane2 >> 4)
  unsigned tvo[5];
#pragma unroll
  for (int i = 0; i < 5; ++i) { const unsigned c = (unsigned)((5 * wave + i) * 64 + lane), r = c / 80u; tvo[i] = TSRC(c, r) + (unsigned)(4096 - (i - 2) * 1024); PINV(tvo[i]); }
  int flo = lane == 0 ? wave * 4 : (int)0x80000000u; PINV(flo);
  unsigned xq[4];
#pragma unroll
  for (int q = 0; q < 4; ++q) { xq[q] = lds_addr(lds) + (unsigned)TBUF(0) + (unsigned)(l15 * 1280 + ((4 * q + lq) ^ l15) * 16); PINV(xq[q]); }
#define XFRT(k_) LDS16(xq[(k_) & 3] + ((k_) >> 2) * 256)
  const unsigned fla = lds_addr(lds) + 147456u;
  unsigned flw = fla + 4u * (unsigned)lane, one = 1u; PINV(flw); PINV(one);
  if (wave == 0) *(volatile LAS unsigned*)(size_t)flw = 0u;
  unsigned gw = flw + 256u * (unsigned)(wave + 1), go = (unsigned)((lane % 24) / 6 * 128 + (lane % 6) * 4), gm = fla + 256u * (unsigned)(wave + 1), gl; PINV(gw); PINV(go); PINS(gm);
  const unsigned* gsrc = cnt + CNT(C_PO); PINS(gsrc);
  *(volatile LAS unsigned*)(size_t)gw = 0u;
  u64 gb; int gt;
  u32x4 xa[4]; u32x2 cv[5]; unsigned fl;
  f32x4 acc[5];
#pragma unroll
  for (int t = 0; t < 5; ++t) { acc[t] = ZERO4; cv[t] = (u32x2){0u, 0u}; }
  VMW(0);
  __builtin_amdgcn_raw_buffer_store_b32(2u, rs_fl, flo, 0, 16);
  BAR();
  TDMA(2); TDMA(3);
  xa[0] = XFRT(0); xa[1] = XFRT(1);
#define T_CVT(t_) do { unsigned c0_, c1_; \
      asm volatile("v_cvt_pk_bf16_f32 %0, %2, %3\n v_cvt_pk_bf16_f32 %1, %4, %5" : "=&v"(c0_), "=&v"(c1_) : "v"(acc[t_][0]), "v"(acc[t_][1]), "v"(acc[t_][2]), "v"(acc[t_][3])); \
      cv[t_] = (u32x2){c0_, c1_}; SB(); } while (0)
#define T_ST(i_) do { PINV(vo[0]); if ((i_) < 2) __builtin_amdgcn_raw_buffer_store_b128((u32x4){cv[2 * (i_)].x, cv[2 * (i_)].y, cv[(2 * (i_) + 1) % 5].x, cv[(2 * (i_) + 1) % 5].y}, rs_o, (int)vo[0] + (i_) * 1024, (int)sof, 16); \
      else __builtin_amdgcn_raw_buffer_store_b64(cv[4], rs_o, (int)vo[1], (int)sof, 16); SB(); } while (0)
#define T_REST(five_) do { asm volatile("s_nop 7\n s_nop 7"); T_CVT(1); T_CVT(2); T_CVT(3); T_CVT(4); T_ST(0); T_ST(1); if (five_) T_ST(2); VMW(0); } while (0)
  const char* dsrc = (const char*)(a.Bx + (size_t)TROW(4) * 640) - 4096;
  unsigned tb = TBUF(4) - 24576u, rb = TBUF(0) - 24576u;
  unsigned sol = 3u * K_STEP + 16384u, sof = k_fix(sol, kkey), sinc = 0u - 16384u, tgt = 4u, dstep = 20480u, fv = 0u, dq = 0u;
  const unsigned dd0 = lds_addr(lds) + 24576u + (unsigned)(5 * wave + 2) * 1024u;
#define T_MF(t_) do { mf_t(acc[t_], xa[k & 3], 10 * (k >> 1) + 2 * (t_) + (k & 1), k == 0, T); SB(); } while (0)
#define T_TILE(STRICT, FIVE) do { \
    SFOR(k, 20) \
      constexpr int k2 = k + 2, kn = k2 < 20 ? k2 : k2 - 20; \
      T_MF(0); \
      if (k != 18) { xa[k2 & 3] = XFRT(kn); SB(); } \
      if (k == 0) T_CVT(1); \
      if (k == 18) { xq[0] += dq; PINV(xq[0]); SB(); }        \
      T_MF(1); \
      if (k == 0) T_CVT(2); \
      if (k == 2 && FIVE) { asm volatile("s_mov_b32 m0, %0" :: "s"(gm) : "memory"); SB(); } \
      if (k == 14 && FIVE) { gl = *(const volatile LAS unsigned*)(size_t)gw; SB(); } \
      if (k == 17 && FIVE) {        \
        if (__builtin_expect(gb != 0ull, 0)) { unsigned spins = 0; while (__any((int)__hip_atomic_load((const gu32*)gsrc + (go >> 2), RLX_AGENT) < gt) && spin_check(spins, tmo)) __builtin_amdgcn_s_sleep(24); } \
        MEMB(); SB(); } \
      if (k == 3) { unsigned m_ = dd0 + tb; PINS(m_); asm volatile("s_mov_b32 m0, %0" :: "s"(m_) : "memory"); SB(); } \
      if (k == 16) { fl = *(const volatile LAS unsigned*)(size_t)flw; SB(); } \
      if (k == 18) {        \
        if (__builtin_expect(fs < tgt, 0)) { do fl = *(const volatile LAS unsigned*)(size_t)flw; while ((unsigned)__builtin_amdgcn_readfirstlane(fl) < tgt); } \
        MEMB(); SB(); } \
      if (k == 9) { PINS(tb); tb += 20480u; PINS(tb); SB(); } \
      if (k == 11) { PINS(rb); rb += 20480u; PINS(rb); SB(); } \
      if (k == 12) { PINS(rb); rb = rb == 6u * 20480u ? 0u : rb; PINS(rb); SB(); } \
      if (k == 13) { PINS(sol); PINS(sinc); sol = (sol + sinc) & (NK * K_STEP - 1u); PINS(sol); SB(); } \
      if (k == 15) { PINS(dstep); dstep = j < 116 ? 20480u : 0u; PINS(dstep); SB(); } \
      if (k == 10) { fv = (unsigned)(STRICT ? (j < 1 ? 4 : j + 3) : j + 2) >> 1; PINV(fv); SB(); } \
      if (k == 19) { xq[2] += dq; PINV(xq[2]); SB(); } \
      T_MF(2); \
      if (k == 0) T_CVT(3); \
      if (k == 1 || k == 2 || (k == 3 && !FIVE)) T_ST(k >= 1 && k <= 3 ? k - 1 : 0); \
      if (k == 4) glds_o<-2048>(dsrc, tvo[0]); else if (k == 5) glds_o<-1024>(dsrc, tvo[1]); else if (k == 6) glds_o<0>(dsrc, tvo[2]); else if (k == 7) glds_o<1024>(dsrc, tvo[3]); else if (k == 8) glds_o<2048>(dsrc, tvo[4]); \
      if (k == 9) { PINS(dsrc); dsrc += dstep; PINS(dsrc); SB(); } \
      if (k == 11) { PINS(tb); tb = tb == 6u * 20480u ? 0u : tb; PINS(tb); SB(); } \
      if (k == 14) { PINS(rb); dq = rb == 0u ? 0u - 5u * 20480u : 20480u; PINS(dq); SB(); } \
      if (k == 15) { PINS(sinc); sinc = sinc == 16384u ? K_STEP - 16384u : 16384u; PINS(sinc); SB(); } \
      if (k == 12) {        \
        asm volatile("s_waitcnt vmcnt(%0)" :: "n"(STRICT ? 14 : 23) : "memory"); \
        __hip_atomic_fetch_add((LAS unsigned*)(size_t)flw, one, __ATOMIC_RELAXED, __HIP_MEMORY_SCOPE_WORKGROUP); SB(); \
      } \
      if (k == 13) { __builtin_amdgcn_raw_buffer_store_b32(fv, rs_fl, flo, 0, 16); SB(); } \
      if (k == 17) { fs = (unsigned)__builtin_amdgcn_readfirstlane(fl); PINS(fs); SB(); } \
      if (k == 18) { xa[0] = XFRT(0); SB(); xq[1] += dq; PINV(xq[1]); SB(); } \
      if (k == 19) { PINS(tgt); tgt += 4u; PINS(tgt); xq[3] += dq; PINV(xq[3]); SB(); } \
      T_MF(3); \
      if (k == 0 && !FIVE) T_CVT(4); \
      if (k == 13) { sof = (sol >> 8) & 0x2000u; PINS(sof); SB(); } \
      if (k == 16) { PINS(sof); sof ^= sol ^ kkey; PINS(sof); SB(); } \
      if (k == 2 && FIVE) { asm volatile("global_load_lds_dword %0, %1 sc1" :: "v"(go), "s"(gsrc) : "memory"); SB(); } \
      if (k == 14 && FIVE) { gt = (j >> 1) + 5 - NK; PINS(gt); SB(); } \
      if (k == 15 && FIVE) { asm volatile("v_cmp_gt_i32_e64 %0, %1, %2" : "=s"(gb) : "s"(gt), "v"(gl)); SB(); } \
      if (k == 19) T_CVT(0); \
      if (FIVE) T_MF(4); \
    SEND; } while (0)
  unsigned fs;
  static_assert(TSTRICT % 2 == 0, "");
#define T_ALL(ODD) do { \
    if (ODD) { const int j = 0; T_TILE(true, false); } \
    _Pragma("unroll 1") for (int j2 = ODD; j2 < TSTRICT; j2 += 2) { \
      { const int j = j2; T_TILE(true, true); } \
      { const int j = j2 + 1; T_TILE(true, false); } } \
    _Pragma("unroll 1") for (int j2 = TSTRICT + ODD; j2 < 121; j2 += 2) { \
      const int s = j2 >> 1; (void)s; \
      { const int j = j2; T_TILE(false, true); } \
      { const int j = j2 + 1; T_TILE(false, false); } } \
    if (ODD) { const int j = 121; T_TILE(false, true); } \
    T_REST(ODD); \
    __builtin_amdgcn_raw_buffer_store_b32(64u, rs_fl, flo, 0, 16); } while (0)
  if (wave & 1) T_ALL(1); else T_ALL(0);
}
#undef lane
#undef l15
#undef lq

#define XPO 0
#define OAUX 0
__device__ __forceinline__ void o_role(const Args& a, unsigned char* lds, const int j, const int wave) {
  char* ws = a.ws; asm volatile("" : "+s"(ws));
  unsigned* const cnt = (unsigned*)(ws + O_CNT); unsigned* const tmo = cnt + CNT(C_TMO);
  const int lane = lane_id(), l15 = lane & 15, lq = lane >> 4;
  const unsigned pol = wait_ge<32>(cnt + CNT(C_POL), 1u, tmo) & 1u;
  const int xq = j / 6, c0 = 10 * xq + 5 * (j & 1), s0 = (j - 6 * xq) >> 1;
  const u32x4 rs_u = mkdesc(ws + O_UO + wave * 8192);
  const __amdgpu_buffer_rsrc_t rs_z = mkrs(ws + O_ZB + (size_t)c0 * 16384 + wave * 2048), rs_p = mkrs(ws + O_PT + wave * 16384), rs_m = mkrs(ws + O_PT + 65536 + wave * 4096), rs_out = mkrs(a.out + 640 + 16 * c0);
  unsigned xv = (unsigned)lane * 16u, xh = (unsigned)lane * 8u;
  unsigned wa = (unsigned)(wave * 10880 + lq * 272 + l15 * 16), ra[2], oo[2];
#pragma unroll
  for (int i = 0; i < 2; ++i) {
    const int n = i == 0 ? lane : 64 + l15, tok = 8 * wave + n / 10, oc = n % 10;
    ra[i] = (unsigned)((((oc >> 1) * 2 + (tok >> 4)) * 4 + 2 * (oc & 1)) * 272 + (tok & 15) * 16);
    oo[i] = i == 0 || lane < 16 ? (unsigned)((tok * 1280 + 8 * oc) * 2) : 0x80000000u;
    PINV(ra[i]); PINV(oo[i]);
  }
  PINV(xv); PINV(xh); PINV(wa);
  WReg W;
  u32x2 wp = {0u, 0u};
  wait_ge<32>(cnt + CNT(C_FILL), NFILL, tmo);
#pragma unroll 1
  for (int s = s0; s < 64; s += 3) {
    if (wave == 0) {
      const unsigned tk = __hip_atomic_load((const gu32*)(cnt + CNT(C_TOK + 4 * s + xq)), RLX_AGENT);
      if (__any(min(wp.x, wp.y) < (unsigned)(s + 1))) {
        unsigned spins = 0;
        do wp = ld8(mkrs(cnt + CNT(s == 0 ? C_PZ : C_PW)), (unsigned)((lane & 31) * 128 + (lane >> 5) * 8), 0u); while (__any(min(wp.x, wp.y) < (unsigned)(s + 1)) && (__builtin_amdgcn_s_sleep(16), spin_check(spins, tmo)));
      }
      wait_ge<8>(cnt + CNT(C_PS + j), (unsigned)(s < 63 ? s + 1 : 63), tmo);
      *(volatile LAS unsigned*)(lds + 65536) = tk;
    }
    BAR();
    const bool behind = (unsigned)__builtin_amdgcn_readfirstlane(*(volatile LAS unsigned*)(lds + 65536)) >= 32u;
    const int np = behind || s >= 60 ? 0 : a.d[14];
    SFOR(i, 20) ld_w(rs_u, xv, u_off((unsigned)s, (unsigned)(c0 + (i >> 2))) + (unsigned)(((i >> 1) & 1) * 4096), (i & 1) * 2048, i, (i & 3) == 0, W); nap_rt(np); SEND;
    u32x4 z[5][2], p1[8], pm[2][2]; u32x2 p0[8];
    const unsigned zs = half_of(ws + O_ZB, ZSLOT(s), 0u, pol), ps = TSLOT(s);
    unsigned spins = 0;
    for (;;) {
      unsigned e = ~0u;
      PINV(xv);
#pragma unroll
      for (int n = 0; n < 10; ++n) { z[n >> 1][n & 1] = ld16(rs_z, xv + (unsigned)((n & 1) * 1024), zs + (unsigned)((n >> 1) * 16384)); e = sent_min(e, z[n >> 1][n & 1]); }
      if (!__any(e == 0u) || !spin_check(spins, tmo)) break;
      __builtin_amdgcn_s_sleep(4);
    }
    if (!behind) {
      if (wave == 0) { if (s >= 60) wait_ge<1>(cnt + CNT(C_TOK + 4 * s + xq), 32u, tmo); else wait_ge<12>(cnt + CNT(C_TOK + 4 * s + xq), 32u, tmo); }
      BAR();
    }
    PINV(xv); PINV(xh);
#pragma unroll
    for (int n = 0; n < 8; ++n) { p1[n] = ld16(rs_p, xv + (unsigned)((n & 1) * 2048 + 1024), ps + (unsigned)((n >> 1) * 4096)); p0[n] = ld8(rs_p, xh + (unsigned)((n & 1) * 2048), ps + (unsigned)((n >> 1) * 4096)); SB(); }
#pragma unroll
    for (int n = 0; n < 4; ++n) pm[n >> 1][n & 1] = ld16(rs_m, xv + (unsigned)(n * 1024), ps);
    SB();
    f32x4 acc[5][2];
    SFOR(n, 8)
      u32x4 q0 = {p0[n].x, p0[n].y, 0u, 0u}; PINV(q0);
      SFOR(t, 5) mf_w<true>(acc[t][1], p1[n], 8 * t + n, n == 0, W); SB(); SEND;
      SFOR(t, 5) mf_w<true>(acc[t][0], q0, 8 * t + n, n == 0, W); SB(); SEND;
    SEND;
#pragma unroll
    for (int n = 0; n < 4; ++n)
#pragma unroll
      for (int t = 0; t < 5; ++t) asm volatile("v_mfma_f32_16x16x32_bf16 %0, %1, %2, %0" : "+v"(acc[t][n & 1]) : "v"(z[t][n >> 1]), "v"(pm[n >> 1][n & 1]));
    asm volatile("s_nop 7" : "+v"(acc[0][1]), "+v"(acc[1][1]), "+v"(acc[2][1]), "+v"(acc[3][1]), "+v"(acc[4][1]));
#pragma unroll
    for (int n = 0; n < 10; ++n) *(f32x4*)(lds + wa + (n % 5 * 2 + n / 5) * 1088) = acc[n % 5][n / 5];
    BAR();
    if (wave == 0 && lane == 0) __hip_atomic_store((gu32*)(cnt + CNT(C_PO + xq) + j - 6 * xq), (unsigned)(s + 3), RLX_AGENT);
#pragma unroll
    for (int i = 0; i < 2; ++i) {
      f32x4 v0 = *(const f32x4*)(lds + ra[i]), v1 = *(const f32x4*)(lds + ra[i] + 272);
#pragma unroll
      for (int w = 1; w < 4; ++w) { v0 += *(const f32x4*)(lds + ra[i] + w * 10880); v1 += *(const f32x4*)(lds + ra[i] + w * 10880 + 272); }
      const u32x4 w_ = {cvtpk(v0[0], v0[1]), cvtpk(v0[2], v0[3]), cvtpk(v1[0], v1[1]), cvtpk(v1[2], v1[3])};
      __builtin_amdgcn_raw_buffer_store_b128(w_, rs_out, (int)oo[i], 32 * s * 1280 * 2, OAUX);
      asm volatile("s_nop 1" :: "v"(w_) : "memory");
    }
  }
}

#define XPF 200
__device__ __forceinline__ void att_role(const Args& a, unsigned char* lds, const int h, const int wave) {
  char* ws = a.ws;
  unsigned* const cnt = (unsigned*)(ws + O_CNT); unsigned* const tmo = cnt + CNT(C_TMO);
  const int lane = lane_id(), l15 = lane & 15, lq = lane >> 4, par = wave >> 1;
  const __amdgpu_buffer_rsrc_t rs_q = mkrs(ws + O_QF + (size_t)h * 32768), rs_k = mkrs(ws + O_KF + (size_t)h * 32768), rs_km = mkrs(ws + O_KM + (size_t)h * 3072), rs_pt = mkrs(ws + O_PT);
  const float scale = 0.07216878364870323f, LOG2E = 1.4426950408889634f;
  unsigned xv = (unsigned)lane * 16u; PINV(xv);
#define KM_EMPTY(s_) do { _Pragma("unroll") for (int i_ = 0; i_ < 3; ++i_) st16(rs_km, xv + (unsigned)(i_ * 1024), MSLOT(s_), SENT4); } while (0)
#pragma unroll 1
  for (int s = par; s < NZ; s += 2) KM_EMPTY(s);
  VMW(0);
  if (lane == 0) __hip_atomic_fetch_add((gu32*)(cnt + CNT(C_FILL)), 1u, RLX_AGENT);
  nap_rt(XPF);
  touch_rows(a, 128 + 2 * h + par);
  { const unsigned v = touch(a.Awo, 640 * 1280, 2 * h + par) ^ touch(a.Awo, 640 * 1280, 64 + 2 * h + par); asm volatile("" :: "v"(v)); }
  const unsigned pol = wait_ge<32>(cnt + CNT(C_POL), 1u, tmo) & 1u, kkey = K_KEY();
  u32x4 qf[2][6], kf[2][6];
#define QK_LOAD(s_) do { PINV(xv); _Pragma("unroll") for (int i = 0; i < 12; ++i) { const unsigned so = KSLOT(s_) + (unsigned)(i / 6 * 16384 + i % 6 / 3 * 3072), vo = xv + (unsigned)(i % 3 * 1024); \
      kf[i / 6][i % 6] = ld16(rs_k, vo, so); qf[i / 6][i % 6] = ld16(rs_q, vo, so); } SB(); } while (0)
  const __amdgpu_buffer_rsrc_t rs_tv = mkrs(cnt + CNT(C_PT_ + h));
  int tvo = (lane & 3) * 4 + ((lane >> 2) & 1) * 4 * CNT(C_PV - C_PT_); PINV(tvo);
#define TV_WAIT(s_) do { unsigned spins = 0; while (__any(__builtin_amdgcn_raw_buffer_load_b32(rs_tv, tvo, 0, 16) < (unsigned)(s_)) && spin_check(spins, tmo)) __builtin_amdgcn_s_sleep(16); MEMB(); } while (0)
  unsigned kmo = (unsigned)(lq * 128 + ((l15 & 1) + (l15 & 2) * 2 + ((l15 >> 1) & 2)) * 16), tlast = 0; PINV(kmo);
  if (par == 0) light(rs_km, lane == 0 ? 0u : 0x80000000u, MSLOT(0), tmo, 1); else TV_WAIT(1);
  QK_LOAD(par);
#pragma unroll 1
  for (int s = par; s < 64; s += 2) {
    f32x4 sc[2][3];
    float mx[2];
    {
#pragma unroll
      for (int q = 0; q < 2; ++q) {
        mx[q] = -3.0e38f;
#pragma unroll
        for (int t = 0; t <= q; ++t) {
          sc[q][t] = ZERO4;
#pragma unroll
          for (int kk = 0; kk < 6; ++kk) sc[q][t] = mfma(kf[t][kk], qf[q][kk], sc[q][t]);
#pragma unroll
          for (int r = 0; r < 4; ++r) {
            const float v = rbf(rbf(sc[q][t][r]) * scale);
            sc[q][t][r] = (t < q || 4 * lq + r <= l15) ? v : -3.0e38f;
            mx[q] = fmaxf(mx[q], sc[q][t][r]);
          }
        }
        PINV(mx[q]);
      }
    }
    SB();
    unsigned tv;
    {
      u32x4 km[6];
      unsigned spins = 0;
      const unsigned t0 = wait_rt(tlast + (unsigned)a.d[15]);
      for (;;) {
        unsigned m = ~0u;
        PINV(kmo);
        tv = __builtin_amdgcn_raw_buffer_load_b32(rs_tv, tvo, 0, 16);
#pragma unroll
        for (int kk = 0; kk < 6; ++kk) { km[kk] = ld16(rs_km, kmo + (unsigned)(kk * 512), MSLOT(s)); m = sent_min(m, km[kk]); }
        if (!__any(m == 0u) || !spin_check(spins, tmo)) break;
        if (spins > 4u) __builtin_amdgcn_s_sleep(12); else __builtin_amdgcn_s_sleep(2);
      }
      tlast = t0 - 100u;
      if (spins) { u64 t_; RT_MARK(t_); tlast = (unsigned)t_; }
#pragma unroll
      for (int q = 0; q < 2; ++q) {
        sc[q][2] = ZERO4;
#pragma unroll
        for (int kk = 0; kk < 6; ++kk) sc[q][2] = mfma(km[kk], qf[q][kk], sc[q][2]);
      }
    }
    asm volatile("" : "+v"(sc[0][2]), "+v"(sc[1][2]));
    const bool ahead = s + 2 < 64 && !__any(tv < (unsigned)(s + 2));
    if (ahead) QK_LOAD(s + 2);
#pragma unroll
    for (int q = 0; q < 2; ++q) {
#pragma unroll
      for (int r = 0; r < 4; ++r) { sc[q][2][r] = rbf(rbf(sc[q][2][r]) * scale); mx[q] = fmaxf(mx[q], sc[q][2][r]); }
      const float m = max32(max16(mx[q])), nml = -m * LOG2E;
      float sum = 0.f, sm = 0.f;
#pragma unroll
      for (int t = 0; t <= q; ++t)
#pragma unroll
        for (int r = 0; r < 4; ++r) { const float e = (t < q || 4 * lq + r <= l15) ? __builtin_amdgcn_exp2f(__builtin_fmaf(sc[q][t][r], LOG2E, nml)) : 0.f; sc[q][t][r] = e; sum += e; }
#pragma unroll
      for (int r = 0; r < 4; ++r) { sc[q][2][r] = __builtin_amdgcn_exp2f(__builtin_fmaf(sc[q][2][r], LOG2E, nml)); sm += sc[q][2][r]; }
      sum = sum32(sum16(__builtin_fmaf(sm, 0.5f, sum)));
      const float inv = 1.0f / sum;
      if (q == 0) st8(rs_pt, (unsigned)(h * 2048 + lane * 8), TSLOT(s), cvtpk(sc[0][0][0] * inv, sc[0][0][1] * inv), cvtpk(sc[0][0][2] * inv, sc[0][0][3] * inv));
      else st16(rs_pt, (unsigned)(h * 2048 + 1024 + lane * 16), TSLOT(s), (u32x4){cvtpk(sc[1][0][0] * inv, sc[1][0][1] * inv), cvtpk(sc[1][0][2] * inv, sc[1][0][3] * inv), cvtpk(sc[1][1][0] * inv, sc[1][1][1] * inv), cvtpk(sc[1][1][2] * inv, sc[1][1][3] * inv)});
      st8(rs_pt, lq < 2 ? (unsigned)(65536 + (h >> 2) * 2048 + q * 1024 + (h & 3) * 256 + l15 * 16 + lq * 8) : 0x80000000u, TSLOT(s), cvtpk(sc[q][2][0] * inv, sc[q][2][1] * inv), cvtpk(sc[q][2][2] * inv, sc[q][2][3] * inv));
    }
    if (s + NZ < 64) KM_EMPTY(s);
    VMW(0);
    if (lane < 4) __hip_atomic_fetch_add((gu32*)(cnt + CNT(C_TOK + 4 * s + lane)), 1u, RLX_AGENT);
    if (!ahead && s + 2 < 64) { TV_WAIT(s + 2); QK_LOAD(s + 2); }
  }
}

__device__ __forceinline__ float dot2bf(unsigned x, unsigned y, float c) { return __builtin_amdgcn_fdot2_f32_bf16(__builtin_bit_cast(bf16x2_t, x), __builtin_bit_cast(bf16x2_t, y), c, false); }
__device__ __forceinline__ void a_role(const Args& a, unsigned char* lds, const int j, const int wave) {
  char* ws = a.ws; asm volatile("" : "+s"(ws));
  unsigned* const cnt = (unsigned*)(ws + O_CNT); unsigned* const tmo = cnt + CNT(C_TMO);
  const int lane = lane_id(), l15 = lane & 15, lq = lane >> 4;
  const int c0 = 40 * j - 8 * (j & 1);
  u32x4 wt[3][20];
#define A_WDMA(src_, ld_, off_) do { const int ln_ = lane_id(); _Pragma("unroll") for (int q_ = 0; q_ < 15; ++q_) { const int ins = wave + 4 * q_; const unsigned c = (unsigned)(ins * 64 + ln_), r = c / 6u, pc = c - r * 6u; \
      glds(src_, r * (unsigned)((ld_) * 2) + 16u * pc, lds_addr(lds) + (unsigned)((off_) + ins * 1024)); } } while (0)
#define A_WREAD(off_) do { unsigned o_ = (unsigned)((off_) + (8 * lq + (l15 >> 2)) * 96 + 8 * (l15 & 3)); PINV(o_); \
    _Pragma("unroll") for (int t = 0; t < 3; ++t) { _Pragma("unroll") for (int kk = 0; kk < 20; ++kk) wt[t][kk] = trrd2(lds, o_ + (unsigned)(kk * 3072 + 32 * t), 384u); SB(); } } while (0)
  A_WDMA(a.Awqkv + c0, 1920, 0); A_WDMA(a.Awqkv + 640 + c0, 1920, 61440);
  VMW(0); BAR();
  if (wave < 2) A_WREAD(wave * 61440);
  BAR();
  A_WDMA(a.Awqkv + 1280 + c0, 1920, 0);
  VMW(0); BAR();
  if (wave == 2) A_WREAD(0);
  BAR();
  constexpr int LX = 0, LR = 122880, RS = 304, NR = 128;
  unsigned dvo[10];
#pragma unroll
  for (int i = 0; i < 10; ++i) { const unsigned c = (unsigned)((wave + 4 * i) * 64 + lane), r = c / 80u; dvo[i] = (c ^ (r & 15u)) * 16u; PINV(dvo[i]); }
#define A_XDMA(src_, b_) do { _Pragma("unroll") for (int xi_ = 0; xi_ < 10; ++xi_) glds(src_, dvo[xi_], lds_addr(lds) + (unsigned)(LX + ((b_) & 1) * 40960 + wave * 1024 + xi_ * 4096)); } while (0)
  unsigned xr[4];
#pragma unroll
  for (int q = 0; q < 4; ++q) xr[q] = (unsigned)(LX + l15 * 1280 + ((4 * q + lq) ^ l15) * 16);
  unsigned dv3[14];
#pragma unroll
  for (int i = 0; i < 14; ++i) { const unsigned c = (unsigned)((13 * (wave < 3 ? wave : 0) + i) * 64 + lane), r = c / 80u; dv3[i] = (c ^ (r & 15u)) * 16u + (unsigned)(4096 - (i % 7 - 3) * 1024); PINV(dv3[i]); }
  const unsigned am0 = lds_addr(lds) + (unsigned)(LX + (13 * wave + 3) * 1024);
#define A_XD3(src_, lb_, i_) do { if ((i_) % 7 == 0) m0_set(am0 + (lb_) + (unsigned)((i_) / 7 * 7168)); \
    switch ((i_) % 7) { case 0: glds_o<-3072>(src_, dv3[i_]); break; case 1: glds_o<-2048>(src_, dv3[i_]); break; case 2: glds_o<-1024>(src_, dv3[i_]); break; case 3: glds_o<0>(src_, dv3[i_]); break; \
      case 4: glds_o<1024>(src_, dv3[i_]); break; case 5: glds_o<2048>(src_, dv3[i_]); break; default: glds_o<3072>(src_, dv3[i_]); break; } } while (0)
  A_XDMA(a.Ax, 0);
  A_XDMA(a.Ax + 32 * 640, 1);
  unsigned wo_[3];
#pragma unroll
  for (int t = 0; t < 3; ++t) { const int v = 4 * t + lq - 2 * (j & 1); wo_[t] = (unsigned)(LR + l15 * RS + wave * 96 + (v < 0 ? 40 + (v + 2) * 48 : (v >= 10 ? 40 + (v - 10) * 48 : (v >= 5 ? 8 : 0) + v * 8))); }
  const __amdgpu_buffer_rsrc_t rs_oa = mkrs(ws + O_OA);
#pragma unroll 1
  for (int b = 0; b <= 64; ++b) {
    const int s = b; (void)s;
    if (b == 0) VMW(0); else if (wave < 3) VMW(14);
    BAR();
    const char* xsrc = (const char*)(a.Ax + (size_t)(32 * (b < 62 ? b + 2 : 63)) * 640) - 4096;
    const unsigned xb = (unsigned)(b % 3) * 40960u, xb2 = (unsigned)((b + 2) % 3) * 40960u;
    if (wave < 3) {
      if (b < 64) {
        unsigned xq[4];
#pragma unroll
        for (int q = 0; q < 4; ++q) { xq[q] = xr[q] + xb; PINV(xq[q]); }
#define A_XF(rt_, k_) (*(const u32x4*)(lds + xq[(k_) & 3] + (rt_) * 20480 + ((k_) >> 2) * 256))
        f32x4 acc[2][3]; u32x4 xa[3][2];
#pragma unroll
        for (int k = 0; k < 2; ++k) { xa[k][0] = A_XF(0, k); xa[k][1] = A_XF(1, k); }
#pragma unroll
        for (int k = 0; k < 20; ++k) {
          if (k + 2 < 20) { xa[(k + 2) % 3][0] = A_XF(0, k + 2); xa[(k + 2) % 3][1] = A_XF(1, k + 2); }
#pragma unroll
          for (int t = 0; t < 3; ++t) { acc[0][t] = mfma(wt[t][k], xa[k % 3][0], k == 0 ? ZERO4 : acc[0][t]); acc[1][t] = mfma(wt[t][k], xa[k % 3][1], k == 0 ? ZERO4 : acc[1][t]);
            if (t == 1 && k < 14) { asm volatile("" : "+a"(acc[0][0]), "+a"(acc[0][1]), "+a"(acc[1][0]), "+a"(acc[1][1])); A_XD3(xsrc, xb2, k < 14 ? k : 0); SB(); } }
          if (k >= 14) IL(6, 1);
          asm volatile("" : "+a"(acc[0][0]), "+a"(acc[0][1]), "+a"(acc[0][2]), "+a"(acc[1][0]), "+a"(acc[1][1]), "+a"(acc[1][2])); SB();
        }
        const unsigned ro = (unsigned)(((32 * b) & (NR - 1)) * RS);
#pragma unroll
        for (int t = 0; t < 3; ++t)
#pragma unroll
          for (int rt = 0; rt < 2; ++rt) *(u32x2*)(lds + wo_[t] + ro + rt * 16 * RS) = (u32x2){cvtpk(acc[rt][t][0], acc[rt][t][1]), cvtpk(acc[rt][t][2], acc[rt][t][3])};
      }
    } else {
      const int row = 32 * (b > 0 ? b - 1 : 0) + (lane & 31), hh = lane >> 5;
      const unsigned ho = (unsigned)(LR + 48 * hh);
      unsigned ra[8];
#pragma unroll
      for (int w = 0; w < 8; ++w) { const int p = row - 2 * w; ra[w] = ho + (unsigned)(((p > 0 ? p : 0) & (NR - 1)) * RS); }
#define A_LD10(d_, ad_) do { const u32x4 x0 = *(const u32x4*)(lds + (ad_)), x1 = *(const u32x4*)(lds + (ad_) + 16); const u32x2 x2 = *(const u32x2*)(lds + (ad_) + 32); \
        d_[0] = x0.x; d_[1] = x0.y; d_[2] = x0.z; d_[3] = x0.w; d_[4] = x1.x; d_[5] = x1.y; d_[6] = x1.z; d_[7] = x1.w; d_[8] = x2.x; d_[9] = x2.y; } while (0)
      unsigned q[10], kv[8][10], vv[8][10];
      A_LD10(q, ra[0]);
#pragma unroll
      for (int w = 0; w < 8; ++w) A_LD10(kv[w], ra[w] + 96);
#pragma unroll
      for (int w = 0; w < 8; ++w) A_LD10(vv[w], ra[w] + 192);
      SB();
      float lg[8], mx = -3.0e38f;
#pragma unroll
      for (int w = 0; w < 8; ++w) lg[w] = 0.f;
#pragma unroll
      for (int i = 0; i < 10; ++i)
#pragma unroll
        for (int w = 0; w < 8; ++w) lg[w] = dot2bf(q[i], kv[w][i], lg[w]);
#pragma unroll
      for (int w = 0; w < 8; ++w) { lg[w] = row - 2 * w >= 0 ? rbf(rbf(lg[w]) * 0.22360679774997896f) : rbf(-1000000000.0f); mx = fmaxf(mx, lg[w]); }
      float sum = 0.f;
#pragma unroll
      for (int w = 0; w < 8; ++w) { lg[w] = __expf(lg[w] - mx); sum += lg[w]; }
      const float inv = __builtin_amdgcn_rcpf(sum);
      float ov[20];
#pragma unroll
      for (int d = 0; d < 20; ++d) ov[d] = 0.f;
#pragma unroll
      for (int w = 0; w < 8; w += 2) {
        const unsigned pw = cvtpk(lg[w] * inv, lg[w + 1] * inv);
#pragma unroll
        for (int i = 0; i < 10; ++i) {
          ov[2 * i] = dot2bf(__builtin_amdgcn_perm(vv[w + 1][i], vv[w][i], 0x05040100u), pw, ov[2 * i]);
          ov[2 * i + 1] = dot2bf(__builtin_amdgcn_perm(vv[w + 1][i], vv[w][i], 0x07060302u), pw, ov[2 * i + 1]);
        }
      }
      const unsigned oo = b > 0 ? (unsigned)((row * 640 + 40 * j + 20 * hh) * 2) : 0x80000000u;
#pragma unroll
      for (int i = 0; i < 5; ++i) st8(rs_oa, oo + 8u * i, 0u, cvtpk(ov[4 * i], ov[4 * i + 1]), cvtpk(ov[4 * i + 2], ov[4 * i + 3]));
      if (b > 0 && (b & 15) == 0) { VMW(0); if (lane == 0) __hip_atomic_fetch_add((gu32*)(cnt + CNT(C_AO + (b >> 4) - 1)), 1u, RLX_AGENT); }
    }
  }
  VMW(0);
}
__device__ __forceinline__ void a2_role(const Args& a, unsigned char* lds, const int j, const int wave) {
  char* ws = a.ws; asm volatile("" : "+s"(ws));
  unsigned* const cnt = (unsigned*)(ws + O_CNT); unsigned* const tmo = cnt + CNT(C_TMO);
  VMW(0);
  BAR();
  {
    const int ln = lane_id(), m15 = ln & 15, mq = ln >> 4, cg = j & 3, rg = j >> 2;
    WReg W;
    {
      unsigned wv[10];
#pragma unroll
      for (int q = 0; q < 10; ++q) { const unsigned c = (unsigned)((wave + 4 * q) * 64 + ln), r = c / 20u; wv[q] = r * 1280u + (c - r * 20u) * 16u; PINV(wv[q]); }
#define A2_WDMA(i_) do { _Pragma("unroll") for (int q_ = 0; q_ < 10; ++q_) glds(a.Awo + (size_t)(128 * (i_)) * 640 + 160 * cg, wv[q_], lds_addr(lds) + (unsigned)(((i_) & 3) * 40960 + (wave + 4 * q_) * 1024)); } while (0)
      A2_WDMA(0); A2_WDMA(1); A2_WDMA(2); A2_WDMA(3);
      const unsigned rb = lds_addr(lds) + (unsigned)((8 * mq + (m15 >> 2)) * 320);
      const unsigned ad[3] = {rb + (unsigned)(64 * wave + 16 * (m15 & 3)), rb + (unsigned)(64 * wave + 16 * (m15 & 3) + 8), rb + (unsigned)(256 + 32 * (wave >> 1) + 8 * (m15 & 3))};
      SFOR(i, 5)
        if (i < 2) VMW(30); else if (i == 2) VMW(20); else if (i == 3) VMW(10); else VMW(0);
        BAR();
        SFOR(n, 6) tr4_w(ad[n >> 1] + (unsigned)((i & 3) * 40960), (n & 1) * 20480, (n & 1) * 20480 + 1280, (n & 1) * 20480 + 10240, (n & 1) * 20480 + 11520, 6 * i + n, W); SEND;
        if (i == 0) { BAR(); A2_WDMA(4); }
      SEND;
    }
    unsigned dv[10], xq[4], x5[4];
#pragma unroll
    for (int i = 0; i < 10; ++i) { const unsigned c = (unsigned)((10 * wave + i) * 64 + ln), r = c / 80u; dv[i] = (c ^ (r & 15u)) * 16u + (unsigned)(4096 - (i % 5 - 2) * 1024); PINV(dv[i]); }
    const char* src = ws + O_OA + (size_t)(128 * rg) * 1280 - 4096;
    const unsigned dm = lds_addr(lds) + (unsigned)((10 * wave + 2) * 1024);
#define A2_X(p_, lb_, i_) do { if ((i_) % 5 == 0) m0_set(dm + (lb_) + (unsigned)((i_) / 5 * 5120)); \
      switch ((i_) % 5) { case 0: glds_x<-2048>(p_, dv[i_]); break; case 1: glds_x<-1024>(p_, dv[i_]); break; case 2: glds_x<0>(p_, dv[i_]); break; case 3: glds_x<1024>(p_, dv[i_]); break; default: glds_x<2048>(p_, dv[i_]); break; } } while (0)
    const __amdgpu_buffer_rsrc_t rs_out = mkrs(a.out + (size_t)(128 * rg) * 1280 + 160 * cg);
    unsigned o2 = (unsigned)((m15 * 1280 + 32 * wave + 8 * mq) * 2), o5 = (unsigned)(((16 * (wave & 1) + m15) * 1280 + 128 + 16 * (wave >> 1) + 4 * mq) * 2); PINV(o2); PINV(o5);
    if (wave == 0) wait_ge<16>(cnt + CNT(C_AO + (rg >> 2)), 16u, tmo);
    BAR();
#pragma unroll
    for (int b = 0; b < 4; ++b) { const char* p_ = src + b * 40960; asm volatile("s_nop 4" : "+s"(p_));
#pragma unroll
      for (int i = 0; i < 10; ++i) A2_X(p_, (unsigned)(b * 40960), i); }
#pragma unroll 1
    for (int b = 0; b < 4; ++b) {
      if (b == 0) VMW(30); else if (b == 1) VMW(23); else if (b == 2) VMW(16); else VMW(9);
      BAR();
#pragma unroll
      for (int q = 0; q < 4; ++q) { xq[q] = lds_addr(lds) + (unsigned)((b & 3) * 40960 + m15 * 1280 + ((4 * q + mq) ^ m15) * 16); x5[q] = xq[q] + (unsigned)((wave & 1) * 20480); PINV(xq[q]); PINV(x5[q]); }
      f32x4 acc[5]; u32x4 xa[3][3];
#define A2_XF(n_, k_) LDS16(((n_) == 2 ? x5[(k_) & 3] : xq[(k_) & 3] + (n_) * 20480) + ((k_) >> 2) * 256)
#pragma unroll
      for (int n = 0; n < 6; ++n) xa[n / 3][n % 3] = A2_XF(n % 3, n / 3);
      SFOR(k, 20)
        SFOR(n, 5)
          mf_w<true>(acc[n], xa[k % 3][n >> 1], 12 * (k >> 2) + 4 * (n == 4 ? 2 : (n & 1)) + (k & 3), k == 0, W); SB();
          if (n < 3 && k + 2 < 20) { xa[(k + 2) % 3][n] = A2_XF(n, k + 2); SB(); }
        SEND;
      SEND;
      asm volatile("s_nop 7" : "+v"(acc[0]), "+v"(acc[1]), "+v"(acc[2]), "+v"(acc[3]), "+v"(acc[4]));
#pragma unroll
      for (int rt = 0; rt < 2; ++rt) { const u32x4 w = {cvtpk(acc[2 * rt][0], acc[2 * rt][1]), cvtpk(acc[2 * rt][2], acc[2 * rt][3]), cvtpk(acc[2 * rt + 1][0], acc[2 * rt + 1][1]), cvtpk(acc[2 * rt + 1][2], acc[2 * rt + 1][3])};
        __builtin_amdgcn_raw_buffer_store_b128(w, rs_out, (int)o2, (32 * b + 16 * rt) * 2560, OAUX); asm volatile("s_nop 1" :: "v"(w) : "memory"); }
      __builtin_amdgcn_raw_buffer_store_b64((u32x2){cvtpk(acc[4][0], acc[4][1]), cvtpk(acc[4][2], acc[4][3])}, rs_out, (int)o5, 32 * b * 2560, OAUX);
    }
    VMW(0);
  }
}

#define CALN 6
__device__ __forceinline__ void calibrate(char* ws, int* vote, const int wave) {
  char* cal = ws + O_CAL + wave * 16384;
  unsigned best0 = ~0u, best1 = ~0u, acc = 0;
  const int lane = lane_id();
  for (int r = 0; r < CALN; ++r) {
#pragma unroll
    for (int hf = 0; hf < 2; ++hf) {
      const unsigned long long t0 = __builtin_amdgcn_s_memtime();
      if (lane == 0) acc += __hip_atomic_fetch_add((gu32*)(cal + hf * 8192 + (acc & 0u)), 1u, RLX_AGENT);
      asm volatile("s_waitcnt vmcnt(0)" ::: "memory");
      const unsigned d = (unsigned)(__builtin_amdgcn_s_memtime() - t0);
      if (hf == 0) best0 = d < best0 ? d : best0; else best1 = d < best1 ? d : best1;
    }
  }
  int dl = (int)best1 - (int)best0 + (int)(acc & 0u);
  dl = dl > 400 ? 400 : (dl < -400 ? -400 : dl);
  if ((((unsigned)((size_t)cal >> 21) ^ xcc_side()) & 1u) != 0u) dl = -dl;
  if (lane == 0) vote[wave] = dl;
  __syncthreads();
  if (wave == 0 && lane == 0) __hip_atomic_store((gu32*)((unsigned*)(ws + O_CNT) + CNT(C_POL)), vote[0] + vote[1] + vote[2] + vote[3] < 0 ? 3u : 2u, RLX_AGENT);
  __syncthreads();
}

#define TCH 32
#define XOFF 0
#define CPF 3
__global__ __launch_bounds__(256, 1) void k_mega(Args a) {
  extern __shared__ __attribute__((aligned(16))) unsigned char shm[];
  volatile LAS int* sl = (volatile LAS int*)shm;
  if (threadIdx.x >= 64) {
    u64 pc; unsigned off, len;
    asm volatile("s_getpc_b64 %0\n.Lcpf_mega:\n s_mov_b32 %1, .Lcpf_mega-_Z6k_mega4Args\n s_mov_b32 %2, _Z6k_prepPcPKcS1_S1_S1_S1_-_Z6k_mega4Args" : "=s"(pc), "=s"(off), "=s"(len));
    const __amdgpu_buffer_rsrc_t rs = __builtin_amdgcn_make_buffer_rsrc((void*)(pc - off), 0, (int)len, 0x00020000);
    const unsigned g = ((blockIdx.x >> 3) * 192u + threadIdx.x - 64u) * 16u;
    const u32x4 v0 = __builtin_amdgcn_raw_buffer_load_b128(rs, (int)g, 0, 0), v1 = __builtin_amdgcn_raw_buffer_load_b128(rs, (int)g, 98304, 0);
    asm volatile("" :: "v"(v0), "v"(v1));
  }
  if (threadIdx.x >= 64) {
    const unsigned o = (threadIdx.x - 64u) * (unsigned)(TCH * 65536) + (blockIdx.x >> 3) * 2048u;
    const unsigned v = __builtin_amdgcn_raw_buffer_load_b32(__builtin_amdgcn_make_buffer_rsrc((void*)(a.ws + O_X1), 0, (int)(O_END - O_X1), 0x00020000), (int)o, 0, 0);
    asm volatile("" :: "v"(v));
  }
  if (threadIdx.x == 0) {
    const unsigned x0 = (unsigned)__builtin_amdgcn_s_getreg((3 << 11) | 20) & 7u;
    int slot = 0;
    for (unsigned i = 0; i < 8u; ++i) {
      const unsigned x = (x0 + i) & 7u;
      const unsigned t = __hip_atomic_fetch_add((gu32*)((unsigned*)(a.ws + O_CNT) + CNT(C_SLOT + x)), 1u, RLX_AGENT);
      if (t < 32u) { slot = (int)(x * 32u + t); break; }
    }
    *sl = slot;
  }
  __syncthreads();
  const int slot = __builtin_amdgcn_readfirstlane(*sl);
  __syncthreads();
  const int xq = (slot >> 5) & 3, kq = slot & 31, wave = __builtin_amdgcn_readfirstlane(threadIdx.x >> 6);
#define ENTER __builtin_amdgcn_s_waitcnt(0)
  const int h = xq * 8 + (kq & 7);
  if (slot < 128) {
    if (kq < 8) { ENTER; s_role(a, shm, h, wave);
    } else if (kq < 16) { ENTER; z_role(a, shm, h, wave); }
    else if (kq < 26) { ENTER; r_role(a, shm, xq * 10 + kq - 16, wave); }
    else { if (slot == 26) calibrate(a.ws, (int*)shm, wave); ENTER; if (!(XOFF & 1)) o_role(a, shm, 6 * xq + kq - 26, wave); }
  } else {
    if (kq < 16) { ENTER; t_role(a, shm, h, kq >> 3, wave); ENTER; if (!(XOFF & 4)) a2_role(a, shm, 16 * xq + kq, wave); }
    else if (kq < 24) { ENTER; w_role(a, shm, h, wave); }
    else if (kq < 28) { ENTER; if (!(XOFF & 4)) a_role(a, shm, xq * 4 + kq - 24, wave); }
    else { ENTER; if (!(XOFF & 2)) att_role(a, shm, xq * 8 + (kq - 28) * 2 + (wave & 1), wave); }
  }
}

#define PFW 3
#define PFN 15
constexpr int PB0 = (PFW & 1) ? 1920 / PFN : 0, PB1 = (PFW & 2) ? 1920 / PFN : 0, PB2 = (PFW & 4) ? 1920 / PFN : 0, PB3 = (PFW & 8) ? 1920 / PFN : 0, PF_B = PB0 + PB1 + PB2 + PB3;
__global__ __launch_bounds__(256) void k_prep(char* ws, const char* w0, const char* w1, const char* w2, const char* w3, const char* w4) {
  if (blockIdx.x < 15) { ((u32x4*)(ws + O_CNT))[blockIdx.x * 256 + threadIdx.x] = (u32x4){0u, 0u, 0u, 0u}; return; }
  if (blockIdx.x < 15 + PF_B) {
    const unsigned bi = blockIdx.x - 15;
    const char* w = bi < PB0 ? w0 : (bi < PB0 + PB1 ? w1 : (bi < PB0 + PB1 + PB2 ? w2 : w3));
    const unsigned bb = bi < PB0 ? bi : (bi < PB0 + PB1 ? bi - PB0 : (bi < PB0 + PB1 + PB2 ? bi - PB0 - PB1 : bi - PB0 - PB1 - PB2));
    const unsigned g = bb * (PFN * 4096u) + threadIdx.x * 16u;
    const __amdgpu_buffer_rsrc_t rs = __builtin_amdgcn_make_buffer_rsrc((void*)w, 0, 1920 * 4096, 0x00020000);
    u32x4 v[PFN];
#pragma unroll
    for (int i = 0; i < PFN; ++i) v[i] = __builtin_amdgcn_raw_buffer_load_b128(rs, (int)g, i * 4096, 0);
    __builtin_amdgcn_sched_barrier(0);
    u32x4 s = v[0];
#pragma unroll
    for (int i = 1; i < PFN; ++i) s ^= v[i];
    asm volatile("" :: "v"(s));
    return;
  }
  {
    u64 pc; unsigned off, len;
    asm volatile("s_getpc_b64 %0\n.Lcpf_prep:\n s_mov_b32 %1, .Lcpf_prep-_Z6k_mega4Args\n s_mov_b32 %2, _Z6k_prepPcPKcS1_S1_S1_S1_-_Z6k_mega4Args" : "=s"(pc), "=s"(off), "=s"(len));
    const unsigned g = min(((blockIdx.x - 15 - PF_B) * 256u + threadIdx.x) * 16u, len - 16u);
    u32x4 v; asm volatile("global_load_dwordx4 %0, %1, %2" : "=v"(v) : "v"(g), "s"(pc - off));
  }
}
extern "C" void kernel_launch(void* const* d_in, const int* in_sizes, int n_in, void* d_out, int out_size, void* d_ws, size_t ws_size, hipStream_t stream) {
  Args a;
  a.Ax = (const u16*)d_in[0]; a.Awqkv = (const u16*)d_in[1]; a.Awo = (const u16*)d_in[2]; a.Bx = (const u16*)d_in[3]; a.mem0 = (const u16*)d_in[4];
  a.Wq = (const u16*)d_in[5]; a.Wk = (const u16*)d_in[6]; a.Wv = (const u16*)d_in[7]; a.Wo = (const u16*)d_in[8];
  a.out = (u16*)d_out;
  a.ws = (char*)(((size_t)d_ws + 2 * MB - 1) & ~(size_t)(2 * MB - 1));
  if ((size_t)(a.ws - (char*)d_ws) + O_END > ws_size) return;
  const int dd[16] = {DDV};
  for (int i = 0; i < 16; ++i) a.d[i] = dd[i];
  static_assert(CNT(C_TOTAL) * 4 == 15 * 4096, "");
  k_prep<<<15 + PF_B + ((CPF & 2) ? 48 : 0), 256, 0, stream>>>(a.ws, (const char*)a.Wq, (const char*)a.Wk, (const char*)a.Wv, (const char*)a.Wo, (const char*)a.Bx);
  (void)hipFuncSetAttribute((const void*)k_mega, hipFuncAttributeMaxDynamicSharedMemorySize, 163840);
  k_mega<<<256, 256, 163840, stream>>>(a);
}
```

```cpp
#include <hip/hip_runtime.h>
#include <hip/hip_bf16.h>
#include <stdint.h>

typedef unsigned short u16;
typedef unsigned long long u64;
typedef __attribute__((ext_vector_type(4))) float f32x4;
typedef __attribute__((address_space(1))) u64 gu64;
typedef __attribute__((address_space(1))) unsigned gu32;
typedef unsigned u32x8 __attribute__((ext_vector_type(8)));
typedef unsigned u32x4 __attribute__((ext_vector_type(4)));
typedef unsigned u32x2 __attribute__((ext_vector_type(2)));
typedef __attribute__((ext_vector_type(4))) short bf16x4;
typedef __attribute__((ext_vector_type(8))) __bf16 bfv8;
typedef __attribute__((ext_vector_type(16))) __bf16 bfv16;
typedef __bf16 bf16x2_t __attribute__((ext_vector_type(2)));
typedef float f32x2_t __attribute__((ext_vector_type(2)));
typedef unsigned v2u_t __attribute__((ext_vector_type(2)));
#define RLX_AGENT __ATOMIC_RELAXED, __HIP_MEMORY_SCOPE_AGENT
#define LAS __attribute__((address_space(3)))

__device__ __forceinline__ float bf2f(u16 v) { return __uint_as_float(((unsigned)v) << 16); }
__device__ __forceinline__ u16 f2bf(float f) { return __builtin_bit_cast(u16, (__bf16)f); }
__device__ __forceinline__ float rbf(float f) { return bf2f(f2bf(f)); }
__device__ __forceinline__ unsigned cvtpk(float lo, float hi) { f32x2_t v = {lo, hi}; return __builtin_bit_cast(unsigned, __builtin_convertvector(v, bf16x2_t)); }
__device__ __forceinline__ __amdgpu_buffer_rsrc_t mkrs(const void* p) { return __builtin_amdgcn_make_buffer_rsrc((void*)p, 0, 0x7fffffff, 0x00020000); }
__device__ __forceinline__ u32x4 ld16(__amdgpu_buffer_rsrc_t rs, unsigned voff, unsigned soff) { return __builtin_amdgcn_raw_buffer_load_b128(rs, (int)voff, (int)soff, 16); }
#define OLA 0
__device__ __forceinline__ u32x4 ld16c(__amdgpu_buffer_rsrc_t rs, unsigned voff, unsigned soff) { return __builtin_amdgcn_raw_buffer_load_b128(rs, (int)voff, (int)soff, OLA); }
__device__ __forceinline__ u32x2 ld8(__amdgpu_buffer_rsrc_t rs, unsigned voff, unsigned soff) { return __builtin_amdgcn_raw_buffer_load_b64(rs, (int)voff, (int)soff, 16); }
__device__ __forceinline__ void st16(__amdgpu_buffer_rsrc_t rs, unsigned voff, unsigned soff, u32x4 w) { __builtin_amdgcn_raw_buffer_store_b128(w, rs, (int)voff, (int)soff, 16); asm volatile("s_nop 1" :: "v"(w) : "memory"); }
__device__ __forceinline__ void st8(__amdgpu_buffer_rsrc_t rs, unsigned voff, unsigned soff, unsigned lo, unsigned hi) { u32x2 w = {lo, hi}; __builtin_amdgcn_raw_buffer_store_b64(w, rs, (int)voff, (int)soff, 16); }
__device__ __forceinline__ bool full16(u32x4 v) { return ((v.x & v.y) != ~0u) & ((v.z & v.w) != ~0u); }
__device__ __forceinline__ float max16(float x) { v2u_t r = __builtin_amdgcn_permlane16_swap(__float_as_uint(x), __float_as_uint(x), false, false); return fmaxf(__uint_as_float(r[0]), __uint_as_float(r[1])); }
__device__ __forceinline__ float max32(float x) { v2u_t r = __builtin_amdgcn_permlane32_swap(__float_as_uint(x), __float_as_uint(x), false, false); return fmaxf(__uint_as_float(r[0]), __uint_as_float(r[1])); }
__device__ __forceinline__ float sum16(float x) { v2u_t r = __builtin_amdgcn_permlane16_swap(__float_as_uint(x), __float_as_uint(x), false, false); return __uint_as_float(r[0]) + __uint_as_float(r[1]); }
__device__ __forceinline__ float sum32(float x) { v2u_t r = __builtin_amdgcn_permlane32_swap(__float_as_uint(x), __float_as_uint(x), false, false); return __uint_as_float(r[0]) + __uint_as_float(r[1]); }
__device__ __forceinline__ float tred32(float a, float b) { v2u_t r = __builtin_amdgcn_permlane32_swap(__float_as_uint(a), __float_as_uint(b), false, false); return __uint_as_float(r[0]) + __uint_as_float(r[1]); }
__device__ __forceinline__ float tred16(float a, float b) { v2u_t r = __builtin_amdgcn_permlane16_swap(__float_as_uint(a), __float_as_uint(b), false, false); return __uint_as_float(r[0]) + __uint_as_float(r[1]); }
__device__ __forceinline__ unsigned ror8(unsigned x) { return (unsigned)__builtin_amdgcn_mov_dpp((int)x, 0x128, 0xf, 0xf, true); }
template <int N> __device__ __forceinline__ void nap() { if constexpr (N > 0) { if constexpr (N > 127) { __builtin_amdgcn_s_sleep(127); nap<N - 127>(); } else __builtin_amdgcn_s_sleep(N); } }
__device__ __forceinline__ unsigned half_of(const char* base, unsigned off, unsigned side, unsigned pol) {
  return off + ((((unsigned)(((size_t)base + off) >> 21) ^ side ^ pol) & 1u) << 13);
}
__device__ __forceinline__ unsigned side_map(const void* base, unsigned L, unsigned side, unsigned pol) { return half_of((const char*)base, (L >> 13) << 14, side, pol) + (L & 8191u); }
constexpr unsigned CH_SPIN_CAP = 1u << 18;
__device__ __forceinline__ bool spin_check(unsigned& spins, unsigned* tmop) {
  asm volatile("" ::: "memory");
  ++spins;
  if ((spins & 63u) == 0u) {
    gu32* tmo = (gu32*)tmop;
    if (__builtin_amdgcn_readfirstlane(__hip_atomic_load(tmo, RLX_AGENT))) return false;
    if (spins > CH_SPIN_CAP) { __hip_atomic_store(tmo, 1u, RLX_AGENT); return false; }
  }
  return true;
}
__device__ __forceinline__ unsigned xcc_side() { return ((unsigned)__builtin_amdgcn_s_getreg((3 << 11) | 20) >> 2) & 1u; }
__device__ __forceinline__ f32x4 mfma(u32x4 a, u32x4 b, f32x4 c) { return __builtin_amdgcn_mfma_f32_16x16x32_bf16(__builtin_bit_cast(bfv8, a), __builtin_bit_cast(bfv8, b), c, 0, 0, 0); }
__device__ __forceinline__ f32x4 smf(u32x4 a, u32x8 b, f32x4 c, int ix) { return __builtin_amdgcn_smfmac_f32_16x16x64_bf16(__builtin_bit_cast(bfv8, a), __builtin_bit_cast(bfv16, b), c, ix, 0, 0); }
__device__ __forceinline__ u32x4 lo4(u32x8 v) { return __builtin_shufflevector(v, v, 0, 1, 2, 3); }
__device__ __forceinline__ u32x4 hi4(u32x8 v) { return __builtin_shufflevector(v, v, 4, 5, 6, 7); }
__device__ __forceinline__ u32x8 cat8(u32x4 a, u32x4 b) { return __builtin_shufflevector(a, b, 0, 1, 2, 3, 4, 5, 6, 7); }
#define BAR() do { asm volatile("s_waitcnt lgkmcnt(0)" ::: "memory"); __builtin_amdgcn_s_barrier(); asm volatile("" ::: "memory"); } while (0)
#define SB() __builtin_amdgcn_sched_barrier(0)
#define VMW(n) asm volatile("s_waitcnt vmcnt(" #n ")" ::: "memory")
#define ZERO4 ((f32x4){0.f, 0.f, 0.f, 0.f})
__device__ __forceinline__ u32x2 trrd(const unsigned char* lds, unsigned off) { return __builtin_bit_cast(u32x2, __builtin_amdgcn_ds_read_tr16_b64_v4i16((LAS bf16x4*)(lds + off))); }
__device__ __forceinline__ u32x4 trrd2(const unsigned char* lds, unsigned off, unsigned d) { const u32x2 lo = trrd(lds, off), hi = trrd(lds, off + d); return (u32x4){lo.x, lo.y, hi.x, hi.y}; }


#include <stdlib.h>
#define SAG 16
#define DDV 84,84,10,0,30,0,127,99,105,115,25,30,20,12,0,330
#define XLN 4
#define XLS 2
struct Args {
  const u16* mem0; const u16* Wq; const u16* Wk; const u16* Wv; const u16* Wo; const u16* Bx; const u16* Ax; const u16* Awqkv; const u16* Awo;
  u16* out; char* ws;
  int d[16];
};
constexpr size_t MB = 1u << 20;
#define WOFF 0
#define NK 16
#define NU 16
#define NZ 16
#define NP 16
#define KSIDE 1
constexpr unsigned X1_STEP = 32768, Z_STEP = 1u << 20, K_STEP = 1u << 20;
constexpr size_t O_PROF = 0, O_CNT = (WOFF + 1) * MB, O_CAL = O_CNT + 65536, O_TS = O_CAL + 65536, O_X1 = (WOFF + 2) * MB, O_PS = O_X1 + 1 * MB, O_KM = O_PS + 1 * MB, O_PT = O_KM + 2 * MB, O_OA = O_PT + NZ * 81920,
                 O_KF = O_PT + 4 * MB, O_QF = O_KF + NK * (size_t)K_STEP, O_VF = O_QF + NK * (size_t)K_STEP, O_ZB = KSIDE == 1 ? O_KF : O_VF + NK * MB, O_UO = O_VF + (KSIDE == 1 ? NK : NK + NZ) * MB, O_END = O_UO + NU * 40 * (size_t)32768;
static_assert(O_ZB % (2 * MB) == 0 && NP <= 16 && NZ <= 16 && NZ <= NK && NK % 4 == 0 && O_OA + 2048 * 1280 <= O_KF, "");
#define MSLOT(s_) (((unsigned)(s_) & (NZ - 1u)) * 98304u)
#define TSLOT(s_) (((unsigned)(s_) & (NZ - 1u)) * 81920u)
__device__ __forceinline__ unsigned k_fix(const unsigned o, const unsigned key) { return o ^ ((o >> 8) & 0x2000u) ^ key; }
#define K_KEY() ((((unsigned)((size_t)(ws + O_KF) >> 21) ^ (KSIDE == 2 ? (unsigned)h : KSIDE ^ pol)) & 1u) << 13)
#define KSLOT(s_) k_fix(((unsigned)(s_) & (NK - 1u)) * K_STEP, kkey)
#define ZSLOT(s_) (((unsigned)(s_) & (NZ - 1u)) * Z_STEP)
#define XSLOT(s_) (((unsigned)(s_) & (NP - 1u)) * X1_STEP)
#define CNT(i) (32 * (i))
__device__ __forceinline__ unsigned u_off(const unsigned s, const unsigned c) { return ((s & (NU - 1u)) * 40u + c) << 15; }
constexpr int PRE = 4;
#define WGATE 11
static_assert(WGATE + 3 <= NK && WGATE <= NU && WGATE + PRE + 1 <= NZ && NP >= PRE + 4, "");
constexpr unsigned NFILL = 40 + 64;
constexpr unsigned SENT = 0xFF81FF81u;
#define SENT4 ((u32x4){SENT, SENT, SENT, SENT})
#define PSLOT(s_) (((s_) & (NP - 1)) * 65536)
constexpr int C_SLOT = 0, C_TMO = 8, C_POL = 9, C_FILL = 10, C_PO = 12, C_PT_ = 32, C_PV = 64, C_PS = 96, C_PW = 128, C_PZ = 160, C_TOK = 192, C_AO = 464, C_TOTAL = 480;
__device__ __forceinline__ float nopk(float x) { asm("" : "+v"(x)); return x; }
__device__ __forceinline__ float fmx(float a, float b) { float big = __builtin_inff(); asm("" : "+s"(big)); return __builtin_amdgcn_fmed3f(a, b, big); }
__device__ __forceinline__ float mx16(float x) { v2u_t r = __builtin_amdgcn_permlane16_swap(__float_as_uint(x), __float_as_uint(x), false, false); return fmx(__uint_as_float(r[0]), __uint_as_float(r[1])); }
__device__ __forceinline__ float mx32(float x) { v2u_t r = __builtin_amdgcn_permlane32_swap(__float_as_uint(x), __float_as_uint(x), false, false); return fmx(__uint_as_float(r[0]), __uint_as_float(r[1])); }
__device__ __forceinline__ int lane_id() { int l; asm volatile("v_mbcnt_lo_u32_b32 %0, -1, 0\n v_mbcnt_hi_u32_b32 %0, -1, %0" : "=v"(l)); return l; }
__device__ __forceinline__ unsigned sent_min(unsigned m, unsigned x, unsigned y) { unsigned r = min(min(m, x ^ SENT), y ^ SENT); asm("" : "+v"(r)); return r; }
__device__ __forceinline__ unsigned sent_min(unsigned m, u32x4 v) { return sent_min(sent_min(m, v.x, v.y), v.z, v.w); }
#define RT_MARK(t_) asm volatile("s_memrealtime %0\n s_waitcnt lgkmcnt(0)" : "=s"(t_) :: "memory")
__device__ __forceinline__ unsigned wait_rt(unsigned target) {
  unsigned t;
  target = (unsigned)__builtin_amdgcn_readfirstlane(target);
  asm volatile("s_branch .Lwb%=\n.Lwa%=:\n s_sleep 1\n.Lwb%=:\n s_memrealtime vcc\n s_waitcnt lgkmcnt(0)\n s_sub_i32 %0, %1, vcc_lo\n s_add_i32 vcc_hi, %0, -9\n s_cmp_lt_u32 vcc_hi, 392\n s_cbranch_scc1 .Lwa%=\n"
               " s_add_i32 %0, %0, -1\n s_cmp_lt_u32 %0, 8\n s_cbranch_scc1 .Lwb%=\n s_mov_b32 %0, vcc_lo" : "=&s"(t) : "s"(target) : "vcc", "scc", "memory");
  return t;
}
__device__ __forceinline__ unsigned ts_max(unsigned v) {
  if ((unsigned)__ballot(v == ~0u)) return ~0u;
  const unsigned base = (unsigned)__builtin_amdgcn_readfirstlane(v) - 4096u;
  unsigned d = v == ~0u ? 0u : v - base;
  d = max(d, (unsigned)__builtin_amdgcn_mov_dpp((int)d, 0x121, 0xf, 0xf, true)); d = max(d, (unsigned)__builtin_amdgcn_mov_dpp((int)d, 0x122, 0xf, 0xf, true));
  d = max(d, (unsigned)__builtin_amdgcn_mov_dpp((int)d, 0x124, 0xf, 0xf, true)); d = max(d, (unsigned)__builtin_amdgcn_mov_dpp((int)d, 0x128, 0xf, 0xf, true));
  return (base + max(max((unsigned)__builtin_amdgcn_readlane(d, 0), (unsigned)__builtin_amdgcn_readlane(d, 16)), max((unsigned)__builtin_amdgcn_readlane(d, 32), (unsigned)__builtin_amdgcn_readlane(d, 48)))) & ~1u;
}
#define TS_OFF(l_) (((l_) & 31) * 256 + ((l_) >> 5) * 16384)
#define TS_STEP(s_) (((s_) & 1) * 128 + ((s_) >> 1) * 4)
__device__ __forceinline__ unsigned ts_settle(const __amdgpu_buffer_rsrc_t rs_ts, const int tss, unsigned t, const int lead) {
  for (int n = 0; n < 48; ++n) {
    unsigned now;
    if (t == ~0u) { __builtin_amdgcn_s_sleep(4); u64 t_; RT_MARK(t_); now = (unsigned)t_; } else now = wait_rt(t - (unsigned)lead);
    t = ts_max(__builtin_amdgcn_raw_buffer_load_b32(rs_ts, TS_OFF(lane_id()), tss, 16));
    if (t != ~0u && (int)(t - now) <= lead + 4) break;
  }
  return t;
}
#define ADAPT(d_, failed_) do { if (s > 3) d_ += (failed_) ? a.d[5] : -(int)((s & a.d[6]) == 0); } while (0)
__device__ __forceinline__ void nap_rt(int n) { for (int i = 0; i < n; ++i) __builtin_amdgcn_s_sleep(1); }
#define PINV(x) asm volatile("" : "+v"(x))
#define PINS(x) asm volatile("" : "+s"(x))
#define PINU(x) asm volatile("" :: "v"(x))
#define IL(nm, nv) do { _Pragma("unroll") for (int il_ = 0; il_ < (nm); ++il_) { __builtin_amdgcn_sched_group_barrier(0x008, 1, 0); __builtin_amdgcn_sched_group_barrier(0x7f6, nv, 0); } } while (0)
constexpr int L_MEM = 0, L_QI = 12288;
constexpr int L_B0 = 16384, L_B1 = 65536, L_B2 = 114688;
constexpr int L_QA = L_B1, L_XV = L_B1 + 9216, L_ST = L_B1 + 10240, L_FLG = L_B1 + 10752, L_XR = L_B1 + 12288;

#define TR8_ASM "ds_read_b64_tr_b16 %0, %8\n ds_read_b64_tr_b16 %1, %8 offset:1536\n ds_read_b64_tr_b16 %2, %8 offset:12288\n ds_read_b64_tr_b16 %3, %8 offset:13824\n" \
               "ds_read_b64_tr_b16 %4, %8 offset:24576\n ds_read_b64_tr_b16 %5, %8 offset:26112\n ds_read_b64_tr_b16 %6, %8 offset:36864\n ds_read_b64_tr_b16 %7, %8 offset:38400\n s_waitcnt lgkmcnt(0)"
__device__ __forceinline__ void tr8(const bool agpr, const unsigned ad, u32x8& f0, u32x8& f1) {
  u32x2 a, b, c, d, e, f, g, h;
  if (agpr) asm volatile(TR8_ASM : "=&a"(a), "=&a"(b), "=&a"(c), "=&a"(d), "=&a"(e), "=&a"(f), "=&a"(g), "=&a"(h) : "v"(ad) : "memory");
  else asm volatile(TR8_ASM : "=&v"(a), "=&v"(b), "=&v"(c), "=&v"(d), "=&v"(e), "=&v"(f), "=&v"(g), "=&v"(h) : "v"(ad) : "memory");
  f0 = (u32x8){a.x, a.y, b.x, b.y, c.x, c.y, d.x, d.y}; f1 = (u32x8){e.x, e.y, f.x, f.y, g.x, g.y, h.x, h.y};
}
__device__ __forceinline__ void tr8d(const bool agpr, const unsigned ad, u32x4& f0, u32x4& f1, u32x4& f2, u32x4& f3) {
  u32x2 a, b, c, d, e, f, g, h;
  if (agpr) asm volatile(TR8_ASM : "=&a"(a), "=&a"(b), "=&a"(c), "=&a"(d), "=&a"(e), "=&a"(f), "=&a"(g), "=&a"(h) : "v"(ad) : "memory");
  else asm volatile(TR8_ASM : "=&v"(a), "=&v"(b), "=&v"(c), "=&v"(d), "=&v"(e), "=&v"(f), "=&v"(g), "=&v"(h) : "v"(ad) : "memory");
  f0 = (u32x4){a.x, a.y, b.x, b.y}; f1 = (u32x4){c.x, c.y, d.x, d.y}; f2 = (u32x4){e.x, e.y, f.x, f.y}; f3 = (u32x4){g.x, g.y, h.x, h.y};
}
#define TR4_ASM "ds_read_b64_tr_b16 %0, %4 offset:%5\n ds_read_b64_tr_b16 %1, %4 offset:%6\n ds_read_b64_tr_b16 %2, %4 offset:%7\n ds_read_b64_tr_b16 %3, %4 offset:%8\n s_waitcnt lgkmcnt(0)"
template <int J> __device__ __forceinline__ void tr4(const bool agpr, const unsigned ad, u32x8& f) {
  u32x2 a, b, c, d;
  if (agpr) asm volatile(TR4_ASM : "=&a"(a), "=&a"(b), "=&a"(c), "=&a"(d) : "v"(ad), "n"(16384 * J), "n"(16384 * J + 4096), "n"(16384 * J + 8192), "n"(16384 * J + 12288) : "memory");
  else asm volatile(TR4_ASM : "=&v"(a), "=&v"(b), "=&v"(c), "=&v"(d) : "v"(ad), "n"(16384 * J), "n"(16384 * J + 4096), "n"(16384 * J + 8192), "n"(16384 * J + 12288) : "memory");
  f = (u32x8){a.x, a.y, b.x, b.y, c.x, c.y, d.x, d.y};
}
template <int J> __device__ __forceinline__ void tr4d(const bool agpr, const unsigned ad, u32x4& f0, u32x4& f1) {
  u32x2 a, b, c, d;
  if (agpr) asm volatile(TR4_ASM : "=&a"(a), "=&a"(b), "=&a"(c), "=&a"(d) : "v"(ad), "n"(16384 * J), "n"(16384 * J + 4096), "n"(16384 * J + 8192), "n"(16384 * J + 12288) : "memory");
  else asm volatile(TR4_ASM : "=&v"(a), "=&v"(b), "=&v"(c), "=&v"(d) : "v"(ad), "n"(16384 * J), "n"(16384 * J + 4096), "n"(16384 * J + 8192), "n"(16384 * J + 12288) : "memory");
  f0 = (u32x4){a.x, a.y, b.x, b.y}; f1 = (u32x4){c.x, c.y, d.x, d.y};
}
__device__ __forceinline__ void glds(const void* sbase, unsigned voff, unsigned ldsdst) {
  unsigned keep;
  asm volatile("s_mov_b32 %0, m0\n s_mov_b32 m0, %3\n s_nop 0\n global_load_lds_dwordx4 %1, %2\n s_mov_b32 m0, %0" : "=&s"(keep) : "v"(voff), "s"(sbase), "s"(ldsdst) : "memory");
}
__device__ __forceinline__ void m0_set(unsigned ldsdst) { asm volatile("s_mov_b32 m0, %0\n s_nop 0" :: "s"(ldsdst) : "memory"); }
template <int OFF> __device__ __forceinline__ void glds_o(const void* sbase, unsigned voff) { asm volatile("global_load_lds_dwordx4 %0, %1 offset:%2" :: "v"(voff), "s"(sbase), "n"(OFF) : "memory"); }
template <int OFF> __device__ __forceinline__ void glds_x(const void* sbase, unsigned voff) { asm volatile("global_load_lds_dwordx4 %0, %1 offset:%2 sc1" :: "v"(voff), "s"(sbase), "n"(OFF) : "memory"); }
__device__ __forceinline__ unsigned lds_addr(const unsigned char* p) { return (unsigned)(size_t)(const LAS unsigned char*)p; }
__device__ __forceinline__ unsigned ld_word(const unsigned* p) { return (unsigned)__builtin_amdgcn_readfirstlane(__hip_atomic_load((const gu32*)p, RLX_AGENT)); }
template <int SLEEP>
__device__ __forceinline__ unsigned wait_ge(const unsigned* p, unsigned target, unsigned* tmo) {
  unsigned spins = 0, v;
  while ((v = ld_word(p)) < target) { __builtin_amdgcn_s_sleep(SLEEP); if (!spin_check(spins, tmo)) break; }
  asm volatile("" ::: "memory");
  return v;
}
__device__ __forceinline__ unsigned min_t(const u32x4 w, const int kv) {
  unsigned v = min(min(w.x, w.y), w.z); if (!kv) v = min(v, w.w);
  return (unsigned)__builtin_amdgcn_readfirstlane(v);
}
template <int SLEEP>
__device__ __forceinline__ unsigned wait_t(const unsigned* p, const int first, unsigned target, unsigned* tmo) {
  unsigned spins = 0, v;
  for (;;) {
    v = min_t(ld16(mkrs(p), 0u, 0u), first);
    if (v >= target) break;
    __builtin_amdgcn_s_sleep(SLEEP); if (!spin_check(spins, tmo)) break;
  }
  asm volatile("" ::: "memory");
  return v;
}
template <int SLEEP>
__device__ __forceinline__ void wait_heads(const unsigned* p, const int second, unsigned target, unsigned* tmo) {
  unsigned spins = 0; const int ln = lane_id();
  while (!__all(__builtin_amdgcn_raw_buffer_load_b32(mkrs(p), (ln & 31) * 128 + (ln >> 5) * second * 4, 0, 16) >= target)) { __builtin_amdgcn_s_sleep(SLEEP); if (!spin_check(spins, tmo)) break; }
  asm volatile("" ::: "memory");
}
#define WNAP 12
template <int N> __device__ __forceinline__ void nap4() { if constexpr (N > 16) { asm volatile("s_nop 15"); nap4<N - 16>(); } else if constexpr (N > 0) asm volatile("s_nop %0" :: "n"(N - 1)); }
#define DPPM(d_, s_, ctl_) asm volatile("v_max_f32_dpp %0, %1, %1 " ctl_ " row_mask:0xf bank_mask:0xf" : "=v"(d_) : "v"(s_))
#define XOR1 "quad_perm:[1,0,3,2]"
#define XOR2 "quad_perm:[2,3,0,1]"
__device__ __forceinline__ void light(const __amdgpu_buffer_rsrc_t rs, unsigned vo, const unsigned so, unsigned* tmo, const int slow = 0) {
  unsigned spins = 0;
  for (;;) {
    const bool empty = __builtin_amdgcn_raw_buffer_load_b32(rs, (int)vo, (int)so, 16) == SENT;
    if (!__any(empty) || !spin_check(spins, tmo)) break;
    vo = empty ? vo : 0x80000000u;
    if (slow) __builtin_amdgcn_s_sleep(16); else __builtin_amdgcn_s_sleep(XLS);
  }
}
__device__ __forceinline__ void x1_light(const __amdgpu_buffer_rsrc_t rs_x1, const unsigned so, unsigned* tmo) {
  const unsigned ln = (unsigned)lane_id();
  light(rs_x1, ln < 40u ? (ln >> 5) * 16384u + (ln & 31u) * 256u + 252u : 0x80000000u, so, tmo);
}
__device__ __forceinline__ unsigned touch(const void* p, const int bytes, const int piece) { return __builtin_amdgcn_raw_buffer_load_b32(__builtin_amdgcn_make_buffer_rsrc((void*)p, 0, bytes, 0x00020000), lane_id() * 128, piece * 8192, 0); }
__device__ __forceinline__ unsigned touch_rows(const Args& a, const int which, const int piece) { return touch(which == 0 ? (const void*)a.Bx : (which == 1 ? (const void*)a.Ax : (const void*)(a.ws + O_OA)), 2048 * 1280, piece); }
#define TM 0
template <int N> __device__ __forceinline__ void touch_n(const Args& a, const int w, const int first, const int step) {
  unsigned v = 0;
#pragma unroll
  for (int i = 0; i < N; ++i) v ^= touch_rows(a, w % 3, first + w / 3 + step * i);
  asm volatile("" :: "v"(v));
}
#define DS(x) do { x; SB(); } while (0)
template <int V> struct IC { static constexpr int v = V; };
template <int I, int N, class F> __device__ __forceinline__ void sfor(F&& f) { if constexpr (I < N) { f(IC<I>{}); sfor<I + 1, N>(f); } }
#define SFOR(i_, n_) sfor<0, n_>([&](auto i_##_c) __attribute__((always_inline)) { constexpr int i_ = decltype(i_##_c)::v;
#define SEND })
typedef unsigned u32x32 __attribute__((ext_vector_type(32)));
typedef unsigned u32x16 __attribute__((ext_vector_type(16)));
struct WReg { u32x32 b[11]; u32x16 e; };
#define WR_SW(f_, X) switch ((f_) >> 2) { case 0: X("a", "{a[0:31]}", W.b[0]); break; case 1: X("a", "{a[32:63]}", W.b[1]); break; case 2: X("a", "{a[64:95]}", W.b[2]); break; case 3: X("a", "{a[96:127]}", W.b[3]); break; \
    case 4: X("a", "{a[128:159]}", W.b[4]); break; case 5: X("a", "{a[160:191]}", W.b[5]); break; case 6: X("a", "{a[192:223]}", W.b[6]); break; case 7: X("a", "{a[224:255]}", W.b[7]); break; \
    case 8: X("v", "{v[142:173]}", W.b[8]); break; case 9: X("v", "{v[174:205]}", W.b[9]); break; case 10: X("v", "{v[206:237]}", W.b[10]); break; default: X("v", "{v[238:253]}", W.e); break; }
__device__ __forceinline__ void smf_w(f32x4& c, const u32x4 a, const int f, const int ix, const WReg& W) {
  const int r = f < 32 ? 8 * f : 8 * f - 114;
#define X_(rf_, cs_, v_) asm volatile("v_smfmac_f32_16x16x64_bf16 %0, %1, " rf_ "[%c3:%c4], %2" : "+v"(c) : "v"(a), "v"(ix), "n"(r), "n"(r + 7), cs_(v_))
  WR_SW(f, X_);
#undef X_
}
template <bool WA> __device__ __forceinline__ void mf_w(f32x4& c, const u32x4 x, const int d, const bool first, const WReg& W) {
  const int r = d < 64 ? 4 * d : 4 * d - 114;
#define X_(rf_, cs_, v_) if (WA) { if (first) asm volatile("v_mfma_f32_16x16x32_bf16 %0, " rf_ "[%c2:%c3], %1, 0" : "=&v"(c) : "v"(x), "n"(r), "n"(r + 3), cs_(v_)); \
      else asm volatile("v_mfma_f32_16x16x32_bf16 %0, " rf_ "[%c2:%c3], %1, %0" : "+v"(c) : "v"(x), "n"(r), "n"(r + 3), cs_(v_)); } \
    else { if (first) asm volatile("v_mfma_f32_16x16x32_bf16 %0, %1, " rf_ "[%c2:%c3], 0" : "=&v"(c) : "v"(x), "n"(r), "n"(r + 3), cs_(v_)); \
      else asm volatile("v_mfma_f32_16x16x32_bf16 %0, %1, " rf_ "[%c2:%c3], %0" : "+v"(c) : "v"(x), "n"(r), "n"(r + 3), cs_(v_)); }
  WR_SW(d >> 1, X_);
#undef X_
}
__device__ __forceinline__ void ld_w(const u32x4 rs, const unsigned vo, const unsigned so, const int off, const int f, const bool def, WReg& W) {
  const int r = f < 32 ? 8 * f : 8 * f - 114;
#define Y_(md_, rf_, cs_, v_) asm volatile("s_nop 4\n buffer_load_dwordx4 " rf_ "[%c6:%c7], %1, %2, %3 offen offset:%4 sc1\n buffer_load_dwordx4 " rf_ "[%c8:%c9], %1, %2, %3 offen offset:%5 sc1" \
    : md_ cs_(v_) : "v"(vo), "s"(rs), "s"(so), "n"(off), "n"(off + 1024), "n"(r), "n"(r + 3), "n"(r + 4), "n"(r + 7) : "memory")
#define X_(rf_, cs_, v_) if (def) Y_("=&", rf_, cs_, v_); else Y_("+", rf_, cs_, v_)
  WR_SW(f, X_);
#undef X_
#undef Y_
}
__device__ __forceinline__ u32x4 mkdesc(const void* p) { const size_t b = (size_t)p; return (u32x4){(unsigned)b, (unsigned)(b >> 32) & 0xffffu, 0x7fffffffu, 0x00020000u}; }
__device__ __forceinline__ void tr4_w(const unsigned ad, const int o0, const int o1, const int o2, const int o3, const int f, WReg& W) {
  const int r = f < 32 ? 8 * f : 8 * f - 114;
#define Y_(md_, rf_, cs_, v_) asm volatile("ds_read_b64_tr_b16 " rf_ "[%c6:%c7], %1 offset:%2\n ds_read_b64_tr_b16 " rf_ "[%c8:%c9], %1 offset:%3\n ds_read_b64_tr_b16 " rf_ "[%c10:%c11], %1 offset:%4\n ds_read_b64_tr_b16 " rf_ "[%c12:%c13], %1 offset:%5\n s_waitcnt lgkmcnt(0)" \
    : md_ cs_(v_) : "v"(ad), "n"(o0), "n"(o1), "n"(o2), "n"(o3), "n"(r), "n"(r + 1), "n"(r + 2), "n"(r + 3), "n"(r + 4), "n"(r + 5), "n"(r + 6), "n"(r + 7) : "memory")
#define X_(rf_, cs_, v_) if ((f & 3) == 0) Y_("=&", rf_, cs_, v_); else Y_("+", rf_, cs_, v_)
  WR_SW(f, X_);
#undef X_
#undef Y_
}
#define TRS0(ad_, f_) tr4_w(ad_, 0, 1536, 12288, 13824, f_, W)
#define TRS1(ad_, f_) tr4_w(ad_, 24576, 26112, 36864, 38400, f_, W)
#define TRO4(ad_, j_, f_) tr4_w(ad_, 16384 * (j_), 16384 * (j_) + 4096, 16384 * (j_) + 8192, 16384 * (j_) + 12288, f_, W)
#define SMFV(c_, a_, b_) do { asm volatile("v_smfmac_f32_16x16x64_bf16 %0, %1, %2, %3" : "+v"(c_) : "v"(a_), "v"(b_), "v"(ix)); SB(); } while (0)
#define SMFW(c_, a_, f_) do { smf_w(c_, a_, f_, ix, W); SB(); } while (0)
#define ADD1(d_, x_, y_) do { asm volatile("v_add_f32 %0, %1, %2" : "=v"(d_) : "v"(x_), "v"(y_)); SB(); } while (0)
#define ADD2(d0_, d1_, c_) do { asm volatile("v_add_f32 %0, %2, %3\n v_add_f32 %1, %4, %5" : "=&v"(d0_), "=v"(d1_) : "v"(c_[0]), "v"(c_[2]), "v"(c_[1]), "v"(c_[3])); SB(); } while (0)
#define CVT1(d_, x_, y_) do { asm volatile("v_cvt_pk_bf16_f32 %0, %1, %2" : "=v"(d_) : "v"(x_), "v"(y_)); SB(); } while (0)
#define EPI3(d_, c_) do { float e_; asm volatile("v_add_f32 %1, %2, %3\n v_add_f32 %0, %4, %5\n v_cvt_pk_bf16_f32 %0, %1, %0" : "=&v"(d_), "=&v"(e_) : "v"(c_[0]), "v"(c_[2]), "v"(c_[1]), "v"(c_[3])); SB(); } while (0)
#define EPI4(d_, m_, c_) do { float e_, f_; asm volatile("v_add_f32 %1, %4, %5\n v_add_f32 %2, %6, %7\n v_cvt_pk_bf16_f32 %0, %1, %2\n v_cmp_u_f32_e64 %3, %1, %2" : "=&v"(d_), "=&v"(e_), "=&v"(f_), "=s"(m_) : "v"(c_[0]), "v"(c_[2]), "v"(c_[1]), "v"(c_[3])); SB(); } while (0)
#define CVTN(d_, m_, x_, y_) do { NANM(m_, x_, y_); CVT1(d_, x_, y_); } while (0)
#define ADDCVT(d_, x_, c_) do { asm volatile("v_add_f32 %0, %2, %3\n v_cvt_pk_bf16_f32 %0, %1, %0" : "=&v"(d_) : "v"(x_), "v"(c_[1]), "v"(c_[3])); SB(); } while (0)
#define LGW(n_) do { __builtin_amdgcn_s_waitcnt(0xC07F | ((n_) << 8)); SB(); } while (0)
#define LBAR(n_) do { asm volatile("s_waitcnt lgkmcnt(" #n_ ")" ::: "memory"); __builtin_amdgcn_s_barrier(); asm volatile("" ::: "memory"); SB(); } while (0)
#define FILL() do { __builtin_amdgcn_s_setprio(0); SB(); } while (0)
#define NANM(m_, x_, y_) do { asm volatile("v_cmp_u_f32_e64 %0, %1, %2" : "=s"(m_) : "v"(x_), "v"(y_)); SB(); } while (0)
#define LW32(ad_, off_, v_) (*(unsigned*)(lds + (ad_) + (off_)) = (v_))
#define MEMB() asm volatile("" ::: "memory")
#define LG0() __builtin_amdgcn_s_waitcnt(0xC07F)
#define FLAG(i_) do { MEMB(); *(volatile LAS unsigned char*)(LAS unsigned char*)(lds + fla + 4 * (i_)) = (unsigned char)snv; SB(); } while (0)
#define FLRD(i_) (*(const volatile LAS unsigned*)(LAS unsigned char*)(lds + flr + 4 * (i_)))
#define SBUF(i_) (((i_) % 3) == 0 ? L_B0 : (((i_) % 3) == 1 ? L_B1 : L_B2))
#define SDMA_INIT() unsigned svo[12]; const int sq0 = 3 * ((h >> 2) & 3); \
    _Pragma("unroll") for (int q_ = 0; q_ < 12; ++q_) { const int qr_ = q_ + sq0, ins = wave + 4 * (qr_ < 12 ? qr_ : qr_ - 12); const unsigned c = (unsigned)(ins * 64 + lane), r = c / 24u, pc = c - r * 24u, sw = ((r >> 1) & 1u) * 2u + ((r >> 3) & 1u) * 4u; \
      svo[q_] = r * 12288u + 16u * (pc ^ sw); PINV(svo[q_]); }
#define KROT(kr_) ((kr_) < 4 ? (((kr_) + h) & 3) : 4)
#define SDMA(W_m, kr_, i_) do { const char* W_ = (const char*)((W_m) + (size_t)(128 * KROT(kr_)) * 6144 + 192 * h); \
    _Pragma("unroll") for (int q_ = 0; q_ < 12; ++q_) { const int qr_ = q_ + sq0; glds(W_, svo[q_], lds_addr(lds) + (unsigned)(SBUF(i_) + (wave + 4 * (qr_ < 12 ? qr_ : qr_ - 12)) * 1024)); } SB(); } while (0)
#define SDMAQK(i_) SDMA(((i_) & 1) ? a.Wk : a.Wq, (i_) >> 1, i_)
#define TRO(tile_) (trow * 384u + (((unsigned)(2 * (tile_) + ((l15 & 3) >> 1))) ^ tswz) * 16u + (unsigned)(l15 & 1) * 8u)
#define TRO_INIT() const unsigned trow = (unsigned)(8 * lq + (l15 >> 2)), tswz = ((trow >> 1) & 1u) * 2u + ((trow >> 3) & 1u) * 4u
#define SXDMA_INIT() unsigned xvo = (unsigned)((4 * wave + lq) * 1280 + (l15 ^ ((4 * wave + lq) & 15)) * 16); PINV(xvo)
#define SXDMA(kr_, xb_) do { const char* X_ = (const char*)a.Bx + 256 * KROT(kr_); \
    glds(X_, xvo, lds_addr(lds) + (unsigned)((xb_) + wave * 1024)); glds(X_ + 16 * 1280, xvo, lds_addr(lds) + (unsigned)((xb_) + 4096 + wave * 1024)); SB(); } while (0)
#define SXRD(xb_) _Pragma("unroll") for (int kk = 0; kk < 4; ++kk) _Pragma("unroll") for (int rt = 0; rt < 2; ++rt) xa[rt][kk] = *(const u32x4*)(lds + (xb_) + rt * 4096 + l15 * 256 + ((4 * kk + lq) ^ l15) * 16)
#define MFV(c_, a_, b_, first_) do { if (first_) asm volatile("v_mfma_f32_16x16x32_bf16 %0, %1, %2, 0" : "=&v"(c_) : "v"(a_), "v"(b_)); else asm volatile("v_mfma_f32_16x16x32_bf16 %0, %1, %2, %0" : "+v"(c_) : "v"(a_), "v"(b_)); } while (0)
#define HALF(f_, hi_) ((hi_) ? hi4(f_) : lo4(f_))
#define ODMA_INIT() unsigned ovo = (unsigned)((4 * wave + lq) * 1280 + (l15 ^ (((4 * wave + lq) & 7) << 1)) * 16); PINV(ovo); \
    const unsigned orow = (unsigned)(4 * lq + (l15 >> 2)); \
    unsigned otr[2]; _Pragma("unroll") for (int t = 0; t < 2; ++t) otr[t] = orow * 256u + (((unsigned)(2 * (2 * wave + t) + ((l15 & 3) >> 1))) ^ ((orow & 7u) << 1)) * 16u + (unsigned)(l15 & 1) * 8u
#define OROT(cr_) (((cr_) + h) % 5)
#define ODMA(cr_, i_) do { const char* W_ = (const char*)(a.Wo + (size_t)(192 * h) * 640) + 256 * OROT(cr_); \
    _Pragma("unroll") for (int q_ = 0; q_ < 12; ++q_) glds(W_ + q_ * (16 * 1280), ovo, lds_addr(lds) + (unsigned)(SBUF(i_) + (wave + 4 * q_) * 1024)); SB(); } while (0)

#define KXL 8
#define SAL 2
#define SOF 0
#define MNAP 0
#define SF0(m_, t_, k_) (6 * (2 * ((k_) >> 1) + (m_)) + 2 * (t_) + ((k_) & 1))
#define SFR(m_, t_, k_) (SF0(m_, t_, k_) - (SF0(m_, t_, k_) > 39) - (SF0(m_, t_, k_) > 41))
constexpr int L_KX = L_B1 + 28672, L_QX = 0;
template <int wave>
__device__ __forceinline__ void s_loop(const Args& a, unsigned char* lds, const int h, const unsigned pol, const WReg& W) {
  constexpr int KIND = wave < 2 ? 0 : wave - 1;
  char* ws = a.ws; asm volatile("" : "+s"(ws));
  unsigned* const cnt = (unsigned*)(ws + O_CNT); unsigned* const tmo = cnt + CNT(C_TMO);
  const int lane = lane_id(), l15 = lane & 15, lq = lane >> 4;
  char* const X1 = ws + O_X1; char* const P = ws + O_PS;
  u32x8 kxf[3];
  if (KIND == 0) {
#pragma unroll
    for (int t = 0; t < 3; ++t) kxf[t] = cat8(*(const u32x4*)(lds + L_KX + wave * 6144 + t * 2048 + lane * 16), *(const u32x4*)(lds + L_KX + wave * 6144 + t * 2048 + 1024 + lane * 16));
  }
  const __amdgpu_buffer_rsrc_t rs_x1 = mkrs(X1), rs_p = mkrs(P), rs_kf = mkrs(ws + O_KF);
  unsigned tprog = 0;
  const unsigned kkey = K_KEY();
#define KX_LOAD(sg_) do { PINV(xv); const unsigned kb_ = KSLOT(sg_) + (unsigned)(h * 32768 + wave * 16384); \
    _Pragma("unroll") for (int t_ = 0; t_ < 3; ++t_) kxf[t_] = cat8(ld16(rs_kf, xv + (unsigned)((t_ & 1) * 2048), kb_ + (unsigned)((t_ >> 1) * 4096)), ld16(rs_kf, xv + (unsigned)((t_ & 1) * 2048 + 1024), kb_ + (unsigned)((t_ >> 1) * 4096))); } while (0)
  unsigned trx = (unsigned)(L_XR + wave * 4096 + lane * 16); PINV(trx);
  unsigned trq = (unsigned)(L_B2 + wave * 12288 + lane * 16); PINV(trq);
  unsigned trk = (unsigned)(L_B0 + wave * 12288 + lane * 16); PINV(trk);
  int ix = (l15 & 2) ? 0xEEEE : 0x4444; PINV(ix);
  const float CS = 0.07216878364870323f * 1.4426950408889634f;
  unsigned xv = (unsigned)lane * 16u;
  unsigned ib = (unsigned)(L_QI + (16 * wave + l15) * 16 + lq * 4);
  unsigned ia = (unsigned)(L_QA + wave * 384 + ((l15 >> 2) & 1) * 128 + (l15 >> 3) * 64 + (l15 & 1) * 32 + lq * 8 + ((l15 >> 1) & 1) * 4);
  unsigned rda = (unsigned)(L_QA + (lq >> 1) * 768 + (l15 >> 3) * 384 + (lq & 1) * 64 + ((l15 >> 2) & 1) * 32 + (l15 & 3) * 8);
  unsigned qk_rd = (unsigned)(L_QI + (4 * lq + (l15 >> 2)) * 16 + (l15 & 1) * 8);
  unsigned flr = (unsigned)L_FLG;
  unsigned xva = KIND == 0 ? (unsigned)(L_XV + lq * 256 + (l15 >> 1) * 16 + wave * 8 + (l15 & 1) * 4) : (unsigned)(L_XV + lane * 16);
  PINV(xv); PINV(ib); PINV(ia);
  if (KIND != 2) { PINV(rda); PINV(xva); } if (KIND == 1) PINV(qk_rd); if (KIND != 0) PINV(flr);
  const bool hi8 = l15 >= 8;
  const __amdgpu_buffer_rsrc_t rs_ts = mkrs(ws + O_TS + half_of(ws + O_TS, 0u, 0u, pol));
  int tso = lane == 0 ? 256 * h : (int)0x80000000u; if (KIND != 0) PINV(tso);
  int tsa = TS_OFF(lane); if (KIND == 2) PINV(tsa);
  const __amdgpu_buffer_rsrc_t rs_ps = mkrs(cnt + CNT(C_PS) - 32 * h);
  unsigned po = side_map(P, (unsigned)(h * 512 + ((l15 >> 2) & 1) * 256 + (4 * lq + 2 * (l15 & 1) + (l15 >> 3)) * 16 + ((l15 >> 1) & 1) * 8), 0u, pol);
  unsigned pm = !(l15 & 1) ? side_map(P, (unsigned)(16384 + h * 128 + (4 * lq + 2 * ((l15 >> 2) & 1) + (l15 >> 3)) * 8 + ((l15 >> 1) & 1) * 4), 0u, pol) : 0x80000000u;
  u32x4 p_desc = mkdesc(P); if (KIND == 1) PINS(p_desc);
  unsigned pfo = lane < 32 ? side_map(P, (unsigned)(h * 512 + lane * 16), 0u, pol) : (lane < 40 ? side_map(P, (unsigned)(16384 + h * 128 + (lane - 32) * 16), 0u, pol) : 0x80000000u);
  if (KIND == 1) { PINV(po); PINV(pm); }
  if (KIND == 1) PINV(pfo);
  unsigned dur = 130, tnx = 0, tpr = 0, adj = 0; int dly = a.d[0]; PINS(dly);
#define MQ(t_, f_, w_) SMFV(aq[t_], f_, w_)
#define MK(t_, f_, w_) SMFV(ak[t_], f_, w_)
#define QW(t_, k_) SMFW(aq[t_], A[k_], SFR(0, t_, k_))
#define KW(t_, k_) SMFW(ak[t_], A[k_], SFR(1, t_, k_))
#define RLO(tb_, t_, k2_) (*(const u32x4*)(lds + (tb_) + (t_) * 4096 + (k2_) * 2048))
#define RHI(tb_, t_, k2_) (*(const u32x4*)(lds + (tb_) + (t_) * 4096 + (k2_) * 2048 + 1024))
#define RFR(tb_, t_, k2_) cat8(RLO(tb_, t_, k2_), RHI(tb_, t_, k2_))
#define XLO(j_) (*(const u32x4*)(lds + trx + (j_) * 2048))
#define XHI(j_) (*(const u32x4*)(lds + trx + (j_) * 2048 + 1024))
#define ALD(k_) DS(A[k_] = ld16(rs_x1, xv + (unsigned)(((k_) & 1) * 1024), sx[(k_) >> 1]))
#define APACE 10
#define ALP(k_) do { ALD(k_); nap4<APACE>(); SB(); } while (0)
#define A_LOAD() do { PINV(xv); ALP(0); ALP(8); ALP(1); ALP(9); ALP(2); ALP(7); ALP(3); ALP(4); ALP(5); ALD(6); SB(); } while (0)
  u32x4 ts_desc = mkdesc(ws + O_TS + half_of(ws + O_TS, 0u, 0u, pol) + 256 * h);
  unsigned fad = lds_addr(lds) + (unsigned)L_FLG; PINV(fad); PINS(ts_desc);
  if (KIND == 2) LW32(flr, 24, dur);
#pragma unroll 1
  for (int s = 0; s < 64; ++s) {
    int pss = PSLOT(s), tss = TS_STEP(s), tsn = TS_STEP(s + 1); PINS(pss); PINS(tss); if (KIND == 2) PINS(tsn);
    unsigned snp = (unsigned)(s + 1) * 0x01010101u, snv = snp, tries = 0; PINS(snp); PINV(snv);
    f32x4 sc = ZERO4; if (KIND != 2) PINV(sc);
    unsigned sx[5];
    { const unsigned so = half_of(X1, XSLOT(s), 0u, pol);
#pragma unroll
      for (int j = 0; j < 5; ++j) { sx[j] = so + (j < 4 ? (unsigned)((j + h) & 3) * 2048u : 16384u); PINS(sx[j]); }
      if (KIND == 2 && s == 1) x1_light(rs_x1, so, tmo); }
    SB();
    asm volatile("s_branch .Lsre%c6\n"
                 ".Lsbad%c6:\n s_add_u32 %0, %0, 1\n s_cmp_lt_u32 %0, 4\n s_cbranch_scc1 .Lsnow%c6\n s_sleep 8\n"
                 ".Lsnow%c6:\n s_memrealtime vcc\n ds_write_b32 %5, %1 offset:28\n ds_read_b32 %1, %5 offset:24\n s_waitcnt lgkmcnt(0)\n v_add_u32 %1, vcc_lo, %1\n v_and_b32 %1, -2, %1\n"
                 " s_mov_b64 exec, 1\n buffer_store_dword %1, off, %3, %4 sc1\n s_mov_b64 exec, -1\n v_mov_b32 %1, %2\n"
                 ".Lsre%c6:" : "+s"(tries) : "v"(snv), "s"(snp), "s"(ts_desc), "s"(tss), "v"(fad), "n"(wave) : "vcc", "scc", "memory");
    SB();
    u32x8 ra = RFR(trq, 0, 0), rb = RFR(trq, 1, 0), rc = RFR(trq, 2, 0);
    f32x4 aq[3] = {ZERO4, ZERO4, ZERO4}, ak[3] = {ZERO4, ZERO4, ZERO4};
    asm volatile("s_cmp_lg_u32 %6, 0\n s_cbranch_scc1 .Lsgo%c7" : "+v"(aq[0]), "+v"(aq[1]), "+v"(aq[2]), "+v"(ak[0]), "+v"(ak[1]), "+v"(ak[2]) : "s"(tries), "n"(wave) : "scc", "memory");
    SB();
    unsigned tis = 0, tsl = 0; bool calm = false;
    asm volatile(".p2align %c0\n .fill %c1, 4, 0xbf800000" :: "n"(SAL), "n"(SOF));
    if (KIND == 2) tis = wait_rt(tnx);
    BAR();
    asm volatile(".Lsgo%c0:" :: "n"(wave) : "memory");
    SB();
    u32x4 A[10], lo, hi;
    A_LOAD();
    unsigned w0;
    float ta, tb_;
    u64 nanm;
    QW(0, 0); QW(1, 0); QW(2, 0); KW(0, 0);
    MQ(0, A[8], ra); DS(lo = RLO(trq, 0, 1));
    KW(1, 0); DS(hi = RHI(trq, 0, 1)); ra = cat8(lo, hi);
    MQ(1, A[8], rb); DS(lo = RLO(trq, 1, 1));
    KW(2, 0); DS(hi = RHI(trq, 1, 1)); rb = cat8(lo, hi);
    QW(0, 1);
    MQ(2, A[8], rc); DS(lo = RLO(trq, 2, 1));
    QW(1, 1); DS(hi = RHI(trq, 2, 1)); rc = cat8(lo, hi);
    QW(2, 1);
    MQ(0, A[9], ra); DS(lo = RLO(trk, 0, 0));
    KW(0, 1); DS(hi = RHI(trk, 0, 0)); ra = cat8(lo, hi);
    MQ(1, A[9], rb); DS(lo = XLO(0));
    KW(1, 1); DS(hi = XHI(0)); rb = cat8(lo, hi);
    QW(0, 2);
    MQ(2, A[9], rc); DS(lo = XLO(1));
    QW(1, 2); DS(hi = XHI(1)); rc = cat8(lo, hi);
    QW(2, 2);
    MK(0, A[8], ra); DS(lo = RLO(trk, 1, 0));
    QW(0, 7); DS(hi = RHI(trk, 1, 0)); ra = cat8(lo, hi);
    MQ(1, A[7], rb); DS(lo = RLO(trk, 0, 1));
    KW(0, 2); DS(hi = RHI(trk, 0, 1)); rb = cat8(lo, hi);
    QW(0, 3);
    MQ(2, A[7], rc); DS(lo = RLO(trk, 2, 0));
    QW(1, 3); DS(hi = RHI(trk, 2, 0)); rc = cat8(lo, hi);
    QW(2, 3);
    QW(0, 4);
    MK(1, A[8], ra); DS(lo = RLO(trk, 1, 1));
    QW(1, 4); DS(hi = RHI(trk, 1, 1)); ra = cat8(lo, hi);
    QW(2, 4);
    QW(0, 5);
    MK(0, A[9], rb); DS(lo = RLO(trk, 2, 1));
    QW(1, 5); DS(hi = RHI(trk, 2, 1)); rb = cat8(lo, hi);
    QW(2, 5);
    QW(0, 6); QW(1, 6); QW(2, 6); LG0(); SB();
    KW(0, 3); FILL(); ADD1(ta, aq[0][0], aq[0][2]);
    KW(0, 4); FILL(); ADD1(tb_, aq[0][1], aq[0][3]);
    KW(0, 5); FILL(); CVTN(w0, nanm, ta, tb_);
    KW(0, 6); DS(LW32(ia, 0, w0)); ADD1(ta, aq[1][0], aq[1][2]);
    KW(0, 7); ADD1(tb_, aq[1][1], aq[1][3]);
    KW(1, 2);
    if (__builtin_expect(nanm != 0ull, 0)) {
      unsigned m = ~0u;
#define S_BACK() asm volatile("v_cmp_eq_u32 vcc, 0, %0\n s_cmp_lt_u32 %1, %3\n s_cselect_b64 vcc, vcc, 0\n s_cbranch_vccnz .Lsbad%c2" : "+v"(m) : "s"(tries), "n"(wave), "n"(CH_SPIN_CAP) : "vcc", "scc")
#pragma unroll
      for (int k = 0; k < 10; k += 2) m = sent_min(m, A[k].x, A[k + 1].x);
      S_BACK();
#pragma unroll
      for (int k = 0; k < 10; ++k) m = sent_min(m, A[k]);
      S_BACK();
    }
    asm volatile("" :: "s"(sx[0]), "s"(sx[1]), "s"(sx[2]), "s"(sx[3]), "s"(sx[4]), "s"(tries), "s"(tss), "s"(snp), "s"(ts_desc), "v"(fad), "v"(snv));
    CVT1(w0, ta, tb_);
    KW(1, 3); DS(LW32(ia, 1536, w0)); ADD1(ta, aq[2][0], aq[2][2]);
    KW(1, 4); FILL(); ADD1(tb_, aq[2][1], aq[2][3]);
    KW(1, 5); FILL(); CVT1(w0, ta, tb_);
    KW(1, 6); DS(LW32(ia, 3072, w0)); ADD1(ta, ak[0][0], ak[0][2]);
    KW(1, 7); FILL(); ADD1(tb_, ak[0][1], ak[0][3]);
    MK(1, A[9], ra); CVT1(w0, ta, tb_);
    KW(2, 1); DS(LW32(ib, 0, w0));
    u32x2 g[6];
#define QAR(i_) do { if (KIND != 2) DS(g[i_] = trrd(lds, rda + (unsigned)(((i_) >> 1) * 1536 + ((i_) & 1) * 128))); else if ((i_) >= 1 && (i_) <= 3) FILL(); } while (0)
#define QA(t_) ((u32x4){g[2 * (t_)].x, g[2 * (t_)].y, g[2 * (t_) + 1].x, g[2 * (t_) + 1].y})
    KW(2, 2); LBAR(1); QAR(0);
    KW(2, 3); QAR(1); ADD1(ta, ak[1][0], ak[1][2]);
    KW(2, 4); QAR(2); ADD1(tb_, ak[1][1], ak[1][3]);
    KW(2, 5); QAR(3); CVT1(w0, ta, tb_);
    KW(2, 6); DS(LW32(ib, 1024, w0));
    KW(2, 7); QAR(4); QAR(5);
    if (KIND == 2) DS(tsl = __builtin_amdgcn_raw_buffer_load_b32(rs_ts, tsa, tss, 16));
    MK(2, A[8], rc);
    MK(2, A[9], rb);
    if (KIND == 0) {
      float la, lb;
      { const u32x4 q_ = QA(0); SMFV(sc, q_, kxf[0]); } LBAR(2);
      { const u32x4 q_ = QA(1); SMFV(sc, q_, kxf[1]); }
      { const u32x4 q_ = QA(2); SMFV(sc, q_, kxf[2]); }
      EPI3(w0, ak[2]); DS(LW32(ib, 2048, w0));
      asm volatile("s_nop 3\n v_add_f32 %0, %2, %3\n v_add_f32 %1, %4, %5" : "=&v"(la), "=v"(lb) : "v"(sc[0]), "v"(sc[2]), "v"(sc[1]), "v"(sc[3])); SB();
      DS(LW32(xva, 0, __float_as_uint(la)); LW32(xva, 128, __float_as_uint(lb)));
      LBAR(1); LBAR(0);
    } else if (KIND == 2) {
      LBAR(0);
      asm volatile("s_nop 5"); SB();
      EPI3(w0, ak[2]); DS(LW32(ib, 2048, w0));
      LBAR(0); LBAR(0);
      tsl = ts_max(tsl);
      calm = s > 1 && tsl != ~0u && (int)(tsl - tpr) <= a.d[10];
      if (calm && s < 63) __builtin_amdgcn_raw_buffer_store_b32((tsl + (unsigned)dly + dur) & ~1u, rs_ts, tso, tsn, 16);
    } else {
      u32x2 kb[12];
#define KBR(i_) DS(kb[i_] = trrd(lds, qk_rd + (unsigned)((i_) * 256)))
#define ML(t_) do { const u32x4 a_ = QA(t_); \
        const u32x8 b_ = {kb[4 * (t_)].x, kb[4 * (t_)].y, kb[4 * (t_) + 1].x, kb[4 * (t_) + 1].y, kb[4 * (t_) + 2].x, kb[4 * (t_) + 2].y, kb[4 * (t_) + 3].x, kb[4 * (t_) + 3].y}; SMFV(sc, a_, b_); } while (0)
      LBAR(2);
      KBR(0); KBR(1); KBR(2); KBR(3); KBR(4); KBR(5);
      EPI3(w0, ak[2]); DS(LW32(ib, 2048, w0));
      KBR(6); KBR(7);
      LBAR(2);
      KBR(8); KBR(9); KBR(10); KBR(11);
      ML(0);
      asm volatile("s_barrier" ::: "memory"); SB();
      f32x4 t; DS(t = *(const f32x4*)(lds + xva));
      ML(1); ML(2);
      float tl, nl, x, m, u, e, d;
      asm volatile("v_max3_f32 %4, %0, %1, %2\n v_max_f32 %4, %4, %3\n v_mul_f32 %5, %15, %4\n v_fma_f32 %0, %16, %0, %5\n v_fma_f32 %1, %16, %1, %5\n v_fma_f32 %2, %16, %2, %5\n v_fma_f32 %3, %16, %3, %5\n s_nop 0\n"
                   "v_add_f32_dpp %6, %11, %13 quad_perm:[0,1,2,3] row_mask:0xf bank_mask:0x3\n v_add_f32_dpp %6, %12, %14 quad_perm:[0,1,2,3] row_mask:0xf bank_mask:0xc\n v_max_f32 %7, %6, %4\n"
                   "v_exp_f32 %0, %0\n v_exp_f32 %1, %1\n v_max_f32_dpp %7, %7, %7 quad_perm:[1,0,3,2] row_mask:0xf bank_mask:0xf\n"
                   "v_exp_f32 %2, %2\n v_exp_f32 %3, %3\n v_max_f32_dpp %7, %7, %7 quad_perm:[2,3,0,1] row_mask:0xf bank_mask:0xf\n"
                   "v_add_f32 %8, %0, %1\n v_add_f32 %10, %2, %3\n v_max_f32_dpp %7, %7, %7 row_half_mirror row_mask:0xf bank_mask:0xf\n"
                   "v_mul_f32 %7, %15, %7\n v_sub_f32 %4, %7, %5\n v_fma_f32 %9, %16, %6, %7\n v_exp_f32 %4, %4\n v_exp_f32 %9, %9\n v_add_f32 %8, %8, %10\n v_fma_f32 %10, %4, %8, %9\n"
                   "v_mul_f32 %0, %0, %4\n v_mul_f32 %1, %1, %4\n v_add_f32_dpp %10, %10, %10 quad_perm:[1,0,3,2] row_mask:0xf bank_mask:0xf\n"
                   "v_mul_f32 %2, %2, %4\n v_mul_f32 %3, %3, %4\n v_add_f32_dpp %10, %10, %10 quad_perm:[2,3,0,1] row_mask:0xf bank_mask:0xf\n"
                   "v_mov_b32_dpp %6, %9 quad_perm:[1,0,3,2] row_mask:0xf bank_mask:0xf\n s_nop 0\n v_add_f32_dpp %10, %10, %10 row_half_mirror row_mask:0xf bank_mask:0xf\n v_rcp_f32 %10, %10\n s_nop 0\n"
                   "v_mul_f32 %0, %0, %10\n v_mul_f32 %1, %1, %10\n v_cvt_pk_bf16_f32 v0, %0, %1\n v_mul_f32 %2, %2, %10\n v_mul_f32 %3, %3, %10\n v_cvt_pk_bf16_f32 v1, %2, %3\n"
                   "buffer_store_dwordx2 v[0:1], %17, %19, %20 offen sc1\n v_mul_f32 %9, %9, %10\n v_mul_f32 %6, %6, %10\n v_cvt_pk_bf16_f32 %9, %9, %6\n buffer_store_dword %9, %18, %19, %20 offen sc1"
                   : "+v"(t[0]), "+v"(t[1]), "+v"(t[2]), "+v"(t[3]), "=&v"(tl), "=&v"(nl), "=&v"(x), "=&v"(m), "=&v"(u), "=&v"(e), "=&v"(d)
                   : "v"(sc[0]), "v"(sc[1]), "v"(sc[2]), "v"(sc[3]), "s"(-CS), "s"(CS), "v"(po), "v"(pm), "s"(p_desc), "s"(pss) : "v0", "v1", "memory");
      SB();
      { u64 t_; RT_MARK(t_); LW32(flr, 20, (unsigned)t_); }
    }
    BAR();
    if (KIND == 0 && s == 0) {
#pragma unroll
      for (int n = 0; n < 12; ++n) st16(mkrs(ws + (wave == 0 ? O_KF : O_QF)), xv + (unsigned)(n % 3 * 1024), KSLOT(0) + (unsigned)(h * 32768 + n / 6 * 16384 + n % 6 / 3 * 3072), *(const u32x4*)(lds + (wave == 0 ? L_KX : L_QX) + n * 1024 + xv));
    }
    if (KIND == 0) {
      nap_rt(KXL - 1);
      if (s < 63) {
        const unsigned tneed = (unsigned)(s + 1);
        if (tprog < tneed) tprog = wait_t<4>(cnt + CNT(C_PT_ + h), 1, tneed, tmo);
        KX_LOAD(s + 1);
      }
    }
    if (KIND == 0) tprog = min_t(ld16(mkrs(cnt + CNT(C_PT_ + h)), 0u, 0u), 1);
    if (KIND == 2 && s < 63) {
      const unsigned f7 = FLRD(7), f5 = FLRD(5);
      const unsigned tb2 = (unsigned)__builtin_amdgcn_readfirstlane(f5);
      adj = __builtin_amdgcn_readfirstlane(f7) == snp;
      if (__builtin_expect(!calm || adj, 0)) {
        if (s < 2) __builtin_amdgcn_raw_buffer_store_b32(((unsigned)tb2 + (unsigned)a.d[11]) & ~1u, rs_ts, tso, tss, 16);
        tsl = ts_settle(rs_ts, tss, tsl, a.d[4]) + (unsigned)a.d[12];
        if (s > 0) __builtin_amdgcn_raw_buffer_store_b32((tsl + (unsigned)dly + dur) & ~1u, rs_ts, tso, tsn, 16);
      }
      tnx = tsl + (unsigned)dly; tpr = tnx + dur;
      dur = min(dur + 1u, (unsigned)tb2 - tis); PINS(dur);
      LW32(flr, 24, dur + (unsigned)a.d[13]);
      ADAPT(dly, adj);
    }
    if (KIND == 1) {
      const int ln = lane_id();
      const __amdgpu_buffer_rsrc_t rs_km = mkrs(ws + O_KM);
      if (s + PRE < 63) st16(rs_p, pfo, (unsigned)PSLOT(s + PRE), SENT4);
#pragma unroll
      for (int kk = 0; kk < 6; kk += 2) {
        const u32x4 v = trrd2(lds, qk_rd + kk * 512, 256u), v2 = trrd2(lds, qk_rd + (kk + 1) * 512, 256u);
        st16(rs_km, (unsigned)((kk + ((ln >> 3) & 1)) * 512 + (ln >> 4) * 128 + (ln & 7) * 16), MSLOT(s) + (unsigned)h * 3072u, hi8 ? v2 : v);
      }
      __builtin_amdgcn_raw_buffer_store_b32((unsigned)(s + 1), rs_ps, tso, 0, 16);
    }
  }
}
__device__ __forceinline__ void s_role(const Args& a, unsigned char* lds, const int h, const int wave) {
  char* ws = a.ws; asm volatile("" : "+s"(ws));
  unsigned* const cnt = (unsigned*)(ws + O_CNT); unsigned* const tmo = cnt + CNT(C_TMO);
  const int lane = lane_id(), l15 = lane & 15, lq = lane >> 4;
  WReg W; u32x8 wx[3];
  unsigned tro[3];
  { TRO_INIT();
#pragma unroll
    for (int t = 0; t < 3; ++t) tro[t] = TRO(4 * t + wave); }
  SDMA_INIT();
  SXDMA_INIT();
  SXDMA(0, 0); SDMAQK(0); SDMAQK(1); SXDMA(1, 8192); SDMAQK(2);
  unsigned pf;
  f32x4 pac[2][2][3];
#define kacc pac[1]
#define qacc pac[0]
  SFOR(i, 10)
    if (i < 2 || i == 3 || i == 5 || i == 7) VMW(26); else if (i < 8) VMW(24); else if (i == 8) VMW(12); else VMW(0);
    BAR();
    u32x8 res[3][2];
    SFOR(t, 3)
      const unsigned ad = lds_addr(lds) + tro[t] + (unsigned)SBUF(i);
      if (i < 8) { TRS0(ad, SFR(i & 1, t, i & 6)); if (i == 6 && t > 0) tr8(false, ad, res[t][0], wx[t]); else TRS1(ad, SFR(i & 1, t, (i & 6) + 1)); }
      else tr8(false, ad, res[t][0], res[t][1]);
    SEND;
    BAR();
    if (i == 2 || i == 4 || i == 6) SXDMA((i >> 1) + 1, (((i >> 1) + 1) & 1) * 8192);
    if (i + 3 < 10) SDMAQK(i + 3);
    if (i >= 8) {
#pragma unroll
      for (int t = 0; t < 3; ++t)
#pragma unroll
        for (int k = 0; k < 2; ++k) {
          unsigned char* dst = lds + SBUF(i) + wave * 12288 + t * 4096 + k * 2048 + lane * 16;
          *(u32x4*)dst = lo4(res[t][k]); *(u32x4*)(dst + 1024) = hi4(res[t][k]);
        }
    }
    if (i == 8) asm volatile("s_nop 4\n global_load_dword %0, %1, %2 sc1" : "=v"(pf) : "v"(lane < 32 ? 0u : 128u), "s"(cnt + CNT(C_POL)) : "memory");
    if (i == 9) PINV(pf);
    if (i == 7) {
#pragma unroll
      for (int t = 1; t < 3; ++t) { unsigned char* dst = lds + L_XR + wave * 4096 + lane * 16 + (t - 1) * 2048; *(u32x4*)dst = lo4(wx[t]); *(u32x4*)(dst + 1024) = hi4(wx[t]); }
    }
    SB();
    SFOR(kk, 4)
      u32x4 xa[2];
#pragma unroll
      for (int rt = 0; rt < 2; ++rt) xa[rt] = *(const u32x4*)(lds + ((i >> 1) & 1) * 8192 + rt * 4096 + l15 * 256 + ((4 * kk + lq) ^ l15) * 16);
      SFOR(n, 6)
        constexpr int t = n >> 1, rt = n & 1;
        if (i >= 8) { const u32x4 w_ = HALF(res[t][kk >> 1], kk & 1); MFV(pac[i & 1][rt][t], w_, xa[rt], false); }
        else if (i == 6 && t > 0 && kk >= 2) { const u32x4 w_ = HALF(wx[t], kk & 1); MFV(pac[0][rt][t], w_, xa[rt], false); }
        else mf_w<true>(pac[i & 1][rt][t], xa[rt], 2 * SFR(i & 1, t, (i & 6) + (kk >> 1)) + (kk & 1), i < 2 && kk == 0, W);
      SEND;
    SEND;
    SB();
  SEND;
  unsigned pol;
  {
    asm volatile("s_nop 7\n s_nop 7" : "+v"(kacc[0][0]), "+v"(kacc[0][1]), "+v"(kacc[0][2]), "+v"(kacc[1][0]), "+v"(kacc[1][1]), "+v"(kacc[1][2]));
    pol = (unsigned)__builtin_amdgcn_readlane((int)pf, 0);
    if (__builtin_expect((unsigned)__builtin_amdgcn_readlane((int)pf, 32) < NFILL || pol == 0u, 0)) { wait_ge<8>(cnt + CNT(C_FILL), NFILL, tmo); pol = ld_word(cnt + CNT(C_POL)); }
    pol &= 1u;
    BAR();
#pragma unroll
    for (int rt = 0; rt < 2; ++rt)
#pragma unroll
      for (int t = 0; t < 3; ++t) { const int ix = 4 * t + wave, o_ = rt * 6144 + (ix >> 1) * 1024;
        *(u32x2*)(lds + L_KX + o_ + lane * 16 + (ix & 1) * 8) = (u32x2){cvtpk(kacc[rt][t][0], kacc[rt][t][1]), cvtpk(kacc[rt][t][2], kacc[rt][t][3])};
        *(u32x2*)(lds + L_QX + o_ + ((ix >> 1) % 3 == 2 ? lane * 8 + (ix & 1) * 512 : lane * 16 + (ix & 1) * 8)) = (u32x2){cvtpk(qacc[rt][t][0], qacc[rt][t][1]), cvtpk(qacc[rt][t][2], qacc[rt][t][3])}; }
    if (wave == 2) { if (lane < 8) *(unsigned*)(lds + L_FLG + 4 * lane) = 0u; }
    BAR();
  }
  if (wave == 0) s_loop<0>(a, lds, h, pol, W); else if (wave == 1) s_loop<1>(a, lds, h, pol, W); else if (wave == 2) s_loop<2>(a, lds, h, pol, W); else s_loop<3>(a, lds, h, pol, W);
}
#undef kacc
#undef qacc

__device__ __forceinline__ void z_role(const Args& a, unsigned char* lds, const int h, const int wave) {
  char* ws = a.ws; asm volatile("" : "+s"(ws));
  unsigned* const cnt = (unsigned*)(ws + O_CNT); unsigned* const tmo = cnt + CNT(C_TMO);
  char* const X1 = ws + O_X1;
#define ZFV(t_, k_) (8 * (t_) + (k_))
#define ZFO(n_, j_) ((n_) < 6 ? 24 + 3 * (n_) + (j_) : 30 + 2 * (n_) + ((j_) >> 1))
  WReg W;
  const __amdgpu_buffer_rsrc_t rs_u = mkrs(ws + O_UO + h * 1024);
  unsigned xv = (unsigned)lane_id() * 16u; PINV(xv);
  unsigned pkv, pk3, pk4, pf;
  {
    const int lane = lane_id(), l15 = lane & 15, lq = lane >> 4;
    unsigned tro[3];
    { TRO_INIT();
#pragma unroll
      for (int t = 0; t < 3; ++t) tro[t] = TRO(4 * t + wave); }
    SDMA_INIT(); SXDMA_INIT(); ODMA_INIT();
    SDMA(a.Wv, 0, 0); SXDMA(0, 0); SDMA(a.Wv, 1, 1); SXDMA(1, 8192); SDMA(a.Wv, 2, 2);
    f32x4 kacc[2][3];
    u32x8 wl[3][2];
    SFOR(i, 5)
      if (i < 4) VMW(26); else VMW(24);
      BAR();
      u32x4 xa[2][4];
      SXRD((i & 1) * 8192);
      SFOR(t, 3)
        if (i < 4) { TRS0(lds_addr(lds) + tro[t] + (unsigned)SBUF(i), ZFV(t, 2 * (i & 3))); TRS1(lds_addr(lds) + tro[t] + (unsigned)SBUF(i), ZFV(t, 2 * (i & 3) + 1)); }
        else tr8(false, lds_addr(lds) + tro[t] + (unsigned)SBUF(i), wl[t][0], wl[t][1]);
      SEND;
#pragma unroll
      for (int n = 0; n < 8; ++n) PINV(xa[n >> 2][n & 3]);
      BAR();
      if (i + 2 < 5) SXDMA(i + 2, (i & 1) * 8192);
      if (i + 3 < 5) SDMA(a.Wv, i + 3, i + 3); else ODMA(i - 2, i + 3);
      SFOR(n, 24)
        constexpr int kk = n / 6, t = (n % 6) >> 1, rt = n & 1;
        if (i < 4) mf_w<true>(kacc[rt][t], xa[rt][kk], 2 * ZFV(t, 2 * (i & 3) + (kk >> 1)) + (kk & 1), i == 0 && kk == 0, W);
        else { const u32x4 w_ = HALF(wl[t][kk >> 1], kk & 1); MFV(kacc[rt][t], w_, xa[rt][kk], false); }
      SEND;
      SB();
    SEND;
    u32x4 vx[2][6];
    asm volatile("s_nop 7\n s_nop 7" : "+v"(kacc[0][0]), "+v"(kacc[0][1]), "+v"(kacc[0][2]), "+v"(kacc[1][0]), "+v"(kacc[1][1]), "+v"(kacc[1][2]));
#pragma unroll
    for (int rt = 0; rt < 2; ++rt)
#pragma unroll
      for (int t = 0; t < 3; ++t)
        *(u32x2*)(lds + rt * 6144 + (2 * t + (wave >> 1)) * 1024 + xv + (wave & 1) * 8) = (u32x2){cvtpk(kacc[rt][t][0], kacc[rt][t][1]), cvtpk(kacc[rt][t][2], kacc[rt][t][3])};
    BAR();
#pragma unroll
    for (int n = 0; n < 12; ++n) { vx[n / 6][n % 6] = *(const u32x4*)(lds + n * 1024 + xv); PINV(vx[n / 6][n % 6]); }
    SFOR(i5, 5)
      constexpr int i = i5 + 5;
      if (i == 5) VMW(24); else if (i == 6) VMW(26); else if (i == 7) VMW(28); else if (i == 8) VMW(18); else VMW(6);
      BAR();
      u32x8 res[2][3];
      SFOR(t, 2)
        const unsigned ad = lds_addr(lds) + otr[t] + (unsigned)SBUF(i);
        constexpr int n = 2 * i5 + t;
        if (n < 8) TRO4(ad, 0, ZFO(n & 7, 0)); else tr4<0>(false, ad, res[t][0]);
        if (n < 6) TRO4(ad, 1, ZFO(n & 7, 1)); else tr4<1>(false, ad, res[t][1]);
        if (n < 8) TRO4(ad, 2, ZFO(n & 7, 2)); else tr4<2>(false, ad, res[t][2]);
      SEND;
      BAR();
      if (i + 3 < 10) ODMA(i - 2, i + 3);
      if (i == 9) asm volatile("s_nop 4\n global_load_dword %0, %1, %2 sc1" : "=v"(pf) : "v"((xv >> 9) << 7), "s"(cnt + CNT(C_POL)) : "memory");
      f32x4 ua[2][2];
      SFOR(m, 24)
        constexpr int e = m >> 2, t = (m >> 1) & 1, rt = m & 1, n = 2 * i5 + t;
        if (n >= 8 || (n >= 6 && (e >> 1) == 1)) { const u32x4 w_ = HALF(res[t][e >> 1], e & 1); MFV(ua[t][rt], vx[rt][e], w_, e == 0); }
        else mf_w<false>(ua[t][rt], vx[rt][e], 2 * ZFO(n & 7, e >> 1) + (e & 1), e == 0, W);
      SEND;
      asm volatile("s_nop 7" : "+v"(ua[0][0]), "+v"(ua[0][1]), "+v"(ua[1][0]), "+v"(ua[1][1]));
#pragma unroll
      for (int t = 0; t < 2; ++t)
        st16(rs_u, xv, u_off(0u, (unsigned)(8 * OROT(i5) + 2 * wave + t)), (u32x4){cvtpk(ua[t][0][0], ua[t][0][1]), cvtpk(ua[t][0][2], ua[t][0][3]), cvtpk(ua[t][1][0], ua[t][1][1]), cvtpk(ua[t][1][2], ua[t][1][3])});
      SB();
      if (i == 7) {
        PINV(xv); pkv = xv + (unsigned)(L_B1 + wave * 12288);
#pragma unroll
        for (int n = 0; n < 6; ++n) { *(u32x4*)(lds + pkv + n * 2048) = lo4(wl[n >> 1][n & 1]); *(u32x4*)(lds + pkv + n * 2048 + 1024) = hi4(wl[n >> 1][n & 1]); }
      }
      if (i == 8) {
        PINV(xv); pk3 = xv + (unsigned)(L_B2 + wave * 12288);
#pragma unroll
        for (int t = 0; t < 2; ++t) { *(u32x4*)(lds + pk3 + t * 2048) = lo4(res[t][1]); *(u32x4*)(lds + pk3 + t * 2048 + 1024) = hi4(res[t][1]); }
      }
      if (i == 9) {
        PINV(xv); pk4 = xv + (unsigned)(L_B0 + wave * 12288);
#pragma unroll
        for (int n = 0; n < 6; ++n) { *(u32x4*)(lds + pk4 + n * 2048) = lo4(res[n & 1][n >> 1]); *(u32x4*)(lds + pk4 + n * 2048 + 1024) = hi4(res[n & 1][n >> 1]); }
      }
      SB();
    SEND;
  }
  const int lane = lane_id(), l15 = lane & 15, lq = lane >> 4;
  constexpr int L_VA = 10240;
  unsigned pol;
  {
    VMW(0); PINV(pf);
    pol = (unsigned)__builtin_amdgcn_readlane((int)pf, 0);
    __builtin_amdgcn_raw_buffer_store_b32(1u, mkrs(cnt + CNT(C_PZ + h)), lane == 0 ? 4 * wave : (int)0x80000000u, 0, 16);
    if (__builtin_expect((unsigned)__builtin_amdgcn_readlane((int)pf, 32) < NFILL || pol == 0u, 0)) { wait_ge<8>(cnt + CNT(C_FILL), NFILL, tmo); pol = ld_word(cnt + CNT(C_POL)); }
    pol &= 1u;
  }
  const __amdgpu_buffer_rsrc_t rs_x1 = mkrs(X1), rs_z = mkrs(ws + O_ZB), rs_ts = mkrs(ws + O_TS + half_of(ws + O_TS, 0u, 0u, pol));
  int ix = (l15 & 2) ? 0xEEEE : 0x4444; PINV(ix);
  unsigned ia = (unsigned)(L_VA + wave * 384 + ((l15 >> 2) & 1) * 128 + (l15 >> 3) * 64 + (l15 & 1) * 32 + lq * 8 + ((l15 >> 1) & 1) * 4);
  unsigned rda = (unsigned)(L_VA + (lq >> 1) * 768 + (l15 >> 3) * 384 + (lq & 1) * 64 + ((l15 >> 2) & 1) * 32 + (l15 & 3) * 8);
  unsigned zvo = (unsigned)((2 * wave + (lq >> 1)) * 16384 + h * 256 + l15 * 16 + (lq & 1) * 8);
  unsigned evo = (unsigned)((2 * wave + (lq & 1) + 8 * (lq >> 1)) * 16384 + h * 256 + l15 * 16);
  unsigned gvo = xv + (unsigned)(wave * 2048), gv2 = xv + 16384u + (unsigned)((wave >> 1) * 1024);
  u64 hmask = 0xffffffffull << (32 * (wave & 1)); PINS(hmask);
  const unsigned gm0 = lds_addr(lds) + (unsigned)(L_MEM + ((wave - h) & 3) * 2048), gm2 = lds_addr(lds) + (unsigned)(L_MEM + 8192 + (wave >> 1) * 1024);
  PINV(ia); PINV(rda); PINV(zvo); PINV(gvo); PINV(gv2); PINV(pkv); PINV(pk3); PINV(pk4);
  unsigned tsv = ~0u, tries = 0; int dly = a.d[1]; PINS(dly);
#define PLO(b_, n_) (*(const u32x4*)(lds + (b_) + (n_) * 2048))
#define PHI(b_, n_) (*(const u32x4*)(lds + (b_) + (n_) * 2048 + 1024))
#define MV(t_, f_, w_) SMFV(av[t_], f_, w_)
#define VW(t_, k_) SMFW(av[t_], A[k_], ZFV(t_, k_))
#define MO(n_, j_, w_) SMFV(az[n_], va[j_], w_)
#define OW(n_, j_) SMFW(az[n_], va[j_], ZFO(n_, j_))
#define AFR(k_) (*(const u32x4*)(lds + afb + (k_) * 1024))
  unsigned afb = xv + L_MEM; PINV(afb);
#define G_LOAD() do { asm volatile("s_mov_b32 m0, %3\n s_nop 0\n global_load_lds_dwordx4 %0, %4 sc1\n global_load_lds_dwordx4 %0, %4 offset:1024 sc1\n s_mov_b32 m0, %2\n s_mov_b64 exec, %5\n global_load_lds_dwordx4 %1, %4 sc1\n s_mov_b64 exec, -1\n s_waitcnt vmcnt(2)\n s_barrier" \
      :: "v"(gvo), "v"(gv2), "s"(gm2), "s"(gm0), "s"(xs), "s"(hmask) : "memory"); SB(); } while (0)
  u32x4 ts_desc = mkdesc(ws + O_TS + half_of(ws + O_TS, 0u, 0u, pol) + 16384 + 256 * h);
  int w3m = wave, dzs = a.d[9]; PINS(w3m); PINS(dzs); PINS(ts_desc);
  u32x4 z_desc = mkdesc(ws + O_ZB); PINS(z_desc);
#pragma unroll 1
  for (int s = 0; s < 64; ++s) {
    f32x4 av[3], az[10];
#pragma unroll
    for (int n = 0; n < 10; ++n) { az[n] = ZERO4; PINV(az[n]); }
    unsigned snp = (unsigned)(s + 1) * 0x01010101u, snv = snp, adj = 0; PINS(snp); PINV(snv);
    const unsigned so = half_of(X1, XSLOT(s), 0u, pol);
    const char* xs = X1 + so; int tss = TS_STEP(s); PINS(xs); PINS(tss);
    unsigned zs[5], tnx = (unsigned)__builtin_amdgcn_readfirstlane(wave == 3 && tsv != ~0u ? tsv + (unsigned)dly : 0u); PINS(tnx);
    { const unsigned z0 = half_of(ws + O_ZB, ZSLOT(s), 0u, pol);
#pragma unroll
      for (int i = 0; i < 5; ++i) { zs[i] = z0 + (unsigned)OROT(i) * (8u * 16384u); PINS(zs[i]); } }
    if (s == 1 && wave == 3) x1_light(rs_x1, so, tmo);
    SB();
    asm volatile("s_branch .Lzre\n"
                 ".Lzbad:\n s_waitcnt lgkmcnt(0)\n s_add_u32 %0, %0, 1\n s_add_u32 %1, %1, 1\n s_cmp_lt_u32 %0, 4\n s_cbranch_scc1 .Lznow\n s_sleep 8\n"
                 ".Lznow:\n s_cmp_lg_u32 %6, 3\n s_cbranch_scc1 .Lzre\n s_memrealtime vcc\n s_waitcnt lgkmcnt(0)\n s_add_u32 vcc_lo, vcc_lo, %7\n s_and_b32 vcc_lo, vcc_lo, -2\n v_mov_b32 %2, vcc_lo\n"
                 " s_mov_b64 exec, 1\n buffer_store_dword %2, off, %4, %5 sc1\n s_mov_b64 exec, -1\n v_mov_b32 %2, %3\n"
                 ".Lzre:" : "+s"(adj), "+s"(tries) : "v"(snv), "s"(snp), "s"(ts_desc), "s"(tss), "s"(w3m), "s"(dzs) : "vcc", "scc", "memory");
    SB();
    u32x8 ra = cat8(PLO(pkv, 0), PHI(pkv, 0)), rb = cat8(PLO(pkv, 1), PHI(pkv, 1));
    av[0] = ZERO4; av[1] = ZERO4; av[2] = ZERO4;
    asm volatile("" : "+v"(av[0]), "+v"(av[1]), "+v"(av[2]));
    SB();
    wait_rt(tnx);
    BAR();
    G_LOAD();
    u32x4 A[10], lo, hi, va[3];
    unsigned w0, w1;
    float ta, tb_;
    u64 nanm;
    DS(A[0] = AFR(0)); DS(A[2] = AFR(2)); DS(A[4] = AFR(4)); DS(A[6] = AFR(6));
    VW(0, 0); VW(1, 0); asm volatile("s_waitcnt vmcnt(0)\n s_barrier" ::: "memory"); SB();
    VW(2, 0); DS(A[8] = AFR(8));
    VW(0, 2); DS(A[9] = AFR(9));
    VW(1, 2); DS(A[1] = AFR(1));
    VW(2, 2); DS(A[3] = AFR(3));
    VW(0, 4); DS(A[5] = AFR(5));
    VW(0, 6); DS(A[7] = AFR(7));
    MV(0, A[8], ra); DS(lo = PLO(pkv, 2));
    MV(0, A[9], rb); DS(hi = PHI(pkv, 2)); ra = cat8(lo, hi);
    VW(0, 1); DS(lo = PLO(pkv, 3));
    VW(0, 3); DS(hi = PHI(pkv, 3)); rb = cat8(lo, hi);
    VW(0, 5); VW(0, 7);
    VW(1, 4); VW(1, 6);
    MV(1, A[8], ra); DS(lo = PLO(pkv, 4)); ADD1(ta, av[0][0], av[0][2]);
    VW(1, 1); DS(hi = PHI(pkv, 4)); ra = cat8(lo, hi); ADD1(tb_, av[0][1], av[0][3]);
    VW(1, 3); LGW(2); CVT1(w0, ta, tb_);
    MV(1, A[9], rb); DS(LW32(ia, 0, w0));
    VW(1, 5); DS(lo = PLO(pkv, 5)); NANM(nanm, ta, tb_);
    VW(1, 7); DS(hi = PHI(pkv, 5)); rb = cat8(lo, hi);
    VW(2, 4);
    if (__builtin_expect(nanm != 0ull, 0)) {
      unsigned m = ~0u;
#define Z_BACK() asm volatile("v_cmp_eq_u32 vcc, 0, %0\n s_cmp_lt_u32 %1, %2\n s_cselect_b64 vcc, vcc, 0\n s_cbranch_vccnz .Lzbad" : "+v"(m) : "s"(tries), "n"(CH_SPIN_CAP) : "vcc", "scc")
#pragma unroll
      for (int k = 0; k < 10; k += 2) m = sent_min(m, A[k].x, A[k + 1].x);
      Z_BACK();
#pragma unroll
      for (int k = 0; k < 10; ++k) m = sent_min(m, A[k]);
      Z_BACK();
    }
    asm volatile("" :: "s"(xs), "s"(tnx), "s"(tries), "s"(tss), "s"(snp), "s"(ts_desc), "s"(w3m), "s"(dzs), "v"(snv));
    PINS(adj);
    u32x2 g0, g1;
#define VR0(j_) DS(g0 = trrd(lds, rda + (unsigned)((j_) * 1536)))
#define VR1(j_) do { DS(g1 = trrd(lds, rda + (unsigned)((j_) * 1536 + 128))); va[j_] = (u32x4){g0.x, g0.y, g1.x, g1.y}; } while (0)
    VW(2, 6); LBAR(2);
    MV(2, A[8], ra); VR0(0); ADD1(ta, av[1][0], av[1][2]);
    VW(2, 1); VR1(0); ADD1(tb_, av[1][1], av[1][3]);
    VW(2, 3); DS(lo = PLO(pk4, 0)); CVT1(w0, ta, tb_);
    VW(2, 5); DS(LW32(ia, 1536, w0));
    MV(2, A[9], rb); DS(hi = PHI(pk4, 0)); ra = cat8(lo, hi);
    VW(2, 7); DS(lo = PLO(pk4, 1));
    OW(0, 0); DS(hi = PHI(pk4, 1)); rb = cat8(lo, hi);
    OW(1, 0); LBAR(3);
    OW(2, 0); VR0(1); ADD1(ta, av[2][0], av[2][2]);
    OW(3, 0); VR1(1); ADD1(tb_, av[2][1], av[2][3]);
    OW(4, 0); LGW(2); CVT1(w0, ta, tb_);
    MO(8, 0, ra); DS(LW32(ia, 3072, w0));
    MO(9, 0, rb); DS(lo = PLO(pk3, 0));
    OW(5, 0); DS(hi = PHI(pk3, 0)); ra = cat8(lo, hi);
    OW(6, 0); DS(lo = PLO(pk3, 1));
    OW(7, 0); LGW(3); MEMB(); __builtin_amdgcn_s_barrier(); MEMB(); SB();
    OW(0, 1); VR0(2); VR1(2);
    OW(1, 1); DS(hi = PHI(pk3, 1)); rb = cat8(lo, hi);
    OW(2, 1);
    MO(6, 1, ra); DS(lo = PLO(pk4, 2));
    OW(3, 1); DS(hi = PHI(pk4, 2)); ra = cat8(lo, hi);
#define ZSW() do { asm volatile("v_permlane32_swap_b32 %0, %1" : "+v"(w0), "+v"(w1)); SB(); } while (0)
#define ZST(i_) do { const u32x2 w_ = {w0, w1}; asm volatile("buffer_store_dwordx2 %0, %1, %2, %3 offen sc1" :: "v"(w_), "v"(zvo), "s"(z_desc), "s"(zs[i_]) : "memory"); SB(); } while (0)
    OW(0, 2);
    OW(1, 2);
    MO(7, 1, rb); DS(lo = PLO(pk4, 3));
    OW(2, 2); DS(hi = PHI(pk4, 3)); rb = cat8(lo, hi); ADD2(ta, tb_, az[0]);
    OW(3, 2); LGW(2); CVT1(w0, ta, tb_); ADD1(ta, az[1][0], az[1][2]);
    MO(8, 1, ra); DS(lo = PLO(pk4, 4)); ADDCVT(w1, ta, az[1]);
    OW(6, 2); DS(hi = PHI(pk4, 4)); ra = cat8(lo, hi); ZSW();
    OW(7, 2); ZST(0); ADD2(ta, tb_, az[2]);
    OW(4, 1); LGW(2); CVT1(w0, ta, tb_); ADD1(ta, az[3][0], az[3][2]);
    MO(9, 1, rb); DS(lo = PLO(pk4, 5)); ADDCVT(w1, ta, az[3]);
    OW(5, 1); DS(hi = PHI(pk4, 5)); rb = cat8(lo, hi); ZSW();
    OW(4, 2); ZST(1); ADD2(ta, tb_, az[6]);
    OW(5, 2); LGW(2); CVT1(w0, ta, tb_); ADD1(ta, az[7][0], az[7][2]);
    MO(8, 2, ra); LGW(0); ADDCVT(w1, ta, az[7]);
    MO(9, 2, rb);
    asm volatile("v_add_f32 v2, %2, %4\n v_permlane32_swap_b32 %0, %1\n v_add_f32 v0, %3, %5\n v_cvt_pk_bf16_f32 v0, v2, v0\n v_add_f32 v2, %6, %8\n v_add_f32 v1, %7, %9\n v_cvt_pk_bf16_f32 v1, v2, v1"
                 : "+v"(w0), "+v"(w1) : "v"(az[4][0]), "v"(az[4][1]), "v"(az[4][2]), "v"(az[4][3]), "v"(az[5][0]), "v"(az[5][1]), "v"(az[5][2]), "v"(az[5][3]) : "v0", "v1", "v2", "v3");
    { const u32x2 w_ = {w0, w1};
      asm volatile("buffer_store_dwordx2 %0, %1, %2, %3 offen sc1\n v_add_f32 v3, %6, %8\n v_permlane32_swap_b32 v0, v1\n v_add_f32 v2, %7, %9\n buffer_store_dwordx2 v[0:1], %1, %2, %4 offen sc1\n v_cvt_pk_bf16_f32 v2, v3, v2\n"
                   "v_add_f32 %10, %10, %12\n v_add_f32 v3, %11, %13\n v_cvt_pk_bf16_f32 v3, %10, v3\n s_nop 1\n v_permlane32_swap_b32 v2, v3\n buffer_store_dwordx2 v[2:3], %1, %2, %5 offen sc1"
                   :: "v"(w_), "v"(zvo), "s"(z_desc), "s"(zs[3]), "s"(zs[2]), "s"(zs[4]), "v"(az[8][0]), "v"(az[8][1]), "v"(az[8][2]), "v"(az[8][3]), "v"(az[9][0]), "v"(az[9][1]), "v"(az[9][2]), "v"(az[9][3]) : "v0", "v1", "v2", "v3", "memory"); }
    SB();
    ADAPT(dly, adj);
    if (s < 63) {
      if (s + PRE < 64) {
        const unsigned e0 = half_of(ws + O_ZB, ZSLOT((s + PRE)), 0u, pol);
        st16(rs_z, evo, e0, SENT4); st16(rs_z, evo, e0 + 16u * 16384u, SENT4); st16(rs_z, lane < 32 ? evo : 0x80000000u, e0 + 32u * 16384u, SENT4);
      }
      if (wave == 3) { tsv = ts_settle(rs_ts, tss, 0u, a.d[4]); if (tsv != ~0u && (int)(tsv - tnx) > dzs + a.d[10]) tsv += (unsigned)a.d[12]; }
    }
  }
}

#define WCK 3
#define UNAP 0
__device__ __forceinline__ void r_role(const Args& a, unsigned char* lds, const int c, const int wave) {
  char* ws = a.ws; asm volatile("" : "+s"(ws));
  unsigned* const cnt = (unsigned*)(ws + O_CNT); unsigned* const tmo = cnt + CNT(C_TMO);
  char* const X1 = ws + O_X1; char* const P = ws + O_PS;
  const int lane = lane_id(), l15 = lane & 15, lq = lane >> 4;
  const unsigned pol = wait_ge<8>(cnt + CNT(C_POL), 1u, tmo) & 1u;
  const __amdgpu_buffer_rsrc_t rs_x1 = mkrs(X1 + (c >> 5) * 16384 + (c & 31) * 256), rs_p = mkrs(P + half_of(P, 0u, 0u, pol)), rs_z = mkrs(ws + O_ZB + (size_t)c * 16384);
  const __amdgpu_buffer_rsrc_t rs_ts = mkrs(ws + O_TS + half_of(ws + O_TS, 0u, 0u, pol));
  unsigned xv = (unsigned)lane * 16u; PINV(xv);
  {
    const u32x4 ones = {~0u, ~0u, ~0u, ~0u};
    static_assert(PRE == 4, "");
#pragma unroll
    for (int i = 0; i < 8; ++i) st16(rs_z, xv + (unsigned)((i & 3) * 1024), half_of(ws + O_ZB, ZSLOT(wave), 0u, pol) + (unsigned)((i >> 2) * 4096), SENT4);
    st16(rs_p, lane < 32 ? xv : 0x80000000u, (unsigned)(PSLOT(wave) + (c >> 4) * 16384 + (c & 15) * 512), SENT4);
    st16(rs_x1, lane < 16 ? xv : 0x80000000u, half_of(X1, XSLOT((wave + 1)), 0u, pol), SENT4);
    if (c < 4) st16(rs_ts, xv, (unsigned)((c >> 1) * 16384 + (c & 1) * 4096 + wave * 1024), ones);
    if (wave == 0) {
      const int q = lane >> 2, r = (q >> 2) * 2 + (q & 1);
      const unsigned v = *(const unsigned*)(a.mem0 + r * 640 + 16 * c + 4 * (lane & 3) + (q & 2));
      __builtin_amdgcn_raw_buffer_store_b32(v == SENT ? ~0u : v, rs_x1, lane * 4, (int)half_of(X1, 0u, 0u, pol), 16);
    }
    VMW(0);
    BAR();
    if (wave != 0) return;
    if (lane == 0) __hip_atomic_fetch_add((gu32*)(cnt + CNT(C_FILL)), 1u, RLX_AGENT);
  }
  const u32x4 rs_u = mkdesc(ws + O_UO);
  const __amdgpu_buffer_rsrc_t rs_pw = mkrs(cnt + CNT(C_PW) + ((c >> 1) & 3));
  int ix = (l15 & 2) ? 0xEEEE : 0x4444; PINV(ix);
  unsigned pm = (unsigned)(lq * 256 + l15 * 8);
  int xo = (4 * lq + (l15 & 3)) * 16 + (l15 >> 2) * 4; unsigned sel = (l15 & 1) ? 0x07060302u : 0x01000504u;
  int tsa = (lane & 31) * 4;
  PINV(pm); PINV(xo); PINV(sel); PINV(tsa);
  WReg W;
#define U_LOAD(sg_, def_) SFOR(i, 16) ld_w(rs_u, xv, u_off((unsigned)(sg_), (unsigned)c) + (unsigned)((i >> 1) * 4096), (i & 1) * 2048, i, (def_) && (i & 3) == 0, W); nap4<UNAP>(); SEND
  wait_ge<8>(cnt + CNT(C_FILL), NFILL, tmo);
  wait_heads<8>(cnt + CNT(C_PZ) + ((c >> 1) & 3), 0, 1u, tmo);
  unsigned wl = __builtin_amdgcn_raw_buffer_load_b32(rs_pw, tsa * 32, 0, 16), tsl = 0; int tso = TS_OFF(lane), two = tsa * 32; PINV(tso); PINV(two); PINV(wl);
  const u32x4 ts_desc = mkdesc(ws + O_TS + half_of(ws + O_TS, 0u, 0u, pol));
  U_LOAD(0, true);
  unsigned tsv = ~0u, tpub; int dlp = a.d[2]; PINS(dlp);
  { u64 t_; RT_MARK(t_); tpub = (unsigned)t_ - 4096u; }
#pragma unroll 1
  for (int s = 0; s < 63; ++s) {
    unsigned ps[5], zs[2], xs;
#pragma unroll
    for (int j = 0; j < 5; ++j) { ps[j] = (unsigned)PSLOT(s) + (unsigned)((j >> 1) * 16384 + (j & 1) * 4096); PINS(ps[j]); }
    zs[0] = half_of(ws + O_ZB, ZSLOT(s), 0u, pol); zs[1] = zs[0] + 4096u; xs = half_of(X1, XSLOT((s + 1)), 0u, pol); PINS(zs[0]); PINS(zs[1]); PINS(xs);
    int tss = TS_STEP(s); PINS(tss);
    asm volatile("s_waitcnt vmcnt(32)" : "+v"(tsl) :: "memory");
    tsv = s < 2 ? ~0u : ts_max(tsl);
    if (__builtin_expect(s >= 2 && (tsv == ~0u || (int)(tsv - tpub) < a.d[8]), 0)) { tsv = ts_settle(rs_ts, tss, tsv, a.d[4]); }
    int wo = __all(wl >= (unsigned)(s + 2)) ? (int)0x80000000u : two; PINV(wo);
    f32x4 o = ZERO4; PINV(o);
    unsigned tis = tsv + (unsigned)dlp; (void)tis;
#define R_LIGHT() do { const unsigned ln = (unsigned)lane_id(); light(mkrs(ws + O_ZB), ln < 32u ? zs[0] + (unsigned)c * 16384u + ln * 256u : (unsigned)(O_PS - O_ZB) + half_of(P, 0u, 0u, pol) + ps[4] + (ln - 32u) * 128u, 0u, tmo); } while (0)
    if (__builtin_expect(tsv & 1u, 0)) { R_LIGHT(); } else tis = wait_rt(tsv + (unsigned)dlp);
    unsigned adj = 0, pk, tsx, wn; u64 nanm;
    u32x4 z[8], p[16]; u32x2 m[8];
#define R_LOAD() do { PINV(xv); PINV(pm); \
      _Pragma("unroll") for (int i = 0; i < 8; ++i) DS(z[i] = ld16(rs_z, xv + (unsigned)((i & 3) * 1024), zs[i >> 2])); \
      _Pragma("unroll") for (int i = 0; i < 16; ++i) DS(p[i] = ld16(rs_p, xv + (unsigned)((i & 3) * 1024), ps[i >> 2])); \
      _Pragma("unroll") for (int i = 0; i < 8; ++i) DS(m[i] = ld8(rs_p, pm + (unsigned)((i >> 1) * 1024 + (i & 1) * 128), ps[4])); \
      DS(tsx = __builtin_amdgcn_raw_buffer_load_b32(rs_ts, tso, tss, 16));        \
      DS(wn = __builtin_amdgcn_raw_buffer_load_b32(rs_pw, wo, 0, 16)); } while (0)
#define R_PROD() do { \
      SFOR(i, 16) SMFW(o, p[i], i); SEND; \
      _Pragma("unroll") for (int i = 0; i < 4; ++i) { const u32x4 a_ = {m[2 * i].x, m[2 * i].y, m[2 * i + 1].x, m[2 * i + 1].y}; const u32x8 b_ = cat8(z[2 * i], z[2 * i + 1]); SMFV(o, a_, b_); } \
      float ta, tb_; unsigned q; \
      asm volatile("s_nop 7\n v_add_f32 %2, %5, %7\n v_add_f32 %3, %6, %8\n v_cvt_pk_bf16_f32 %0, %2, %3\n v_cmp_u_f32_e64 %1, %2, %3\n s_nop 0\n" \
                   "v_mov_b32_dpp %4, %0 quad_perm:[1,0,3,2] row_mask:0xf bank_mask:0xf\n v_perm_b32 %0, %0, %4, %9" \
                   : "=&v"(pk), "=&s"(nanm), "=&v"(ta), "=&v"(tb_), "=&v"(q) : "v"(o[0]), "v"(o[1]), "v"(o[2]), "v"(o[3]), "v"(sel)); } while (0)
    R_LOAD();
    R_PROD();
    if (__builtin_expect(nanm != 0ull, 0)) {
      while (spin_check(adj, tmo)) {
        if (adj > 2u) R_LIGHT();
        else { const unsigned t_ = ts_max(tsx); if (t_ != ~0u) wait_rt(t_ + (unsigned)dlp); }
        R_LOAD();
        o = ZERO4; asm volatile("s_nop 1" : "+v"(o)); SB();
        R_PROD();
        if (nanm == 0ull) break;
        if ((adj & 7u) == 0u) {
          unsigned e = ~0u;
#pragma unroll
          for (int i = 0; i < 16; ++i) e = sent_min(e, p[i]);
#pragma unroll
          for (int i = 0; i < 8; ++i) e = sent_min(sent_min(e, z[i]), m[i].x, m[i].y);
          if (!__any(e == 0u)) break;
        }
      }
    }
    __builtin_amdgcn_raw_buffer_store_b32(pk, rs_x1, xo, (int)xs, 16);
    SB();
    PINU(tsx);
    { u64 t_; RT_MARK(t_); tpub = (unsigned)t_; }
    ADAPT(dlp, adj);
    if (s + PRE + 1 < 64) st16(rs_x1, lane < 16 ? xv : 0x80000000u, half_of(X1, XSLOT((s + PRE + 1)), 0u, pol), SENT4);
    {
      wl = max(wl, wn);
      if (__builtin_expect(!__all(wl >= (unsigned)(s + 2)), 0)) { wait_heads<4>(cnt + CNT(C_PW) + ((c >> 1) & 3), 0, (unsigned)(s + 2), tmo); }
      wait_rt(tpub + (unsigned)a.d[3]);
      asm volatile("s_nop 4\n buffer_load_dword %0, %1, %2, %3 offen sc1" : "=v"(tsl) : "v"(tso), "s"(ts_desc), "s"(TS_STEP(s + 1)) : "memory");
      U_LOAD(s + 1, false);
    }
  }
  VMW(0);
}

__device__ __forceinline__ void w_role(const Args& a, unsigned char* lds, const int h, const int wave) {
  char* ws = a.ws; asm volatile("" : "+s"(ws));
  unsigned* const cnt = (unsigned*)(ws + O_CNT); unsigned* const tmo = cnt + CNT(C_TMO);
  const int lane = lane_id(), l15 = lane & 15, lq = lane >> 4;
  WReg W;
  {
    ODMA_INIT();
    ODMA(0, 0); ODMA(1, 1); ODMA(2, 2);
#pragma unroll
    for (int i = 0; i < 5; ++i) {
      if (i < 3) VMW(24); else if (i == 3) VMW(12); else VMW(0);
      BAR();
#pragma unroll
      for (int t = 0; t < 2; ++t) {
        const unsigned ad = lds_addr(lds) + otr[t] + (unsigned)SBUF(i);
        TRO4(ad, 0, 6 * i + 3 * t); TRO4(ad, 1, 6 * i + 3 * t + 1); TRO4(ad, 2, 6 * i + 3 * t + 2);
      }
      if (i + 3 < 5) { BAR(); ODMA(i + 3, i + 3); }
    }
  }
  const __amdgpu_buffer_rsrc_t rs_u = mkrs(ws + O_UO + h * 1024);
  const __amdgpu_buffer_rsrc_t rs_pv = mkrs(cnt + CNT(C_PV + h)), rs_pw = mkrs(cnt + CNT(C_PW + h));
  unsigned xv = (unsigned)lane * 16u; int flo = lane == 0 ? 4 * wave : (int)0x80000000u; PINV(xv); PINV(flo);
  unsigned uo[10];
#pragma unroll
  for (int i = 0; i < 10; ++i) { uo[i] = (unsigned)(8 * OROT(i >> 1) + 2 * wave + (i & 1)) << 15; PINS(uo[i]); }
  const __amdgpu_buffer_rsrc_t rs_po = mkrs(cnt + CNT(C_PO)); int poo = wave == 0 ? (lane % 24) / 6 * 128 + (lane % 6) * 4 : (int)0x80000000u; PINV(poo);
  const unsigned pol = wait_ge<8>(cnt + CNT(C_POL), 1u, tmo) & 1u, kkey = K_KEY();
  const char* vsrc = ws + O_VF + (size_t)h * 32768 + (wave >> 1) * 16384 + (wave & 1) * 3072;
#define V_DMA(sg_) do { m0_set(lds_addr(lds) + (unsigned)(((sg_) % 3) * 12288 + wave * 3072)); const char* p_ = vsrc + KSLOT(sg_); asm volatile("s_nop 4" : "+s"(p_)); glds_x<0>(p_, xv); glds_x<1024>(p_, xv); glds_x<2048>(p_, xv); } while (0)
  if (TM == 0) touch_n<5>(a, 4 * h + wave, 0, 64);
  if (TM == 3) touch_n<4>(a, 4 * h + wave, 0, 96);
  BAR();
  wait_t<2>(cnt + CNT(C_PV + h), 1, 2u, tmo);
  V_DMA(1); V_DMA(2);
  VMW(3);
  int nxt = 3;
#pragma unroll 1
  for (int s = 1; s < 64; ++s) {
    unsigned ub = u_off((unsigned)s, 0u), sv = (unsigned)s, va = lds_addr(lds) + (unsigned)((s % 3) * 12288) + xv; PINS(ub); PINV(sv); PINV(va);
    f32x4 acc[2][2]; u32x4 vx[12], pw, w; unsigned sp; int vprog; bool late, far;
    BAR();
#pragma unroll
    for (int n = 0; n < 12; ++n) DS(vx[(n >> 1) + 6 * (n & 1)] = *(const LAS u32x4*)(size_t)(va + ((n >> 1) + 6 * (n & 1)) * 1024));
    SFOR(t, 11)
      SFOR(m, 12)
        constexpr int e = m >> 1, rt = m & 1, c = (t + 1) & 1;
        if (t < 10) { mf_w<false>(acc[t & 1][rt], vx[6 * rt + e], 6 * (t % 10) + e, e == 0, W); SB(); }
        else if (m == 0) { asm volatile("s_nop 7" : "+v"(acc[1][0]), "+v"(acc[1][1])); SB(); }
        if (t > 0 && m >= 2 && m < 6) { if (t < 10) FILL(); CVT1(w[(m + 2) & 3], acc[c][(m >> 1) - 1 & 1][2 * (m & 1)], acc[c][(m >> 1) - 1 & 1][2 * (m & 1) + 1]); }
        if (t > 0 && m == 6) { if (t < 10) __builtin_amdgcn_raw_buffer_store_b128(w, rs_u, (int)xv, (int)(ub + uo[(t + 9) % 10]), 16); else st16(rs_u, xv, ub + uo[9], w); SB(); }
        if (t == 0 && m == 1) { pw = ld16(rs_pv, 0u, 0u); SB(); }
        if (t == 0 && m == 4) { sp = __builtin_amdgcn_raw_buffer_load_b32(rs_po, poo, 0, 16); SB(); }
        if (t == WCK && m == 8) { vprog = (int)min_t(pw, 1); PINS(vprog); far = wave == 0 && __any((int)sp < s + 2 - WGATE); MEMB(); SB(); }
        if (t == WCK && m == 10) { __builtin_amdgcn_raw_buffer_store_b32(sv, rs_pw, flo, 0, 16); SB(); }
        if (t == WCK + 1 && m == 0) {
          late = nxt <= s + 1;
          const int upto = min(min(vprog, s + 2), 63);
          while (nxt <= upto) { V_DMA(nxt); ++nxt; }
          SB(); }
        if (t == 6 && m == 8) { pw = ld16(rs_pv, 0u, 0u); SB(); }
      SEND;
    SEND;
    if (__builtin_expect(nxt <= s + 1 || far, 0)) {
      const int upto = min(min((int)min_t(pw, 1), s + 2), 63);
      while (nxt <= upto) { V_DMA(nxt); ++nxt; }
      VMW(0); __builtin_amdgcn_raw_buffer_store_b32((unsigned)(s + 1), rs_pw, flo, 0, 16);
      if (s < 63) {
        if (far) { unsigned spins = 0; while (__any((int)__builtin_amdgcn_raw_buffer_load_b32(rs_po, poo, 0, 16) < s + 2 - WGATE) && spin_check(spins, tmo)) __builtin_amdgcn_s_sleep(24); MEMB(); }
        if (nxt <= s + 1) { wait_t<1>(cnt + CNT(C_PV + h), 1, (unsigned)(s + 1), tmo); V_DMA(nxt); ++nxt; VMW(0); }
      }
    } else if (late) asm volatile("s_waitcnt vmcnt(%0)" :: "n"(11 - WCK) : "memory");
  }
}

#define TSTRICT 24
#define LDS16(ad_) (*(LAS u32x4*)(size_t)(ad_))
struct TReg { u32x32 b[12]; u32x16 e; };
#define TR_SW(g_, X) switch (g_) { case 0: X("a", "{a[0:31]}", T.b[0]); break; case 1: X("a", "{a[32:63]}", T.b[1]); break; case 2: X("a", "{a[64:95]}", T.b[2]); break; case 3: X("a", "{a[96:127]}", T.b[3]); break; \
    case 4: X("a", "{a[128:159]}", T.b[4]); break; case 5: X("a", "{a[160:191]}", T.b[5]); break; case 6: X("a", "{a[192:223]}", T.b[6]); break; case 7: X("a", "{a[224:255]}", T.b[7]); break; \
    case 8: X("v", "{v[110:141]}", T.b[8]); break; case 9: X("v", "{v[142:173]}", T.b[9]); break; case 10: X("v", "{v[174:205]}", T.b[10]); break; case 11: X("v", "{v[206:237]}", T.b[11]); break; default: X("v", "{v[238:253]}", T.e); break; }
__device__ __forceinline__ void mf_t(f32x4& c, const u32x4 x, const int d, const bool first, const TReg& T) {
  const int r = d < 64 ? 4 * d : 4 * d - 146;
#define X_(rf_, cs_, v_) if (first) asm volatile("v_mfma_f32_16x16x32_bf16 %0, " rf_ "[%c2:%c3], %1, 0" : "=&v"(c) : "v"(x), "n"(r), "n"(r + 3), cs_(v_)); \
      else asm volatile("v_mfma_f32_16x16x32_bf16 %0, " rf_ "[%c2:%c3], %1, %0" : "+v"(c) : "v"(x), "n"(r), "n"(r + 3), cs_(v_))
  TR_SW(d >> 3, X_);
#undef X_
}
__device__ __forceinline__ void tr_t(const unsigned ad0, const unsigned ad1, const int off, const int d, TReg& T) {
  const int r = d < 64 ? 4 * d : 4 * d - 146;
#define Y_(md_, rf_, cs_, v_) asm volatile("ds_read_b64_tr_b16 " rf_ "[%c5:%c6], %1 offset:%3\n ds_read_b64_tr_b16 " rf_ "[%c7:%c8], %1 offset:%4\n ds_read_b64_tr_b16 " rf_ "[%c9:%c10], %2 offset:%3\n ds_read_b64_tr_b16 " rf_ "[%c11:%c12], %2 offset:%4\n s_waitcnt lgkmcnt(0)" \
    : md_ cs_(v_) : "v"(ad0), "v"(ad1), "n"(off), "n"(off + 1536), "n"(r), "n"(r + 1), "n"(r + 2), "n"(r + 3), "n"(r + 4), "n"(r + 5), "n"(r + 6), "n"(r + 7) : "memory")
#define X_(rf_, cs_, v_) if ((d & 7) == 0) Y_("=&", rf_, cs_, v_); else Y_("+", rf_, cs_, v_)
  TR_SW(d >> 3, X_);
#undef X_
#undef Y_
}
__device__ __forceinline__ void t_role(const Args& a, unsigned char* lds, const int h, const int isv, const int wave) {
  char* ws = a.ws; asm volatile("" : "+s"(ws));
  unsigned* const cnt = (unsigned*)(ws + O_CNT); unsigned* const tmo = cnt + CNT(C_TMO);
  const int lane = lane_id(), l15 = lane & 15, lq = lane >> 4;
  TReg T;
#define TBUF(j_) (24576u + (unsigned)(((j_) + 3) % 6) * 20480u)
#define TSRC(c_, r_) ((((c_) - (r_) * 80u < 64u ? (r_) * 80u + (((c_) - (r_) * 80u + 16u * (unsigned)h) & 63u) : (c_)) ^ ((r_) & 15u)) * 16u)
#define TDMA1(src_, dst_, i_) do { const unsigned c = (unsigned)((wave + 4 * (i_)) * 64 + lane_id()), r = c / 80u; glds(src_, TSRC(c, r), (dst_) + (i_) * 4096u); } while (0)
#define TROW(j_) (16 * ((j_) + 6))
#define TDMA(j_) do { _Pragma("unroll") for (int xi_ = 0; xi_ < 5; ++xi_) TDMA1(a.Bx + (size_t)TROW(j_) * 640, lds_addr(lds) + TBUF(j_) + (unsigned)wave * 1024u, xi_); } while (0)
  const __amdgpu_buffer_rsrc_t rs_fl = mkrs(cnt + CNT((isv ? C_PV : C_PT_) + h)), rs_o = mkrs(ws + O_KF + (size_t)h * 32768);
  unsigned vo[2], kkey;
  {
    unsigned ad0[5], ad1[5], mvo[6], qvo[3], xvo, xr, x5;
    { TRO_INIT();
#pragma unroll
      for (int t = 0; t < 5; ++t) { const bool isq = t == 4 || wave == 3; const unsigned tile = t == 4 ? 4u + (unsigned)(wave >> 1) : (wave == 3 ? (unsigned)t : (unsigned)(4 * wave + t));
        ad0[t] = lds_addr(lds) + 32768u + TRO(tile) + (isq ? 24576u : 0u); ad1[t] = lds_addr(lds) + 32768u + (isq ? TRO(tile + 6u) + 24576u : TRO(tile) + 12288u); PINV(ad0[t]); PINV(ad1[t]); } }
#pragma unroll
    for (int q = 0; q < 6; ++q) { const unsigned c = (unsigned)((wave + 4 * q) * 64 + lane), r = c / 24u, pc = (c - r * 24u) ^ (((r >> 1) & 1u) * 2u + ((r >> 3) & 1u) * 4u);
      mvo[q] = r * 12288u + 16u * pc; PINV(mvo[q]);
      if (q < 3) { qvo[q] = pc < 12u ? mvo[q] : mvo[q] + 32u * 12288u - 192u; PINV(qvo[q]); } }
    xvo = (unsigned)((8 * wave + (lane >> 3)) * 1280 + ((lane & 7) ^ (lane >> 3)) * 16);
    xr = lds_addr(lds) + (unsigned)(l15 * 128 + (lq ^ (l15 & 7)) * 16); x5 = xr + (unsigned)((wave & 1) * 2048);
    PINV(xvo); PINV(xr); PINV(x5);
    const u16* Wm = (isv ? a.Wv : a.Wk) + 192 * h; const u16* Wh = a.Wq + 192 * h + 96 * isv;
#define TW_DMA(i_) do { const int r_ = 128 * KROT((i_) >> 1) + 64 * ((i_) & 1); const unsigned d_ = lds_addr(lds) + (unsigned)(32768 + ((i_) % 3) * 36864 + wave * 1024); \
      _Pragma("unroll") for (int q_ = 0; q_ < 2; ++q_) glds(a.Bx + (32 + 32 * q_) * 640 + r_, xvo, lds_addr(lds) + (unsigned)(((i_) & 3) * 8192 + q_ * 4096 + wave * 1024)); \
      _Pragma("unroll") for (int q_ = 0; q_ < 6; ++q_) glds(Wm + (size_t)r_ * 6144, mvo[q_], d_ + (unsigned)(q_ * 4096)); \
      _Pragma("unroll") for (int q_ = 0; q_ < 3; ++q_) glds(Wh + (size_t)r_ * 6144, qvo[q_], d_ + (unsigned)(24576 + q_ * 4096)); SB(); } while (0)
    TW_DMA(0); TW_DMA(1); TW_DMA(2);
    f32x4 pa[4][4], p5[2];
    SFOR(i, 10)
      if (i < 8) VMW(22); else if (i == 8) VMW(16); else VMW(10);
      BAR();
      SFOR(t, 5)
        if (i % 3 < 2) tr_t(ad0[t], ad1[t], (i % 3) * 36864, 10 * i + 2 * t, T); else tr_t(ad0[t] + 73728u, ad1[t] + 73728u, 0, 10 * i + 2 * t, T);
      SEND;
      if (i < 9) BAR();
      if (i + 3 < 10) TW_DMA(i + 3);
      if (i == 7) TDMA(0);
      if (i == 8) TDMA(1);
      SFOR(kk, 2)
        u32x4 x_[4], y_[2];
#pragma unroll
        for (int rt = 0; rt < 4; ++rt) x_[rt] = LDS16((xr ^ (unsigned)(kk * 64)) + (i & 3) * 8192 + rt * 2048);
#pragma unroll
        for (int g = 0; g < 2; ++g) y_[g] = LDS16((x5 ^ (unsigned)(kk * 64)) + (i & 3) * 8192 + g * 4096);
        SFOR(n, 16) mf_t(pa[n >> 2][n & 3], x_[n >> 2], 10 * i + 2 * (n & 3) + kk, i == 0 && kk == 0, T); SEND;
        SFOR(g, 2) mf_t(p5[g], y_[g], 10 * i + 8 + kk, i == 0 && kk == 0, T); SEND;
        SB();
      SEND;
    SEND;
    { const unsigned pol = wait_ge<8>(cnt + CNT(C_POL), 1u, tmo) & 1u; kkey = K_KEY(); PINS(kkey); }
    const int ln = lane_id();
    vo[0] = (unsigned)ln * 16u + (wave == 3 ? (unsigned)(O_QF - O_KF) + (unsigned)(3 * isv) * 1024u : (isv ? (unsigned)(O_VF - O_KF) : 0u) + (unsigned)wave * 2048u);
    vo[1] = (unsigned)ln * 8u + (unsigned)(O_QF - O_KF) + (unsigned)(3 * isv + 2) * 1024u + (unsigned)(wave >> 1) * 512u;
    PINV(vo[0]); PINV(vo[1]);
    asm volatile("s_nop 7" : "+v"(pa[3][0]), "+v"(pa[3][1]), "+v"(pa[3][2]), "+v"(pa[3][3]), "+v"(p5[0]), "+v"(p5[1]));
#pragma unroll
    for (int g = 0; g < 2; ++g) {
#pragma unroll
      for (int n = 0; n < 4; ++n) { const f32x4 c0 = pa[2 * g + (n >> 1)][2 * (n & 1)], c1 = pa[2 * g + (n >> 1)][2 * (n & 1) + 1];
        st16(rs_o, vo[0] + (unsigned)((n & 1) * 1024), KSLOT(1 + g) + (unsigned)((n >> 1) * 16384), (u32x4){cvtpk(c0[0], c0[1]), cvtpk(c0[2], c0[3]), cvtpk(c1[0], c1[1]), cvtpk(c1[2], c1[3])}); }
      st8(rs_o, vo[1], KSLOT(1 + g) + (unsigned)((wave & 1) * 16384), cvtpk(p5[g][0], p5[g][1]), cvtpk(p5[g][2], p5[g][3]));
    }
  }
  const int lane2 = lane_id();
#define lane lane2
#define l15 (lane2 & 15)
#define lq (lane2 >> 4)
  unsigned tvo[5];
#pragma unroll
  for (int i = 0; i < 5; ++i) { const unsigned c = (unsigned)((5 * wave + i) * 64 + lane), r = c / 80u; tvo[i] = TSRC(c, r) + (unsigned)(4096 - (i - 2) * 1024); PINV(tvo[i]); }
  int flo = lane == 0 ? wave * 4 : (int)0x80000000u; PINV(flo);
  unsigned xq[4];
#pragma unroll
  for (int q = 0; q < 4; ++q) { xq[q] = lds_addr(lds) + (unsigned)TBUF(0) + (unsigned)(l15 * 1280 + ((4 * q + lq) ^ l15) * 16); PINV(xq[q]); }
#define XFRT(k_) LDS16(xq[(k_) & 3] + ((k_) >> 2) * 256)
  const unsigned fla = lds_addr(lds) + 147456u;
  unsigned flw = fla + 4u * (unsigned)lane, one = 1u; PINV(flw); PINV(one);
  if (wave == 0) *(volatile LAS unsigned*)(size_t)flw = 0u;
  unsigned gw = flw + 256u * (unsigned)(wave + 1), go = (unsigned)((lane % 24) / 6 * 128 + (lane % 6) * 4), gm = fla + 256u * (unsigned)(wave + 1), gl; PINV(gw); PINV(go); PINS(gm);
  const unsigned* gsrc = cnt + CNT(C_PO); PINS(gsrc);
  *(volatile LAS unsigned*)(size_t)gw = 0u;
  u64 gb; int gt;
  u32x4 xa[4]; u32x2 cv[5]; unsigned fl;
  f32x4 acc[5];
#pragma unroll
  for (int t = 0; t < 5; ++t) { acc[t] = ZERO4; cv[t] = (u32x2){0u, 0u}; }
  VMW(0);
  __builtin_amdgcn_raw_buffer_store_b32(2u, rs_fl, flo, 0, 16);
  BAR();
  TDMA(2); TDMA(3);
  xa[0] = XFRT(0); xa[1] = XFRT(1);
#define T_CVT(t_) do { unsigned c0_, c1_; \
      asm volatile("v_cvt_pk_bf16_f32 %0, %2, %3\n v_cvt_pk_bf16_f32 %1, %4, %5" : "=&v"(c0_), "=&v"(c1_) : "v"(acc[t_][0]), "v"(acc[t_][1]), "v"(acc[t_][2]), "v"(acc[t_][3])); \
      cv[t_] = (u32x2){c0_, c1_}; SB(); } while (0)
#define T_ST(i_) do { PINV(vo[0]); if ((i_) < 2) __builtin_amdgcn_raw_buffer_store_b128((u32x4){cv[2 * (i_)].x, cv[2 * (i_)].y, cv[(2 * (i_) + 1) % 5].x, cv[(2 * (i_) + 1) % 5].y}, rs_o, (int)vo[0] + (i_) * 1024, (int)sof, 16); \
      else __builtin_amdgcn_raw_buffer_store_b64(cv[4], rs_o, (int)vo[1], (int)sof, 16); SB(); } while (0)
#define T_REST(five_) do { asm volatile("s_nop 7\n s_nop 7"); T_CVT(1); T_CVT(2); T_CVT(3); T_CVT(4); T_ST(0); T_ST(1); if (five_) T_ST(2); VMW(0); } while (0)
  const char* dsrc = (const char*)(a.Bx + (size_t)TROW(4) * 640) - 4096;
  unsigned tb = TBUF(4) - 24576u, rb = TBUF(0) - 24576u;
  unsigned sol = 3u * K_STEP + 16384u, sof = k_fix(sol, kkey), sinc = 0u - 16384u, tgt = 4u, dstep = 20480u, fv = 0u, dq = 0u;
  const unsigned dd0 = lds_addr(lds) + 24576u + (unsigned)(5 * wave + 2) * 1024u;
#define T_MF(t_) do { mf_t(acc[t_], xa[k & 3], 10 * (k >> 1) + 2 * (t_) + (k & 1), k == 0, T); SB(); } while (0)
#define T_TILE(STRICT, FIVE) do { \
    SFOR(k, 20) \
      constexpr int k2 = k + 2, kn = k2 < 20 ? k2 : k2 - 20; \
      T_MF(0); \
      if (k != 18) { xa[k2 & 3] = XFRT(kn); SB(); } \
      if (k == 0) T_CVT(1); \
      if (k == 18) { xq[0] += dq; PINV(xq[0]); SB(); }        \
      T_MF(1); \
      if (k == 0) T_CVT(2); \
      if (k == 2 && FIVE) { asm volatile("s_mov_b32 m0, %0" :: "s"(gm) : "memory"); SB(); } \
      if (k == 14 && FIVE) { gl = *(const volatile LAS unsigned*)(size_t)gw; SB(); } \
      if (k == 17 && FIVE) {        \
        if (__builtin_expect(gb != 0ull, 0)) { unsigned spins = 0; while (__any((int)__hip_atomic_load((const gu32*)gsrc + (go >> 2), RLX_AGENT) < gt) && spin_check(spins, tmo)) __builtin_amdgcn_s_sleep(24); } \
        MEMB(); SB(); } \
      if (k == 3) { unsigned m_ = dd0 + tb; PINS(m_); asm volatile("s_mov_b32 m0, %0" :: "s"(m_) : "memory"); SB(); } \
      if (k == 16) { fl = *(const volatile LAS unsigned*)(size_t)flw; SB(); } \
      if (k == 18) {        \
        if (__builtin_expect(fs < tgt, 0)) { do fl = *(const volatile LAS unsigned*)(size_t)flw; while ((unsigned)__builtin_amdgcn_readfirstlane(fl) < tgt); } \
        MEMB(); SB(); } \
      if (k == 9) { PINS(tb); tb += 20480u; PINS(tb); SB(); } \
      if (k == 11) { PINS(rb); rb += 20480u; PINS(rb); SB(); } \
      if (k == 12) { PINS(rb); rb = rb == 6u * 20480u ? 0u : rb; PINS(rb); SB(); } \
      if (k == 13) { PINS(sol); PINS(sinc); sol = (sol + sinc) & (NK * K_STEP - 1u); PINS(sol); SB(); } \
      if (k == 15) { PINS(dstep); dstep = j < 116 ? 20480u : 0u; PINS(dstep); SB(); } \
      if (k == 10) { fv = (unsigned)(STRICT ? (j < 1 ? 4 : j + 3) : j + 2) >> 1; PINV(fv); SB(); } \
      if (k == 19) { xq[2] += dq; PINV(xq[2]); SB(); } \
      T_MF(2); \
      if (k == 0) T_CVT(3); \
      if (k == 1 || k == 2 || (k == 3 && !FIVE)) T_ST(k >= 1 && k <= 3 ? k - 1 : 0); \
      if (k == 4) glds_o<-2048>(dsrc, tvo[0]); else if (k == 5) glds_o<-1024>(dsrc, tvo[1]); else if (k == 6) glds_o<0>(dsrc, tvo[2]); else if (k == 7) glds_o<1024>(dsrc, tvo[3]); else if (k == 8) glds_o<2048>(dsrc, tvo[4]); \
      if (k == 9) { PINS(dsrc); dsrc += dstep; PINS(dsrc); SB(); } \
      if (k == 11) { PINS(tb); tb = tb == 6u * 20480u ? 0u : tb; PINS(tb); SB(); } \
      if (k == 14) { PINS(rb); dq = rb == 0u ? 0u - 5u * 20480u : 20480u; PINS(dq); SB(); } \
      if (k == 15) { PINS(sinc); sinc = sinc == 16384u ? K_STEP - 16384u : 16384u; PINS(sinc); SB(); } \
      if (k == 12) {        \
        asm volatile("s_waitcnt vmcnt(%0)" :: "n"(STRICT ? 14 : 23) : "memory"); \
        __hip_atomic_fetch_add((LAS unsigned*)(size_t)flw, one, __ATOMIC_RELAXED, __HIP_MEMORY_SCOPE_WORKGROUP); SB(); \
      } \
      if (k == 13) { __builtin_amdgcn_raw_buffer_store_b32(fv, rs_fl, flo, 0, 16); SB(); } \
      if (k == 17) { fs = (unsigned)__builtin_amdgcn_readfirstlane(fl); PINS(fs); SB(); } \
      if (k == 18) { xa[0] = XFRT(0); SB(); xq[1] += dq; PINV(xq[1]); SB(); } \
      if (k == 19) { PINS(tgt); tgt += 4u; PINS(tgt); xq[3] += dq; PINV(xq[3]); SB(); } \
      T_MF(3); \
      if (k == 0 && !FIVE) T_CVT(4); \
      if (k == 13) { sof = (sol >> 8) & 0x2000u; PINS(sof); SB(); } \
      if (k == 16) { PINS(sof); sof ^= sol ^ kkey; PINS(sof); SB(); } \
      if (k == 2 && FIVE) { asm volatile("global_load_lds_dword %0, %1 sc1" :: "v"(go), "s"(gsrc) : "memory"); SB(); } \
      if (k == 14 && FIVE) { gt = (j >> 1) + 5 - NK; PINS(gt); SB(); } \
      if (k == 15 && FIVE) { asm volatile("v_cmp_gt_i32_e64 %0, %1, %2" : "=s"(gb) : "s"(gt), "v"(gl)); SB(); } \
      if (k == 19) T_CVT(0); \
      if (FIVE) T_MF(4); \
    SEND; } while (0)
  unsigned fs;
  static_assert(TSTRICT % 2 == 0, "");
#define T_ALL(ODD) do { \
    if (ODD) { const int j = 0; T_TILE(true, false); } \
    _Pragma("unroll 1") for (int j2 = ODD; j2 < TSTRICT; j2 += 2) { \
      { const int j = j2; T_TILE(true, true); } \
      { const int j = j2 + 1; T_TILE(true, false); } } \
    _Pragma("unroll 1") for (int j2 = TSTRICT + ODD; j2 < 121; j2 += 2) { \
      const int s = j2 >> 1; (void)s; \
      { const int j = j2; T_TILE(false, true); } \
      { const int j = j2 + 1; T_TILE(false, false); } } \
    if (ODD) { const int j = 121; T_TILE(false, true); } \
    T_REST(ODD); \
    __builtin_amdgcn_raw_buffer_store_b32(64u, rs_fl, flo, 0, 16); } while (0)
  if (wave & 1) T_ALL(1); else T_ALL(0);
}
#undef lane
#undef l15
#undef lq

#define XPO 0
#define OAUX 0
__device__ __forceinline__ void o_role(const Args& a, unsigned char* lds, const int j, const int wave) {
  char* ws = a.ws; asm volatile("" : "+s"(ws));
  unsigned* const cnt = (unsigned*)(ws + O_CNT); unsigned* const tmo = cnt + CNT(C_TMO);
  const int lane = lane_id(), l15 = lane & 15, lq = lane >> 4;
  const unsigned pol = wait_ge<32>(cnt + CNT(C_POL), 1u, tmo) & 1u;
  const int xq = j / 6, c0 = 10 * xq + 5 * (j & 1), s0 = (j - 6 * xq) >> 1;
  const u32x4 rs_u = mkdesc(ws + O_UO + wave * 8192);
  const __amdgpu_buffer_rsrc_t rs_z = mkrs(ws + O_ZB + (size_t)c0 * 16384 + wave * 2048), rs_p = mkrs(ws + O_PT + wave * 16384), rs_m = mkrs(ws + O_PT + 65536 + wave * 4096), rs_out = mkrs(a.out + 640 + 16 * c0);
  unsigned xv = (unsigned)lane * 16u, xh = (unsigned)lane * 8u;
  unsigned wa = (unsigned)(wave * 10880 + lq * 272 + l15 * 16), ra[2], oo[2];
#pragma unroll
  for (int i = 0; i < 2; ++i) {
    const int n = i == 0 ? lane : 64 + l15, tok = 8 * wave + n / 10, oc = n % 10;
    ra[i] = (unsigned)((((oc >> 1) * 2 + (tok >> 4)) * 4 + 2 * (oc & 1)) * 272 + (tok & 15) * 16);
    oo[i] = i == 0 || lane < 16 ? (unsigned)((tok * 1280 + 8 * oc) * 2) : 0x80000000u;
    PINV(ra[i]); PINV(oo[i]);
  }
  PINV(xv); PINV(xh); PINV(wa);
  WReg W;
  if (TM == 2) touch_n<10>(a, 4 * j + wave, 0, 32);
  if (TM == 3) touch_n<4>(a, 192 + 4 * j + wave, 0, 96);
  if (TM == 1) {
    const int w = 4 * j + wave;
    wait_ge<32>(cnt + CNT(C_PZ + 8 * xq + (w & 7)), 1u, tmo);
    unsigned v = 0;
#pragma unroll
    for (int i = 0; i < 8; ++i) v ^= touch_rows(a, w % 3, 64 + w / 3 + 32 * i);
    v ^= touch(a.Awo, 640 * 1280, w) ^ touch(a.Awo, 640 * 1280, 96 + w);
    asm volatile("" :: "v"(v));
  }
  u32x2 wp = {0u, 0u};
  wait_ge<32>(cnt + CNT(C_FILL), NFILL, tmo);
#pragma unroll 1
  for (int s = s0; s < 64; s += 3) {
    if (wave == 0) {
      const unsigned tk = __hip_atomic_load((const gu32*)(cnt + CNT(C_TOK + 4 * s + xq)), RLX_AGENT);
      if (__any(min(wp.x, wp.y) < (unsigned)(s + 1))) {
        unsigned spins = 0;
        do wp = ld8(mkrs(cnt + CNT(s == 0 ? C_PZ : C_PW)), (unsigned)((lane & 31) * 128 + (lane >> 5) * 8), 0u); while (__any(min(wp.x, wp.y) < (unsigned)(s + 1)) && (__builtin_amdgcn_s_sleep(16), spin_check(spins, tmo)));
      }
      wait_ge<8>(cnt + CNT(C_PS + j), (unsigned)(s < 63 ? s + 1 : 63), tmo);
      *(volatile LAS unsigned*)(lds + 65536) = tk;
    }
    BAR();
    const bool behind = (unsigned)__builtin_amdgcn_readfirstlane(*(volatile LAS unsigned*)(lds + 65536)) >= 32u;
    const int np = behind || s >= 60 ? 0 : a.d[14];
    SFOR(i, 20) ld_w(rs_u, xv, u_off((unsigned)s, (unsigned)(c0 + (i >> 2))) + (unsigned)(((i >> 1) & 1) * 4096), (i & 1) * 2048, i, (i & 3) == 0, W); nap_rt(np); SEND;
    u32x4 z[5][2], p1[8], pm[2][2]; u32x2 p0[8];
    const unsigned zs = half_of(ws + O_ZB, ZSLOT(s), 0u, pol), ps = TSLOT(s);
    unsigned spins = 0;
    for (;;) {
      unsigned e = ~0u;
      PINV(xv);
#pragma unroll
      for (int n = 0; n < 10; ++n) { z[n >> 1][n & 1] = ld16(rs_z, xv + (unsigned)((n & 1) * 1024), zs + (unsigned)((n >> 1) * 16384)); e = sent_min(e, z[n >> 1][n & 1]); }
      if (!__any(e == 0u) || !spin_check(spins, tmo)) break;
      __builtin_amdgcn_s_sleep(4);
    }
    if (!behind) {
      if (wave == 0) { if (s >= 60) wait_ge<1>(cnt + CNT(C_TOK + 4 * s + xq), 32u, tmo); else wait_ge<12>(cnt + CNT(C_TOK + 4 * s + xq), 32u, tmo); }
      BAR();
    }
    PINV(xv); PINV(xh);
#pragma unroll
    for (int n = 0; n < 8; ++n) { p1[n] = ld16(rs_p, xv + (unsigned)((n & 1) * 2048 + 1024), ps + (unsigned)((n >> 1) * 4096)); p0[n] = ld8(rs_p, xh + (unsigned)((n & 1) * 2048), ps + (unsigned)((n >> 1) * 4096)); SB(); }
#pragma unroll
    for (int n = 0; n < 4; ++n) pm[n >> 1][n & 1] = ld16(rs_m, xv + (unsigned)(n * 1024), ps);
    SB();
    f32x4 acc[5][2];
    SFOR(n, 8)
      u32x4 q0 = {p0[n].x, p0[n].y, 0u, 0u}; PINV(q0);
      SFOR(t, 5) mf_w<true>(acc[t][1], p1[n], 8 * t + n, n == 0, W); SB(); SEND;
      SFOR(t, 5) mf_w<true>(acc[t][0], q0, 8 * t + n, n == 0, W); SB(); SEND;
    SEND;
#pragma unroll
    for (int n = 0; n < 4; ++n)
#pragma unroll
      for (int t = 0; t < 5; ++t) asm volatile("v_mfma_f32_16x16x32_bf16 %0, %1, %2, %0" : "+v"(acc[t][n & 1]) : "v"(z[t][n >> 1]), "v"(pm[n >> 1][n & 1]));
    asm volatile("s_nop 7" : "+v"(acc[0][1]), "+v"(acc[1][1]), "+v"(acc[2][1]), "+v"(acc[3][1]), "+v"(acc[4][1]));
#pragma unroll
    for (int n = 0; n < 10; ++n) *(f32x4*)(lds + wa + (n % 5 * 2 + n / 5) * 1088) = acc[n % 5][n / 5];
    BAR();
    if (wave == 0 && lane == 0) __hip_atomic_store((gu32*)(cnt + CNT(C_PO + xq) + j - 6 * xq), (unsigned)(s + 3), RLX_AGENT);
#pragma unroll
    for (int i = 0; i < 2; ++i) {
      f32x4 v0 = *(const f32x4*)(lds + ra[i]), v1 = *(const f32x4*)(lds + ra[i] + 272);
#pragma unroll
      for (int w = 1; w < 4; ++w) { v0 += *(const f32x4*)(lds + ra[i] + w * 10880); v1 += *(const f32x4*)(lds + ra[i] + w * 10880 + 272); }
      const u32x4 w_ = {cvtpk(v0[0], v0[1]), cvtpk(v0[2], v0[3]), cvtpk(v1[0], v1[1]), cvtpk(v1[2], v1[3])};
      __builtin_amdgcn_raw_buffer_store_b128(w_, rs_out, (int)oo[i], 32 * s * 1280 * 2, OAUX);
      asm volatile("s_nop 1" :: "v"(w_) : "memory");
    }
  }
}

#define XPF 100
__device__ __forceinline__ void att_role(const Args& a, unsigned char* lds, const int h, const int wave) {
  char* ws = a.ws;
  unsigned* const cnt = (unsigned*)(ws + O_CNT); unsigned* const tmo = cnt + CNT(C_TMO);
  const int lane = lane_id(), l15 = lane & 15, lq = lane >> 4, par = wave >> 1;
  const __amdgpu_buffer_rsrc_t rs_q = mkrs(ws + O_QF + (size_t)h * 32768), rs_k = mkrs(ws + O_KF + (size_t)h * 32768), rs_km = mkrs(ws + O_KM + (size_t)h * 3072), rs_pt = mkrs(ws + O_PT);
  const float scale = 0.07216878364870323f, LOG2E = 1.4426950408889634f;
  unsigned xv = (unsigned)lane * 16u; PINV(xv);
#define KM_EMPTY(s_) do { _Pragma("unroll") for (int i_ = 0; i_ < 3; ++i_) st16(rs_km, xv + (unsigned)(i_ * 1024), MSLOT(s_), SENT4); } while (0)
#pragma unroll 1
  for (int s = par; s < NZ; s += 2) KM_EMPTY(s);
  VMW(0);
  if (lane == 0) __hip_atomic_fetch_add((gu32*)(cnt + CNT(C_FILL)), 1u, RLX_AGENT);
  nap_rt(XPF);
  if (TM == 0) touch_n<5>(a, 128 + 2 * h + par, 0, 64);
  if (TM == 3) touch_n<4>(a, 128 + 2 * h + par, 0, 96);
  if (TM != 1) { const unsigned v = touch(a.Awo, 640 * 1280, 2 * h + par) ^ touch(a.Awo, 640 * 1280, 64 + 2 * h + par); asm volatile("" :: "v"(v)); }
  if (TM == 1) { const unsigned v = touch_rows(a, 1, 2 * h + par) ^ touch_rows(a, 0, 2 * h + par) ^ touch_rows(a, 2, 2 * h + par); asm volatile("" :: "v"(v)); }
  const unsigned pol = wait_ge<32>(cnt + CNT(C_POL), 1u, tmo) & 1u, kkey = K_KEY();
  u32x4 qf[2][6], kf[2][6];
  unsigned xh = (unsigned)lane * 8u;
#define QK_LOAD(s_) do { PINV(xv); PINV(xh); _Pragma("unroll") for (int i = 0; i < 12; ++i) { const unsigned so = KSLOT(s_) + (unsigned)(i / 6 * 16384 + i % 6 / 3 * 3072), vo = xv + (unsigned)(i % 3 * 1024); \
      kf[i / 6][i % 6] = ld16(rs_k, vo, so); \
      if (i % 3 < 2) qf[i / 6][i % 6] = ld16(rs_q, vo, so); else { const u32x2 l_ = ld8(rs_q, xh + 2048u, so), h_ = ld8(rs_q, xh + 2560u, so); qf[i / 6][i % 6] = (u32x4){l_.x, l_.y, h_.x, h_.y}; } } SB(); } while (0)
  const __amdgpu_buffer_rsrc_t rs_tv = mkrs(cnt + CNT(C_PT_ + h));
  int tvo = (lane & 3) * 4 + ((lane >> 2) & 1) * 4 * CNT(C_PV - C_PT_); PINV(tvo);
#define TV_WAIT(s_) do { unsigned spins = 0; while (__any(__builtin_amdgcn_raw_buffer_load_b32(rs_tv, tvo, 0, 16) < (unsigned)(s_)) && spin_check(spins, tmo)) __builtin_amdgcn_s_sleep(16); MEMB(); } while (0)
  unsigned kmo = (unsigned)(lq * 128 + ((l15 & 1) + (l15 & 2) * 2 + ((l15 >> 1) & 2)) * 16), tlast = 0; PINV(kmo);
  if (par == 0) wait_ge<32>(cnt + CNT(C_PS + h), 2u, tmo); else TV_WAIT(1);
  QK_LOAD(par);
#pragma unroll 1
  for (int s = par; s < 64; s += 2) {
    f32x4 sc[2][3];
    float mx[2];
    {
#pragma unroll
      for (int q = 0; q < 2; ++q) {
        mx[q] = -3.0e38f;
#pragma unroll
        for (int t = 0; t <= q; ++t) {
          sc[q][t] = ZERO4;
#pragma unroll
          for (int kk = 0; kk < 6; ++kk) sc[q][t] = mfma(kf[t][kk], qf[q][kk], sc[q][t]);
#pragma unroll
          for (int r = 0; r < 4; ++r) {
            const float v = rbf(rbf(sc[q][t][r]) * scale);
            sc[q][t][r] = (t < q || 4 * lq + r <= l15) ? v : -3.0e38f;
            mx[q] = fmaxf(mx[q], sc[q][t][r]);
          }
        }
        PINV(mx[q]);
      }
    }
    SB();
    unsigned tv;
    {
      u32x4 km[6];
      unsigned spins = 0;
      const unsigned t0 = wait_rt(tlast + (unsigned)a.d[15]);
      for (;;) {
        unsigned m = ~0u;
        PINV(kmo);
        tv = __builtin_amdgcn_raw_buffer_load_b32(rs_tv, tvo, 0, 16);
#pragma unroll
        for (int kk = 0; kk < 6; ++kk) { km[kk] = ld16(rs_km, kmo + (unsigned)(kk * 512), MSLOT(s)); m = sent_min(m, km[kk]); }
        if (!__any(m == 0u) || !spin_check(spins, tmo)) break;
        if (spins > 4u) __builtin_amdgcn_s_sleep(12); else __builtin_amdgcn_s_sleep(2);
      }
      tlast = t0 - 100u;
      if (spins) { u64 t_; RT_MARK(t_); tlast = (unsigned)t_; }
#pragma unroll
      for (int q = 0; q < 2; ++q) {
        sc[q][2] = ZERO4;
#pragma unroll
        for (int kk = 0; kk < 6; ++kk) sc[q][2] = mfma(km[kk], qf[q][kk], sc[q][2]);
      }
    }
    asm volatile("" : "+v"(sc[0][2]), "+v"(sc[1][2]));
    const bool ahead = s + 2 < 64 && !__any(tv < (unsigned)(s + 2));
    if (ahead) QK_LOAD(s + 2);
#pragma unroll
    for (int q = 0; q < 2; ++q) {
#pragma unroll
      for (int r = 0; r < 4; ++r) { sc[q][2][r] = rbf(rbf(sc[q][2][r]) * scale); mx[q] = fmaxf(mx[q], sc[q][2][r]); }
      const float m = max32(max16(mx[q])), nml = -m * LOG2E;
      float sum = 0.f, sm = 0.f;
#pragma unroll
      for (int t = 0; t <= q; ++t)
#pragma unroll
        for (int r = 0; r < 4; ++r) { const float e = (t < q || 4 * lq + r <= l15) ? __builtin_amdgcn_exp2f(__builtin_fmaf(sc[q][t][r], LOG2E, nml)) : 0.f; sc[q][t][r] = e; sum += e; }
#pragma unroll
      for (int r = 0; r < 4; ++r) { sc[q][2][r] = __builtin_amdgcn_exp2f(__builtin_fmaf(sc[q][2][r], LOG2E, nml)); sm += sc[q][2][r]; }
      sum = sum32(sum16(__builtin_fmaf(sm, 0.5f, sum)));
      const float inv = 1.0f / sum;
      if (q == 0) st8(rs_pt, (unsigned)(h * 2048 + lane * 8), TSLOT(s), cvtpk(sc[0][0][0] * inv, sc[0][0][1] * inv), cvtpk(sc[0][0][2] * inv, sc[0][0][3] * inv));
      else st16(rs_pt, (unsigned)(h * 2048 + 1024 + lane * 16), TSLOT(s), (u32x4){cvtpk(sc[1][0][0] * inv, sc[1][0][1] * inv), cvtpk(sc[1][0][2] * inv, sc[1][0][3] * inv), cvtpk(sc[1][1][0] * inv, sc[1][1][1] * inv), cvtpk(sc[1][1][2] * inv, sc[1][1][3] * inv)});
      st8(rs_pt, lq < 2 ? (unsigned)(65536 + (h >> 2) * 2048 + q * 1024 + (h & 3) * 256 + l15 * 16 + lq * 8) : 0x80000000u, TSLOT(s), cvtpk(sc[q][2][0] * inv, sc[q][2][1] * inv), cvtpk(sc[q][2][2] * inv, sc[q][2][3] * inv));
    }
    if (s + NZ < 64) KM_EMPTY(s);
    VMW(0);
    if (lane < 4) __hip_atomic_fetch_add((gu32*)(cnt + CNT(C_TOK + 4 * s + lane)), 1u, RLX_AGENT);
    if (!ahead && s + 2 < 64) { TV_WAIT(s + 2); QK_LOAD(s + 2); }
  }
}

__device__ __forceinline__ float dot2bf(unsigned x, unsigned y, float c) { return __builtin_amdgcn_fdot2_f32_bf16(__builtin_bit_cast(bf16x2_t, x), __builtin_bit_cast(bf16x2_t, y), c, false); }
__device__ __forceinline__ void a_role(const Args& a, unsigned char* lds, const int j, const int wave) {
  char* ws = a.ws; asm volatile("" : "+s"(ws));
  unsigned* const cnt = (unsigned*)(ws + O_CNT); unsigned* const tmo = cnt + CNT(C_TMO);
  const int lane = lane_id(), l15 = lane & 15, lq = lane >> 4;
  const int c0 = 40 * j - 8 * (j & 1);
  u32x4 wt[3][20];
#define A_WDMA(src_, ld_, off_) do { const int ln_ = lane_id(); _Pragma("unroll") for (int q_ = 0; q_ < 15; ++q_) { const int ins = wave + 4 * q_; const unsigned c = (unsigned)(ins * 64 + ln_), r = c / 6u, pc = c - r * 6u; \
      glds(src_, r * (unsigned)((ld_) * 2) + 16u * pc, lds_addr(lds) + (unsigned)((off_) + ins * 1024)); } } while (0)
#define A_WREAD(off_) do { unsigned o_ = (unsigned)((off_) + (8 * lq + (l15 >> 2)) * 96 + 8 * (l15 & 3)); PINV(o_); \
    _Pragma("unroll") for (int t = 0; t < 3; ++t) { _Pragma("unroll") for (int kk = 0; kk < 20; ++kk) wt[t][kk] = trrd2(lds, o_ + (unsigned)(kk * 3072 + 32 * t), 384u); SB(); } } while (0)
  A_WDMA(a.Awqkv + c0, 1920, 0); A_WDMA(a.Awqkv + 640 + c0, 1920, 61440);
  VMW(0); BAR();
  if (wave < 2) A_WREAD(wave * 61440);
  BAR();
  A_WDMA(a.Awqkv + 1280 + c0, 1920, 0);
  VMW(0); BAR();
  if (wave == 2) A_WREAD(0);
  BAR();
  constexpr int LX = 0, LR = 122880, RS = 304, NR = 128;
  unsigned dvo[10];
#pragma unroll
  for (int i = 0; i < 10; ++i) { const unsigned c = (unsigned)((wave + 4 * i) * 64 + lane), r = c / 80u; dvo[i] = (c ^ (r & 15u)) * 16u; PINV(dvo[i]); }
#define A_XDMA(src_, b_) do { _Pragma("unroll") for (int xi_ = 0; xi_ < 10; ++xi_) glds(src_, dvo[xi_], lds_addr(lds) + (unsigned)(LX + ((b_) & 1) * 40960 + wave * 1024 + xi_ * 4096)); } while (0)
  unsigned xr[4];
#pragma unroll
  for (int q = 0; q < 4; ++q) xr[q] = (unsigned)(LX + l15 * 1280 + ((4 * q + lq) ^ l15) * 16);
  unsigned dv3[14];
#pragma unroll
  for (int i = 0; i < 14; ++i) { const unsigned c = (unsigned)((13 * (wave < 3 ? wave : 0) + i) * 64 + lane), r = c / 80u; dv3[i] = (c ^ (r & 15u)) * 16u + (unsigned)(4096 - (i % 7 - 3) * 1024); PINV(dv3[i]); }
  const unsigned am0 = lds_addr(lds) + (unsigned)(LX + (13 * wave + 3) * 1024);
#define A_XD3(src_, lb_, i_) do { if ((i_) % 7 == 0) m0_set(am0 + (lb_) + (unsigned)((i_) / 7 * 7168)); \
    switch ((i_) % 7) { case 0: glds_o<-3072>(src_, dv3[i_]); break; case 1: glds_o<-2048>(src_, dv3[i_]); break; case 2: glds_o<-1024>(src_, dv3[i_]); break; case 3: glds_o<0>(src_, dv3[i_]); break; \
      case 4: glds_o<1024>(src_, dv3[i_]); break; case 5: glds_o<2048>(src_, dv3[i_]); break; default: glds_o<3072>(src_, dv3[i_]); break; } } while (0)
  A_XDMA(a.Ax, 0);
  A_XDMA(a.Ax + 32 * 640, 1);
  unsigned wo_[3];
#pragma unroll
  for (int t = 0; t < 3; ++t) { const int v = 4 * t + lq - 2 * (j & 1); wo_[t] = (unsigned)(LR + l15 * RS + wave * 96 + (v < 0 ? 40 + (v + 2) * 48 : (v >= 10 ? 40 + (v - 10) * 48 : (v >= 5 ? 8 : 0) + v * 8))); }
  const __amdgpu_buffer_rsrc_t rs_oa = mkrs(ws + O_OA);
#pragma unroll 1
  for (int b = 0; b <= 64; ++b) {
    const int s = b; (void)s;
    if (b == 0) VMW(0); else if (wave < 3) VMW(14);
    BAR();
    const char* xsrc = (const char*)(a.Ax + (size_t)(32 * (b < 62 ? b + 2 : 63)) * 640) - 4096;
    const unsigned xb = (unsigned)(b % 3) * 40960u, xb2 = (unsigned)((b + 2) % 3) * 40960u;
    if (wave < 3) {
      if (b < 64) {
        unsigned xq[4];
#pragma unroll
        for (int q = 0; q < 4; ++q) { xq[q] = xr[q] + xb; PINV(xq[q]); }
#define A_XF(rt_, k_) (*(const u32x4*)(lds + xq[(k_) & 3] + (rt_) * 20480 + ((k_) >> 2) * 256))
        f32x4 acc[2][3]; u32x4 xa[3][2];
#pragma unroll
        for (int k = 0; k < 2; ++k) { xa[k][0] = A_XF(0, k); xa[k][1] = A_XF(1, k); }
#pragma unroll
        for (int k = 0; k < 20; ++k) {
          if (k + 2 < 20) { xa[(k + 2) % 3][0] = A_XF(0, k + 2); xa[(k + 2) % 3][1] = A_XF(1, k + 2); }
#pragma unroll
          for (int t = 0; t < 3; ++t) { acc[0][t] = mfma(wt[t][k], xa[k % 3][0], k == 0 ? ZERO4 : acc[0][t]); acc[1][t] = mfma(wt[t][k], xa[k % 3][1], k == 0 ? ZERO4 : acc[1][t]);
            if (t == 1 && k < 14) { asm volatile("" : "+a"(acc[0][0]), "+a"(acc[0][1]), "+a"(acc[1][0]), "+a"(acc[1][1])); A_XD3(xsrc, xb2, k < 14 ? k : 0); SB(); } }
          if (k >= 14) IL(6, 1);
          asm volatile("" : "+a"(acc[0][0]), "+a"(acc[0][1]), "+a"(acc[0][2]), "+a"(acc[1][0]), "+a"(acc[1][1]), "+a"(acc[1][2])); SB();
        }
        const unsigned ro = (unsigned)(((32 * b) & (NR - 1)) * RS);
#pragma unroll
        for (int t = 0; t < 3; ++t)
#pragma unroll
          for (int rt = 0; rt < 2; ++rt) *(u32x2*)(lds + wo_[t] + ro + rt * 16 * RS) = (u32x2){cvtpk(acc[rt][t][0], acc[rt][t][1]), cvtpk(acc[rt][t][2], acc[rt][t][3])};
      }
    } else {
      const int row = 32 * (b > 0 ? b - 1 : 0) + (lane & 31), hh = lane >> 5;
      const unsigned ho = (unsigned)(LR + 48 * hh);
      unsigned ra[8];
#pragma unroll
      for (int w = 0; w < 8; ++w) { const int p = row - 2 * w; ra[w] = ho + (unsigned)(((p > 0 ? p : 0) & (NR - 1)) * RS); }
#define A_LD10(d_, ad_) do { const u32x4 x0 = *(const u32x4*)(lds + (ad_)), x1 = *(const u32x4*)(lds + (ad_) + 16); const u32x2 x2 = *(const u32x2*)(lds + (ad_) + 32); \
        d_[0] = x0.x; d_[1] = x0.y; d_[2] = x0.z; d_[3] = x0.w; d_[4] = x1.x; d_[5] = x1.y; d_[6] = x1.z; d_[7] = x1.w; d_[8] = x2.x; d_[9] = x2.y; } while (0)
      unsigned q[10], kv[8][10], vv[8][10];
      A_LD10(q, ra[0]);
#pragma unroll
      for (int w = 0; w < 8; ++w) A_LD10(kv[w], ra[w] + 96);
#pragma unroll
      for (int w = 0; w < 8; ++w) A_LD10(vv[w], ra[w] + 192);
      SB();
      float lg[8], mx = -3.0e38f;
#pragma unroll
      for (int w = 0; w < 8; ++w) lg[w] = 0.f;
#pragma unroll
      for (int i = 0; i < 10; ++i)
#pragma unroll
        for (int w = 0; w < 8; ++w) lg[w] = dot2bf(q[i], kv[w][i], lg[w]);
#pragma unroll
      for (int w = 0; w < 8; ++w) { lg[w] = row - 2 * w >= 0 ? rbf(rbf(lg[w]) * 0.22360679774997896f) : rbf(-1000000000.0f); mx = fmaxf(mx, lg[w]); }
      float sum = 0.f;
#pragma unroll
      for (int w = 0; w < 8; ++w) { lg[w] = __expf(lg[w] - mx); sum += lg[w]; }
      const float inv = __builtin_amdgcn_rcpf(sum);
      float ov[20];
#pragma unroll
      for (int d = 0; d < 20; ++d) ov[d] = 0.f;
#pragma unroll
      for (int w = 0; w < 8; w += 2) {
        const unsigned pw = cvtpk(lg[w] * inv, lg[w + 1] * inv);
#pragma unroll
        for (int i = 0; i < 10; ++i) {
          ov[2 * i] = dot2bf(__builtin_amdgcn_perm(vv[w + 1][i], vv[w][i], 0x05040100u), pw, ov[2 * i]);
          ov[2 * i + 1] = dot2bf(__builtin_amdgcn_perm(vv[w + 1][i], vv[w][i], 0x07060302u), pw, ov[2 * i + 1]);
        }
      }
      const unsigned oo = b > 0 ? (unsigned)((row * 640 + 40 * j + 20 * hh) * 2) : 0x80000000u;
#pragma unroll
      for (int i = 0; i < 5; ++i) st8(rs_oa, oo + 8u * i, 0u, cvtpk(ov[4 * i], ov[4 * i + 1]), cvtpk(ov[4 * i + 2], ov[4 * i + 3]));
      if (b > 0 && (b & 15) == 0) { VMW(0); if (lane == 0) __hip_atomic_fetch_add((gu32*)(cnt + CNT(C_AO + (b >> 4) - 1)), 1u, RLX_AGENT); }
    }
  }
  VMW(0);
}
__device__ __forceinline__ void a2_role(const Args& a, unsigned char* lds, const int j, const int wave) {
  char* ws = a.ws; asm volatile("" : "+s"(ws));
  unsigned* const cnt = (unsigned*)(ws + O_CNT); unsigned* const tmo = cnt + CNT(C_TMO);
  VMW(0);
  BAR();
  {
    const int ln = lane_id(), m15 = ln & 15, mq = ln >> 4, cg = j & 3, rg = j >> 2;
    WReg W;
    {
      unsigned wv[10];
#pragma unroll
      for (int q = 0; q < 10; ++q) { const unsigned c = (unsigned)((wave + 4 * q) * 64 + ln), r = c / 20u; wv[q] = r * 1280u + (c - r * 20u) * 16u; PINV(wv[q]); }
#define A2_WDMA(i_) do { _Pragma("unroll") for (int q_ = 0; q_ < 10; ++q_) glds(a.Awo + (size_t)(128 * (i_)) * 640 + 160 * cg, wv[q_], lds_addr(lds) + (unsigned)(((i_) & 3) * 40960 + (wave + 4 * q_) * 1024)); } while (0)
      A2_WDMA(0); A2_WDMA(1); A2_WDMA(2); A2_WDMA(3);
      const unsigned rb = lds_addr(lds) + (unsigned)((8 * mq + (m15 >> 2)) * 320);
      const unsigned ad[3] = {rb + (unsigned)(64 * wave + 16 * (m15 & 3)), rb + (unsigned)(64 * wave + 16 * (m15 & 3) + 8), rb + (unsigned)(256 + 32 * (wave >> 1) + 8 * (m15 & 3))};
      SFOR(i, 5)
        if (i < 2) VMW(30); else if (i == 2) VMW(20); else if (i == 3) VMW(10); else VMW(0);
        BAR();
        SFOR(n, 6) tr4_w(ad[n >> 1] + (unsigned)((i & 3) * 40960), (n & 1) * 20480, (n & 1) * 20480 + 1280, (n & 1) * 20480 + 10240, (n & 1) * 20480 + 11520, 6 * i + n, W); SEND;
        if (i == 0) { BAR(); A2_WDMA(4); }
      SEND;
    }
    unsigned dv[10], xq[4], x5[4];
#pragma unroll
    for (int i = 0; i < 10; ++i) { const unsigned c = (unsigned)((10 * wave + i) * 64 + ln), r = c / 80u; dv[i] = (c ^ (r & 15u)) * 16u + (unsigned)(4096 - (i % 5 - 2) * 1024); PINV(dv[i]); }
    const char* src = ws + O_OA + (size_t)(128 * rg) * 1280 - 4096;
    const unsigned dm = lds_addr(lds) + (unsigned)((10 * wave + 2) * 1024);
#define A2_X(p_, lb_, i_) do { if ((i_) % 5 == 0) m0_set(dm + (lb_) + (unsigned)((i_) / 5 * 5120)); \
      switch ((i_) % 5) { case 0: glds_x<-2048>(p_, dv[i_]); break; case 1: glds_x<-1024>(p_, dv[i_]); break; case 2: glds_x<0>(p_, dv[i_]); break; case 3: glds_x<1024>(p_, dv[i_]); break; default: glds_x<2048>(p_, dv[i_]); break; } } while (0)
    const __amdgpu_buffer_rsrc_t rs_out = mkrs(a.out + (size_t)(128 * rg) * 1280 + 160 * cg);
    unsigned o2 = (unsigned)((m15 * 1280 + 32 * wave + 8 * mq) * 2), o5 = (unsigned)(((16 * (wave & 1) + m15) * 1280 + 128 + 16 * (wave >> 1) + 4 * mq) * 2); PINV(o2); PINV(o5);
    if (wave == 0) wait_ge<16>(cnt + CNT(C_AO + (rg >> 2)), 16u, tmo);
    BAR();
#pragma unroll
    for (int b = 0; b < 4; ++b) { const char* p_ = src + b * 40960; asm volatile("s_nop 4" : "+s"(p_));
#pragma unroll
      for (int i = 0; i < 10; ++i) A2_X(p_, (unsigned)(b * 40960), i); }
#pragma unroll 1
    for (int b = 0; b < 4; ++b) {
      if (b == 0) VMW(30); else if (b == 1) VMW(23); else if (b == 2) VMW(16); else VMW(9);
      BAR();
#pragma unroll
      for (int q = 0; q < 4; ++q) { xq[q] = lds_addr(lds) + (unsigned)((b & 3) * 40960 + m15 * 1280 + ((4 * q + mq) ^ m15) * 16); x5[q] = xq[q] + (unsigned)((wave & 1) * 20480); PINV(xq[q]); PINV(x5[q]); }
      f32x4 acc[5]; u32x4 xa[3][3];
#define A2_XF(n_, k_) LDS16(((n_) == 2 ? x5[(k_) & 3] : xq[(k_) & 3] + (n_) * 20480) + ((k_) >> 2) * 256)
#pragma unroll
      for (int n = 0; n < 6; ++n) xa[n / 3][n % 3] = A2_XF(n % 3, n / 3);
      SFOR(k, 20)
        SFOR(n, 5)
          mf_w<true>(acc[n], xa[k % 3][n >> 1], 12 * (k >> 2) + 4 * (n == 4 ? 2 : (n & 1)) + (k & 3), k == 0, W); SB();
          if (n < 3 && k + 2 < 20) { xa[(k + 2) % 3][n] = A2_XF(n, k + 2); SB(); }
        SEND;
      SEND;
      asm volatile("s_nop 7" : "+v"(acc[0]), "+v"(acc[1]), "+v"(acc[2]), "+v"(acc[3]), "+v"(acc[4]));
#pragma unroll
      for (int rt = 0; rt < 2; ++rt) { const u32x4 w = {cvtpk(acc[2 * rt][0], acc[2 * rt][1]), cvtpk(acc[2 * rt][2], acc[2 * rt][3]), cvtpk(acc[2 * rt + 1][0], acc[2 * rt + 1][1]), cvtpk(acc[2 * rt + 1][2], acc[2 * rt + 1][3])};
        __builtin_amdgcn_raw_buffer_store_b128(w, rs_out, (int)o2, (32 * b + 16 * rt) * 2560, OAUX); asm volatile("s_nop 1" :: "v"(w) : "memory"); }
      __builtin_amdgcn_raw_buffer_store_b64((u32x2){cvtpk(acc[4][0], acc[4][1]), cvtpk(acc[4][2], acc[4][3])}, rs_out, (int)o5, 32 * b * 2560, OAUX);
    }
    VMW(0);
  }
}

#define CALN 6
__device__ __forceinline__ void calibrate(char* ws, int* vote, const int wave) {
  char* cal = ws + O_CAL + wave * 16384;
  unsigned best0 = ~0u, best1 = ~0u, acc = 0;
  const int lane = lane_id();
  for (int r = 0; r < CALN; ++r) {
#pragma unroll
    for (int hf = 0; hf < 2; ++hf) {
      const unsigned long long t0 = __builtin_amdgcn_s_memtime();
      if (lane == 0) acc += __hip_atomic_fetch_add((gu32*)(cal + hf * 8192 + (acc & 0u)), 1u, RLX_AGENT);
      asm volatile("s_waitcnt vmcnt(0)" ::: "memory");
      const unsigned d = (unsigned)(__builtin_amdgcn_s_memtime() - t0);
      if (hf == 0) best0 = d < best0 ? d : best0; else best1 = d < best1 ? d : best1;
    }
  }
  int dl = (int)best1 - (int)best0 + (int)(acc & 0u);
  dl = dl > 400 ? 400 : (dl < -400 ? -400 : dl);
  if ((((unsigned)((size_t)cal >> 21) ^ xcc_side()) & 1u) != 0u) dl = -dl;
  if (lane == 0) vote[wave] = dl;
  __syncthreads();
  if (wave == 0 && lane == 0) __hip_atomic_store((gu32*)((unsigned*)(ws + O_CNT) + CNT(C_POL)), vote[0] + vote[1] + vote[2] + vote[3] < 0 ? 3u : 2u, RLX_AGENT);
  __syncthreads();
}

#define TCH 32
#define XOFF 0
#define CPF 3
__global__ __launch_bounds__(256, 1) void k_mega(Args a) {
  extern __shared__ __attribute__((aligned(16))) unsigned char shm[];
  volatile LAS int* sl = (volatile LAS int*)shm;
  if (threadIdx.x >= 64) {
    u64 pc; unsigned off, len;
    asm volatile("s_getpc_b64 %0\n.Lcpf_mega:\n s_mov_b32 %1, .Lcpf_mega-_Z6k_mega4Args\n s_mov_b32 %2, _Z6k_prepPcPKcS1_S1_S1_S1_-_Z6k_mega4Args" : "=s"(pc), "=s"(off), "=s"(len));
    const __amdgpu_buffer_rsrc_t rs = __builtin_amdgcn_make_buffer_rsrc((void*)(pc - off), 0, (int)len, 0x00020000);
    const unsigned g = ((blockIdx.x >> 3) * 192u + threadIdx.x - 64u) * 16u;
    const u32x4 v0 = __builtin_amdgcn_raw_buffer_load_b128(rs, (int)g, 0, 0), v1 = __builtin_amdgcn_raw_buffer_load_b128(rs, (int)g, 98304, 0);
    asm volatile("" :: "v"(v0), "v"(v1));
  }
  if (threadIdx.x >= 64) {
    const unsigned o = (threadIdx.x - 64u) * (unsigned)(TCH * 65536) + (blockIdx.x >> 3) * 2048u;
    const unsigned v = __builtin_amdgcn_raw_buffer_load_b32(__builtin_amdgcn_make_buffer_rsrc((void*)(a.ws + O_X1), 0, (int)(O_END - O_X1), 0x00020000), (int)o, 0, 0);
    asm volatile("" :: "v"(v));
  }
  if (threadIdx.x == 0) {
    const unsigned x0 = (unsigned)__builtin_amdgcn_s_getreg((3 << 11) | 20) & 7u;
    int slot = 0;
    for (unsigned i = 0; i < 8u; ++i) {
      const unsigned x = (x0 + i) & 7u;
      const unsigned t = __hip_atomic_fetch_add((gu32*)((unsigned*)(a.ws + O_CNT) + CNT(C_SLOT + x)), 1u, RLX_AGENT);
      if (t < 32u) { slot = (int)(x * 32u + t); break; }
    }
    *sl = slot;
  }
  __syncthreads();
  const int slot = __builtin_amdgcn_readfirstlane(*sl);
  __syncthreads();
  const int xq = (slot >> 5) & 3, kq = slot & 31, wave = __builtin_amdgcn_readfirstlane(threadIdx.x >> 6);
#define ENTER __builtin_amdgcn_s_waitcnt(0)
  const int h = xq * 8 + (kq & 7);
  if (slot < 128) {
    if (kq < 8) { ENTER; s_role(a, shm, h, wave);
    } else if (kq < 16) { ENTER; z_role(a, shm, h, wave); }
    else if (kq < 26) { ENTER; r_role(a, shm, xq * 10 + kq - 16, wave); }
    else { if (slot == 26) calibrate(a.ws, (int*)shm, wave); ENTER; if (!(XOFF & 1)) o_role(a, shm, 6 * xq + kq - 26, wave); }
  } else {
    if (kq < 16) { ENTER; t_role(a, shm, h, kq >> 3, wave); ENTER; if (!(XOFF & 4)) a2_role(a, shm, 16 * xq + kq, wave); }
    else if (kq < 24) { ENTER; w_role(a, shm, h, wave); }
    else if (kq < 28) { ENTER; if (!(XOFF & 4)) a_role(a, shm, xq * 4 + kq - 24, wave); }
    else { ENTER; if (!(XOFF & 2)) att_role(a, shm, xq * 8 + (kq - 28) * 2 + (wave & 1), wave); }
  }
}

#define PFW 3
#define PFN 15
constexpr int PB0 = (PFW & 1) ? 1920 / PFN : 0, PB1 = (PFW & 2) ? 1920 / PFN : 0, PB2 = (PFW & 4) ? 1920 / PFN : 0, PB3 = (PFW & 8) ? 1920 / PFN : 0, PF_B = PB0 + PB1 + PB2 + PB3;
__global__ __launch_bounds__(256) void k_prep(char* ws, const char* w0, const char* w1, const char* w2, const char* w3, const char* w4) {
  if (blockIdx.x < 15) { ((u32x4*)(ws + O_CNT))[blockIdx.x * 256 + threadIdx.x] = (u32x4){0u, 0u, 0u, 0u}; return; }
  if (blockIdx.x < 15 + PF_B) {
    const unsigned bi = blockIdx.x - 15;
    const char* w = bi < PB0 ? w0 : (bi < PB0 + PB1 ? w1 : (bi < PB0 + PB1 + PB2 ? w2 : w3));
    const unsigned bb = bi < PB0 ? bi : (bi < PB0 + PB1 ? bi - PB0 : (bi < PB0 + PB1 + PB2 ? bi - PB0 - PB1 : bi - PB0 - PB1 - PB2));
    const unsigned g = bb * (PFN * 4096u) + threadIdx.x * 16u;
    const __amdgpu_buffer_rsrc_t rs = __builtin_amdgcn_make_buffer_rsrc((void*)w, 0, 1920 * 4096, 0x00020000);
    u32x4 v[PFN];
#pragma unroll
    for (int i = 0; i < PFN; ++i) v[i] = __builtin_amdgcn_raw_buffer_load_b128(rs, (int)g, i * 4096, 0);
    __builtin_amdgcn_sched_barrier(0);
    u32x4 s = v[0];
#pragma unroll
    for (int i = 1; i < PFN; ++i) s ^= v[i];
    asm volatile("" :: "v"(s));
    return;
  }
  {
    u64 pc; unsigned off, len;
    asm volatile("s_getpc_b64 %0\n.Lcpf_prep:\n s_mov_b32 %1, .Lcpf_prep-_Z6k_mega4Args\n s_mov_b32 %2, _Z6k_prepPcPKcS1_S1_S1_S1_-_Z6k_mega4Args" : "=s"(pc), "=s"(off), "=s"(len));
    const unsigned g = min(((blockIdx.x - 15 - PF_B) * 256u + threadIdx.x) * 16u, len - 16u);
    u32x4 v; asm volatile("global_load_dwordx4 %0, %1, %2" : "=v"(v) : "v"(g), "s"(pc - off));
  }
}
extern "C" void kernel_launch(void* const* d_in, const int* in_sizes, int n_in, void* d_out, int out_size, void* d_ws, size_t ws_size, hipStream_t stream) {
  Args a;
  a.Ax = (const u16*)d_in[0]; a.Awqkv = (const u16*)d_in[1]; a.Awo = (const u16*)d_in[2]; a.Bx = (const u16*)d_in[3]; a.mem0 = (const u16*)d_in[4];
  a.Wq = (const u16*)d_in[5]; a.Wk = (const u16*)d_in[6]; a.Wv = (const u16*)d_in[7]; a.Wo = (const u16*)d_in[8];
  a.out = (u16*)d_out;
  a.ws = (char*)(((size_t)d_ws + 2 * MB - 1) & ~(size_t)(2 * MB - 1));
  if ((size_t)(a.ws - (char*)d_ws) + O_END > ws_size) return;
  const int dd[16] = {DDV};
  for (int i = 0; i < 16; ++i) a.d[i] = dd[i];
  static_assert(CNT(C_TOTAL) * 4 == 15 * 4096, "");
  k_prep<<<15 + PF_B + ((CPF & 2) ? 48 : 0), 256, 0, stream>>>(a.ws, (const char*)a.Wq, (const char*)a.Wk, (const char*)a.Wv, (const char*)a.Wo, (const char*)a.Bx);
  (void)hipFuncSetAttribute((const void*)k_mega, hipFuncAttributeMaxDynamicSharedMemorySize, 163840);
  k_mega<<<256, 256, 163840, stream>>>(a);
}
```

```cpp
#include <hip/hip_runtime.h>
#include <hip/hip_bf16.h>
#include <stdint.h>

typedef unsigned short u16;
typedef unsigned long long u64;
typedef __attribute__((ext_vector_type(4))) float f32x4;
typedef __attribute__((address_space(1))) u64 gu64;
typedef __attribute__((address_space(1))) unsigned gu32;
typedef unsigned u32x8 __attribute__((ext_vector_type(8)));
typedef unsigned u32x4 __attribute__((ext_vector_type(4)));
typedef unsigned u32x2 __attribute__((ext_vector_type(2)));
typedef __attribute__((ext_vector_type(4))) short bf16x4;
typedef __attribute__((ext_vector_type(8))) __bf16 bfv8;
typedef __attribute__((ext_vector_type(16))) __bf16 bfv16;
typedef __bf16 bf16x2_t __attribute__((ext_vector_type(2)));
typedef float f32x2_t __attribute__((ext_vector_type(2)));
typedef unsigned v2u_t __attribute__((ext_vector_type(2)));
#define RLX_AGENT __ATOMIC_RELAXED, __HIP_MEMORY_SCOPE_AGENT
#define LAS __attribute__((address_space(3)))

__device__ __forceinline__ float bf2f(u16 v) { return __uint_as_float(((unsigned)v) << 16); }
__device__ __forceinline__ u16 f2bf(float f) { return __builtin_bit_cast(u16, (__bf16)f); }
__device__ __forceinline__ float rbf(float f) { return bf2f(f2bf(f)); }
__device__ __forceinline__ unsigned cvtpk(float lo, float hi) { f32x2_t v = {lo, hi}; return __builtin_bit_cast(unsigned, __builtin_convertvector(v, bf16x2_t)); }
__device__ __forceinline__ __amdgpu_buffer_rsrc_t mkrs(const void* p) { return __builtin_amdgcn_make_buffer_rsrc((void*)p, 0, 0x7fffffff, 0x00020000); }
__device__ __forceinline__ u32x4 ld16(__amdgpu_buffer_rsrc_t rs, unsigned voff, unsigned soff) { return __builtin_amdgcn_raw_buffer_load_b128(rs, (int)voff, (int)soff, 16); }
#define OLA 0
__device__ __forceinline__ u32x4 ld16c(__amdgpu_buffer_rsrc_t rs, unsigned voff, unsigned soff) { return __builtin_amdgcn_raw_buffer_load_b128(rs, (int)voff, (int)soff, OLA); }
__device__ __forceinline__ u32x2 ld8(__amdgpu_buffer_rsrc_t rs, unsigned voff, unsigned soff) { return __builtin_amdgcn_raw_buffer_load_b64(rs, (int)voff, (int)soff, 16); }
__device__ __forceinline__ void st16(__amdgpu_buffer_rsrc_t rs, unsigned voff, unsigned soff, u32x4 w) { __builtin_amdgcn_raw_buffer_store_b128(w, rs, (int)voff, (int)soff, 16); asm volatile("s_nop 1" :: "v"(w) : "memory"); }
__device__ __forceinline__ void st8(__amdgpu_buffer_rsrc_t rs, unsigned voff, unsigned soff, unsigned lo, unsigned hi) { u32x2 w = {lo, hi}; __builtin_amdgcn_raw_buffer_store_b64(w, rs, (int)voff, (int)soff, 16); }
__device__ __forceinline__ bool full16(u32x4 v) { return ((v.x & v.y) != ~0u) & ((v.z & v.w) != ~0u); }
__device__ __forceinline__ float max16(float x) { v2u_t r = __builtin_amdgcn_permlane16_swap(__float_as_uint(x), __float_as_uint(x), false, false); return fmaxf(__uint_as_float(r[0]), __uint_as_float(r[1])); }
__device__ __forceinline__ float max32(float x) { v2u_t r = __builtin_amdgcn_permlane32_swap(__float_as_uint(x), __float_as_uint(x), false, false); return fmaxf(__uint_as_float(r[0]), __uint_as_float(r[1])); }
__device__ __forceinline__ float sum16(float x) { v2u_t r = __builtin_amdgcn_permlane16_swap(__float_as_uint(x), __float_as_uint(x), false, false); return __uint_as_float(r[0]) + __uint_as_float(r[1]); }
__device__ __forceinline__ float sum32(float x) { v2u_t r = __builtin_amdgcn_permlane32_swap(__float_as_uint(x), __float_as_uint(x), false, false); return __uint_as_float(r[0]) + __uint_as_float(r[1]); }
__device__ __forceinline__ float tred32(float a, float b) { v2u_t r = __builtin_amdgcn_permlane32_swap(__float_as_uint(a), __float_as_uint(b), false, false); return __uint_as_float(r[0]) + __uint_as_float(r[1]); }
__device__ __forceinline__ float tred16(float a, float b) { v2u_t r = __builtin_amdgcn_permlane16_swap(__float_as_uint(a), __float_as_uint(b), false, false); return __uint_as_float(r[0]) + __uint_as_float(r[1]); }
__device__ __forceinline__ unsigned ror8(unsigned x) { return (unsigned)__builtin_amdgcn_mov_dpp((int)x, 0x128, 0xf, 0xf, true); }
template <int N> __device__ __forceinline__ void nap() { if constexpr (N > 0) { if constexpr (N > 127) { __builtin_amdgcn_s_sleep(127); nap<N - 127>(); } else __builtin_amdgcn_s_sleep(N); } }
__device__ __forceinline__ unsigned half_of(const char* base, unsigned off, unsigned side, unsigned pol) {
  return off + ((((unsigned)(((size_t)base + off) >> 21) ^ side ^ pol) & 1u) << 13);
}
__device__ __forceinline__ unsigned side_map(const void* base, unsigned L, unsigned side, unsigned pol) { return half_of((const char*)base, (L >> 13) << 14, side, pol) + (L & 8191u); }
constexpr unsigned CH_SPIN_CAP = 1u << 18;
__device__ __forceinline__ bool spin_check(unsigned& spins, unsigned* tmop) {
  asm volatile("" ::: "memory");
  ++spins;
  if ((spins & 63u) == 0u) {
    gu32* tmo = (gu32*)tmop;
    if (__builtin_amdgcn_readfirstlane(__hip_atomic_load(tmo, RLX_AGENT))) return false;
    if (spins > CH_SPIN_CAP) { __hip_atomic_store(tmo, 1u, RLX_AGENT); return false; }
  }
  return true;
}
__device__ __forceinline__ unsigned xcc_side() { return ((unsigned)__builtin_amdgcn_s_getreg((3 << 11) | 20) >> 2) & 1u; }
__device__ __forceinline__ f32x4 mfma(u32x4 a, u32x4 b, f32x4 c) { return __builtin_amdgcn_mfma_f32_16x16x32_bf16(__builtin_bit_cast(bfv8, a), __builtin_bit_cast(bfv8, b), c, 0, 0, 0); }
__device__ __forceinline__ f32x4 smf(u32x4 a, u32x8 b, f32x4 c, int ix) { return __builtin_amdgcn_smfmac_f32_16x16x64_bf16(__builtin_bit_cast(bfv8, a), __builtin_bit_cast(bfv16, b), c, ix, 0, 0); }
__device__ __forceinline__ u32x4 lo4(u32x8 v) { return __builtin_shufflevector(v, v, 0, 1, 2, 3); }
__device__ __forceinline__ u32x4 hi4(u32x8 v) { return __builtin_shufflevector(v, v, 4, 5, 6, 7); }
__device__ __forceinline__ u32x8 cat8(u32x4 a, u32x4 b) { return __builtin_shufflevector(a, b, 0, 1, 2, 3, 4, 5, 6, 7); }
#define BAR() do { asm volatile("s_waitcnt lgkmcnt(0)" ::: "memory"); __builtin_amdgcn_s_barrier(); asm volatile("" ::: "memory"); } while (0)
#define SB() __builtin_amdgcn_sched_barrier(0)
#define VMW(n) asm volatile("s_waitcnt vmcnt(" #n ")" ::: "memory")
#define ZERO4 ((f32x4){0.f, 0.f, 0.f, 0.f})
__device__ __forceinline__ u32x2 trrd(const unsigned char* lds, unsigned off) { return __builtin_bit_cast(u32x2, __builtin_amdgcn_ds_read_tr16_b64_v4i16((LAS bf16x4*)(lds + off))); }
__device__ __forceinline__ u32x4 trrd2(const unsigned char* lds, unsigned off, unsigned d) { const u32x2 lo = trrd(lds, off), hi = trrd(lds, off + d); return (u32x4){lo.x, lo.y, hi.x, hi.y}; }


#include <stdlib.h>
#define SAG 16
#define DDV 84,84,10,0,30,0,127,99,105,115,25,30,20,12,0,330
#define XLN 4
#define XLS 2
struct Args {
  const u16* mem0; const u16* Wq; const u16* Wk; const u16* Wv; const u16* Wo; const u16* Bx; const u16* Ax; const u16* Awqkv; const u16* Awo;
  u16* out; char* ws;
  int d[16];
};
constexpr size_t MB = 1u << 20;
#define WOFF 0
#define NK 16
#define NU 16
#define NZ 16
#define NP 16
#define KSIDE 1
constexpr unsigned X1_STEP = 32768, Z_STEP = 1u << 20, K_STEP = 1u << 20;
constexpr size_t O_PROF = 0, O_CNT = (WOFF + 1) * MB, O_CAL = O_CNT + 65536, O_TS = O_CAL + 65536, O_X1 = (WOFF + 2) * MB, O_PS = O_X1 + 1 * MB, O_KM = O_PS + 1 * MB, O_PT = O_KM + 2 * MB, O_OA = O_PT + NZ * 81920,
                 O_KF = O_PT + 4 * MB, O_QF = O_KF + NK * (size_t)K_STEP, O_VF = O_QF + NK * (size_t)K_STEP, O_ZB = KSIDE == 1 ? O_KF : O_VF + NK * MB, O_UO = O_VF + (KSIDE == 1 ? NK : NK + NZ) * MB, O_END = O_UO + NU * 40 * (size_t)32768;
static_assert(O_ZB % (2 * MB) == 0 && NP <= 16 && NZ <= 16 && NZ <= NK && NK % 4 == 0 && O_OA + 2048 * 1280 <= O_KF, "");
#define MSLOT(s_) (((unsigned)(s_) & (NZ - 1u)) * 98304u)
#define TSLOT(s_) (((unsigned)(s_) & (NZ - 1u)) * 81920u)
__device__ __forceinline__ unsigned k_fix(const unsigned o, const unsigned key) { return o ^ ((o >> 8) & 0x2000u) ^ key; }
#define K_KEY() ((((unsigned)((size_t)(ws + O_KF) >> 21) ^ (KSIDE == 2 ? (unsigned)h : KSIDE ^ pol)) & 1u) << 13)
#define KSLOT(s_) k_fix(((unsigned)(s_) & (NK - 1u)) * K_STEP, kkey)
#define ZSLOT(s_) (((unsigned)(s_) & (NZ - 1u)) * Z_STEP)
#define XSLOT(s_) (((unsigned)(s_) & (NP - 1u)) * X1_STEP)
#define CNT(i) (32 * (i))
__device__ __forceinline__ unsigned u_off(const unsigned s, const unsigned c) { return ((s & (NU - 1u)) * 40u + c) << 15; }
constexpr int PRE = 4;
#define WGATE 11
static_assert(WGATE + 3 <= NK && WGATE <= NU && WGATE + PRE + 1 <= NZ && NP >= PRE + 4, "");
constexpr unsigned NFILL = 40 + 64;
constexpr unsigned SENT = 0xFF81FF81u;
#define SENT4 ((u32x4){SENT, SENT, SENT, SENT})
#define PSLOT(s_) (((s_) & (NP - 1)) * 65536)
constexpr int C_SLOT = 0, C_TMO = 8, C_POL = 9, C_FILL = 10, C_PO = 12, C_PT_ = 32, C_PV = 64, C_PS = 96, C_PW = 128, C_PZ = 160, C_TOK = 192, C_AO = 464, C_TOTAL = 480;
__device__ __forceinline__ float nopk(float x) { asm("" : "+v"(x)); return x; }
__device__ __forceinline__ float fmx(float a, float b) { float big = __builtin_inff(); asm("" : "+s"(big)); return __builtin_amdgcn_fmed3f(a, b, big); }
__device__ __forceinline__ float mx16(float x) { v2u_t r = __builtin_amdgcn_permlane16_swap(__float_as_uint(x), __float_as_uint(x), false, false); return fmx(__uint_as_float(r[0]), __uint_as_float(r[1])); }
__device__ __forceinline__ float mx32(float x) { v2u_t r = __builtin_amdgcn_permlane32_swap(__float_as_uint(x), __float_as_uint(x), false, false); return fmx(__uint_as_float(r[0]), __uint_as_float(r[1])); }
__device__ __forceinline__ int lane_id() { int l; asm volatile("v_mbcnt_lo_u32_b32 %0, -1, 0\n v_mbcnt_hi_u32_b32 %0, -1, %0" : "=v"(l)); return l; }
__device__ __forceinline__ unsigned sent_min(unsigned m, unsigned x, unsigned y) { unsigned r = min(min(m, x ^ SENT), y ^ SENT); asm("" : "+v"(r)); return r; }
__device__ __forceinline__ unsigned sent_min(unsigned m, u32x4 v) { return sent_min(sent_min(m, v.x, v.y), v.z, v.w); }
#define RT_MARK(t_) asm volatile("s_memrealtime %0\n s_waitcnt lgkmcnt(0)" : "=s"(t_) :: "memory")
__device__ __forceinline__ unsigned wait_rt(unsigned target) {
  unsigned t;
  target = (unsigned)__builtin_amdgcn_readfirstlane(target);
  asm volatile("s_branch .Lwb%=\n.Lwa%=:\n s_sleep 1\n.Lwb%=:\n s_memrealtime vcc\n s_waitcnt lgkmcnt(0)\n s_sub_i32 %0, %1, vcc_lo\n s_add_i32 vcc_hi, %0, -9\n s_cmp_lt_u32 vcc_hi, 392\n s_cbranch_scc1 .Lwa%=\n"
               " s_add_i32 %0, %0, -1\n s_cmp_lt_u32 %0, 8\n s_cbranch_scc1 .Lwb%=\n s_mov_b32 %0, vcc_lo" : "=&s"(t) : "s"(target) : "vcc", "scc", "memory");
  return t;
}
__device__ __forceinline__ unsigned ts_max(unsigned v) {
  if ((unsigned)__ballot(v == ~0u)) return ~0u;
  const unsigned base = (unsigned)__builtin_amdgcn_readfirstlane(v) - 4096u;
  unsigned d = v == ~0u ? 0u : v - base;
  d = max(d, (unsigned)__builtin_amdgcn_mov_dpp((int)d, 0x121, 0xf, 0xf, true)); d = max(d, (unsigned)__builtin_amdgcn_mov_dpp((int)d, 0x122, 0xf, 0xf, true));
  d = max(d, (unsigned)__builtin_amdgcn_mov_dpp((int)d, 0x124, 0xf, 0xf, true)); d = max(d, (unsigned)__builtin_amdgcn_mov_dpp((int)d, 0x128, 0xf, 0xf, true));
  return (base + max(max((unsigned)__builtin_amdgcn_readlane(d, 0), (unsigned)__builtin_amdgcn_readlane(d, 16)), max((unsigned)__builtin_amdgcn_readlane(d, 32), (unsigned)__builtin_amdgcn_readlane(d, 48)))) & ~1u;
}
#define TS_OFF(l_) (((l_) & 31) * 256 + ((l_) >> 5) * 16384)
#define TS_STEP(s_) (((s_) & 1) * 128 + ((s_) >> 1) * 4)
__device__ __forceinline__ unsigned ts_settle(const __amdgpu_buffer_rsrc_t rs_ts, const int tss, unsigned t, const int lead) {
  for (int n = 0; n < 48; ++n) {
    unsigned now;
    if (t == ~0u) { __builtin_amdgcn_s_sleep(4); u64 t_; RT_MARK(t_); now = (unsigned)t_; } else now = wait_rt(t - (unsigned)lead);
    t = ts_max(__builtin_amdgcn_raw_buffer_load_b32(rs_ts, TS_OFF(lane_id()), tss, 16));
    if (t != ~0u && (int)(t - now) <= lead + 4) break;
  }
  return t;
}
#define ADAPT(d_, failed_) do { if (s > 3) d_ += (failed_) ? a.d[5] : -(int)((s & a.d[6]) == 0); } while (0)
__device__ __forceinline__ void nap_rt(int n) { for (int i = 0; i < n; ++i) __builtin_amdgcn_s_sleep(1); }
#define PINV(x) asm volatile("" : "+v"(x))
#define PINS(x) asm volatile("" : "+s"(x))
#define PINU(x) asm volatile("" :: "v"(x))
#define IL(nm, nv) do { _Pragma("unroll") for (int il_ = 0; il_ < (nm); ++il_) { __builtin_amdgcn_sched_group_barrier(0x008, 1, 0); __builtin_amdgcn_sched_group_barrier(0x7f6, nv, 0); } } while (0)
constexpr int L_MEM = 0, L_QI = 12288;
constexpr int L_B0 = 16384, L_B1 = 65536, L_B2 = 114688;
constexpr int L_QA = L_B1, L_XV = L_B1 + 9216, L_ST = L_B1 + 10240, L_FLG = L_B1 + 10752, L_XR = L_B1 + 12288;

#define TR8_ASM "ds_read_b64_tr_b16 %0, %8\n ds_read_b64_tr_b16 %1, %8 offset:1536\n ds_read_b64_tr_b16 %2, %8 offset:12288\n ds_read_b64_tr_b16 %3, %8 offset:13824\n" \
               "ds_read_b64_tr_b16 %4, %8 offset:24576\n ds_read_b64_tr_b16 %5, %8 offset:26112\n ds_read_b64_tr_b16 %6, %8 offset:36864\n ds_read_b64_tr_b16 %7, %8 offset:38400\n s_waitcnt lgkmcnt(0)"
__device__ __forceinline__ void tr8(const bool agpr, const unsigned ad, u32x8& f0, u32x8& f1) {
  u32x2 a, b, c, d, e, f, g, h;
  if (agpr) asm volatile(TR8_ASM : "=&a"(a), "=&a"(b), "=&a"(c), "=&a"(d), "=&a"(e), "=&a"(f), "=&a"(g), "=&a"(h) : "v"(ad) : "memory");
  else asm volatile(TR8_ASM : "=&v"(a), "=&v"(b), "=&v"(c), "=&v"(d), "=&v"(e), "=&v"(f), "=&v"(g), "=&v"(h) : "v"(ad) : "memory");
  f0 = (u32x8){a.x, a.y, b.x, b.y, c.x, c.y, d.x, d.y}; f1 = (u32x8){e.x, e.y, f.x, f.y, g.x, g.y, h.x, h.y};
}
__device__ __forceinline__ void tr8d(const bool agpr, const unsigned ad, u32x4& f0, u32x4& f1, u32x4& f2, u32x4& f3) {
  u32x2 a, b, c, d, e, f, g, h;
  if (agpr) asm volatile(TR8_ASM : "=&a"(a), "=&a"(b), "=&a"(c), "=&a"(d), "=&a"(e), "=&a"(f), "=&a"(g), "=&a"(h) : "v"(ad) : "memory");
  else asm volatile(TR8_ASM : "=&v"(a), "=&v"(b), "=&v"(c), "=&v"(d), "=&v"(e), "=&v"(f), "=&v"(g), "=&v"(h) : "v"(ad) : "memory");
  f0 = (u32x4){a.x, a.y, b.x, b.y}; f1 = (u32x4){c.x, c.y, d.x, d.y}; f2 = (u32x4){e.x, e.y, f.x, f.y}; f3 = (u32x4){g.x, g.y, h.x, h.y};
}
#define TR4_ASM "ds_read_b64_tr_b16 %0, %4 offset:%5\n ds_read_b64_tr_b16 %1, %4 offset:%6\n ds_read_b64_tr_b16 %2, %4 offset:%7\n ds_read_b64_tr_b16 %3, %4 offset:%8\n s_waitcnt lgkmcnt(0)"
template <int J> __device__ __forceinline__ void tr4(const bool agpr, const unsigned ad, u32x8& f) {
  u32x2 a, b, c, d;
  if (agpr) asm volatile(TR4_ASM : "=&a"(a), "=&a"(b), "=&a"(c), "=&a"(d) : "v"(ad), "n"(16384 * J), "n"(16384 * J + 4096), "n"(16384 * J + 8192), "n"(16384 * J + 12288) : "memory");
  else asm volatile(TR4_ASM : "=&v"(a), "=&v"(b), "=&v"(c), "=&v"(d) : "v"(ad), "n"(16384 * J), "n"(16384 * J + 4096), "n"(16384 * J + 8192), "n"(16384 * J + 12288) : "memory");
  f = (u32x8){a.x, a.y, b.x, b.y, c.x, c.y, d.x, d.y};
}
template <int J> __device__ __forceinline__ void tr4d(const bool agpr, const unsigned ad, u32x4& f0, u32x4& f1) {
  u32x2 a, b, c, d;
  if (agpr) asm volatile(TR4_ASM : "=&a"(a), "=&a"(b), "=&a"(c), "=&a"(d) : "v"(ad), "n"(16384 * J), "n"(16384 * J + 4096), "n"(16384 * J + 8192), "n"(16384 * J + 12288) : "memory");
  else asm volatile(TR4_ASM : "=&v"(a), "=&v"(b), "=&v"(c), "=&v"(d) : "v"(ad), "n"(16384 * J), "n"(16384 * J + 4096), "n"(16384 * J + 8192), "n"(16384 * J + 12288) : "memory");
  f0 = (u32x4){a.x, a.y, b.x, b.y}; f1 = (u32x4){c.x, c.y, d.x, d.y};
}
__device__ __forceinline__ void glds(const void* sbase, unsigned voff, unsigned ldsdst) {
  unsigned keep;
  asm volatile("s_mov_b32 %0, m0\n s_mov_b32 m0, %3\n s_nop 0\n global_load_lds_dwordx4 %1, %2\n s_mov_b32 m0, %0" : "=&s"(keep) : "v"(voff), "s"(sbase), "s"(ldsdst) : "memory");
}
__device__ __forceinline__ void m0_set(unsigned ldsdst) { asm volatile("s_mov_b32 m0, %0\n s_nop 0" :: "s"(ldsdst) : "memory"); }
template <int OFF> __device__ __forceinline__ void glds_o(const void* sbase, unsigned voff) { asm volatile("global_load_lds_dwordx4 %0, %1 offset:%2" :: "v"(voff), "s"(sbase), "n"(OFF) : "memory"); }
template <int OFF> __device__ __forceinline__ void glds_x(const void* sbase, unsigned voff) { asm volatile("global_load_lds_dwordx4 %0, %1 offset:%2 sc1" :: "v"(voff), "s"(sbase), "n"(OFF) : "memory"); }
__device__ __forceinline__ unsigned lds_addr(const unsigned char* p) { return (unsigned)(size_t)(const LAS unsigned char*)p; }
__device__ __forceinline__ unsigned ld_word(const unsigned* p) { return (unsigned)__builtin_amdgcn_readfirstlane(__hip_atomic_load((const gu32*)p, RLX_AGENT)); }
template <int SLEEP>
__device__ __forceinline__ unsigned wait_ge(const unsigned* p, unsigned target, unsigned* tmo) {
  unsigned spins = 0, v;
  while ((v = ld_word(p)) < target) { __builtin_amdgcn_s_sleep(SLEEP); if (!spin_check(spins, tmo)) break; }
  asm volatile("" ::: "memory");
  return v;
}
__device__ __forceinline__ unsigned min_t(const u32x4 w, const int kv) {
  unsigned v = min(min(w.x, w.y), w.z); if (!kv) v = min(v, w.w);
  return (unsigned)__builtin_amdgcn_readfirstlane(v);
}
template <int SLEEP>
__device__ __forceinline__ unsigned wait_t(const unsigned* p, const int first, unsigned target, unsigned* tmo) {
  unsigned spins = 0, v;
  for (;;) {
    v = min_t(ld16(mkrs(p), 0u, 0u), first);
    if (v >= target) break;
    __builtin_amdgcn_s_sleep(SLEEP); if (!spin_check(spins, tmo)) break;
  }
  asm volatile("" ::: "memory");
  return v;
}
template <int SLEEP>
__device__ __forceinline__ void wait_heads(const unsigned* p, const int second, unsigned target, unsigned* tmo) {
  unsigned spins = 0; const int ln = lane_id();
  while (!__all(__builtin_amdgcn_raw_buffer_load_b32(mkrs(p), (ln & 31) * 128 + (ln >> 5) * second * 4, 0, 16) >= target)) { __builtin_amdgcn_s_sleep(SLEEP); if (!spin_check(spins, tmo)) break; }
  asm volatile("" ::: "memory");
}
#define WNAP 12
template <int N> __device__ __forceinline__ void nap4() { if constexpr (N > 16) { asm volatile("s_nop 15"); nap4<N - 16>(); } else if constexpr (N > 0) asm volatile("s_nop %0" :: "n"(N - 1)); }
#define DPPM(d_, s_, ctl_) asm volatile("v_max_f32_dpp %0, %1, %1 " ctl_ " row_mask:0xf bank_mask:0xf" : "=v"(d_) : "v"(s_))
#define XOR1 "quad_perm:[1,0,3,2]"
#define XOR2 "quad_perm:[2,3,0,1]"
__device__ __forceinline__ void light(const __amdgpu_buffer_rsrc_t rs, unsigned vo, const unsigned so, unsigned* tmo, const int slow = 0) {
  unsigned spins = 0;
  for (;;) {
    const bool empty = __builtin_amdgcn_raw_buffer_load_b32(rs, (int)vo, (int)so, 16) == SENT;
    if (!__any(empty) || !spin_check(spins, tmo)) break;
    vo = empty ? vo : 0x80000000u;
    if (slow) __builtin_amdgcn_s_sleep(16); else __builtin_amdgcn_s_sleep(XLS);
  }
}
__device__ __forceinline__ void x1_light(const __amdgpu_buffer_rsrc_t rs_x1, const unsigned so, unsigned* tmo) {
  const unsigned ln = (unsigned)lane_id();
  light(rs_x1, ln < 40u ? (ln >> 5) * 16384u + (ln & 31u) * 256u + 252u : 0x80000000u, so, tmo);
}
__device__ __forceinline__ unsigned touch(const void* p, const int bytes, const int piece) { return __builtin_amdgcn_raw_buffer_load_b32(__builtin_amdgcn_make_buffer_rsrc((void*)p, 0, bytes, 0x00020000), lane_id() * 128, piece * 8192, 0); }
__device__ __forceinline__ unsigned touch_rows(const Args& a, const int which, const int piece) { return touch(which == 0 ? (const void*)a.Bx : (which == 1 ? (const void*)a.Ax : (const void*)(a.ws + O_OA)), 2048 * 1280, piece); }
#define TM 0
template <int N> __device__ __forceinline__ void touch_n(const Args& a, const int w, const int first, const int step) {
  unsigned v = 0;
#pragma unroll
  for (int i = 0; i < N; ++i) v ^= touch_rows(a, w % 3, first + w / 3 + step * i);
  asm volatile("" :: "v"(v));
}
#define DS(x) do { x; SB(); } while (0)
template <int V> struct IC { static constexpr int v = V; };
template <int I, int N, class F> __device__ __forceinline__ void sfor(F&& f) { if constexpr (I < N) { f(IC<I>{}); sfor<I + 1, N>(f); } }
#define SFOR(i_, n_) sfor<0, n_>([&](auto i_##_c) __attribute__((always_inline)) { constexpr int i_ = decltype(i_##_c)::v;
#define SEND })
typedef unsigned u32x32 __attribute__((ext_vector_type(32)));
typedef unsigned u32x16 __attribute__((ext_vector_type(16)));
struct WReg { u32x32 b[11]; u32x16 e; };
#define WR_SW(f_, X) switch ((f_) >> 2) { case 0: X("a", "{a[0:31]}", W.b[0]); break; case 1: X("a", "{a[32:63]}", W.b[1]); break; case 2: X("a", "{a[64:95]}", W.b[2]); break; case 3: X("a", "{a[96:127]}", W.b[3]); break; \
    case 4: X("a", "{a[128:159]}", W.b[4]); break; case 5: X("a", "{a[160:191]}", W.b[5]); break; case 6: X("a", "{a[192:223]}", W.b[6]); break; case 7: X("a", "{a[224:255]}", W.b[7]); break; \
    case 8: X("v", "{v[142:173]}", W.b[8]); break; case 9: X("v", "{v[174:205]}", W.b[9]); break; case 10: X("v", "{v[206:237]}", W.b[10]); break; default: X("v", "{v[238:253]}", W.e); break; }
__device__ __forceinline__ void smf_w(f32x4& c, const u32x4 a, const int f, const int ix, const WReg& W) {
  const int r = f < 32 ? 8 * f : 8 * f - 114;
#define X_(rf_, cs_, v_) asm volatile("v_smfmac_f32_16x16x64_bf16 %0, %1, " rf_ "[%c3:%c4], %2" : "+v"(c) : "v"(a), "v"(ix), "n"(r), "n"(r + 7), cs_(v_))
  WR_SW(f, X_);
#undef X_
}
template <bool WA> __device__ __forceinline__ void mf_w(f32x4& c, const u32x4 x, const int d, const bool first, const WReg& W) {
  const int r = d < 64 ? 4 * d : 4 * d - 114;
#define X_(rf_, cs_, v_) if (WA) { if (first) asm volatile("v_mfma_f32_16x16x32_bf16 %0, " rf_ "[%c2:%c3], %1, 0" : "=&v"(c) : "v"(x), "n"(r), "n"(r + 3), cs_(v_)); \
      else asm volatile("v_mfma_f32_16x16x32_bf16 %0, " rf_ "[%c2:%c3], %1, %0" : "+v"(c) : "v"(x), "n"(r), "n"(r + 3), cs_(v_)); } \
    else { if (first) asm volatile("v_mfma_f32_16x16x32_bf16 %0, %1, " rf_ "[%c2:%c3], 0" : "=&v"(c) : "v"(x), "n"(r), "n"(r + 3), cs_(v_)); \
      else asm volatile("v_mfma_f32_16x16x32_bf16 %0, %1, " rf_ "[%c2:%c3], %0" : "+v"(c) : "v"(x), "n"(r), "n"(r + 3), cs_(v_)); }
  WR_SW(d >> 1, X_);
#undef X_
}
__device__ __forceinline__ void ld_w(const u32x4 rs, const unsigned vo, const unsigned so, const int off, const int f, const bool def, WReg& W) {
  const int r = f < 32 ? 8 * f : 8 * f - 114;
#define Y_(md_, rf_, cs_, v_) asm volatile("s_nop 4\n buffer_load_dwordx4 " rf_ "[%c6:%c7], %1, %2, %3 offen offset:%4 sc1\n buffer_load_dwordx4 " rf_ "[%c8:%c9], %1, %2, %3 offen offset:%5 sc1" \
    : md_ cs_(v_) : "v"(vo), "s"(rs), "s"(so), "n"(off), "n"(off + 1024), "n"(r), "n"(r + 3), "n"(r + 4), "n"(r + 7) : "memory")
#define X_(rf_, cs_, v_) if (def) Y_("=&", rf_, cs_, v_); else Y_("+", rf_, cs_, v_)
  WR_SW(f, X_);
#undef X_
#undef Y_
}
__device__ __forceinline__ u32x4 mkdesc(const void* p) { const size_t b = (size_t)p; return (u32x4){(unsigned)b, (unsigned)(b >> 32) & 0xffffu, 0x7fffffffu, 0x00020000u}; }
__device__ __forceinline__ void tr4_w(const unsigned ad, const int o0, const int o1, const int o2, const int o3, const int f, WReg& W) {
  const int r = f < 32 ? 8 * f : 8 * f - 114;
#define Y_(md_, rf_, cs_, v_) asm volatile("ds_read_b64_tr_b16 " rf_ "[%c6:%c7], %1 offset:%2\n ds_read_b64_tr_b16 " rf_ "[%c8:%c9], %1 offset:%3\n ds_read_b64_tr_b16 " rf_ "[%c10:%c11], %1 offset:%4\n ds_read_b64_tr_b16 " rf_ "[%c12:%c13], %1 offset:%5\n s_waitcnt lgkmcnt(0)" \
    : md_ cs_(v_) : "v"(ad), "n"(o0), "n"(o1), "n"(o2), "n"(o3), "n"(r), "n"(r + 1), "n"(r + 2), "n"(r + 3), "n"(r + 4), "n"(r + 5), "n"(r + 6), "n"(r + 7) : "memory")
#define X_(rf_, cs_, v_) if ((f & 3) == 0) Y_("=&", rf_, cs_, v_); else Y_("+", rf_, cs_, v_)
  WR_SW(f, X_);
#undef X_
#undef Y_
}
#define TRS0(ad_, f_) tr4_w(ad_, 0, 1536, 12288, 13824, f_, W)
#define TRS1(ad_, f_) tr4_w(ad_, 24576, 26112, 36864, 38400, f_, W)
#define TRO4(ad_, j_, f_) tr4_w(ad_, 16384 * (j_), 16384 * (j_) + 4096, 16384 * (j_) + 8192, 16384 * (j_) + 12288, f_, W)
#define SMFV(c_, a_, b_) do { asm volatile("v_smfmac_f32_16x16x64_bf16 %0, %1, %2, %3" : "+v"(c_) : "v"(a_), "v"(b_), "v"(ix)); SB(); } while (0)
#define SMFW(c_, a_, f_) do { smf_w(c_, a_, f_, ix, W); SB(); } while (0)
#define ADD1(d_, x_, y_) do { asm volatile("v_add_f32 %0, %1, %2" : "=v"(d_) : "v"(x_), "v"(y_)); SB(); } while (0)
#define ADD2(d0_, d1_, c_) do { asm volatile("v_add_f32 %0, %2, %3\n v_add_f32 %1, %4, %5" : "=&v"(d0_), "=v"(d1_) : "v"(c_[0]), "v"(c_[2]), "v"(c_[1]), "v"(c_[3])); SB(); } while (0)
#define CVT1(d_, x_, y_) do { asm volatile("v_cvt_pk_bf16_f32 %0, %1, %2" : "=v"(d_) : "v"(x_), "v"(y_)); SB(); } while (0)
#define EPI3(d_, c_) do { float e_; asm volatile("v_add_f32 %1, %2, %3\n v_add_f32 %0, %4, %5\n v_cvt_pk_bf16_f32 %0, %1, %0" : "=&v"(d_), "=&v"(e_) : "v"(c_[0]), "v"(c_[2]), "v"(c_[1]), "v"(c_[3])); SB(); } while (0)
#define EPI4(d_, m_, c_) do { float e_, f_; asm volatile("v_add_f32 %1, %4, %5\n v_add_f32 %2, %6, %7\n v_cvt_pk_bf16_f32 %0, %1, %2\n v_cmp_u_f32_e64 %3, %1, %2" : "=&v"(d_), "=&v"(e_), "=&v"(f_), "=s"(m_) : "v"(c_[0]), "v"(c_[2]), "v"(c_[1]), "v"(c_[3])); SB(); } while (0)
#define CVTN(d_, m_, x_, y_) do { NANM(m_, x_, y_); CVT1(d_, x_, y_); } while (0)
#define ADDCVT(d_, x_, c_) do { asm volatile("v_add_f32 %0, %2, %3\n v_cvt_pk_bf16_f32 %0, %1, %0" : "=&v"(d_) : "v"(x_), "v"(c_[1]), "v"(c_[3])); SB(); } while (0)
#define LGW(n_) do { __builtin_amdgcn_s_waitcnt(0xC07F | ((n_) << 8)); SB(); } while (0)
#define LBAR(n_) do { asm volatile("s_waitcnt lgkmcnt(" #n_ ")" ::: "memory"); __builtin_amdgcn_s_barrier(); asm volatile("" ::: "memory"); SB(); } while (0)
#define FILL() do { __builtin_amdgcn_s_setprio(0); SB(); } while (0)
#define NANM(m_, x_, y_) do { asm volatile("v_cmp_u_f32_e64 %0, %1, %2" : "=s"(m_) : "v"(x_), "v"(y_)); SB(); } while (0)
#define LW32(ad_, off_, v_) (*(unsigned*)(lds + (ad_) + (off_)) = (v_))
#define MEMB() asm volatile("" ::: "memory")
#define LG0() __builtin_amdgcn_s_waitcnt(0xC07F)
#define FLAG(i_) do { MEMB(); *(volatile LAS unsigned char*)(LAS unsigned char*)(lds + fla + 4 * (i_)) = (unsigned char)snv; SB(); } while (0)
#define FLRD(i_) (*(const volatile LAS unsigned*)(LAS unsigned char*)(lds + flr + 4 * (i_)))
#define SBUF(i_) (((i_) % 3) == 0 ? L_B0 : (((i_) % 3) == 1 ? L_B1 : L_B2))
#define SDMA_INIT() unsigned svo[12]; const int sq0 = 3 * ((h >> 2) & 3); \
    _Pragma("unroll") for (int q_ = 0; q_ < 12; ++q_) { const int qr_ = q_ + sq0, ins = wave + 4 * (qr_ < 12 ? qr_ : qr_ - 12); const unsigned c = (unsigned)(ins * 64 + lane), r = c / 24u, pc = c - r * 24u, sw = ((r >> 1) & 1u) * 2u + ((r >> 3) & 1u) * 4u; \
      svo[q_] = r * 12288u + 16u * (pc ^ sw); PINV(svo[q_]); }
#define KROT(kr_) ((kr_) < 4 ? (((kr_) + h) & 3) : 4)
#define SDMA(W_m, kr_, i_) do { const char* W_ = (const char*)((W_m) + (size_t)(128 * KROT(kr_)) * 6144 + 192 * h); \
    _Pragma("unroll") for (int q_ = 0; q_ < 12; ++q_) { const int qr_ = q_ + sq0; glds(W_, svo[q_], lds_addr(lds) + (unsigned)(SBUF(i_) + (wave + 4 * (qr_ < 12 ? qr_ : qr_ - 12)) * 1024)); } SB(); } while (0)
#define SDMAQK(i_) SDMA(((i_) & 1) ? a.Wk : a.Wq, (i_) >> 1, i_)
#define TRO(tile_) (trow * 384u + (((unsigned)(2 * (tile_) + ((l15 & 3) >> 1))) ^ tswz) * 16u + (unsigned)(l15 & 1) * 8u)
#define TRO_INIT() const unsigned trow = (unsigned)(8 * lq + (l15 >> 2)), tswz = ((trow >> 1) & 1u) * 2u + ((trow >> 3) & 1u) * 4u
#define SXDMA_INIT() unsigned xvo = (unsigned)((4 * wave + lq) * 1280 + (l15 ^ ((4 * wave + lq) & 15)) * 16); PINV(xvo)
#define SXDMA(kr_, xb_) do { const char* X_ = (const char*)a.Bx + 256 * KROT(kr_); \
    glds(X_, xvo, lds_addr(lds) + (unsigned)((xb_) + wave * 1024)); glds(X_ + 16 * 1280, xvo, lds_addr(lds) + (unsigned)((xb_) + 4096 + wave * 1024)); SB(); } while (0)
#define SXRD(xb_) _Pragma("unroll") for (int kk = 0; kk < 4; ++kk) _Pragma("unroll") for (int rt = 0; rt < 2; ++rt) xa[rt][kk] = *(const u32x4*)(lds + (xb_) + rt * 4096 + l15 * 256 + ((4 * kk + lq) ^ l15) * 16)
#define MFV(c_, a_, b_, first_) do { if (first_) asm volatile("v_mfma_f32_16x16x32_bf16 %0, %1, %2, 0" : "=&v"(c_) : "v"(a_), "v"(b_)); else asm volatile("v_mfma_f32_16x16x32_bf16 %0, %1, %2, %0" : "+v"(c_) : "v"(a_), "v"(b_)); } while (0)
#define HALF(f_, hi_) ((hi_) ? hi4(f_) : lo4(f_))
#define ODMA_INIT() unsigned ovo = (unsigned)((4 * wave + lq) * 1280 + (l15 ^ (((4 * wave + lq) & 7) << 1)) * 16); PINV(ovo); \
    const unsigned orow = (unsigned)(4 * lq + (l15 >> 2)); \
    unsigned otr[2]; _Pragma("unroll") for (int t = 0; t < 2; ++t) otr[t] = orow * 256u + (((unsigned)(2 * (2 * wave + t) + ((l15 & 3) >> 1))) ^ ((orow & 7u) << 1)) * 16u + (unsigned)(l15 & 1) * 8u
#define OROT(cr_) (((cr_) + h) % 5)
#define ODMA(cr_, i_) do { const char* W_ = (const char*)(a.Wo + (size_t)(192 * h) * 640) + 256 * OROT(cr_); \
    _Pragma("unroll") for (int q_ = 0; q_ < 12; ++q_) glds(W_ + q_ * (16 * 1280), ovo, lds_addr(lds) + (unsigned)(SBUF(i_) + (wave + 4 * q_) * 1024)); SB(); } while (0)

#define KXL 8
#define SAL 2
#define SOF 0
#define MNAP 0
#define SF0(m_, t_, k_) (6 * (2 * ((k_) >> 1) + (m_)) + 2 * (t_) + ((k_) & 1))
#define SFR(m_, t_, k_) (SF0(m_, t_, k_) - (SF0(m_, t_, k_) > 39) - (SF0(m_, t_, k_) > 41))
constexpr int L_KX = L_B1 + 28672, L_QX = 0;
template <int wave>
__device__ __forceinline__ void s_loop(const Args& a, unsigned char* lds, const int h, const unsigned pol, const WReg& W) {
  constexpr int KIND = wave < 2 ? 0 : wave - 1;
  char* ws = a.ws; asm volatile("" : "+s"(ws));
  unsigned* const cnt = (unsigned*)(ws + O_CNT); unsigned* const tmo = cnt + CNT(C_TMO);
  const int lane = lane_id(), l15 = lane & 15, lq = lane >> 4;
  char* const X1 = ws + O_X1; char* const P = ws + O_PS;
  u32x8 kxf[3];
  if (KIND == 0) {
#pragma unroll
    for (int t = 0; t < 3; ++t) kxf[t] = cat8(*(const u32x4*)(lds + L_KX + wave * 6144 + t * 2048 + lane * 16), *(const u32x4*)(lds + L_KX + wave * 6144 + t * 2048 + 1024 + lane * 16));
  }
  const __amdgpu_buffer_rsrc_t rs_x1 = mkrs(X1), rs_p = mkrs(P), rs_kf = mkrs(ws + O_KF);
  unsigned tprog = 0;
  const unsigned kkey = K_KEY();
#define KX_LOAD(sg_) do { PINV(xv); const unsigned kb_ = KSLOT(sg_) + (unsigned)(h * 32768 + wave * 16384); \
    _Pragma("unroll") for (int t_ = 0; t_ < 3; ++t_) kxf[t_] = cat8(ld16(rs_kf, xv + (unsigned)((t_ & 1) * 2048), kb_ + (unsigned)((t_ >> 1) * 4096)), ld16(rs_kf, xv + (unsigned)((t_ & 1) * 2048 + 1024), kb_ + (unsigned)((t_ >> 1) * 4096))); } while (0)
  unsigned trx = (unsigned)(L_XR + wave * 4096 + lane * 16); PINV(trx);
  unsigned trq = (unsigned)(L_B2 + wave * 12288 + lane * 16); PINV(trq);
  unsigned trk = (unsigned)(L_B0 + wave * 12288 + lane * 16); PINV(trk);
  int ix = (l15 & 2) ? 0xEEEE : 0x4444; PINV(ix);
  const float CS = 0.07216878364870323f * 1.4426950408889634f;
  unsigned xv = (unsigned)lane * 16u;
  unsigned ib = (unsigned)(L_QI + (16 * wave + l15) * 16 + lq * 4);
  unsigned ia = (unsigned)(L_QA + wave * 384 + ((l15 >> 2) & 1) * 128 + (l15 >> 3) * 64 + (l15 & 1) * 32 + lq * 8 + ((l15 >> 1) & 1) * 4);
  unsigned rda = (unsigned)(L_QA + (lq >> 1) * 768 + (l15 >> 3) * 384 + (lq & 1) * 64 + ((l15 >> 2) & 1) * 32 + (l15 & 3) * 8);
  unsigned qk_rd = (unsigned)(L_QI + (4 * lq + (l15 >> 2)) * 16 + (l15 & 1) * 8);
  unsigned flr = (unsigned)L_FLG;
  unsigned xva = KIND == 0 ? (unsigned)(L_XV + lq * 256 + (l15 >> 1) * 16 + wave * 8 + (l15 & 1) * 4) : (unsigned)(L_XV + lane * 16);
  PINV(xv); PINV(ib); PINV(ia);
  if (KIND != 2) { PINV(rda); PINV(xva); } if (KIND == 1) PINV(qk_rd); if (KIND != 0) PINV(flr);
  const bool hi8 = l15 >= 8;
  const __amdgpu_buffer_rsrc_t rs_ts = mkrs(ws + O_TS + half_of(ws + O_TS, 0u, 0u, pol));
  int tso = lane == 0 ? 256 * h : (int)0x80000000u; if (KIND != 0) PINV(tso);
  int tsa = TS_OFF(lane); if (KIND == 2) PINV(tsa);
  const __amdgpu_buffer_rsrc_t rs_ps = mkrs(cnt + CNT(C_PS) - 32 * h);
  unsigned po = side_map(P, (unsigned)(h * 512 + ((l15 >> 2) & 1) * 256 + (4 * lq + 2 * (l15 & 1) + (l15 >> 3)) * 16 + ((l15 >> 1) & 1) * 8), 0u, pol);
  unsigned pm = !(l15 & 1) ? side_map(P, (unsigned)(16384 + h * 128 + (4 * lq + 2 * ((l15 >> 2) & 1) + (l15 >> 3)) * 8 + ((l15 >> 1) & 1) * 4), 0u, pol) : 0x80000000u;
  u32x4 p_desc = mkdesc(P); if (KIND == 1) PINS(p_desc);
  unsigned pfo = lane < 32 ? side_map(P, (unsigned)(h * 512 + lane * 16), 0u, pol) : (lane < 40 ? side_map(P, (unsigned)(16384 + h * 128 + (lane - 32) * 16), 0u, pol) : 0x80000000u);
  if (KIND == 1) { PINV(po); PINV(pm); }
  if (KIND == 1) PINV(pfo);
  unsigned dur = 130, tnx = 0, tpr = 0, adj = 0; int dly = a.d[0]; PINS(dly);
#define MQ(t_, f_, w_) SMFV(aq[t_], f_, w_)
#define MK(t_, f_, w_) SMFV(ak[t_], f_, w_)
#define QW(t_, k_) SMFW(aq[t_], A[k_], SFR(0, t_, k_))
#define KW(t_, k_) SMFW(ak[t_], A[k_], SFR(1, t_, k_))
#define RLO(tb_, t_, k2_) (*(const u32x4*)(lds + (tb_) + (t_) * 4096 + (k2_) * 2048))
#define RHI(tb_, t_, k2_) (*(const u32x4*)(lds + (tb_) + (t_) * 4096 + (k2_) * 2048 + 1024))
#define RFR(tb_, t_, k2_) cat8(RLO(tb_, t_, k2_), RHI(tb_, t_, k2_))
#define XLO(j_) (*(const u32x4*)(lds + trx + (j_) * 2048))
#define XHI(j_) (*(const u32x4*)(lds + trx + (j_) * 2048 + 1024))
#define ALD(k_) DS(A[k_] = ld16(rs_x1, xv + (unsigned)(((k_) & 1) * 1024), sx[(k_) >> 1]))
#define APACE 10
#define ALP(k_) do { ALD(k_); nap4<APACE>(); SB(); } while (0)
#define A_LOAD() do { PINV(xv); ALP(0); ALP(8); ALP(1); ALP(9); ALP(2); ALP(7); ALP(3); ALP(4); ALP(5); ALD(6); SB(); } while (0)
  u32x4 ts_desc = mkdesc(ws + O_TS + half_of(ws + O_TS, 0u, 0u, pol) + 256 * h);
  unsigned fad = lds_addr(lds) + (unsigned)L_FLG; PINV(fad); PINS(ts_desc);
  if (KIND == 2) LW32(flr, 24, dur);
#pragma unroll 1
  for (int s = 0; s < 64; ++s) {
    int pss = PSLOT(s), tss = TS_STEP(s), tsn = TS_STEP(s + 1); PINS(pss); PINS(tss); if (KIND == 2) PINS(tsn);
    unsigned snp = (unsigned)(s + 1) * 0x01010101u, snv = snp, tries = 0; PINS(snp); PINV(snv);
    f32x4 sc = ZERO4; if (KIND != 2) PINV(sc);
    unsigned sx[5];
    { const unsigned so = half_of(X1, XSLOT(s), 0u, pol);
#pragma unroll
      for (int j = 0; j < 5; ++j) { sx[j] = so + (j < 4 ? (unsigned)((j + h) & 3) * 2048u : 16384u); PINS(sx[j]); }
      if (KIND == 2 && s == 1) x1_light(rs_x1, so, tmo); }
    SB();
    asm volatile("s_branch .Lsre%c6\n"
                 ".Lsbad%c6:\n s_add_u32 %0, %0, 1\n s_cmp_lt_u32 %0, 4\n s_cbranch_scc1 .Lsnow%c6\n s_sleep 8\n"
                 ".Lsnow%c6:\n s_memrealtime vcc\n ds_write_b32 %5, %1 offset:28\n ds_read_b32 %1, %5 offset:24\n s_waitcnt lgkmcnt(0)\n v_add_u32 %1, vcc_lo, %1\n v_and_b32 %1, -2, %1\n"
                 " s_mov_b64 exec, 1\n buffer_store_dword %1, off, %3, %4 sc1\n s_mov_b64 exec, -1\n v_mov_b32 %1, %2\n"
                 ".Lsre%c6:" : "+s"(tries) : "v"(snv), "s"(snp), "s"(ts_desc), "s"(tss), "v"(fad), "n"(wave) : "vcc", "scc", "memory");
    SB();
    u32x8 ra = RFR(trq, 0, 0), rb = RFR(trq, 1, 0), rc = RFR(trq, 2, 0);
    f32x4 aq[3] = {ZERO4, ZERO4, ZERO4}, ak[3] = {ZERO4, ZERO4, ZERO4};
    asm volatile("s_cmp_lg_u32 %6, 0\n s_cbranch_scc1 .Lsgo%c7" : "+v"(aq[0]), "+v"(aq[1]), "+v"(aq[2]), "+v"(ak[0]), "+v"(ak[1]), "+v"(ak[2]) : "s"(tries), "n"(wave) : "scc", "memory");
    SB();
    unsigned tis = 0, tsl = 0; bool calm = false;
    asm volatile(".p2align %c0\n .fill %c1, 4, 0xbf800000" :: "n"(SAL), "n"(SOF));
    if (KIND == 2) tis = wait_rt(tnx);
    BAR();
    asm volatile(".Lsgo%c0:" :: "n"(wave) : "memory");
    SB();
    u32x4 A[10], lo, hi;
    A_LOAD();
    unsigned w0;
    float ta, tb_;
    u64 nanm;
    QW(0, 0); QW(1, 0); QW(2, 0); KW(0, 0);
    MQ(0, A[8], ra); DS(lo = RLO(trq, 0, 1));
    KW(1, 0); DS(hi = RHI(trq, 0, 1)); ra = cat8(lo, hi);
    MQ(1, A[8], rb); DS(lo = RLO(trq, 1, 1));
    KW(2, 0); DS(hi = RHI(trq, 1, 1)); rb = cat8(lo, hi);
    QW(0, 1);
    MQ(2, A[8], rc); DS(lo = RLO(trq, 2, 1));
    QW(1, 1); DS(hi = RHI(trq, 2, 1)); rc = cat8(lo, hi);
    QW(2, 1);
    MQ(0, A[9], ra); DS(lo = RLO(trk, 0, 0));
    KW(0, 1); DS(hi = RHI(trk, 0, 0)); ra = cat8(lo, hi);
    MQ(1, A[9], rb); DS(lo = XLO(0));
    KW(1, 1); DS(hi = XHI(0)); rb = cat8(lo, hi);
    QW(0, 2);
    MQ(2, A[9], rc); DS(lo = XLO(1));
    QW(1, 2); DS(hi = XHI(1)); rc = cat8(lo, hi);
    QW(2, 2);
    MK(0, A[8], ra); DS(lo = RLO(trk, 1, 0));
    QW(0, 7); DS(hi = RHI(trk, 1, 0)); ra = cat8(lo, hi);
    MQ(1, A[7], rb); DS(lo = RLO(trk, 0, 1));
    KW(0, 2); DS(hi = RHI(trk, 0, 1)); rb = cat8(lo, hi);
    QW(0, 3);
    MQ(2, A[7], rc); DS(lo = RLO(trk, 2, 0));
    QW(1, 3); DS(hi = RHI(trk, 2, 0)); rc = cat8(lo, hi);
    QW(2, 3);
    QW(0, 4);
    MK(1, A[8], ra); DS(lo = RLO(trk, 1, 1));
    QW(1, 4); DS(hi = RHI(trk, 1, 1)); ra = cat8(lo, hi);
    QW(2, 4);
    QW(0, 5);
    MK(0, A[9], rb); DS(lo = RLO(trk, 2, 1));
    QW(1, 5); DS(hi = RHI(trk, 2, 1)); rb = cat8(lo, hi);
    QW(2, 5);
    QW(0, 6); QW(1, 6); QW(2, 6); LG0(); SB();
    KW(0, 3); FILL(); ADD1(ta, aq[0][0], aq[0][2]);
    KW(0, 4); FILL(); ADD1(tb_, aq[0][1], aq[0][3]);
    KW(0, 5); FILL(); CVTN(w0, nanm, ta, tb_);
    KW(0, 6); DS(LW32(ia, 0, w0)); ADD1(ta, aq[1][0], aq[1][2]);
    KW(0, 7); ADD1(tb_, aq[1][1], aq[1][3]);
    KW(1, 2);
    if (__builtin_expect(nanm != 0ull, 0)) {
      unsigned m = ~0u;
#define S_BACK() asm volatile("v_cmp_eq_u32 vcc, 0, %0\n s_cmp_lt_u32 %1, %3\n s_cselect_b64 vcc, vcc, 0\n s_cbranch_vccnz .Lsbad%c2" : "+v"(m) : "s"(tries), "n"(wave), "n"(CH_SPIN_CAP) : "vcc", "scc")
#pragma unroll
      for (int k = 0; k < 10; k += 2) m = sent_min(m, A[k].x, A[k + 1].x);
      S_BACK();
#pragma unroll
      for (int k = 0; k < 10; ++k) m = sent_min(m, A[k]);
      S_BACK();
    }
    asm volatile("" :: "s"(sx[0]), "s"(sx[1]), "s"(sx[2]), "s"(sx[3]), "s"(sx[4]), "s"(tries), "s"(tss), "s"(snp), "s"(ts_desc), "v"(fad), "v"(snv));
    CVT1(w0, ta, tb_);
    KW(1, 3); DS(LW32(ia, 1536, w0)); ADD1(ta, aq[2][0], aq[2][2]);
    KW(1, 4); FILL(); ADD1(tb_, aq[2][1], aq[2][3]);
    KW(1, 5); FILL(); CVT1(w0, ta, tb_);
    KW(1, 6); DS(LW32(ia, 3072, w0)); ADD1(ta, ak[0][0], ak[0][2]);
    KW(1, 7); FILL(); ADD1(tb_, ak[0][1], ak[0][3]);
    MK(1, A[9], ra); CVT1(w0, ta, tb_);
    KW(2, 1); DS(LW32(ib, 0, w0));
    u32x2 g[6];
#define QAR(i_) do { if (KIND != 2) DS(g[i_] = trrd(lds, rda + (unsigned)(((i_) >> 1) * 1536 + ((i_) & 1) * 128))); else if ((i_) >= 1 && (i_) <= 3) FILL(); } while (0)
#define QA(t_) ((u32x4){g[2 * (t_)].x, g[2 * (t_)].y, g[2 * (t_) + 1].x, g[2 * (t_) + 1].y})
    KW(2, 2); LBAR(1); QAR(0);
    KW(2, 3); QAR(1); ADD1(ta, ak[1][0], ak[1][2]);
    KW(2, 4); QAR(2); ADD1(tb_, ak[1][1], ak[1][3]);
    KW(2, 5); QAR(3); CVT1(w0, ta, tb_);
    KW(2, 6); DS(LW32(ib, 1024, w0));
    KW(2, 7); QAR(4); QAR(5);
    if (KIND == 2) DS(tsl = __builtin_amdgcn_raw_buffer_load_b32(rs_ts, tsa, tss, 16));
    MK(2, A[8], rc);
    MK(2, A[9], rb);
    if (KIND == 0) {
      float la, lb;
      { const u32x4 q_ = QA(0); SMFV(sc, q_, kxf[0]); } LBAR(2);
      { const u32x4 q_ = QA(1); SMFV(sc, q_, kxf[1]); }
      { const u32x4 q_ = QA(2); SMFV(sc, q_, kxf[2]); }
      EPI3(w0, ak[2]); DS(LW32(ib, 2048, w0));
      asm volatile("s_nop 3\n v_add_f32 %0, %2, %3\n v_add_f32 %1, %4, %5" : "=&v"(la), "=v"(lb) : "v"(sc[0]), "v"(sc[2]), "v"(sc[1]), "v"(sc[3])); SB();
      DS(LW32(xva, 0, __float_as_uint(la)); LW32(xva, 128, __float_as_uint(lb)));
      LBAR(1); LBAR(0);
    } else if (KIND == 2) {
      LBAR(0);
      asm volatile("s_nop 5"); SB();
      EPI3(w0, ak[2]); DS(LW32(ib, 2048, w0));
      LBAR(0); LBAR(0);
      tsl = ts_max(tsl);
      calm = s > 1 && tsl != ~0u && (int)(tsl - tpr) <= a.d[10];
      if (calm && s < 63) __builtin_amdgcn_raw_buffer_store_b32((tsl + (unsigned)dly + dur) & ~1u, rs_ts, tso, tsn, 16);
    } else {
      u32x2 kb[12];
#define KBR(i_) DS(kb[i_] = trrd(lds, qk_rd + (unsigned)((i_) * 256)))
#define ML(t_) do { const u32x4 a_ = QA(t_); \
        const u32x8 b_ = {kb[4 * (t_)].x, kb[4 * (t_)].y, kb[4 * (t_) + 1].x, kb[4 * (t_) + 1].y, kb[4 * (t_) + 2].x, kb[4 * (t_) + 2].y, kb[4 * (t_) + 3].x, kb[4 * (t_) + 3].y}; SMFV(sc, a_, b_); } while (0)
      LBAR(2);
      KBR(0); KBR(1); KBR(2); KBR(3); KBR(4); KBR(5);
      EPI3(w0, ak[2]); DS(LW32(ib, 2048, w0));
      KBR(6); KBR(7);
      LBAR(2);
      KBR(8); KBR(9); KBR(10); KBR(11);
      ML(0);
      asm volatile("s_barrier" ::: "memory"); SB();
      f32x4 t; DS(t = *(const f32x4*)(lds + xva));
      ML(1); ML(2);
      float tl, nl, x, m, u, e, d;
      asm volatile("v_max3_f32 %4, %0, %1, %2\n v_max_f32 %4, %4, %3\n v_mul_f32 %5, %15, %4\n v_fma_f32 %0, %16, %0, %5\n v_fma_f32 %1, %16, %1, %5\n v_fma_f32 %2, %16, %2, %5\n v_fma_f32 %3, %16, %3, %5\n s_nop 0\n"
                   "v_add_f32_dpp %6, %11, %13 quad_perm:[0,1,2,3] row_mask:0xf bank_mask:0x3\n v_add_f32_dpp %6, %12, %14 quad_perm:[0,1,2,3] row_mask:0xf bank_mask:0xc\n v_max_f32 %7, %6, %4\n"
                   "v_exp_f32 %0, %0\n v_exp_f32 %1, %1\n v_max_f32_dpp %7, %7, %7 quad_perm:[1,0,3,2] row_mask:0xf bank_mask:0xf\n"
                   "v_exp_f32 %2, %2\n v_exp_f32 %3, %3\n v_max_f32_dpp %7, %7, %7 quad_perm:[2,3,0,1] row_mask:0xf bank_mask:0xf\n"
                   "v_add_f32 %8, %0, %1\n v_add_f32 %10, %2, %3\n v_max_f32_dpp %7, %7, %7 row_half_mirror row_mask:0xf bank_mask:0xf\n"
                   "v_mul_f32 %7, %15, %7\n v_sub_f32 %4, %7, %5\n v_fma_f32 %9, %16, %6, %7\n v_exp_f32 %4, %4\n v_exp_f32 %9, %9\n v_add_f32 %8, %8, %10\n v_fma_f32 %10, %4, %8, %9\n"
                   "v_mul_f32 %0, %0, %4\n v_mul_f32 %1, %1, %4\n v_add_f32_dpp %10, %10, %10 quad_perm:[1,0,3,2] row_mask:0xf bank_mask:0xf\n"
                   "v_mul_f32 %2, %2, %4\n v_mul_f32 %3, %3, %4\n v_add_f32_dpp %10, %10, %10 quad_perm:[2,3,0,1] row_mask:0xf bank_mask:0xf\n"
                   "v_mov_b32_dpp %6, %9 quad_perm:[1,0,3,2] row_mask:0xf bank_mask:0xf\n s_nop 0\n v_add_f32_dpp %10, %10, %10 row_half_mirror row_mask:0xf bank_mask:0xf\n v_rcp_f32 %10, %10\n s_nop 0\n"
                   "v_mul_f32 %0, %0, %10\n v_mul_f32 %1, %1, %10\n v_cvt_pk_bf16_f32 v0, %0, %1\n v_mul_f32 %2, %2, %10\n v_mul_f32 %3, %3, %10\n v_cvt_pk_bf16_f32 v1, %2, %3\n"
                   "buffer_store_dwordx2 v[0:1], %17, %19, %20 offen sc1\n v_mul_f32 %9, %9, %10\n v_mul_f32 %6, %6, %10\n v_cvt_pk_bf16_f32 %9, %9, %6\n buffer_store_dword %9, %18, %19, %20 offen sc1"
                   : "+v"(t[0]), "+v"(t[1]), "+v"(t[2]), "+v"(t[3]), "=&v"(tl), "=&v"(nl), "=&v"(x), "=&v"(m), "=&v"(u), "=&v"(e), "=&v"(d)
                   : "v"(sc[0]), "v"(sc[1]), "v"(sc[2]), "v"(sc[3]), "s"(-CS), "s"(CS), "v"(po), "v"(pm), "s"(p_desc), "s"(pss) : "v0", "v1", "memory");
      SB();
      { u64 t_; RT_MARK(t_); LW32(flr, 20, (unsigned)t_); }
    }
    BAR();
    if (KIND == 0 && s == 0) {
#pragma unroll
      for (int n = 0; n < 12; ++n) st16(mkrs(ws + (wave == 0 ? O_KF : O_QF)), xv + (unsigned)(n % 3 * 1024), KSLOT(0) + (unsigned)(h * 32768 + n / 6 * 16384 + n % 6 / 3 * 3072), *(const u32x4*)(lds + (wave == 0 ? L_KX : L_QX) + n * 1024 + xv));
    }
    if (KIND == 0) {
      nap_rt(KXL - 1);
      if (s < 63) {
        const unsigned tneed = (unsigned)(s + 1);
        if (tprog < tneed) tprog = wait_t<4>(cnt + CNT(C_PT_ + h), 1, tneed, tmo);
        KX_LOAD(s + 1);
      }
    }
    if (KIND == 0) tprog = min_t(ld16(mkrs(cnt + CNT(C_PT_ + h)), 0u, 0u), 1);
    if (KIND == 2 && s < 63) {
      const unsigned f7 = FLRD(7), f5 = FLRD(5);
      const unsigned tb2 = (unsigned)__builtin_amdgcn_readfirstlane(f5);
      adj = __builtin_amdgcn_readfirstlane(f7) == snp;
      if (__builtin_expect(!calm || adj, 0)) {
        if (s < 2) __builtin_amdgcn_raw_buffer_store_b32(((unsigned)tb2 + (unsigned)a.d[11]) & ~1u, rs_ts, tso, tss, 16);
        tsl = ts_settle(rs_ts, tss, tsl, a.d[4]) + (unsigned)a.d[12];
        if (s > 0) __builtin_amdgcn_raw_buffer_store_b32((tsl + (unsigned)dly + dur) & ~1u, rs_ts, tso, tsn, 16);
      }
      tnx = tsl + (unsigned)dly; tpr = tnx + dur;
      dur = min(dur + 1u, (unsigned)tb2 - tis); PINS(dur);
      LW32(flr, 24, dur + (unsigned)a.d[13]);
      ADAPT(dly, adj);
    }
    if (KIND == 1) {
      const int ln = lane_id();
      const __amdgpu_buffer_rsrc_t rs_km = mkrs(ws + O_KM);
      if (s + PRE < 63) st16(rs_p, pfo, (unsigned)PSLOT(s + PRE), SENT4);
#pragma unroll
      for (int kk = 0; kk < 6; kk += 2) {
        const u32x4 v = trrd2(lds, qk_rd + kk * 512, 256u), v2 = trrd2(lds, qk_rd + (kk + 1) * 512, 256u);
        st16(rs_km, (unsigned)((kk + ((ln >> 3) & 1)) * 512 + (ln >> 4) * 128 + (ln & 7) * 16), MSLOT(s) + (unsigned)h * 3072u, hi8 ? v2 : v);
      }
      __builtin_amdgcn_raw_buffer_store_b32((unsigned)(s + 1), rs_ps, tso, 0, 16);
    }
  }
}
__device__ __forceinline__ void s_role(const Args& a, unsigned char* lds, const int h, const int wave) {
  char* ws = a.ws; asm volatile("" : "+s"(ws));
  unsigned* const cnt = (unsigned*)(ws + O_CNT); unsigned* const tmo = cnt + CNT(C_TMO);
  const int lane = lane_id(), l15 = lane & 15, lq = lane >> 4;
  WReg W; u32x8 wx[3];
  unsigned tro[3];
  { TRO_INIT();
#pragma unroll
    for (int t = 0; t < 3; ++t) tro[t] = TRO(4 * t + wave); }
  SDMA_INIT();
  SXDMA_INIT();
  SXDMA(0, 0); SDMAQK(0); SDMAQK(1); SXDMA(1, 8192); SDMAQK(2);
  unsigned pf;
  f32x4 pac[2][2][3];
#define kacc pac[1]
#define qacc pac[0]
  SFOR(i, 10)
    if (i < 2 || i == 3 || i == 5 || i == 7) VMW(26); else if (i < 8) VMW(24); else if (i == 8) VMW(12); else VMW(0);
    BAR();
    u32x8 res[3][2];
    SFOR(t, 3)
      const unsigned ad = lds_addr(lds) + tro[t] + (unsigned)SBUF(i);
      if (i < 8) { TRS0(ad, SFR(i & 1, t, i & 6)); if (i == 6 && t > 0) tr8(false, ad, res[t][0], wx[t]); else TRS1(ad, SFR(i & 1, t, (i & 6) + 1)); }
      else tr8(false, ad, res[t][0], res[t][1]);
    SEND;
    BAR();
    if (i == 2 || i == 4 || i == 6) SXDMA((i >> 1) + 1, (((i >> 1) + 1) & 1) * 8192);
    if (i + 3 < 10) SDMAQK(i + 3);
    if (i >= 8) {
#pragma unroll
      for (int t = 0; t < 3; ++t)
#pragma unroll
        for (int k = 0; k < 2; ++k) {
          unsigned char* dst = lds + SBUF(i) + wave * 12288 + t * 4096 + k * 2048 + lane * 16;
          *(u32x4*)dst = lo4(res[t][k]); *(u32x4*)(dst + 1024) = hi4(res[t][k]);
        }
    }
    if (i == 8) asm volatile("s_nop 4\n global_load_dword %0, %1, %2 sc1" : "=v"(pf) : "v"(lane < 32 ? 0u : 128u), "s"(cnt + CNT(C_POL)) : "memory");
    if (i == 9) PINV(pf);
    if (i == 7) {
#pragma unroll
      for (int t = 1; t < 3; ++t) { unsigned char* dst = lds + L_XR + wave * 4096 + lane * 16 + (t - 1) * 2048; *(u32x4*)dst = lo4(wx[t]); *(u32x4*)(dst + 1024) = hi4(wx[t]); }
    }
    SB();
    SFOR(kk, 4)
      u32x4 xa[2];
#pragma unroll
      for (int rt = 0; rt < 2; ++rt) xa[rt] = *(const u32x4*)(lds + ((i >> 1) & 1) * 8192 + rt * 4096 + l15 * 256 + ((4 * kk + lq) ^ l15) * 16);
      SFOR(n, 6)
        constexpr int t = n >> 1, rt = n & 1;
        if (i >= 8) { const u32x4 w_ = HALF(res[t][kk >> 1], kk & 1); MFV(pac[i & 1][rt][t], w_, xa[rt], false); }
        else if (i == 6 && t > 0 && kk >= 2) { const u32x4 w_ = HALF(wx[t], kk & 1); MFV(pac[0][rt][t], w_, xa[rt], false); }
        else mf_w<true>(pac[i & 1][rt][t], xa[rt], 2 * SFR(i & 1, t, (i & 6) + (kk >> 1)) + (kk & 1), i < 2 && kk == 0, W);
      SEND;
    SEND;
    SB();
  SEND;
  unsigned pol;
  {
    asm volatile("s_nop 7\n s_nop 7" : "+v"(kacc[0][0]), "+v"(kacc[0][1]), "+v"(kacc[0][2]), "+v"(kacc[1][0]), "+v"(kacc[1][1]), "+v"(kacc[1][2]));
    pol = (unsigned)__builtin_amdgcn_readlane((int)pf, 0);
    if (__builtin_expect((unsigned)__builtin_amdgcn_readlane((int)pf, 32) < NFILL || pol == 0u, 0)) { wait_ge<8>(cnt + CNT(C_FILL), NFILL, tmo); pol = ld_word(cnt + CNT(C_POL)); }
    pol &= 1u;
    BAR();
#pragma unroll
    for (int rt = 0; rt < 2; ++rt)
#pragma unroll
      for (int t = 0; t < 3; ++t) { const int ix = 4 * t + wave, o_ = rt * 6144 + (ix >> 1) * 1024;
        *(u32x2*)(lds + L_KX + o_ + lane * 16 + (ix & 1) * 8) = (u32x2){cvtpk(kacc[rt][t][0], kacc[rt][t][1]), cvtpk(kacc[rt][t][2], kacc[rt][t][3])};
        *(u32x2*)(lds + L_QX + o_ + ((ix >> 1) % 3 == 2 ? lane * 8 + (ix & 1) * 512 : lane * 16 + (ix & 1) * 8)) = (u32x2){cvtpk(qacc[rt][t][0], qacc[rt][t][1]), cvtpk(qacc[rt][t][2], qacc[rt][t][3])}; }
    if (wave == 2) { if (lane < 8) *(unsigned*)(lds + L_FLG + 4 * lane) = 0u; }
    BAR();
  }
  if (wave == 0) s_loop<0>(a, lds, h, pol, W); else if (wave == 1) s_loop<1>(a, lds, h, pol, W); else if (wave == 2) s_loop<2>(a, lds, h, pol, W); else s_loop<3>(a, lds, h, pol, W);
}
#undef kacc
#undef qacc

__device__ __forceinline__ void z_role(const Args& a, unsigned char* lds, const int h, const int wave) {
  char* ws = a.ws; asm volatile("" : "+s"(ws));
  unsigned* const cnt = (unsigned*)(ws + O_CNT); unsigned* const tmo = cnt + CNT(C_TMO);
  char* const X1 = ws + O_X1;
#define ZFV(t_, k_) (8 * (t_) + (k_))
#define ZFO(n_, j_) ((n_) < 6 ? 24 + 3 * (n_) + (j_) : 30 + 2 * (n_) + ((j_) >> 1))
  WReg W;
  const __amdgpu_buffer_rsrc_t rs_u = mkrs(ws + O_UO + h * 1024);
  unsigned xv = (unsigned)lane_id() * 16u; PINV(xv);
  unsigned pkv, pk3, pk4, pf;
  {
    const int lane = lane_id(), l15 = lane & 15, lq = lane >> 4;
    unsigned tro[3];
    { TRO_INIT();
#pragma unroll
      for (int t = 0; t < 3; ++t) tro[t] = TRO(4 * t + wave); }
    SDMA_INIT(); SXDMA_INIT(); ODMA_INIT();
    SDMA(a.Wv, 0, 0); SXDMA(0, 0); SDMA(a.Wv, 1, 1); SXDMA(1, 8192); SDMA(a.Wv, 2, 2);
    f32x4 kacc[2][3];
    u32x8 wl[3][2];
    SFOR(i, 5)
      if (i < 4) VMW(26); else VMW(24);
      BAR();
      u32x4 xa[2][4];
      SXRD((i & 1) * 8192);
      SFOR(t, 3)
        if (i < 4) { TRS0(lds_addr(lds) + tro[t] + (unsigned)SBUF(i), ZFV(t, 2 * (i & 3))); TRS1(lds_addr(lds) + tro[t] + (unsigned)SBUF(i), ZFV(t, 2 * (i & 3) + 1)); }
        else tr8(false, lds_addr(lds) + tro[t] + (unsigned)SBUF(i), wl[t][0], wl[t][1]);
      SEND;
#pragma unroll
      for (int n = 0; n < 8; ++n) PINV(xa[n >> 2][n & 3]);
      BAR();
      if (i + 2 < 5) SXDMA(i + 2, (i & 1) * 8192);
      if (i + 3 < 5) SDMA(a.Wv, i + 3, i + 3); else ODMA(i - 2, i + 3);
      SFOR(n, 24)
        constexpr int kk = n / 6, t = (n % 6) >> 1, rt = n & 1;
        if (i < 4) mf_w<true>(kacc[rt][t], xa[rt][kk], 2 * ZFV(t, 2 * (i & 3) + (kk >> 1)) + (kk & 1), i == 0 && kk == 0, W);
        else { const u32x4 w_ = HALF(wl[t][kk >> 1], kk & 1); MFV(kacc[rt][t], w_, xa[rt][kk], false); }
      SEND;
      SB();
    SEND;
    u32x4 vx[2][6];
    asm volatile("s_nop 7\n s_nop 7" : "+v"(kacc[0][0]), "+v"(kacc[0][1]), "+v"(kacc[0][2]), "+v"(kacc[1][0]), "+v"(kacc[1][1]), "+v"(kacc[1][2]));
#pragma unroll
    for (int rt = 0; rt < 2; ++rt)
#pragma unroll
      for (int t = 0; t < 3; ++t)
        *(u32x2*)(lds + rt * 6144 + (2 * t + (wave >> 1)) * 1024 + xv + (wave & 1) * 8) = (u32x2){cvtpk(kacc[rt][t][0], kacc[rt][t][1]), cvtpk(kacc[rt][t][2], kacc[rt][t][3])};
    BAR();
#pragma unroll
    for (int n = 0; n < 12; ++n) { vx[n / 6][n % 6] = *(const u32x4*)(lds + n * 1024 + xv); PINV(vx[n / 6][n % 6]); }
    SFOR(i5, 5)
      constexpr int i = i5 + 5;
      if (i == 5) VMW(24); else if (i == 6) VMW(26); else if (i == 7) VMW(28); else if (i == 8) VMW(18); else VMW(6);
      BAR();
      u32x8 res[2][3];
      SFOR(t, 2)
        const unsigned ad = lds_addr(lds) + otr[t] + (unsigned)SBUF(i);
        constexpr int n = 2 * i5 + t;
        if (n < 8) TRO4(ad, 0, ZFO(n & 7, 0)); else tr4<0>(false, ad, res[t][0]);
        if (n < 6) TRO4(ad, 1, ZFO(n & 7, 1)); else tr4<1>(false, ad, res[t][1]);
        if (n < 8) TRO4(ad, 2, ZFO(n & 7, 2)); else tr4<2>(false, ad, res[t][2]);
      SEND;
      BAR();
      if (i + 3 < 10) ODMA(i - 2, i + 3);
      if (i == 9) asm volatile("s_nop 4\n global_load_dword %0, %1, %2 sc1" : "=v"(pf) : "v"((xv >> 9) << 7), "s"(cnt + CNT(C_POL)) : "memory");
      f32x4 ua[2][2];
      SFOR(m, 24)
        constexpr int e = m >> 2, t = (m >> 1) & 1, rt = m & 1, n = 2 * i5 + t;
        if (n >= 8 || (n >= 6 && (e >> 1) == 1)) { const u32x4 w_ = HALF(res[t][e >> 1], e & 1); MFV(ua[t][rt], vx[rt][e], w_, e == 0); }
        else mf_w<false>(ua[t][rt], vx[rt][e], 2 * ZFO(n & 7, e >> 1) + (e & 1), e == 0, W);
      SEND;
      asm volatile("s_nop 7" : "+v"(ua[0][0]), "+v"(ua[0][1]), "+v"(ua[1][0]), "+v"(ua[1][1]));
#pragma unroll
      for (int t = 0; t < 2; ++t)
        st16(rs_u, xv, u_off(0u, (unsigned)(8 * OROT(i5) + 2 * wave + t)), (u32x4){cvtpk(ua[t][0][0], ua[t][0][1]), cvtpk(ua[t][0][2], ua[t][0][3]), cvtpk(ua[t][1][0], ua[t][1][1]), cvtpk(ua[t][1][2], ua[t][1][3])});
      SB();
      if (i == 7) {
        PINV(xv); pkv = xv + (unsigned)(L_B1 + wave * 12288);
#pragma unroll
        for (int n = 0; n < 6; ++n) { *(u32x4*)(lds + pkv + n * 2048) = lo4(wl[n >> 1][n & 1]); *(u32x4*)(lds + pkv + n * 2048 + 1024) = hi4(wl[n >> 1][n & 1]); }
      }
      if (i == 8) {
        PINV(xv); pk3 = xv + (unsigned)(L_B2 + wave * 12288);
#pragma unroll
        for (int t = 0; t < 2; ++t) { *(u32x4*)(lds + pk3 + t * 2048) = lo4(res[t][1]); *(u32x4*)(lds + pk3 + t * 2048 + 1024) = hi4(res[t][1]); }
      }
      if (i == 9) {
        PINV(xv); pk4 = xv + (unsigned)(L_B0 + wave * 12288);
#pragma unroll
        for (int n = 0; n < 6; ++n) { *(u32x4*)(lds + pk4 + n * 2048) = lo4(res[n & 1][n >> 1]); *(u32x4*)(lds + pk4 + n * 2048 + 1024) = hi4(res[n & 1][n >> 1]); }
      }
      SB();
    SEND;
  }
  const int lane = lane_id(), l15 = lane & 15, lq = lane >> 4;
  constexpr int L_VA = 10240;
  unsigned pol;
  {
    VMW(0); PINV(pf);
    pol = (unsigned)__builtin_amdgcn_readlane((int)pf, 0);
    __builtin_amdgcn_raw_buffer_store_b32(1u, mkrs(cnt + CNT(C_PZ + h)), lane == 0 ? 4 * wave : (int)0x80000000u, 0, 16);
    if (__builtin_expect((unsigned)__builtin_amdgcn_readlane((int)pf, 32) < NFILL || pol == 0u, 0)) { wait_ge<8>(cnt + CNT(C_FILL), NFILL, tmo); pol = ld_word(cnt + CNT(C_POL)); }
    pol &= 1u;
  }
  const __amdgpu_buffer_rsrc_t rs_x1 = mkrs(X1), rs_z = mkrs(ws + O_ZB), rs_ts = mkrs(ws + O_TS + half_of(ws + O_TS, 0u, 0u, pol));
  int ix = (l15 & 2) ? 0xEEEE : 0x4444; PINV(ix);
  unsigned ia = (unsigned)(L_VA + wave * 384 + ((l15 >> 2) & 1) * 128 + (l15 >> 3) * 64 + (l15 & 1) * 32 + lq * 8 + ((l15 >> 1) & 1) * 4);
  unsigned rda = (unsigned)(L_VA + (lq >> 1) * 768 + (l15 >> 3) * 384 + (lq & 1) * 64 + ((l15 >> 2) & 1) * 32 + (l15 & 3) * 8);
  unsigned zvo = (unsigned)((2 * wave + (lq >> 1)) * 16384 + h * 256 + l15 * 16 + (lq & 1) * 8);
  unsigned evo = (unsigned)((2 * wave + (lq & 1) + 8 * (lq >> 1)) * 16384 + h * 256 + l15 * 16);
  unsigned gvo = xv + (unsigned)(wave * 2048), gv2 = xv + 16384u + (unsigned)((wave >> 1) * 1024);
  u64 hmask = 0xffffffffull << (32 * (wave & 1)); PINS(hmask);
  const unsigned gm0 = lds_addr(lds) + (unsigned)(L_MEM + ((wave - h) & 3) * 2048), gm2 = lds_addr(lds) + (unsigned)(L_MEM + 8192 + (wave >> 1) * 1024);
  PINV(ia); PINV(rda); PINV(zvo); PINV(gvo); PINV(gv2); PINV(pkv); PINV(pk3); PINV(pk4);
  unsigned tsv = ~0u, tries = 0; int dly = a.d[1]; PINS(dly);
#define PLO(b_, n_) (*(const u32x4*)(lds + (b_) + (n_) * 2048))
#define PHI(b_, n_) (*(const u32x4*)(lds + (b_) + (n_) * 2048 + 1024))
#define MV(t_, f_, w_) SMFV(av[t_], f_, w_)
#define VW(t_, k_) SMFW(av[t_], A[k_], ZFV(t_, k_))
#define MO(n_, j_, w_) SMFV(az[n_], va[j_], w_)
#define OW(n_, j_) SMFW(az[n_], va[j_], ZFO(n_, j_))
#define AFR(k_) (*(const u32x4*)(lds + afb + (k_) * 1024))
  unsigned afb = xv + L_MEM; PINV(afb);
#define G_LOAD() do { asm volatile("s_mov_b32 m0, %3\n s_nop 0\n global_load_lds_dwordx4 %0, %4 sc1\n global_load_lds_dwordx4 %0, %4 offset:1024 sc1\n s_mov_b32 m0, %2\n s_mov_b64 exec, %5\n global_load_lds_dwordx4 %1, %4 sc1\n s_mov_b64 exec, -1\n s_waitcnt vmcnt(2)\n s_barrier" \
      :: "v"(gvo), "v"(gv2), "s"(gm2), "s"(gm0), "s"(xs), "s"(hmask) : "memory"); SB(); } while (0)
  u32x4 ts_desc = mkdesc(ws + O_TS + half_of(ws + O_TS, 0u, 0u, pol) + 16384 + 256 * h);
  int w3m = wave, dzs = a.d[9]; PINS(w3m); PINS(dzs); PINS(ts_desc);
  u32x4 z_desc = mkdesc(ws + O_ZB); PINS(z_desc);
#pragma unroll 1
  for (int s = 0; s < 64; ++s) {
    f32x4 av[3], az[10];
#pragma unroll
    for (int n = 0; n < 10; ++n) { az[n] = ZERO4; PINV(az[n]); }
    unsigned snp = (unsigned)(s + 1) * 0x01010101u, snv = snp, adj = 0; PINS(snp); PINV(snv);
    const unsigned so = half_of(X1, XSLOT(s), 0u, pol);
    const char* xs = X1 + so; int tss = TS_STEP(s); PINS(xs); PINS(tss);
    unsigned zs[5], tnx = (unsigned)__builtin_amdgcn_readfirstlane(wave == 3 && tsv != ~0u ? tsv + (unsigned)dly : 0u); PINS(tnx);
    { const unsigned z0 = half_of(ws + O_ZB, ZSLOT(s), 0u, pol);
#pragma unroll
      for (int i = 0; i < 5; ++i) { zs[i] = z0 + (unsigned)OROT(i) * (8u * 16384u); PINS(zs[i]); } }
    if (s == 1 && wave == 3) x1_light(rs_x1, so, tmo);
    SB();
    asm volatile("s_branch .Lzre\n"
                 ".Lzbad:\n s_waitcnt lgkmcnt(0)\n s_add_u32 %0, %0, 1\n s_add_u32 %1, %1, 1\n s_cmp_lt_u32 %0, 4\n s_cbranch_scc1 .Lznow\n s_sleep 8\n"
                 ".Lznow:\n s_cmp_lg_u32 %6, 3\n s_cbranch_scc1 .Lzre\n s_memrealtime vcc\n s_waitcnt lgkmcnt(0)\n s_add_u32 vcc_lo, vcc_lo, %7\n s_and_b32 vcc_lo, vcc_lo, -2\n v_mov_b32 %2, vcc_lo\n"
                 " s_mov_b64 exec, 1\n buffer_store_dword %2, off, %4, %5 sc1\n s_mov_b64 exec, -1\n v_mov_b32 %2, %3\n"
                 ".Lzre:" : "+s"(adj), "+s"(tries) : "v"(snv), "s"(snp), "s"(ts_desc), "s"(tss), "s"(w3m), "s"(dzs) : "vcc", "scc", "memory");
    SB();
    u32x8 ra = cat8(PLO(pkv, 0), PHI(pkv, 0)), rb = cat8(PLO(pkv, 1), PHI(pkv, 1));
    av[0] = ZERO4; av[1] = ZERO4; av[2] = ZERO4;
    asm volatile("" : "+v"(av[0]), "+v"(av[1]), "+v"(av[2]));
    SB();
    wait_rt(tnx);
    BAR();
    G_LOAD();
    u32x4 A[10], lo, hi, va[3];
    unsigned w0, w1;
    float ta, tb_;
    u64 nanm;
    DS(A[0] = AFR(0)); DS(A[2] = AFR(2)); DS(A[4] = AFR(4)); DS(A[6] = AFR(6));
    VW(0, 0); VW(1, 0); asm volatile("s_waitcnt vmcnt(0)\n s_barrier" ::: "memory"); SB();
    VW(2, 0); DS(A[8] = AFR(8));
    VW(0, 2); DS(A[9] = AFR(9));
    VW(1, 2); DS(A[1] = AFR(1));
    VW(2, 2); DS(A[3] = AFR(3));
    VW(0, 4); DS(A[5] = AFR(5));
    VW(0, 6); DS(A[7] = AFR(7));
    MV(0, A[8], ra); DS(lo = PLO(pkv, 2));
    MV(0, A[9], rb); DS(hi = PHI(pkv, 2)); ra = cat8(lo, hi);
    VW(0, 1); DS(lo = PLO(pkv, 3));
    VW(0, 3); DS(hi = PHI(pkv, 3)); rb = cat8(lo, hi);
    VW(0, 5); VW(0, 7);
    VW(1, 4); VW(1, 6);
    MV(1, A[8], ra); DS(lo = PLO(pkv, 4)); ADD1(ta, av[0][0], av[0][2]);
    VW(1, 1); DS(hi = PHI(pkv, 4)); ra = cat8(lo, hi); ADD1(tb_, av[0][1], av[0][3]);
    VW(1, 3); LGW(2); CVT1(w0, ta, tb_);
    MV(1, A[9], rb); DS(LW32(ia, 0, w0));
    VW(1, 5); DS(lo = PLO(pkv, 5)); NANM(nanm, ta, tb_);
    VW(1, 7); DS(hi = PHI(pkv, 5)); rb = cat8(lo, hi);
    VW(2, 4);
    if (__builtin_expect(nanm != 0ull, 0)) {
      unsigned m = ~0u;
#define Z_BACK() asm volatile("v_cmp_eq_u32 vcc, 0, %0\n s_cmp_lt_u32 %1, %2\n s_cselect_b64 vcc, vcc, 0\n s_cbranch_vccnz .Lzbad" : "+v"(m) : "s"(tries), "n"(CH_SPIN_CAP) : "vcc", "scc")
#pragma unroll
      for (int k = 0; k < 10; k += 2) m = sent_min(m, A[k].x, A[k + 1].x);
      Z_BACK();
#pragma unroll
      for (int k = 0; k < 10; ++k) m = sent_min(m, A[k]);
      Z_BACK();
    }
    asm volatile("" :: "s"(xs), "s"(tnx), "s"(tries), "s"(tss), "s"(snp), "s"(ts_desc), "s"(w3m), "s"(dzs), "v"(snv));
    PINS(adj);
    u32x2 g0, g1;
#define VR0(j_) DS(g0 = trrd(lds, rda + (unsigned)((j_) * 1536)))
#define VR1(j_) do { DS(g1 = trrd(lds, rda + (unsigned)((j_) * 1536 + 128))); va[j_] = (u32x4){g0.x, g0.y, g1.x, g1.y}; } while (0)
    VW(2, 6); LBAR(2);
    MV(2, A[8], ra); VR0(0); ADD1(ta, av[1][0], av[1][2]);
    VW(2, 1); VR1(0); ADD1(tb_, av[1][1], av[1][3]);
    VW(2, 3); DS(lo = PLO(pk4, 0)); CVT1(w0, ta, tb_);
    VW(2, 5); DS(LW32(ia, 1536, w0));
    MV(2, A[9], rb); DS(hi = PHI(pk4, 0)); ra = cat8(lo, hi);
    VW(2, 7); DS(lo = PLO(pk4, 1));
    OW(0, 0); DS(hi = PHI(pk4, 1)); rb = cat8(lo, hi);
    OW(1, 0); LBAR(3);
    OW(2, 0); VR0(1); ADD1(ta, av[2][0], av[2][2]);
    OW(3, 0); VR1(1); ADD1(tb_, av[2][1], av[2][3]);
    OW(4, 0); LGW(2); CVT1(w0, ta, tb_);
    MO(8, 0, ra); DS(LW32(ia, 3072, w0));
    MO(9, 0, rb); DS(lo = PLO(pk3, 0));
    OW(5, 0); DS(hi = PHI(pk3, 0)); ra = cat8(lo, hi);
    OW(6, 0); DS(lo = PLO(pk3, 1));
    OW(7, 0); LGW(3); MEMB(); __builtin_amdgcn_s_barrier(); MEMB(); SB();
    OW(0, 1); VR0(2); VR1(2);
    OW(1, 1); DS(hi = PHI(pk3, 1)); rb = cat8(lo, hi);
    OW(2, 1);
    MO(6, 1, ra); DS(lo = PLO(pk4, 2));
    OW(3, 1); DS(hi = PHI(pk4, 2)); ra = cat8(lo, hi);
#define ZSW() do { asm volatile("v_permlane32_swap_b32 %0, %1" : "+v"(w0), "+v"(w1)); SB(); } while (0)
#define ZST(i_) do { const u32x2 w_ = {w0, w1}; asm volatile("buffer_store_dwordx2 %0, %1, %2, %3 offen sc1" :: "v"(w_), "v"(zvo), "s"(z_desc), "s"(zs[i_]) : "memory"); SB(); } while (0)
    OW(0, 2);
    OW(1, 2);
    MO(7, 1, rb); DS(lo = PLO(pk4, 3));
    OW(2, 2); DS(hi = PHI(pk4, 3)); rb = cat8(lo, hi); ADD2(ta, tb_, az[0]);
    OW(3, 2); LGW(2); CVT1(w0, ta, tb_); ADD1(ta, az[1][0], az[1][2]);
    MO(8, 1, ra); DS(lo = PLO(pk4, 4)); ADDCVT(w1, ta, az[1]);
    OW(6, 2); DS(hi = PHI(pk4, 4)); ra = cat8(lo, hi); ZSW();
    OW(7, 2); ZST(0); ADD2(ta, tb_, az[2]);
    OW(4, 1); LGW(2); CVT1(w0, ta, tb_); ADD1(ta, az[3][0], az[3][2]);
    MO(9, 1, rb); DS(lo = PLO(pk4, 5)); ADDCVT(w1, ta, az[3]);
    OW(5, 1); DS(hi = PHI(pk4, 5)); rb = cat8(lo, hi); ZSW();
    OW(4, 2); ZST(1); ADD2(ta, tb_, az[6]);
    OW(5, 2); LGW(2); CVT1(w0, ta, tb_); ADD1(ta, az[7][0], az[7][2]);
    MO(8, 2, ra); LGW(0); ADDCVT(w1, ta, az[7]);
    MO(9, 2, rb);
    asm volatile("v_add_f32 v2, %2, %4\n v_permlane32_swap_b32 %0, %1\n v_add_f32 v0, %3, %5\n v_cvt_pk_bf16_f32 v0, v2, v0\n v_add_f32 v2, %6, %8\n v_add_f32 v1, %7, %9\n v_cvt_pk_bf16_f32 v1, v2, v1"
                 : "+v"(w0), "+v"(w1) : "v"(az[4][0]), "v"(az[4][1]), "v"(az[4][2]), "v"(az[4][3]), "v"(az[5][0]), "v"(az[5][1]), "v"(az[5][2]), "v"(az[5][3]) : "v0", "v1", "v2", "v3");
    { const u32x2 w_ = {w0, w1};
      asm volatile("buffer_store_dwordx2 %0, %1, %2, %3 offen sc1\n v_add_f32 v3, %6, %8\n v_permlane32_swap_b32 v0, v1\n v_add_f32 v2, %7, %9\n buffer_store_dwordx2 v[0:1], %1, %2, %4 offen sc1\n v_cvt_pk_bf16_f32 v2, v3, v2\n"
                   "v_add_f32 %10, %10, %12\n v_add_f32 v3, %11, %13\n v_cvt_pk_bf16_f32 v3, %10, v3\n s_nop 1\n v_permlane32_swap_b32 v2, v3\n buffer_store_dwordx2 v[2:3], %1, %2, %5 offen sc1"
                   :: "v"(w_), "v"(zvo), "s"(z_desc), "s"(zs[3]), "s"(zs[2]), "s"(zs[4]), "v"(az[8][0]), "v"(az[8][1]), "v"(az[8][2]), "v"(az[8][3]), "v"(az[9][0]), "v"(az[9][1]), "v"(az[9][2]), "v"(az[9][3]) : "v0", "v1", "v2", "v3", "memory"); }
    SB();
    ADAPT(dly, adj);
    if (s < 63) {
      if (s + PRE < 64) {
        const unsigned e0 = half_of(ws + O_ZB, ZSLOT((s + PRE)), 0u, pol);
        st16(rs_z, evo, e0, SENT4); st16(rs_z, evo, e0 + 16u * 16384u, SENT4); st16(rs_z, lane < 32 ? evo : 0x80000000u, e0 + 32u * 16384u, SENT4);
      }
      if (wave == 3) { tsv = ts_settle(rs_ts, tss, 0u, a.d[4]); if (tsv != ~0u && (int)(tsv - tnx) > dzs + a.d[10]) tsv += (unsigned)a.d[12]; }
    }
  }
}

#define WCK 3
#define UNAP 0
__device__ __forceinline__ void r_role(const Args& a, unsigned char* lds, const int c, const int wave) {
  char* ws = a.ws; asm volatile("" : "+s"(ws));
  unsigned* const cnt = (unsigned*)(ws + O_CNT); unsigned* const tmo = cnt + CNT(C_TMO);
  char* const X1 = ws + O_X1; char* const P = ws + O_PS;
  const int lane = lane_id(), l15 = lane & 15, lq = lane >> 4;
  const unsigned pol = wait_ge<8>(cnt + CNT(C_POL), 1u, tmo) & 1u;
  const __amdgpu_buffer_rsrc_t rs_x1 = mkrs(X1 + (c >> 5) * 16384 + (c & 31) * 256), rs_p = mkrs(P + half_of(P, 0u, 0u, pol)), rs_z = mkrs(ws + O_ZB + (size_t)c * 16384);
  const __amdgpu_buffer_rsrc_t rs_ts = mkrs(ws + O_TS + half_of(ws + O_TS, 0u, 0u, pol));
  unsigned xv = (unsigned)lane * 16u; PINV(xv);
  {
    const u32x4 ones = {~0u, ~0u, ~0u, ~0u};
    static_assert(PRE == 4, "");
#pragma unroll
    for (int i = 0; i < 8; ++i) st16(rs_z, xv + (unsigned)((i & 3) * 1024), half_of(ws + O_ZB, ZSLOT(wave), 0u, pol) + (unsigned)((i >> 2) * 4096), SENT4);
    st16(rs_p, lane < 32 ? xv : 0x80000000u, (unsigned)(PSLOT(wave) + (c >> 4) * 16384 + (c & 15) * 512), SENT4);
    st16(rs_x1, lane < 16 ? xv : 0x80000000u, half_of(X1, XSLOT((wave + 1)), 0u, pol), SENT4);
    if (c < 4) st16(rs_ts, xv, (unsigned)((c >> 1) * 16384 + (c & 1) * 4096 + wave * 1024), ones);
    if (wave == 0) {
      const int q = lane >> 2, r = (q >> 2) * 2 + (q & 1);
      const unsigned v = *(const unsigned*)(a.mem0 + r * 640 + 16 * c + 4 * (lane & 3) + (q & 2));
      __builtin_amdgcn_raw_buffer_store_b32(v == SENT ? ~0u : v, rs_x1, lane * 4, (int)half_of(X1, 0u, 0u, pol), 16);
    }
    VMW(0);
    BAR();
    if (wave != 0) return;
    if (lane == 0) __hip_atomic_fetch_add((gu32*)(cnt + CNT(C_FILL)), 1u, RLX_AGENT);
  }
  const u32x4 rs_u = mkdesc(ws + O_UO);
  const __amdgpu_buffer_rsrc_t rs_pw = mkrs(cnt + CNT(C_PW) + ((c >> 1) & 3));
  int ix = (l15 & 2) ? 0xEEEE : 0x4444; PINV(ix);
  unsigned pm = (unsigned)(lq * 256 + l15 * 8);
  int xo = (4 * lq + (l15 & 3)) * 16 + (l15 >> 2) * 4; unsigned sel = (l15 & 1) ? 0x07060302u : 0x01000504u;
  int tsa = (lane & 31) * 4;
  PINV(pm); PINV(xo); PINV(sel); PINV(tsa);
  WReg W;
#define U_LOAD(sg_, def_) SFOR(i, 16) ld_w(rs_u, xv, u_off((unsigned)(sg_), (unsigned)c) + (unsigned)((i >> 1) * 4096), (i & 1) * 2048, i, (def_) && (i & 3) == 0, W); nap4<UNAP>(); SEND
  wait_ge<8>(cnt + CNT(C_FILL), NFILL, tmo);
  wait_heads<8>(cnt + CNT(C_PZ) + ((c >> 1) & 3), 0, 1u, tmo);
  unsigned wl = __builtin_amdgcn_raw_buffer_load_b32(rs_pw, tsa * 32, 0, 16), tsl = 0; int tso = TS_OFF(lane), two = tsa * 32; PINV(tso); PINV(two); PINV(wl);
  const u32x4 ts_desc = mkdesc(ws + O_TS + half_of(ws + O_TS, 0u, 0u, pol));
  U_LOAD(0, true);
  unsigned tsv = ~0u, tpub; int dlp = a.d[2]; PINS(dlp);
  { u64 t_; RT_MARK(t_); tpub = (unsigned)t_ - 4096u; }
#pragma unroll 1
  for (int s = 0; s < 63; ++s) {
    unsigned ps[5], zs[2], xs;
#pragma unroll
    for (int j = 0; j < 5; ++j) { ps[j] = (unsigned)PSLOT(s) + (unsigned)((j >> 1) * 16384 + (j & 1) * 4096); PINS(ps[j]); }
    zs[0] = half_of(ws + O_ZB, ZSLOT(s), 0u, pol); zs[1] = zs[0] + 4096u; xs = half_of(X1, XSLOT((s + 1)), 0u, pol); PINS(zs[0]); PINS(zs[1]); PINS(xs);
    int tss = TS_STEP(s); PINS(tss);
    asm volatile("s_waitcnt vmcnt(32)" : "+v"(tsl) :: "memory");
    tsv = s < 2 ? ~0u : ts_max(tsl);
    if (__builtin_expect(s >= 2 && (tsv == ~0u || (int)(tsv - tpub) < a.d[8]), 0)) { tsv = ts_settle(rs_ts, tss, tsv, a.d[4]); }
    int wo = __all(wl >= (unsigned)(s + 2)) ? (int)0x80000000u : two; PINV(wo);
    f32x4 o = ZERO4; PINV(o);
    unsigned tis = tsv + (unsigned)dlp; (void)tis;
#define R_LIGHT() do { const unsigned ln = (unsigned)lane_id(); light(mkrs(ws + O_ZB), ln < 32u ? zs[0] + (unsigned)c * 16384u + ln * 256u : (unsigned)(O_PS - O_ZB) + half_of(P, 0u, 0u, pol) + ps[4] + (ln - 32u) * 128u, 0u, tmo); } while (0)
    if (__builtin_expect(tsv & 1u, 0)) { R_LIGHT(); } else tis = wait_rt(tsv + (unsigned)dlp);
    unsigned adj = 0, pk, tsx, wn; u64 nanm;
    u32x4 z[8], p[16]; u32x2 m[8];
#define R_LOAD() do { PINV(xv); PINV(pm); \
      _Pragma("unroll") for (int i = 0; i < 8; ++i) DS(z[i] = ld16(rs_z, xv + (unsigned)((i & 3) * 1024), zs[i >> 2])); \
      _Pragma("unroll") for (int i = 0; i < 16; ++i) DS(p[i] = ld16(rs_p, xv + (unsigned)((i & 3) * 1024), ps[i >> 2])); \
      _Pragma("unroll") for (int i = 0; i < 8; ++i) DS(m[i] = ld8(rs_p, pm + (unsigned)((i >> 1) * 1024 + (i & 1) * 128), ps[4])); \
      DS(tsx = __builtin_amdgcn_raw_buffer_load_b32(rs_ts, tso, tss, 16));        \
      DS(wn = __builtin_amdgcn_raw_buffer_load_b32(rs_pw, wo, 0, 16)); } while (0)
#define R_PROD() do { \
      SFOR(i, 16) SMFW(o, p[i], i); SEND; \
      _Pragma("unroll") for (int i = 0; i < 4; ++i) { const u32x4 a_ = {m[2 * i].x, m[2 * i].y, m[2 * i + 1].x, m[2 * i + 1].y}; const u32x8 b_ = cat8(z[2 * i], z[2 * i + 1]); SMFV(o, a_, b_); } \
      float ta, tb_; unsigned q; \
      asm volatile("s_nop 7\n v_add_f32 %2, %5, %7\n v_add_f32 %3, %6, %8\n v_cvt_pk_bf16_f32 %0, %2, %3\n v_cmp_u_f32_e64 %1, %2, %3\n s_nop 0\n" \
                   "v_mov_b32_dpp %4, %0 quad_perm:[1,0,3,2] row_mask:0xf bank_mask:0xf\n v_perm_b32 %0, %0, %4, %9" \
                   : "=&v"(pk), "=&s"(nanm), "=&v"(ta), "=&v"(tb_), "=&v"(q) : "v"(o[0]), "v"(o[1]), "v"(o[2]), "v"(o[3]), "v"(sel)); } while (0)
    R_LOAD();
    R_PROD();
    if (__builtin_expect(nanm != 0ull, 0)) {
      while (spin_check(adj, tmo)) {
        if (adj > 2u) R_LIGHT();
        else { const unsigned t_ = ts_max(tsx); if (t_ != ~0u) wait_rt(t_ + (unsigned)dlp); }
        R_LOAD();
        o = ZERO4; asm volatile("s_nop 1" : "+v"(o)); SB();
        R_PROD();
        if (nanm == 0ull) break;
        if ((adj & 7u) == 0u) {
          unsigned e = ~0u;
#pragma unroll
          for (int i = 0; i < 16; ++i) e = sent_min(e, p[i]);
#pragma unroll
          for (int i = 0; i < 8; ++i) e = sent_min(sent_min(e, z[i]), m[i].x, m[i].y);
          if (!__any(e == 0u)) break;
        }
      }
    }
    __builtin_amdgcn_raw_buffer_store_b32(pk, rs_x1, xo, (int)xs, 16);
    SB();
    PINU(tsx);
    { u64 t_; RT_MARK(t_); tpub = (unsigned)t_; }
    ADAPT(dlp, adj);
    if (s + PRE + 1 < 64) st16(rs_x1, lane < 16 ? xv : 0x80000000u, half_of(X1, XSLOT((s + PRE + 1)), 0u, pol), SENT4);
    {
      wl = max(wl, wn);
      if (__builtin_expect(!__all(wl >= (unsigned)(s + 2)), 0)) { wait_heads<4>(cnt + CNT(C_PW) + ((c >> 1) & 3), 0, (unsigned)(s + 2), tmo); }
      wait_rt(tpub + (unsigned)a.d[3]);
      asm volatile("s_nop 4\n buffer_load_dword %0, %1, %2, %3 offen sc1" : "=v"(tsl) : "v"(tso), "s"(ts_desc), "s"(TS_STEP(s + 1)) : "memory");
      U_LOAD(s + 1, false);
    }
  }
  VMW(0);
}

__device__ __forceinline__ void w_role(const Args& a, unsigned char* lds, const int h, const int wave) {
  char* ws = a.ws; asm volatile("" : "+s"(ws));
  unsigned* const cnt = (unsigned*)(ws + O_CNT); unsigned* const tmo = cnt + CNT(C_TMO);
  const int lane = lane_id(), l15 = lane & 15, lq = lane >> 4;
  WReg W;
  {
    ODMA_INIT();
    ODMA(0, 0); ODMA(1, 1); ODMA(2, 2);
#pragma unroll
    for (int i = 0; i < 5; ++i) {
      if (i < 3) VMW(24); else if (i == 3) VMW(12); else VMW(0);
      BAR();
#pragma unroll
      for (int t = 0; t < 2; ++t) {
        const unsigned ad = lds_addr(lds) + otr[t] + (unsigned)SBUF(i);
        TRO4(ad, 0, 6 * i + 3 * t); TRO4(ad, 1, 6 * i + 3 * t + 1); TRO4(ad, 2, 6 * i + 3 * t + 2);
      }
      if (i + 3 < 5) { BAR(); ODMA(i + 3, i + 3); }
    }
  }
  const __amdgpu_buffer_rsrc_t rs_u = mkrs(ws + O_UO + h * 1024);
  const __amdgpu_buffer_rsrc_t rs_pv = mkrs(cnt + CNT(C_PV + h)), rs_pw = mkrs(cnt + CNT(C_PW + h));
  unsigned xv = (unsigned)lane * 16u; int flo = lane == 0 ? 4 * wave : (int)0x80000000u; PINV(xv); PINV(flo);
  unsigned uo[10];
#pragma unroll
  for (int i = 0; i < 10; ++i) { uo[i] = (unsigned)(8 * OROT(i >> 1) + 2 * wave + (i & 1)) << 15; PINS(uo[i]); }
  const __amdgpu_buffer_rsrc_t rs_po = mkrs(cnt + CNT(C_PO)); int poo = wave == 0 ? (lane % 24) / 6 * 128 + (lane % 6) * 4 : (int)0x80000000u; PINV(poo);
  const unsigned pol = wait_ge<8>(cnt + CNT(C_POL), 1u, tmo) & 1u, kkey = K_KEY();
  const char* vsrc = ws + O_VF + (size_t)h * 32768 + (wave >> 1) * 16384 + (wave & 1) * 3072;
#define V_DMA(sg_) do { m0_set(lds_addr(lds) + (unsigned)(((sg_) % 3) * 12288 + wave * 3072)); const char* p_ = vsrc + KSLOT(sg_); asm volatile("s_nop 4" : "+s"(p_)); glds_x<0>(p_, xv); glds_x<1024>(p_, xv); glds_x<2048>(p_, xv); } while (0)
  if (TM == 0) touch_n<5>(a, 4 * h + wave, 0, 64);
  if (TM == 3) touch_n<4>(a, 4 * h + wave, 0, 96);
  BAR();
  wait_t<2>(cnt + CNT(C_PV + h), 1, 2u, tmo);
  V_DMA(1); V_DMA(2);
  VMW(3);
  int nxt = 3;
#pragma unroll 1
  for (int s = 1; s < 64; ++s) {
    unsigned ub = u_off((unsigned)s, 0u), sv = (unsigned)s, va = lds_addr(lds) + (unsigned)((s % 3) * 12288) + xv; PINS(ub); PINV(sv); PINV(va);
    f32x4 acc[2][2]; u32x4 vx[12], pw, w; unsigned sp; int vprog; bool late, far;
    BAR();
#pragma unroll
    for (int n = 0; n < 12; ++n) DS(vx[(n >> 1) + 6 * (n & 1)] = *(const LAS u32x4*)(size_t)(va + ((n >> 1) + 6 * (n & 1)) * 1024));
    SFOR(t, 11)
      SFOR(m, 12)
        constexpr int e = m >> 1, rt = m & 1, c = (t + 1) & 1;
        if (t < 10) { mf_w<false>(acc[t & 1][rt], vx[6 * rt + e], 6 * (t % 10) + e, e == 0, W); SB(); }
        else if (m == 0) { asm volatile("s_nop 7" : "+v"(acc[1][0]), "+v"(acc[1][1])); SB(); }
        if (t > 0 && m >= 2 && m < 6) { if (t < 10) FILL(); CVT1(w[(m + 2) & 3], acc[c][(m >> 1) - 1 & 1][2 * (m & 1)], acc[c][(m >> 1) - 1 & 1][2 * (m & 1) + 1]); }
        if (t > 0 && m == 6) { if (t < 10) __builtin_amdgcn_raw_buffer_store_b128(w, rs_u, (int)xv, (int)(ub + uo[(t + 9) % 10]), 16); else st16(rs_u, xv, ub + uo[9], w); SB(); }
        if (t == 0 && m == 1) { pw = ld16(rs_pv, 0u, 0u); SB(); }
        if (t == 0 && m == 4) { sp = __builtin_amdgcn_raw_buffer_load_b32(rs_po, poo, 0, 16); SB(); }
        if (t == WCK && m == 8) { vprog = (int)min_t(pw, 1); PINS(vprog); far = wave == 0 && __any((int)sp < s + 2 - WGATE); MEMB(); SB(); }
        if (t == WCK && m == 10) { __builtin_amdgcn_raw_buffer_store_b32(sv, rs_pw, flo, 0, 16); SB(); }
        if (t == WCK + 1 && m == 0) {
          late = nxt <= s + 1;
          const int upto = min(min(vprog, s + 2), 63);
          while (nxt <= upto) { V_DMA(nxt); ++nxt; }
          SB(); }
        if (t == 6 && m == 8) { pw = ld16(rs_pv, 0u, 0u); SB(); }
      SEND;
    SEND;
    if (__builtin_expect(nxt <= s + 1 || far, 0)) {
      const int upto = min(min((int)min_t(pw, 1), s + 2), 63);
      while (nxt <= upto) { V_DMA(nxt); ++nxt; }
      VMW(0); __builtin_amdgcn_raw_buffer_store_b32((unsigned)(s + 1), rs_pw, flo, 0, 16);
      if (s < 63) {
        if (far) { unsigned spins = 0; while (__any((int)__builtin_amdgcn_raw_buffer_load_b32(rs_po, poo, 0, 16) < s + 2 - WGATE) && spin_check(spins, tmo)) __builtin_amdgcn_s_sleep(24); MEMB(); }
        if (nxt <= s + 1) { wait_t<1>(cnt + CNT(C_PV + h), 1, (unsigned)(s + 1), tmo); V_DMA(nxt); ++nxt; VMW(0); }
      }
    } else if (late) asm volatile("s_waitcnt vmcnt(%0)" :: "n"(11 - WCK) : "memory");
  }
}

#define TSTRICT 24
#define LDS16(ad_) (*(LAS u32x4*)(size_t)(ad_))
struct TReg { u32x32 b[12]; u32x16 e; };
#define TR_SW(g_, X) switch (g_) { case 0: X("a", "{a[0:31]}", T.b[0]); break; case 1: X("a", "{a[32:63]}", T.b[1]); break; case 2: X("a", "{a[64:95]}", T.b[2]); break; case 3: X("a", "{a[96:127]}", T.b[3]); break; \
    case 4: X("a", "{a[128:159]}", T.b[4]); break; case 5: X("a", "{a[160:191]}", T.b[5]); break; case 6: X("a", "{a[192:223]}", T.b[6]); break; case 7: X("a", "{a[224:255]}", T.b[7]); break; \
    case 8: X("v", "{v[110:141]}", T.b[8]); break; case 9: X("v", "{v[142:173]}", T.b[9]); break; case 10: X("v", "{v[174:205]}", T.b[10]); break; case 11: X("v", "{v[206:237]}", T.b[11]); break; default: X("v", "{v[238:253]}", T.e); break; }
__device__ __forceinline__ void mf_t(f32x4& c, const u32x4 x, const int d, const bool first, const TReg& T) {
  const int r = d < 64 ? 4 * d : 4 * d - 146;
#define X_(rf_, cs_, v_) if (first) asm volatile("v_mfma_f32_16x16x32_bf16 %0, " rf_ "[%c2:%c3], %1, 0" : "=&v"(c) : "v"(x), "n"(r), "n"(r + 3), cs_(v_)); \
      else asm volatile("v_mfma_f32_16x16x32_bf16 %0, " rf_ "[%c2:%c3], %1, %0" : "+v"(c) : "v"(x), "n"(r), "n"(r + 3), cs_(v_))
  TR_SW(d >> 3, X_);
#undef X_
}
__device__ __forceinline__ void tr_t(const unsigned ad0, const unsigned ad1, const int off, const int d, TReg& T) {
  const int r = d < 64 ? 4 * d : 4 * d - 146;
#define Y_(md_, rf_, cs_, v_) asm volatile("ds_read_b64_tr_b16 " rf_ "[%c5:%c6], %1 offset:%3\n ds_read_b64_tr_b16 " rf_ "[%c7:%c8], %1 offset:%4\n ds_read_b64_tr_b16 " rf_ "[%c9:%c10], %2 offset:%3\n ds_read_b64_tr_b16 " rf_ "[%c11:%c12], %2 offset:%4\n s_waitcnt lgkmcnt(0)" \
    : md_ cs_(v_) : "v"(ad0), "v"(ad1), "n"(off), "n"(off + 1536), "n"(r), "n"(r + 1), "n"(r + 2), "n"(r + 3), "n"(r + 4), "n"(r + 5), "n"(r + 6), "n"(r + 7) : "memory")
#define X_(rf_, cs_, v_) if ((d & 7) == 0) Y_("=&", rf_, cs_, v_); else Y_("+", rf_, cs_, v_)
  TR_SW(d >> 3, X_);
#undef X_
#undef Y_
}
__device__ __forceinline__ void t_role(const Args& a, unsigned char* lds, const int h, const int isv, const int wave) {
  char* ws = a.ws; asm volatile("" : "+s"(ws));
  unsigned* const cnt = (unsigned*)(ws + O_CNT); unsigned* const tmo = cnt + CNT(C_TMO);
  const int lane = lane_id(), l15 = lane & 15, lq = lane >> 4;
  TReg T;
#define TBUF(j_) (24576u + (unsigned)(((j_) + 3) % 6) * 20480u)
#define TSRC(c_, r_) ((((c_) - (r_) * 80u < 64u ? (r_) * 80u + (((c_) - (r_) * 80u + 16u * (unsigned)h) & 63u) : (c_)) ^ ((r_) & 15u)) * 16u)
#define TDMA1(src_, dst_, i_) do { const unsigned c = (unsigned)((wave + 4 * (i_)) * 64 + lane_id()), r = c / 80u; glds(src_, TSRC(c, r), (dst_) + (i_) * 4096u); } while (0)
#define TROW(j_) (16 * ((j_) + 6))
#define TDMA(j_) do { _Pragma("unroll") for (int xi_ = 0; xi_ < 5; ++xi_) TDMA1(a.Bx + (size_t)TROW(j_) * 640, lds_addr(lds) + TBUF(j_) + (unsigned)wave * 1024u, xi_); } while (0)
  const __amdgpu_buffer_rsrc_t rs_fl = mkrs(cnt + CNT((isv ? C_PV : C_PT_) + h)), rs_o = mkrs(ws + O_KF + (size_t)h * 32768);
  unsigned vo[2], kkey;
  {
    unsigned ad0[5], ad1[5], mvo[6], qvo[3], xvo, xr, x5;
    { TRO_INIT();
#pragma unroll
      for (int t = 0; t < 5; ++t) { const bool isq = t == 4 || wave == 3; const unsigned tile = t == 4 ? 4u + (unsigned)(wave >> 1) : (wave == 3 ? (unsigned)t : (unsigned)(4 * wave + t));
        ad0[t] = lds_addr(lds) + 32768u + TRO(tile) + (isq ? 24576u : 0u); ad1[t] = lds_addr(lds) + 32768u + (isq ? TRO(tile + 6u) + 24576u : TRO(tile) + 12288u); PINV(ad0[t]); PINV(ad1[t]); } }
#pragma unroll
    for (int q = 0; q < 6; ++q) { const unsigned c = (unsigned)((wave + 4 * q) * 64 + lane), r = c / 24u, pc = (c - r * 24u) ^ (((r >> 1) & 1u) * 2u + ((r >> 3) & 1u) * 4u);
      mvo[q] = r * 12288u + 16u * pc; PINV(mvo[q]);
      if (q < 3) { qvo[q] = pc < 12u ? mvo[q] : mvo[q] + 32u * 12288u - 192u; PINV(qvo[q]); } }
    xvo = (unsigned)((8 * wave + (lane >> 3)) * 1280 + ((lane & 7) ^ (lane >> 3)) * 16);
    xr = lds_addr(lds) + (unsigned)(l15 * 128 + (lq ^ (l15 & 7)) * 16); x5 = xr + (unsigned)((wave & 1) * 2048);
    PINV(xvo); PINV(xr); PINV(x5);
    const u16* Wm = (isv ? a.Wv : a.Wk) + 192 * h; const u16* Wh = a.Wq + 192 * h + 96 * isv;
#define TW_DMA(i_) do { const int r_ = 128 * KROT((i_) >> 1) + 64 * ((i_) & 1); const unsigned d_ = lds_addr(lds) + (unsigned)(32768 + ((i_) % 3) * 36864 + wave * 1024); \
      _Pragma("unroll") for (int q_ = 0; q_ < 2; ++q_) glds(a.Bx + (32 + 32 * q_) * 640 + r_, xvo, lds_addr(lds) + (unsigned)(((i_) & 3) * 8192 + q_ * 4096 + wave * 1024)); \
      _Pragma("unroll") for (int q_ = 0; q_ < 6; ++q_) glds(Wm + (size_t)r_ * 6144, mvo[q_], d_ + (unsigned)(q_ * 4096)); \
      _Pragma("unroll") for (int q_ = 0; q_ < 3; ++q_) glds(Wh + (size_t)r_ * 6144, qvo[q_], d_ + (unsigned)(24576 + q_ * 4096)); SB(); } while (0)
    TW_DMA(0); TW_DMA(1); TW_DMA(2);
    f32x4 pa[4][4], p5[2];
    SFOR(i, 10)
      if (i < 8) VMW(22); else if (i == 8) VMW(16); else VMW(10);
      BAR();
      SFOR(t, 5)
        if (i % 3 < 2) tr_t(ad0[t], ad1[t], (i % 3) * 36864, 10 * i + 2 * t, T); else tr_t(ad0[t] + 73728u, ad1[t] + 73728u, 0, 10 * i + 2 * t, T);
      SEND;
      if (i < 9) BAR();
      if (i + 3 < 10) TW_DMA(i + 3);
      if (i == 7) TDMA(0);
      if (i == 8) TDMA(1);
      SFOR(kk, 2)
        u32x4 x_[4], y_[2];
#pragma unroll
        for (int rt = 0; rt < 4; ++rt) x_[rt] = LDS16((xr ^ (unsigned)(kk * 64)) + (i & 3) * 8192 + rt * 2048);
#pragma unroll
        for (int g = 0; g < 2; ++g) y_[g] = LDS16((x5 ^ (unsigned)(kk * 64)) + (i & 3) * 8192 + g * 4096);
        SFOR(n, 16) mf_t(pa[n >> 2][n & 3], x_[n >> 2], 10 * i + 2 * (n & 3) + kk, i == 0 && kk == 0, T); SEND;
        SFOR(g, 2) mf_t(p5[g], y_[g], 10 * i + 8 + kk, i == 0 && kk == 0, T); SEND;
        SB();
      SEND;
    SEND;
    { const unsigned pol = wait_ge<8>(cnt + CNT(C_POL), 1u, tmo) & 1u; kkey = K_KEY(); PINS(kkey); }
    const int ln = lane_id();
    vo[0] = (unsigned)ln * 16u + (wave == 3 ? (unsigned)(O_QF - O_KF) + (unsigned)(3 * isv) * 1024u : (isv ? (unsigned)(O_VF - O_KF) : 0u) + (unsigned)wave * 2048u);
    vo[1] = (unsigned)ln * 8u + (unsigned)(O_QF - O_KF) + (unsigned)(3 * isv + 2) * 1024u + (unsigned)(wave >> 1) * 512u;
    PINV(vo[0]); PINV(vo[1]);
    asm volatile("s_nop 7" : "+v"(pa[3][0]), "+v"(pa[3][1]), "+v"(pa[3][2]), "+v"(pa[3][3]), "+v"(p5[0]), "+v"(p5[1]));
#pragma unroll
    for (int g = 0; g < 2; ++g) {
#pragma unroll
      for (int n = 0; n < 4; ++n) { const f32x4 c0 = pa[2 * g + (n >> 1)][2 * (n & 1)], c1 = pa[2 * g + (n >> 1)][2 * (n & 1) + 1];
        st16(rs_o, vo[0] + (unsigned)((n & 1) * 1024), KSLOT(1 + g) + (unsigned)((n >> 1) * 16384), (u32x4){cvtpk(c0[0], c0[1]), cvtpk(c0[2], c0[3]), cvtpk(c1[0], c1[1]), cvtpk(c1[2], c1[3])}); }
      st8(rs_o, vo[1], KSLOT(1 + g) + (unsigned)((wave & 1) * 16384), cvtpk(p5[g][0], p5[g][1]), cvtpk(p5[g][2], p5[g][3]));
    }
  }
  const int lane2 = lane_id();
#define lane lane2
#define l15 (lane2 & 15)
#define lq (lane2 >> 4)
  unsigned tvo[5];
#pragma unroll
  for (int i = 0; i < 5; ++i) { const unsigned c = (unsigned)((5 * wave + i) * 64 + lane), r = c / 80u; tvo[i] = TSRC(c, r) + (unsigned)(4096 - (i - 2) * 1024); PINV(tvo[i]); }
  int flo = lane == 0 ? wave * 4 : (int)0x80000000u; PINV(flo);
  unsigned xq[4];
#pragma unroll
  for (int q = 0; q < 4; ++q) { xq[q] = lds_addr(lds) + (unsigned)TBUF(0) + (unsigned)(l15 * 1280 + ((4 * q + lq) ^ l15) * 16); PINV(xq[q]); }
#define XFRT(k_) LDS16(xq[(k_) & 3] + ((k_) >> 2) * 256)
  const unsigned fla = lds_addr(lds) + 147456u;
  unsigned flw = fla + 4u * (unsigned)lane, one = 1u; PINV(flw); PINV(one);
  if (wave == 0) *(volatile LAS unsigned*)(size_t)flw = 0u;
  unsigned gw = flw + 256u * (unsigned)(wave + 1), go = (unsigned)((lane % 24) / 6 * 128 + (lane % 6) * 4), gm = fla + 256u * (unsigned)(wave + 1), gl; PINV(gw); PINV(go); PINS(gm);
  const unsigned* gsrc = cnt + CNT(C_PO); PINS(gsrc);
  *(volatile LAS unsigned*)(size_t)gw = 0u;
  u64 gb; int gt;
  u32x4 xa[4]; u32x2 cv[5]; unsigned fl;
  f32x4 acc[5];
#pragma unroll
  for (int t = 0; t < 5; ++t) { acc[t] = ZERO4; cv[t] = (u32x2){0u, 0u}; }
  VMW(0);
  __builtin_amdgcn_raw_buffer_store_b32(2u, rs_fl, flo, 0, 16);
  BAR();
  TDMA(2); TDMA(3);
  xa[0] = XFRT(0); xa[1] = XFRT(1);
#define T_CVT(t_) do { unsigned c0_, c1_; \
      asm volatile("v_cvt_pk_bf16_f32 %0, %2, %3\n v_cvt_pk_bf16_f32 %1, %4, %5" : "=&v"(c0_), "=&v"(c1_) : "v"(acc[t_][0]), "v"(acc[t_][1]), "v"(acc[t_][2]), "v"(acc[t_][3])); \
      cv[t_] = (u32x2){c0_, c1_}; SB(); } while (0)
#define T_ST(i_) do { PINV(vo[0]); if ((i_) < 2) __builtin_amdgcn_raw_buffer_store_b128((u32x4){cv[2 * (i_)].x, cv[2 * (i_)].y, cv[(2 * (i_) + 1) % 5].x, cv[(2 * (i_) + 1) % 5].y}, rs_o, (int)vo[0] + (i_) * 1024, (int)sof, 16); \
      else __builtin_amdgcn_raw_buffer_store_b64(cv[4], rs_o, (int)vo[1], (int)sof, 16); SB(); } while (0)
#define T_REST(five_) do { asm volatile("s_nop 7\n s_nop 7"); T_CVT(1); T_CVT(2); T_CVT(3); T_CVT(4); T_ST(0); T_ST(1); if (five_) T_ST(2); VMW(0); } while (0)
  const char* dsrc = (const char*)(a.Bx + (size_t)TROW(4) * 640) - 4096;
  unsigned tb = TBUF(4) - 24576u, rb = TBUF(0) - 24576u;
  unsigned sol = 3u * K_STEP + 16384u, sof = k_fix(sol, kkey), sinc = 0u - 16384u, tgt = 4u, dstep = 20480u, fv = 0u, dq = 0u;
  const unsigned dd0 = lds_addr(lds) + 24576u + (unsigned)(5 * wave + 2) * 1024u;
#define T_MF(t_) do { mf_t(acc[t_], xa[k & 3], 10 * (k >> 1) + 2 * (t_) + (k & 1), k == 0, T); SB(); } while (0)
#define T_TILE(STRICT, FIVE) do { \
    SFOR(k, 20) \
      constexpr int k2 = k + 2, kn = k2 < 20 ? k2 : k2 - 20; \
      T_MF(0); \
      if (k != 18) { xa[k2 & 3] = XFRT(kn); SB(); } \
      if (k == 0) T_CVT(1); \
      if (k == 18) { xq[0] += dq; PINV(xq[0]); SB(); }        \
      T_MF(1); \
      if (k == 0) T_CVT(2); \
      if (k == 2 && FIVE) { asm volatile("s_mov_b32 m0, %0" :: "s"(gm) : "memory"); SB(); } \
      if (k == 14 && FIVE) { gl = *(const volatile LAS unsigned*)(size_t)gw; SB(); } \
      if (k == 17 && FIVE) {        \
        if (__builtin_expect(gb != 0ull, 0)) { unsigned spins = 0; while (__any((int)__hip_atomic_load((const gu32*)gsrc + (go >> 2), RLX_AGENT) < gt) && spin_check(spins, tmo)) __builtin_amdgcn_s_sleep(24); } \
        MEMB(); SB(); } \
      if (k == 3) { unsigned m_ = dd0 + tb; PINS(m_); asm volatile("s_mov_b32 m0, %0" :: "s"(m_) : "memory"); SB(); } \
      if (k == 16) { fl = *(const volatile LAS unsigned*)(size_t)flw; SB(); } \
      if (k == 18) {        \
        if (__builtin_expect(fs < tgt, 0)) { do fl = *(const volatile LAS unsigned*)(size_t)flw; while ((unsigned)__builtin_amdgcn_readfirstlane(fl) < tgt); } \
        MEMB(); SB(); } \
      if (k == 9) { PINS(tb); tb += 20480u; PINS(tb); SB(); } \
      if (k == 11) { PINS(rb); rb += 20480u; PINS(rb); SB(); } \
      if (k == 12) { PINS(rb); rb = rb == 6u * 20480u ? 0u : rb; PINS(rb); SB(); } \
      if (k == 13) { PINS(sol); PINS(sinc); sol = (sol + sinc) & (NK * K_STEP - 1u); PINS(sol); SB(); } \
      if (k == 15) { PINS(dstep); dstep = j < 116 ? 20480u : 0u; PINS(dstep); SB(); } \
      if (k == 10) { fv = (unsigned)(STRICT ? (j < 1 ? 4 : j + 3) : j + 2) >> 1; PINV(fv); SB(); } \
      if (k == 19) { xq[2] += dq; PINV(xq[2]); SB(); } \
      T_MF(2); \
      if (k == 0) T_CVT(3); \
      if (k == 1 || k == 2 || (k == 3 && !FIVE)) T_ST(k >= 1 && k <= 3 ? k - 1 : 0); \
      if (k == 4) glds_o<-2048>(dsrc, tvo[0]); else if (k == 5) glds_o<-1024>(dsrc, tvo[1]); else if (k == 6) glds_o<0>(dsrc, tvo[2]); else if (k == 7) glds_o<1024>(dsrc, tvo[3]); else if (k == 8) glds_o<2048>(dsrc, tvo[4]); \
      if (k == 9) { PINS(dsrc); dsrc += dstep; PINS(dsrc); SB(); } \
      if (k == 11) { PINS(tb); tb = tb == 6u * 20480u ? 0u : tb; PINS(tb); SB(); } \
      if (k == 14) { PINS(rb); dq = rb == 0u ? 0u - 5u * 20480u : 20480u; PINS(dq); SB(); } \
      if (k == 15) { PINS(sinc); sinc = sinc == 16384u ? K_STEP - 16384u : 16384u; PINS(sinc); SB(); } \
      if (k == 12) {        \
        asm volatile("s_waitcnt vmcnt(%0)" :: "n"(STRICT ? 14 : 23) : "memory"); \
        __hip_atomic_fetch_add((LAS unsigned*)(size_t)flw, one, __ATOMIC_RELAXED, __HIP_MEMORY_SCOPE_WORKGROUP); SB(); \
      } \
      if (k == 13) { __builtin_amdgcn_raw_buffer_store_b32(fv, rs_fl, flo, 0, 16); SB(); } \
      if (k == 17) { fs = (unsigned)__builtin_amdgcn_readfirstlane(fl); PINS(fs); SB(); } \
      if (k == 18) { xa[0] = XFRT(0); SB(); xq[1] += dq; PINV(xq[1]); SB(); } \
      if (k == 19) { PINS(tgt); tgt += 4u; PINS(tgt); xq[3] += dq; PINV(xq[3]); SB(); } \
      T_MF(3); \
      if (k == 0 && !FIVE) T_CVT(4); \
      if (k == 13) { sof = (sol >> 8) & 0x2000u; PINS(sof); SB(); } \
      if (k == 16) { PINS(sof); sof ^= sol ^ kkey; PINS(sof); SB(); } \
      if (k == 2 && FIVE) { asm volatile("global_load_lds_dword %0, %1 sc1" :: "v"(go), "s"(gsrc) : "memory"); SB(); } \
      if (k == 14 && FIVE) { gt = (j >> 1) + 5 - NK; PINS(gt); SB(); } \
      if (k == 15 && FIVE) { asm volatile("v_cmp_gt_i32_e64 %0, %1, %2" : "=s"(gb) : "s"(gt), "v"(gl)); SB(); } \
      if (k == 19) T_CVT(0); \
      if (FIVE) T_MF(4); \
    SEND; } while (0)
  unsigned fs;
  static_assert(TSTRICT % 2 == 0, "");
#define T_ALL(ODD) do { \
    if (ODD) { const int j = 0; T_TILE(true, false); } \
    _Pragma("unroll 1") for (int j2 = ODD; j2 < TSTRICT; j2 += 2) { \
      { const int j = j2; T_TILE(true, true); } \
      { const int j = j2 + 1; T_TILE(true, false); } } \
    _Pragma("unroll 1") for (int j2 = TSTRICT + ODD; j2 < 121; j2 += 2) { \
      const int s = j2 >> 1; (void)s; \
      { const int j = j2; T_TILE(false, true); } \
      { const int j = j2 + 1; T_TILE(false, false); } } \
    if (ODD) { const int j = 121; T_TILE(false, true); } \
    T_REST(ODD); \
    __builtin_amdgcn_raw_buffer_store_b32(64u, rs_fl, flo, 0, 16); } while (0)
  if (wave & 1) T_ALL(1); else T_ALL(0);
}
#undef lane
#undef l15
#undef lq

#define XPO 0
#define OAUX 0
__device__ __forceinline__ void o_role(const Args& a, unsigned char* lds, const int j, const int wave) {
  char* ws = a.ws; asm volatile("" : "+s"(ws));
  unsigned* const cnt = (unsigned*)(ws + O_CNT); unsigned* const tmo = cnt + CNT(C_TMO);
  const int lane = lane_id(), l15 = lane & 15, lq = lane >> 4;
  const unsigned pol = wait_ge<32>(cnt + CNT(C_POL), 1u, tmo) & 1u;
  const int xq = j / 6, c0 = 10 * xq + 5 * (j & 1), s0 = (j - 6 * xq) >> 1;
  const u32x4 rs_u = mkdesc(ws + O_UO + wave * 8192);
  const __amdgpu_buffer_rsrc_t rs_z = mkrs(ws + O_ZB + (size_t)c0 * 16384 + wave * 2048), rs_p = mkrs(ws + O_PT + wave * 16384), rs_m = mkrs(ws + O_PT + 65536 + wave * 4096), rs_out = mkrs(a.out + 640 + 16 * c0);
  unsigned xv = (unsigned)lane * 16u, xh = (unsigned)lane * 8u;
  unsigned wa = (unsigned)(wave * 10880 + lq * 272 + l15 * 16), ra[2], oo[2];
#pragma unroll
  for (int i = 0; i < 2; ++i) {
    const int n = i == 0 ? lane : 64 + l15, tok = 8 * wave + n / 10, oc = n % 10;
    ra[i] = (unsigned)((((oc >> 1) * 2 + (tok >> 4)) * 4 + 2 * (oc & 1)) * 272 + (tok & 15) * 16);
    oo[i] = i == 0 || lane < 16 ? (unsigned)((tok * 1280 + 8 * oc) * 2) : 0x80000000u;
    PINV(ra[i]); PINV(oo[i]);
  }
  PINV(xv); PINV(xh); PINV(wa);
  WReg W;
  if (TM == 2) touch_n<10>(a, 4 * j + wave, 0, 32);
  if (TM == 3) touch_n<4>(a, 192 + 4 * j + wave, 0, 96);
  if (TM == 1) {
    const int w = 4 * j + wave;
    wait_ge<32>(cnt + CNT(C_PZ + 8 * xq + (w & 7)), 1u, tmo);
    unsigned v = 0;
#pragma unroll
    for (int i = 0; i < 8; ++i) v ^= touch_rows(a, w % 3, 64 + w / 3 + 32 * i);
    v ^= touch(a.Awo, 640 * 1280, w) ^ touch(a.Awo, 640 * 1280, 96 + w);
    asm volatile("" :: "v"(v));
  }
  u32x2 wp = {0u, 0u};
  wait_ge<32>(cnt + CNT(C_FILL), NFILL, tmo);
#pragma unroll 1
  for (int s = s0; s < 64; s += 3) {
    if (wave == 0) {
      const unsigned tk = __hip_atomic_load((const gu32*)(cnt + CNT(C_TOK + 4 * s + xq)), RLX_AGENT);
      if (__any(min(wp.x, wp.y) < (unsigned)(s + 1))) {
        unsigned spins = 0;
        do wp = ld8(mkrs(cnt + CNT(s == 0 ? C_PZ : C_PW)), (unsigned)((lane & 31) * 128 + (lane >> 5) * 8), 0u); while (__any(min(wp.x, wp.y) < (unsigned)(s + 1)) && (__builtin_amdgcn_s_sleep(16), spin_check(spins, tmo)));
      }
      wait_ge<8>(cnt + CNT(C_PS + j), (unsigned)(s < 63 ? s + 1 : 63), tmo);
      *(volatile LAS unsigned*)(lds + 65536) = tk;
    }
    BAR();
    const bool behind = (unsigned)__builtin_amdgcn_readfirstlane(*(volatile LAS unsigned*)(lds + 65536)) >= 32u;
    const int np = behind || s >= 60 ? 0 : a.d[14];
    SFOR(i, 20) ld_w(rs_u, xv, u_off((unsigned)s, (unsigned)(c0 + (i >> 2))) + (unsigned)(((i >> 1) & 1) * 4096), (i & 1) * 2048, i, (i & 3) == 0, W); nap_rt(np); SEND;
    u32x4 z[5][2], p1[8], pm[2][2]; u32x2 p0[8];
    const unsigned zs = half_of(ws + O_ZB, ZSLOT(s), 0u, pol), ps = TSLOT(s);
    unsigned spins = 0;
    for (;;) {
      unsigned e = ~0u;
      PINV(xv);
#pragma unroll
      for (int n = 0; n < 10; ++n) { z[n >> 1][n & 1] = ld16(rs_z, xv + (unsigned)((n & 1) * 1024), zs + (unsigned)((n >> 1) * 16384)); e = sent_min(e, z[n >> 1][n & 1]); }
      if (!__any(e == 0u) || !spin_check(spins, tmo)) break;
      __builtin_amdgcn_s_sleep(4);
    }
    if (!behind) {
      if (wave == 0) { if (s >= 60) wait_ge<1>(cnt + CNT(C_TOK + 4 * s + xq), 32u, tmo); else wait_ge<12>(cnt + CNT(C_TOK + 4 * s + xq), 32u, tmo); }
      BAR();
    }
    PINV(xv); PINV(xh);
#pragma unroll
    for (int n = 0; n < 8; ++n) { p1[n] = ld16(rs_p, xv + (unsigned)((n & 1) * 2048 + 1024), ps + (unsigned)((n >> 1) * 4096)); p0[n] = ld8(rs_p, xh + (unsigned)((n & 1) * 2048), ps + (unsigned)((n >> 1) * 4096)); SB(); }
#pragma unroll
    for (int n = 0; n < 4; ++n) pm[n >> 1][n & 1] = ld16(rs_m, xv + (unsigned)(n * 1024), ps);
    SB();
    f32x4 acc[5][2];
    SFOR(n, 8)
      u32x4 q0 = {p0[n].x, p0[n].y, 0u, 0u}; PINV(q0);
      SFOR(t, 5) mf_w<true>(acc[t][1], p1[n], 8 * t + n, n == 0, W); SB(); SEND;
      SFOR(t, 5) mf_w<true>(acc[t][0], q0, 8 * t + n, n == 0, W); SB(); SEND;
    SEND;
#pragma unroll
    for (int n = 0; n < 4; ++n)
#pragma unroll
      for (int t = 0; t < 5; ++t) asm volatile("v_mfma_f32_16x16x32_bf16 %0, %1, %2, %0" : "+v"(acc[t][n & 1]) : "v"(z[t][n >> 1]), "v"(pm[n >> 1][n & 1]));
    asm volatile("s_nop 7" : "+v"(acc[0][1]), "+v"(acc[1][1]), "+v"(acc[2][1]), "+v"(acc[3][1]), "+v"(acc[4][1]));
#pragma unroll
    for (int n = 0; n < 10; ++n) *(f32x4*)(lds + wa + (n % 5 * 2 + n / 5) * 1088) = acc[n % 5][n / 5];
    BAR();
    if (wave == 0 && lane == 0) __hip_atomic_store((gu32*)(cnt + CNT(C_PO + xq) + j - 6 * xq), (unsigned)(s + 3), RLX_AGENT);
#pragma unroll
    for (int i = 0; i < 2; ++i) {
      f32x4 v0 = *(const f32x4*)(lds + ra[i]), v1 = *(const f32x4*)(lds + ra[i] + 272);
#pragma unroll
      for (int w = 1; w < 4; ++w) { v0 += *(const f32x4*)(lds + ra[i] + w * 10880); v1 += *(const f32x4*)(lds + ra[i] + w * 10880 + 272); }
      const u32x4 w_ = {cvtpk(v0[0], v0[1]), cvtpk(v0[2], v0[3]), cvtpk(v1[0], v1[1]), cvtpk(v1[2], v1[3])};
      __builtin_amdgcn_raw_buffer_store_b128(w_, rs_out, (int)oo[i], 32 * s * 1280 * 2, OAUX);
      asm volatile("s_nop 1" :: "v"(w_) : "memory");
    }
  }
}

#define XPF 100
__device__ __forceinline__ void att_role(const Args& a, unsigned char* lds, const int h, const int wave) {
  char* ws = a.ws;
  unsigned* const cnt = (unsigned*)(ws + O_CNT); unsigned* const tmo = cnt + CNT(C_TMO);
  const int lane = lane_id(), l15 = lane & 15, lq = lane >> 4, par = wave >> 1;
  const __amdgpu_buffer_rsrc_t rs_q = mkrs(ws + O_QF + (size_t)h * 32768), rs_k = mkrs(ws + O_KF + (size_t)h * 32768), rs_km = mkrs(ws + O_KM + (size_t)h * 3072), rs_pt = mkrs(ws + O_PT);
  const float scale = 0.07216878364870323f, LOG2E = 1.4426950408889634f;
  unsigned xv = (unsigned)lane * 16u; PINV(xv);
#define KM_EMPTY(s_) do { _Pragma("unroll") for (int i_ = 0; i_ < 3; ++i_) st16(rs_km, xv + (unsigned)(i_ * 1024), MSLOT(s_), SENT4); } while (0)
#pragma unroll 1
  for (int s = par; s < NZ; s += 2) KM_EMPTY(s);
  VMW(0);
  if (lane == 0) __hip_atomic_fetch_add((gu32*)(cnt + CNT(C_FILL)), 1u, RLX_AGENT);
  nap_rt(XPF);
  if (TM == 0) touch_n<5>(a, 128 + 2 * h + par, 0, 64);
  if (TM == 3) touch_n<4>(a, 128 + 2 * h + par, 0, 96);
  if (TM != 1) { const unsigned v = touch(a.Awo, 640 * 1280, 2 * h + par) ^ touch(a.Awo, 640 * 1280, 64 + 2 * h + par); asm volatile("" :: "v"(v)); }
  if (TM == 1) { const unsigned v = touch_rows(a, 1, 2 * h + par) ^ touch_rows(a, 0, 2 * h + par) ^ touch_rows(a, 2, 2 * h + par); asm volatile("" :: "v"(v)); }
  const unsigned pol = wait_ge<32>(cnt + CNT(C_POL), 1u, tmo) & 1u, kkey = K_KEY();
  u32x4 qf[2][6], kf[2][6];
  unsigned xh = (unsigned)lane * 8u;
#define QK_LOAD(s_) do { PINV(xv); PINV(xh); _Pragma("unroll") for (int i = 0; i < 12; ++i) { const unsigned so = KSLOT(s_) + (unsigned)(i / 6 * 16384 + i % 6 / 3 * 3072), vo = xv + (unsigned)(i % 3 * 1024); \
      kf[i / 6][i % 6] = ld16(rs_k, vo, so); \
      if (i % 3 < 2) qf[i / 6][i % 6] = ld16(rs_q, vo, so); else { const u32x2 l_ = ld8(rs_q, xh + 2048u, so), h_ = ld8(rs_q, xh + 2560u, so); qf[i / 6][i % 6] = (u32x4){l_.x, l_.y, h_.x, h_.y}; } } SB(); } while (0)
  const __amdgpu_buffer_rsrc_t rs_tv = mkrs(cnt + CNT(C_PT_ + h));
  int tvo = (lane & 3) * 4 + ((lane >> 2) & 1) * 4 * CNT(C_PV - C_PT_); PINV(tvo);
#define TV_WAIT(s_) do { unsigned spins = 0; while (__any(__builtin_amdgcn_raw_buffer_load_b32(rs_tv, tvo, 0, 16) < (unsigned)(s_)) && spin_check(spins, tmo)) __builtin_amdgcn_s_sleep(16); MEMB(); } while (0)
  unsigned kmo = (unsigned)(lq * 128 + ((l15 & 1) + (l15 & 2) * 2 + ((l15 >> 1) & 2)) * 16), tlast = 0; PINV(kmo);
  if (par == 0) wait_ge<32>(cnt + CNT(C_PS + h), 2u, tmo); else TV_WAIT(1);
  QK_LOAD(par);
#pragma unroll 1
  for (int s = par; s < 64; s += 2) {
    f32x4 sc[2][3];
    float mx[2];
    {
#pragma unroll
      for (int q = 0; q < 2; ++q) {
        mx[q] = -3.0e38f;
#pragma unroll
        for (int t = 0; t <= q; ++t) {
          sc[q][t] = ZERO4;
#pragma unroll
          for (int kk = 0; kk < 6; ++kk) sc[q][t] = mfma(kf[t][kk], qf[q][kk], sc[q][t]);
#pragma unroll
          for (int r = 0; r < 4; ++r) {
            const float v = rbf(rbf(sc[q][t][r]) * scale);
            sc[q][t][r] = (t < q || 4 * lq + r <= l15) ? v : -3.0e38f;
            mx[q] = fmaxf(mx[q], sc[q][t][r]);
          }
        }
        PINV(mx[q]);
      }
    }
    SB();
    unsigned tv;
    {
      u32x4 km[6];
      unsigned spins = 0;
      const unsigned t0 = wait_rt(tlast + (unsigned)a.d[15]);
      for (;;) {
        unsigned m = ~0u;
        PINV(kmo);
        tv = __builtin_amdgcn_raw_buffer_load_b32(rs_tv, tvo, 0, 16);
#pragma unroll
        for (int kk = 0; kk < 6; ++kk) { km[kk] = ld16(rs_km, kmo + (unsigned)(kk * 512), MSLOT(s)); m = sent_min(m, km[kk]); }
        if (!__any(m == 0u) || !spin_check(spins, tmo)) break;
        if (spins > 4u) __builtin_amdgcn_s_sleep(12); else __builtin_amdgcn_s_sleep(2);
      }
      tlast = t0 - 100u;
      if (spins) { u64 t_; RT_MARK(t_); tlast = (unsigned)t_; }
#pragma unroll
      for (int q = 0; q < 2; ++q) {
        sc[q][2] = ZERO4;
#pragma unroll
        for (int kk = 0; kk < 6; ++kk) sc[q][2] = mfma(km[kk], qf[q][kk], sc[q][2]);
      }
    }
    asm volatile("" : "+v"(sc[0][2]), "+v"(sc[1][2]));
    const bool ahead = s + 2 < 64 && !__any(tv < (unsigned)(s + 2));
    if (ahead) QK_LOAD(s + 2);
#pragma unroll
    for (int q = 0; q < 2; ++q) {
#pragma unroll
      for (int r = 0; r < 4; ++r) { sc[q][2][r] = rbf(rbf(sc[q][2][r]) * scale); mx[q] = fmaxf(mx[q], sc[q][2][r]); }
      const float m = max32(max16(mx[q])), nml = -m * LOG2E;
      float sum = 0.f, sm = 0.f;
#pragma unroll
      for (int t = 0; t <= q; ++t)
#pragma unroll
        for (int r = 0; r < 4; ++r) { const float e = (t < q || 4 * lq + r <= l15) ? __builtin_amdgcn_exp2f(__builtin_fmaf(sc[q][t][r], LOG2E, nml)) : 0.f; sc[q][t][r] = e; sum += e; }
#pragma unroll
      for (int r = 0; r < 4; ++r) { sc[q][2][r] = __builtin_amdgcn_exp2f(__builtin_fmaf(sc[q][2][r], LOG2E, nml)); sm += sc[q][2][r]; }
      sum = sum32(sum16(__builtin_fmaf(sm, 0.5f, sum)));
      const float inv = 1.0f / sum;
      if (q == 0) st8(rs_pt, (unsigned)(h * 2048 + lane * 8), TSLOT(s), cvtpk(sc[0][0][0] * inv, sc[0][0][1] * inv), cvtpk(sc[0][0][2] * inv, sc[0][0][3] * inv));
      else st16(rs_pt, (unsigned)(h * 2048 + 1024 + lane * 16), TSLOT(s), (u32x4){cvtpk(sc[1][0][0] * inv, sc[1][0][1] * inv), cvtpk(sc[1][0][2] * inv, sc[1][0][3] * inv), cvtpk(sc[1][1][0] * inv, sc[1][1][1] * inv), cvtpk(sc[1][1][2] * inv, sc[1][1][3] * inv)});
      st8(rs_pt, lq < 2 ? (unsigned)(65536 + (h >> 2) * 2048 + q * 1024 + (h & 3) * 256 + l15 * 16 + lq * 8) : 0x80000000u, TSLOT(s), cvtpk(sc[q][2][0] * inv, sc[q][2][1] * inv), cvtpk(sc[q][2][2] * inv, sc[q][2][3] * inv));
    }
    if (s + NZ < 64) KM_EMPTY(s);
    VMW(0);
    if (lane < 4) __hip_atomic_fetch_add((gu32*)(cnt + CNT(C_TOK + 4 * s + lane)), 1u, RLX_AGENT);
    if (!ahead && s + 2 < 64) { TV_WAIT(s + 2); QK_LOAD(s + 2); }
  }
}

__device__ __forceinline__ float dot2bf(unsigned x, unsigned y, float c) { return __builtin_amdgcn_fdot2_f32_bf16(__builtin_bit_cast(bf16x2_t, x), __builtin_bit_cast(bf16x2_t, y), c, false); }
__device__ __forceinline__ void a_role(const Args& a, unsigned char* lds, const int j, const int wave) {
  char* ws = a.ws; asm volatile("" : "+s"(ws));
  unsigned* const cnt = (unsigned*)(ws + O_CNT); unsigned* const tmo = cnt + CNT(C_TMO);
  const int lane = lane_id(), l15 = lane & 15, lq = lane >> 4;
  const int c0 = 40 * j - 8 * (j & 1);
  u32x4 wt[3][20];
#define A_WDMA(src_, ld_, off_) do { const int ln_ = lane_id(); _Pragma("unroll") for (int q_ = 0; q_ < 15; ++q_) { const int ins = wave + 4 * q_; const unsigned c = (unsigned)(ins * 64 + ln_), r = c / 6u, pc = c - r * 6u; \
      glds(src_, r * (unsigned)((ld_) * 2) + 16u * pc, lds_addr(lds) + (unsigned)((off_) + ins * 1024)); } } while (0)
#define A_WREAD(off_) do { unsigned o_ = (unsigned)((off_) + (8 * lq + (l15 >> 2)) * 96 + 8 * (l15 & 3)); PINV(o_); \
    _Pragma("unroll") for (int t = 0; t < 3; ++t) { _Pragma("unroll") for (int kk = 0; kk < 20; ++kk) wt[t][kk] = trrd2(lds, o_ + (unsigned)(kk * 3072 + 32 * t), 384u); SB(); } } while (0)
  A_WDMA(a.Awqkv + c0, 1920, 0); A_WDMA(a.Awqkv + 640 + c0, 1920, 61440);
  VMW(0); BAR();
  if (wave < 2) A_WREAD(wave * 61440);
  BAR();
  A_WDMA(a.Awqkv + 1280 + c0, 1920, 0);
  VMW(0); BAR();
  if (wave == 2) A_WREAD(0);
  BAR();
  constexpr int LX = 0, LR = 122880, RS = 304, NR = 128;
  unsigned dvo[10];
#pragma unroll
  for (int i = 0; i < 10; ++i) { const unsigned c = (unsigned)((wave + 4 * i) * 64 + lane), r = c / 80u; dvo[i] = (c ^ (r & 15u)) * 16u; PINV(dvo[i]); }
#define A_XDMA(src_, b_) do { _Pragma("unroll") for (int xi_ = 0; xi_ < 10; ++xi_) glds(src_, dvo[xi_], lds_addr(lds) + (unsigned)(LX + ((b_) & 1) * 40960 + wave * 1024 + xi_ * 4096)); } while (0)
  unsigned xr[4];
#pragma unroll
  for (int q = 0; q < 4; ++q) xr[q] = (unsigned)(LX + l15 * 1280 + ((4 * q + lq) ^ l15) * 16);
  unsigned dv3[14];
#pragma unroll
  for (int i = 0; i < 14; ++i) { const unsigned c = (unsigned)((13 * (wave < 3 ? wave : 0) + i) * 64 + lane), r = c / 80u; dv3[i] = (c ^ (r & 15u)) * 16u + (unsigned)(4096 - (i % 7 - 3) * 1024); PINV(dv3[i]); }
  const unsigned am0 = lds_addr(lds) + (unsigned)(LX + (13 * wave + 3) * 1024);
#define A_XD3(src_, lb_, i_) do { if ((i_) % 7 == 0) m0_set(am0 + (lb_) + (unsigned)((i_) / 7 * 7168)); \
    switch ((i_) % 7) { case 0: glds_o<-3072>(src_, dv3[i_]); break; case 1: glds_o<-2048>(src_, dv3[i_]); break; case 2: glds_o<-1024>(src_, dv3[i_]); break; case 3: glds_o<0>(src_, dv3[i_]); break; \
      case 4: glds_o<1024>(src_, dv3[i_]); break; case 5: glds_o<2048>(src_, dv3[i_]); break; default: glds_o<3072>(src_, dv3[i_]); break; } } while (0)
  A_XDMA(a.Ax, 0);
  A_XDMA(a.Ax + 32 * 640, 1);
  unsigned wo_[3];
#pragma unroll
  for (int t = 0; t < 3; ++t) { const int v = 4 * t + lq - 2 * (j & 1); wo_[t] = (unsigned)(LR + l15 * RS + wave * 96 + (v < 0 ? 40 + (v + 2) * 48 : (v >= 10 ? 40 + (v - 10) * 48 : (v >= 5 ? 8 : 0) + v * 8))); }
  const __amdgpu_buffer_rsrc_t rs_oa = mkrs(ws + O_OA);
#pragma unroll 1
  for (int b = 0; b <= 64; ++b) {
    const int s = b; (void)s;
    if (b == 0) VMW(0); else if (wave < 3) VMW(14);
    BAR();
    const char* xsrc = (const char*)(a.Ax + (size_t)(32 * (b < 62 ? b + 2 : 63)) * 640) - 4096;
    const unsigned xb = (unsigned)(b % 3) * 40960u, xb2 = (unsigned)((b + 2) % 3) * 40960u;
    if (wave < 3) {
      if (b < 64) {
        unsigned xq[4];
#pragma unroll
        for (int q = 0; q < 4; ++q) { xq[q] = xr[q] + xb; PINV(xq[q]); }
#define A_XF(rt_, k_) (*(const u32x4*)(lds + xq[(k_) & 3] + (rt_) * 20480 + ((k_) >> 2) * 256))
        f32x4 acc[2][3]; u32x4 xa[3][2];
#pragma unroll
        for (int k = 0; k < 2; ++k) { xa[k][0] = A_XF(0, k); xa[k][1] = A_XF(1, k); }
#pragma unroll
        for (int k = 0; k < 20; ++k) {
          if (k + 2 < 20) { xa[(k + 2) % 3][0] = A_XF(0, k + 2); xa[(k + 2) % 3][1] = A_XF(1, k + 2); }
#pragma unroll
          for (int t = 0; t < 3; ++t) { acc[0][t] = mfma(wt[t][k], xa[k % 3][0], k == 0 ? ZERO4 : acc[0][t]); acc[1][t] = mfma(wt[t][k], xa[k % 3][1], k == 0 ? ZERO4 : acc[1][t]);
            if (t == 1 && k < 14) { asm volatile("" : "+a"(acc[0][0]), "+a"(acc[0][1]), "+a"(acc[1][0]), "+a"(acc[1][1])); A_XD3(xsrc, xb2, k < 14 ? k : 0); SB(); } }
          if (k >= 14) IL(6, 1);
          asm volatile("" : "+a"(acc[0][0]), "+a"(acc[0][1]), "+a"(acc[0][2]), "+a"(acc[1][0]), "+a"(acc[1][1]), "+a"(acc[1][2])); SB();
        }
        const unsigned ro = (unsigned)(((32 * b) & (NR - 1)) * RS);
#pragma unroll
        for (int t = 0; t < 3; ++t)
#pragma unroll
          for (int rt = 0; rt < 2; ++rt) *(u32x2*)(lds + wo_[t] + ro + rt * 16 * RS) = (u32x2){cvtpk(acc[rt][t][0], acc[rt][t][1]), cvtpk(acc[rt][t][2], acc[rt][t][3])};
      }
    } else {
      const int row = 32 * (b > 0 ? b - 1 : 0) + (lane & 31), hh = lane >> 5;
      const unsigned ho = (unsigned)(LR + 48 * hh);
      unsigned ra[8];
#pragma unroll
      for (int w = 0; w < 8; ++w) { const int p = row - 2 * w; ra[w] = ho + (unsigned)(((p > 0 ? p : 0) & (NR - 1)) * RS); }
#define A_LD10(d_, ad_) do { const u32x4 x0 = *(const u32x4*)(lds + (ad_)), x1 = *(const u32x4*)(lds + (ad_) + 16); const u32x2 x2 = *(const u32x2*)(lds + (ad_) + 32); \
        d_[0] = x0.x; d_[1] = x0.y; d_[2] = x0.z; d_[3] = x0.w; d_[4] = x1.x; d_[5] = x1.y; d_[6] = x1.z; d_[7] = x1.w; d_[8] = x2.x; d_[9] = x2.y; } while (0)
      unsigned q[10], kv[8][10], vv[8][10];
      A_LD10(q, ra[0]);
#pragma unroll
      for (int w = 0; w < 8; ++w) A_LD10(kv[w], ra[w] + 96);
#pragma unroll
      for (int w = 0; w < 8; ++w) A_LD10(vv[w], ra[w] + 192);
      SB();
      float lg[8], mx = -3.0e38f;
#pragma unroll
      for (int w = 0; w < 8; ++w) lg[w] = 0.f;
#pragma unroll
      for (int i = 0; i < 10; ++i)
#pragma unroll
        for (int w = 0; w < 8; ++w) lg[w] = dot2bf(q[i], kv[w][i], lg[w]);
#pragma unroll
      for (int w = 0; w < 8; ++w) { lg[w] = row - 2 * w >= 0 ? rbf(rbf(lg[w]) * 0.22360679774997896f) : rbf(-1000000000.0f); mx = fmaxf(mx, lg[w]); }
      float sum = 0.f;
#pragma unroll
      for (int w = 0; w < 8; ++w) { lg[w] = __expf(lg[w] - mx); sum += lg[w]; }
      const float inv = __builtin_amdgcn_rcpf(sum);
      float ov[20];
#pragma unroll
      for (int d = 0; d < 20; ++d) ov[d] = 0.f;
#pragma unroll
      for (int w = 0; w < 8; w += 2) {
        const unsigned pw = cvtpk(lg[w] * inv, lg[w + 1] * inv);
#pragma unroll
        for (int i = 0; i < 10; ++i) {
          ov[2 * i] = dot2bf(__builtin_amdgcn_perm(vv[w + 1][i], vv[w][i], 0x05040100u), pw, ov[2 * i]);
          ov[2 * i + 1] = dot2bf(__builtin_amdgcn_perm(vv[w + 1][i], vv[w][i], 0x07060302u), pw, ov[2 * i + 1]);
        }
      }
      const unsigned oo = b > 0 ? (unsigned)((row * 640 + 40 * j + 20 * hh) * 2) : 0x80000000u;
#pragma unroll
      for (int i = 0; i < 5; ++i) st8(rs_oa, oo + 8u * i, 0u, cvtpk(ov[4 * i], ov[4 * i + 1]), cvtpk(ov[4 * i + 2], ov[4 * i + 3]));
      if (b > 0 && (b & 15) == 0) { VMW(0); if (lane == 0) __hip_atomic_fetch_add((gu32*)(cnt + CNT(C_AO + (b >> 4) - 1)), 1u, RLX_AGENT); }
    }
  }
  VMW(0);
}
__device__ __forceinline__ void a2_role(const Args& a, unsigned char* lds, const int j, const int wave) {
  char* ws = a.ws; asm volatile("" : "+s"(ws));
  unsigned* const cnt = (unsigned*)(ws + O_CNT); unsigned* const tmo = cnt + CNT(C_TMO);
  VMW(0);
  BAR();
  {
    const int ln = lane_id(), m15 = ln & 15, mq = ln >> 4, cg = j & 3, rg = j >> 2;
    WReg W;
    {
      unsigned wv[10];
#pragma unroll
      for (int q = 0; q < 10; ++q) { const unsigned c = (unsigned)((wave + 4 * q) * 64 + ln), r = c / 20u; wv[q] = r * 1280u + (c - r * 20u) * 16u; PINV(wv[q]); }
#define A2_WDMA(i_) do { _Pragma("unroll") for (int q_ = 0; q_ < 10; ++q_) glds(a.Awo + (size_t)(128 * (i_)) * 640 + 160 * cg, wv[q_], lds_addr(lds) + (unsigned)(((i_) & 3) * 40960 + (wave + 4 * q_) * 1024)); } while (0)
      A2_WDMA(0); A2_WDMA(1); A2_WDMA(2); A2_WDMA(3);
      const unsigned rb = lds_addr(lds) + (unsigned)((8 * mq + (m15 >> 2)) * 320);
      const unsigned ad[3] = {rb + (unsigned)(64 * wave + 16 * (m15 & 3)), rb + (unsigned)(64 * wave + 16 * (m15 & 3) + 8), rb + (unsigned)(256 + 32 * (wave >> 1) + 8 * (m15 & 3))};
      SFOR(i, 5)
        if (i < 2) VMW(30); else if (i == 2) VMW(20); else if (i == 3) VMW(10); else VMW(0);
        BAR();
        SFOR(n, 6) tr4_w(ad[n >> 1] + (unsigned)((i & 3) * 40960), (n & 1) * 20480, (n & 1) * 20480 + 1280, (n & 1) * 20480 + 10240, (n & 1) * 20480 + 11520, 6 * i + n, W); SEND;
        if (i == 0) { BAR(); A2_WDMA(4); }
      SEND;
    }
    unsigned dv[10], xq[4], x5[4];
#pragma unroll
    for (int i = 0; i < 10; ++i) { const unsigned c = (unsigned)((10 * wave + i) * 64 + ln), r = c / 80u; dv[i] = (c ^ (r & 15u)) * 16u + (unsigned)(4096 - (i % 5 - 2) * 1024); PINV(dv[i]); }
    const char* src = ws + O_OA + (size_t)(128 * rg) * 1280 - 4096;
    const unsigned dm = lds_addr(lds) + (unsigned)((10 * wave + 2) * 1024);
#define A2_X(p_, lb_, i_) do { if ((i_) % 5 == 0) m0_set(dm + (lb_) + (unsigned)((i_) / 5 * 5120)); \
      switch ((i_) % 5) { case 0: glds_x<-2048>(p_, dv[i_]); break; case 1: glds_x<-1024>(p_, dv[i_]); break; case 2: glds_x<0>(p_, dv[i_]); break; case 3: glds_x<1024>(p_, dv[i_]); break; default: glds_x<2048>(p_, dv[i_]); break; } } while (0)
    const __amdgpu_buffer_rsrc_t rs_out = mkrs(a.out + (size_t)(128 * rg) * 1280 + 160 * cg);
    unsigned o2 = (unsigned)((m15 * 1280 + 32 * wave + 8 * mq) * 2), o5 = (unsigned)(((16 * (wave & 1) + m15) * 1280 + 128 + 16 * (wave >> 1) + 4 * mq) * 2); PINV(o2); PINV(o5);
    if (wave == 0) wait_ge<16>(cnt + CNT(C_AO + (rg >> 2)), 16u, tmo);
    BAR();
#pragma unroll
    for (int b = 0; b < 4; ++b) { const char* p_ = src + b * 40960; asm volatile("s_nop 4" : "+s"(p_));
#pragma unroll
      for (int i = 0; i < 10; ++i) A2_X(p_, (unsigned)(b * 40960), i); }
#pragma unroll 1
    for (int b = 0; b < 4; ++b) {
      if (b == 0) VMW(30); else if (b == 1) VMW(23); else if (b == 2) VMW(16); else VMW(9);
      BAR();
#pragma unroll
      for (int q = 0; q < 4; ++q) { xq[q] = lds_addr(lds) + (unsigned)((b & 3) * 40960 + m15 * 1280 + ((4 * q + mq) ^ m15) * 16); x5[q] = xq[q] + (unsigned)((wave & 1) * 20480); PINV(xq[q]); PINV(x5[q]); }
      f32x4 acc[5]; u32x4 xa[3][3];
#define A2_XF(n_, k_) LDS16(((n_) == 2 ? x5[(k_) & 3] : xq[(k_) & 3] + (n_) * 20480) + ((k_) >> 2) * 256)
#pragma unroll
      for (int n = 0; n < 6; ++n) xa[n / 3][n % 3] = A2_XF(n % 3, n / 3);
      SFOR(k, 20)
        SFOR(n, 5)
          mf_w<true>(acc[n], xa[k % 3][n >> 1], 12 * (k >> 2) + 4 * (n == 4 ? 2 : (n & 1)) + (k & 3), k == 0, W); SB();
          if (n < 3 && k + 2 < 20) { xa[(k + 2) % 3][n] = A2_XF(n, k + 2); SB(); }
        SEND;
      SEND;
      asm volatile("s_nop 7" : "+v"(acc[0]), "+v"(acc[1]), "+v"(acc[2]), "+v"(acc[3]), "+v"(acc[4]));
#pragma unroll
      for (int rt = 0; rt < 2; ++rt) { const u32x4 w = {cvtpk(acc[2 * rt][0], acc[2 * rt][1]), cvtpk(acc[2 * rt][2], acc[2 * rt][3]), cvtpk(acc[2 * rt + 1][0], acc[2 * rt + 1][1]), cvtpk(acc[2 * rt + 1][2], acc[2 * rt + 1][3])};
        __builtin_amdgcn_raw_buffer_store_b128(w, rs_out, (int)o2, (32 * b + 16 * rt) * 2560, OAUX); asm volatile("s_nop 1" :: "v"(w) : "memory"); }
      __builtin_amdgcn_raw_buffer_store_b64((u32x2){cvtpk(acc[4][0], acc[4][1]), cvtpk(acc[4][2], acc[4][3])}, rs_out, (int)o5, 32 * b * 2560, OAUX);
    }
    VMW(0);
  }
}

#define CALN 6
__device__ __forceinline__ void calibrate(char* ws, int* vote, const int wave) {
  char* cal = ws + O_CAL + wave * 16384;
  unsigned best0 = ~0u, best1 = ~0u, acc = 0;
  const int lane = lane_id();
  for (int r = 0; r < CALN; ++r) {
#pragma unroll
    for (int hf = 0; hf < 2; ++hf) {
      const unsigned long long t0 = __builtin_amdgcn_s_memtime();
      if (lane == 0) acc += __hip_atomic_fetch_add((gu32*)(cal + hf * 8192 + (acc & 0u)), 1u, RLX_AGENT);
      asm volatile("s_waitcnt vmcnt(0)" ::: "memory");
      const unsigned d = (unsigned)(__builtin_amdgcn_s_memtime() - t0);
      if (hf == 0) best0 = d < best0 ? d : best0; else best1 = d < best1 ? d : best1;
    }
  }
  int dl = (int)best1 - (int)best0 + (int)(acc & 0u);
  dl = dl > 400 ? 400 : (dl < -400 ? -400 : dl);
  if ((((unsigned)((size_t)cal >> 21) ^ xcc_side()) & 1u) != 0u) dl = -dl;
  if (lane == 0) vote[wave] = dl;
  __syncthreads();
  if (wave == 0 && lane == 0) __hip_atomic_store((gu32*)((unsigned*)(ws + O_CNT) + CNT(C_POL)), vote[0] + vote[1] + vote[2] + vote[3] < 0 ? 3u : 2u, RLX_AGENT);
  __syncthreads();
}

#define TCH 32
#define XOFF 0
#define CPF 3
__global__ __launch_bounds__(256, 1) void k_mega(Args a) {
  extern __shared__ __attribute__((aligned(16))) unsigned char shm[];
  volatile LAS int* sl = (volatile LAS int*)shm;
  if (threadIdx.x >= 64) {
    u64 pc; unsigned off, len;
    asm volatile("s_getpc_b64 %0\n.Lcpf_mega:\n s_mov_b32 %1, .Lcpf_mega-_Z6k_mega4Args\n s_mov_b32 %2, _Z6k_prepPcPKcS1_S1_S1_S1_-_Z6k_mega4Args" : "=s"(pc), "=s"(off), "=s"(len));
    const __amdgpu_buffer_rsrc_t rs = __builtin_amdgcn_make_buffer_rsrc((void*)(pc - off), 0, (int)len, 0x00020000);
    const unsigned g = ((blockIdx.x >> 3) * 192u + threadIdx.x - 64u) * 16u;
    const u32x4 v0 = __builtin_amdgcn_raw_buffer_load_b128(rs, (int)g, 0, 0), v1 = __builtin_amdgcn_raw_buffer_load_b128(rs, (int)g, 98304, 0);
    asm volatile("" :: "v"(v0), "v"(v1));
  }
  if (threadIdx.x >= 64) {
    const unsigned o = (threadIdx.x - 64u) * (unsigned)(TCH * 65536) + (blockIdx.x >> 3) * 2048u;
    const unsigned v = __builtin_amdgcn_raw_buffer_load_b32(__builtin_amdgcn_make_buffer_rsrc((void*)(a.ws + O_X1), 0, (int)(O_END - O_X1), 0x00020000), (int)o, 0, 0);
    asm volatile("" :: "v"(v));
  }
  if (threadIdx.x == 0) {
    const unsigned x0 = (unsigned)__builtin_amdgcn_s_getreg((3 << 11) | 20) & 7u;
    int slot = 0;
    for (unsigned i = 0; i < 8u; ++i) {
      const unsigned x = (x0 + i) & 7u;
      const unsigned t = __hip_atomic_fetch_add((gu32*)((unsigned*)(a.ws + O_CNT) + CNT(C_SLOT + x)), 1u, RLX_AGENT);
      if (t < 32u) { slot = (int)(x * 32u + t); break; }
    }
    *sl = slot;
  }
  __syncthreads();
  const int slot = __builtin_amdgcn_readfirstlane(*sl);
  __syncthreads();
  const int xq = (slot >> 5) & 3, kq = slot & 31, wave = __builtin_amdgcn_readfirstlane(threadIdx.x >> 6);
#define ENTER __builtin_amdgcn_s_waitcnt(0)
  const int h = xq * 8 + (kq & 7);
  if (slot < 128) {
    if (kq < 8) { ENTER; s_role(a, shm, h, wave);
    } else if (kq < 16) { ENTER; z_role(a, shm, h, wave); }
    else if (kq < 26) { ENTER; r_role(a, shm, xq * 10 + kq - 16, wave); }
    else { if (slot == 26) calibrate(a.ws, (int*)shm, wave); ENTER; if (!(XOFF & 1)) o_role(a, shm, 6 * xq + kq - 26, wave); }
  } else {
    if (kq < 16) { ENTER; t_role(a, shm, h, kq >> 3, wave); ENTER; if (!(XOFF & 4)) a2_role(a, shm, 16 * xq + kq, wave); }
    else if (kq < 24) { ENTER; w_role(a, shm, h, wave); }
    else if (kq < 28) { ENTER; if (!(XOFF & 4)) a_role(a, shm, xq * 4 + kq - 24, wave); }
    else { ENTER; if (!(XOFF & 2)) att_role(a, shm, xq * 8 + (kq - 28) * 2 + (wave & 1), wave); }
  }
}

#define PFW 3
#define PFN 15
constexpr int PB0 = (PFW & 1) ? 1920 / PFN : 0, PB1 = (PFW & 2) ? 1920 / PFN : 0, PB2 = (PFW & 4) ? 1920 / PFN : 0, PB3 = (PFW & 8) ? 1920 / PFN : 0, PF_B = PB0 + PB1 + PB2 + PB3;
__global__ __launch_bounds__(256) void k_prep(char* ws, const char* w0, const char* w1, const char* w2, const char* w3, const char* w4) {
  if (blockIdx.x < 15) { ((u32x4*)(ws + O_CNT))[blockIdx.x * 256 + threadIdx.x] = (u32x4){0u, 0u, 0u, 0u}; return; }
  if (blockIdx.x < 15 + PF_B) {
    const unsigned bi = blockIdx.x - 15;
    const char* w = bi < PB0 ? w0 : (bi < PB0 + PB1 ? w1 : (bi < PB0 + PB1 + PB2 ? w2 : w3));
    const unsigned bb = bi < PB0 ? bi : (bi < PB0 + PB1 ? bi - PB0 : (bi < PB0 + PB1 + PB2 ? bi - PB0 - PB1 : bi - PB0 - PB1 - PB2));
    const unsigned g = bb * (PFN * 4096u) + threadIdx.x * 16u;
    const __amdgpu_buffer_rsrc_t rs = __builtin_amdgcn_make_buffer_rsrc((void*)w, 0, 1920 * 4096, 0x00020000);
    u32x4 v[PFN];
#pragma unroll
    for (int i = 0; i < PFN; ++i) v[i] = __builtin_amdgcn_raw_buffer_load_b128(rs, (int)g, i * 4096, 0);
    __builtin_amdgcn_sched_barrier(0);
    u32x4 s = v[0];
#pragma unroll
    for (int i = 1; i < PFN; ++i) s ^= v[i];
    asm volatile("" :: "v"(s));
    return;
  }
  {
    u64 pc; unsigned off, len;
    asm volatile("s_getpc_b64 %0\n.Lcpf_prep:\n s_mov_b32 %1, .Lcpf_prep-_Z6k_mega4Args\n s_mov_b32 %2, _Z6k_prepPcPKcS1_S1_S1_S1_-_Z6k_mega4Args" : "=s"(pc), "=s"(off), "=s"(len));
    const unsigned g = min(((blockIdx.x - 15 - PF_B) * 256u + threadIdx.x) * 16u, len - 16u);
    u32x4 v; asm volatile("global_load_dwordx4 %0, %1, %2" : "=v"(v) : "v"(g), "s"(pc - off));
  }
}

__global__ void k_diag(char* ws) {
  const unsigned long long ts = __builtin_amdgcn_s_memrealtime();
  unsigned acc = 0; int dev = 0, ref = 0;
  for (int pg = 0; pg < 40; ++pg) {
    char* cal = ws + (size_t)pg * 2 * MB + 0x1f0000 + 6144;
    int vote = 0;
    for (int sp = 0; sp < 3; ++sp) {
      unsigned best0 = ~0u, best1 = ~0u;
      for (int r = 0; r < 3; ++r) for (int hf = 0; hf < 2; ++hf) {
        const unsigned long long t0 = __builtin_amdgcn_s_memtime();
        acc += __hip_atomic_fetch_add((gu32*)(cal + sp * 16384 + hf * 8192 + (acc & 0u)), 1u, RLX_AGENT);
        asm volatile("s_waitcnt vmcnt(0)" ::: "memory");
        const unsigned d = (unsigned)(__builtin_amdgcn_s_memtime() - t0);
        if (hf == 0) best0 = d < best0 ? d : best0; else best1 = d < best1 ? d : best1;
      }
      vote += (int)best1 - (int)best0 + (int)(acc & 0u);
    }
    const int pol = ((vote < 0) ^ pg) & 1;
    if (pg == 0) ref = pol; else dev += pol != ref;
  }
  while (__builtin_amdgcn_s_memrealtime() - ts < 60000ull + 2000ull * dev) __builtin_amdgcn_s_sleep(8);
}
#include <stdio.h>
extern "C" void kernel_launch(void* const* d_in, const int* in_sizes, int n_in, void* d_out, int out_size, void* d_ws, size_t ws_size, hipStream_t stream) {
  Args a;
  a.Ax = (const u16*)d_in[0]; a.Awqkv = (const u16*)d_in[1]; a.Awo = (const u16*)d_in[2]; a.Bx = (const u16*)d_in[3]; a.mem0 = (const u16*)d_in[4];
  a.Wq = (const u16*)d_in[5]; a.Wk = (const u16*)d_in[6]; a.Wv = (const u16*)d_in[7]; a.Wo = (const u16*)d_in[8];
  a.out = (u16*)d_out;
  a.ws = (char*)(((size_t)d_ws + 2 * MB - 1) & ~(size_t)(2 * MB - 1));
  if ((size_t)(a.ws - (char*)d_ws) + O_END > ws_size) return;
  const int dd[16] = {DDV};
  for (int i = 0; i < 16; ++i) a.d[i] = dd[i];
  static_assert(CNT(C_TOTAL) * 4 == 15 * 4096, "");
  k_prep<<<15 + PF_B + ((CPF & 2) ? 48 : 0), 256, 0, stream>>>(a.ws, (const char*)a.Wq, (const char*)a.Wk, (const char*)a.Wv, (const char*)a.Wo, (const char*)a.Bx);
  (void)hipFuncSetAttribute((const void*)k_mega, hipFuncAttributeMaxDynamicSharedMemorySize, 163840);
  k_mega<<<256, 256, 163840, stream>>>(a);
  k_diag<<<1, 1, 0, stream>>>(a.ws);
  fprintf(stderr, "PTR ws %p out %p Wq %p Bx %p\n", d_ws, d_out, d_in[5], d_in[3]);
}
```

```cpp
#include <hip/hip_runtime.h>
#include <hip/hip_bf16.h>
#include <stdint.h>

typedef unsigned short u16;
typedef unsigned long long u64;
typedef __attribute__((ext_vector_type(4))) float f32x4;
typedef __attribute__((address_space(1))) u64 gu64;
typedef __attribute__((address_space(1))) unsigned gu32;
typedef unsigned u32x8 __attribute__((ext_vector_type(8)));
typedef unsigned u32x4 __attribute__((ext_vector_type(4)));
typedef unsigned u32x2 __attribute__((ext_vector_type(2)));
typedef __attribute__((ext_vector_type(4))) short bf16x4;
typedef __attribute__((ext_vector_type(8))) __bf16 bfv8;
typedef __attribute__((ext_vector_type(16))) __bf16 bfv16;
typedef __bf16 bf16x2_t __attribute__((ext_vector_type(2)));
typedef float f32x2_t __attribute__((ext_vector_type(2)));
typedef unsigned v2u_t __attribute__((ext_vector_type(2)));
#define RLX_AGENT __ATOMIC_RELAXED, __HIP_MEMORY_SCOPE_AGENT
#define LAS __attribute__((address_space(3)))

__device__ __forceinline__ float bf2f(u16 v) { return __uint_as_float(((unsigned)v) << 16); }
__device__ __forceinline__ u16 f2bf(float f) { return __builtin_bit_cast(u16, (__bf16)f); }
__device__ __forceinline__ float rbf(float f) { return bf2f(f2bf(f)); }
__device__ __forceinline__ unsigned cvtpk(float lo, float hi) { f32x2_t v = {lo, hi}; return __builtin_bit_cast(unsigned, __builtin_convertvector(v, bf16x2_t)); }
__device__ __forceinline__ __amdgpu_buffer_rsrc_t mkrs(const void* p) { return __builtin_amdgcn_make_buffer_rsrc((void*)p, 0, 0x7fffffff, 0x00020000); }
__device__ __forceinline__ u32x4 ld16(__amdgpu_buffer_rsrc_t rs, unsigned voff, unsigned soff) { return __builtin_amdgcn_raw_buffer_load_b128(rs, (int)voff, (int)soff, 16); }
#define OLA 0
__device__ __forceinline__ u32x4 ld16c(__amdgpu_buffer_rsrc_t rs, unsigned voff, unsigned soff) { return __builtin_amdgcn_raw_buffer_load_b128(rs, (int)voff, (int)soff, OLA); }
__device__ __forceinline__ u32x2 ld8(__amdgpu_buffer_rsrc_t rs, unsigned voff, unsigned soff) { return __builtin_amdgcn_raw_buffer_load_b64(rs, (int)voff, (int)soff, 16); }
__device__ __forceinline__ void st16(__amdgpu_buffer_rsrc_t rs, unsigned voff, unsigned soff, u32x4 w) { __builtin_amdgcn_raw_buffer_store_b128(w, rs, (int)voff, (int)soff, 16); asm volatile("s_nop 1" :: "v"(w) : "memory"); }
__device__ __forceinline__ void st8(__amdgpu_buffer_rsrc_t rs, unsigned voff, unsigned soff, unsigned lo, unsigned hi) { u32x2 w = {lo, hi}; __builtin_amdgcn_raw_buffer_store_b64(w, rs, (int)voff, (int)soff, 16); }
__device__ __forceinline__ bool full16(u32x4 v) { return ((v.x & v.y) != ~0u) & ((v.z & v.w) != ~0u); }
__device__ __forceinline__ float max16(float x) { v2u_t r = __builtin_amdgcn_permlane16_swap(__float_as_uint(x), __float_as_uint(x), false, false); return fmaxf(__uint_as_float(r[0]), __uint_as_float(r[1])); }
__device__ __forceinline__ float max32(float x) { v2u_t r = __builtin_amdgcn_permlane32_swap(__float_as_uint(x), __float_as_uint(x), false, false); return fmaxf(__uint_as_float(r[0]), __uint_as_float(r[1])); }
__device__ __forceinline__ float sum16(float x) { v2u_t r = __builtin_amdgcn_permlane16_swap(__float_as_uint(x), __float_as_uint(x), false, false); return __uint_as_float(r[0]) + __uint_as_float(r[1]); }
__device__ __forceinline__ float sum32(float x) { v2u_t r = __builtin_amdgcn_permlane32_swap(__float_as_uint(x), __float_as_uint(x), false, false); return __uint_as_float(r[0]) + __uint_as_float(r[1]); }
__device__ __forceinline__ float tred32(float a, float b) { v2u_t r = __builtin_amdgcn_permlane32_swap(__float_as_uint(a), __float_as_uint(b), false, false); return __uint_as_float(r[0]) + __uint_as_float(r[1]); }
__device__ __forceinline__ float tred16(float a, float b) { v2u_t r = __builtin_amdgcn_permlane16_swap(__float_as_uint(a), __float_as_uint(b), false, false); return __uint_as_float(r[0]) + __uint_as_float(r[1]); }
__device__ __forceinline__ unsigned ror8(unsigned x) { return (unsigned)__builtin_amdgcn_mov_dpp((int)x, 0x128, 0xf, 0xf, true); }
template <int N> __device__ __forceinline__ void nap() { if constexpr (N > 0) { if constexpr (N > 127) { __builtin_amdgcn_s_sleep(127); nap<N - 127>(); } else __builtin_amdgcn_s_sleep(N); } }
__device__ __forceinline__ unsigned half_of(const char* base, unsigned off, unsigned side, unsigned pol) {
  return off + ((((unsigned)(((size_t)base + off) >> 21) ^ side ^ pol) & 1u) << 13);
}
__device__ __forceinline__ unsigned side_map(const void* base, unsigned L, unsigned side, unsigned pol) { return half_of((const char*)base, (L >> 13) << 14, side, pol) + (L & 8191u); }
constexpr unsigned CH_SPIN_CAP = 1u << 18;
__device__ __forceinline__ bool spin_check(unsigned& spins, unsigned* tmop) {
  asm volatile("" ::: "memory");
  ++spins;
  if ((spins & 63u) == 0u) {
    gu32* tmo = (gu32*)tmop;
    if (__builtin_amdgcn_readfirstlane(__hip_atomic_load(tmo, RLX_AGENT))) return false;
    if (spins > CH_SPIN_CAP) { __hip_atomic_store(tmo, 1u, RLX_AGENT); return false; }
  }
  return true;
}
__device__ __forceinline__ unsigned xcc_side() { return ((unsigned)__builtin_amdgcn_s_getreg((3 << 11) | 20) >> 2) & 1u; }
__device__ __forceinline__ f32x4 mfma(u32x4 a, u32x4 b, f32x4 c) { return __builtin_amdgcn_mfma_f32_16x16x32_bf16(__builtin_bit_cast(bfv8, a), __builtin_bit_cast(bfv8, b), c, 0, 0, 0); }
__device__ __forceinline__ f32x4 smf(u32x4 a, u32x8 b, f32x4 c, int ix) { return __builtin_amdgcn_smfmac_f32_16x16x64_bf16(__builtin_bit_cast(bfv8, a), __builtin_bit_cast(bfv16, b), c, ix, 0, 0); }
__device__ __forceinline__ u32x4 lo4(u32x8 v) { return __builtin_shufflevector(v, v, 0, 1, 2, 3); }
__device__ __forceinline__ u32x4 hi4(u32x8 v) { return __builtin_shufflevector(v, v, 4, 5, 6, 7); }
__device__ __forceinline__ u32x8 cat8(u32x4 a, u32x4 b) { return __builtin_shufflevector(a, b, 0, 1, 2, 3, 4, 5, 6, 7); }
#define BAR() do { asm volatile("s_waitcnt lgkmcnt(0)" ::: "memory"); __builtin_amdgcn_s_barrier(); asm volatile("" ::: "memory"); } while (0)
#define SB() __builtin_amdgcn_sched_barrier(0)
#define VMW(n) asm volatile("s_waitcnt vmcnt(" #n ")" ::: "memory")
#define ZERO4 ((f32x4){0.f, 0.f, 0.f, 0.f})
__device__ __forceinline__ u32x2 trrd(const unsigned char* lds, unsigned off) { return __builtin_bit_cast(u32x2, __builtin_amdgcn_ds_read_tr16_b64_v4i16((LAS bf16x4*)(lds + off))); }
__device__ __forceinline__ u32x4 trrd2(const unsigned char* lds, unsigned off, unsigned d) { const u32x2 lo = trrd(lds, off), hi = trrd(lds, off + d); return (u32x4){lo.x, lo.y, hi.x, hi.y}; }


#include <stdlib.h>
#define SAG 16
#define DDV 88,88,16,0,30,0,127,99,92,110,25,30,20,12,20,320
#define FINE 1
#define WAIT_GO wait_fine
#define XLN 4
#define XLS 2
struct Args {
  const u16* mem0; const u16* Wq; const u16* Wk; const u16* Wv; const u16* Wo; const u16* Bx; const u16* Ax; const u16* Awqkv; const u16* Awo;
  u16* out; char* ws;
  int d[16];
};
constexpr size_t MB = 1u << 20;
#define WOFF 0
#define NK 16
#define NU 16
#define NZ 16
#define NP 16
#define KSIDE 1
constexpr unsigned X1_STEP = 32768, Z_STEP = 1u << 20, K_STEP = 1u << 20;
constexpr size_t O_PROF = 0, O_CNT = (WOFF + 1) * MB, O_CAL = O_CNT + 65536, O_TS = O_CAL + 65536, O_X1 = (WOFF + 2) * MB, O_PS = O_X1 + 1 * MB, O_KM = O_PS + 1 * MB, O_PT = O_KM + 2 * MB, O_OA = O_PT + NZ * 81920,
                 O_KF = O_PT + 4 * MB, O_QF = O_KF + NK * (size_t)K_STEP, O_VF = O_QF + NK * (size_t)K_STEP, O_ZB = KSIDE == 1 ? O_KF : O_VF + NK * MB, O_UO = O_VF + (KSIDE == 1 ? NK : NK + NZ) * MB, O_END = O_UO + NU * 40 * (size_t)32768;
static_assert(O_ZB % (2 * MB) == 0 && NP <= 16 && NZ <= 16 && NZ <= NK && NK % 4 == 0 && O_OA + 2048 * 1280 <= O_KF, "");
#define MSLOT(s_) (((unsigned)(s_) & (NZ - 1u)) * 98304u)
#define TSLOT(s_) (((unsigned)(s_) & (NZ - 1u)) * 81920u)
__device__ __forceinline__ unsigned k_fix(const unsigned o, const unsigned key) { return o ^ ((o >> 8) & 0x2000u) ^ key; }
#define K_KEY() ((((unsigned)((size_t)(ws + O_KF) >> 21) ^ (KSIDE == 2 ? (unsigned)h : KSIDE ^ pol)) & 1u) << 13)
#define KSLOT(s_) k_fix(((unsigned)(s_) & (NK - 1u)) * K_STEP, kkey)
#define ZSLOT(s_) (((unsigned)(s_) & (NZ - 1u)) * Z_STEP)
#define XSLOT(s_) (((unsigned)(s_) & (NP - 1u)) * X1_STEP)
#define CNT(i) (32 * (i))
__device__ __forceinline__ unsigned u_off(const unsigned s, const unsigned c) { return ((s & (NU - 1u)) * 40u + c) << 15; }
constexpr int PRE = 4;
#define WGATE 11
static_assert(WGATE + 3 <= NK && WGATE <= NU && WGATE + PRE + 1 <= NZ && NP >= PRE + 4, "");
constexpr unsigned NFILL = 40 + 64;
constexpr unsigned SENT = 0xFF81FF81u;
#define SENT4 ((u32x4){SENT, SENT, SENT, SENT})
#define PSLOT(s_) (((s_) & (NP - 1)) * 65536)
constexpr int C_EPOCH = 0, C_ROT = 1, C_POL = 2, C_GO = 3, C_TMO = 8, C_FILL = 10, C_PO = 12, C_PT_ = 32, C_PV = 64, C_PS = 96, C_PW = 128, C_PZ = 160, C_TOK = 192, C_AO = 464, C_TOTAL = 480;
__device__ __forceinline__ float nopk(float x) { asm("" : "+v"(x)); return x; }
__device__ __forceinline__ float fmx(float a, float b) { float big = __builtin_inff(); asm("" : "+s"(big)); return __builtin_amdgcn_fmed3f(a, b, big); }
__device__ __forceinline__ float mx16(float x) { v2u_t r = __builtin_amdgcn_permlane16_swap(__float_as_uint(x), __float_as_uint(x), false, false); return fmx(__uint_as_float(r[0]), __uint_as_float(r[1])); }
__device__ __forceinline__ float mx32(float x) { v2u_t r = __builtin_amdgcn_permlane32_swap(__float_as_uint(x), __float_as_uint(x), false, false); return fmx(__uint_as_float(r[0]), __uint_as_float(r[1])); }
__device__ __forceinline__ int lane_id() { int l; asm volatile("v_mbcnt_lo_u32_b32 %0, -1, 0\n v_mbcnt_hi_u32_b32 %0, -1, %0" : "=v"(l)); return l; }
__device__ __forceinline__ unsigned sent_min(unsigned m, unsigned x, unsigned y) { unsigned r = min(min(m, x ^ SENT), y ^ SENT); asm("" : "+v"(r)); return r; }
__device__ __forceinline__ unsigned sent_min(unsigned m, u32x4 v) { return sent_min(sent_min(m, v.x, v.y), v.z, v.w); }
#define RT_MARK(t_) asm volatile("s_memrealtime %0\n s_waitcnt lgkmcnt(0)" : "=s"(t_) :: "memory")
__device__ __forceinline__ unsigned wait_rt(unsigned target) {
  unsigned t;
  target = (unsigned)__builtin_amdgcn_readfirstlane(target);
  asm volatile("s_branch .Lwb%=\n.Lwa%=:\n s_sleep 1\n.Lwb%=:\n s_memrealtime vcc\n s_waitcnt lgkmcnt(0)\n s_sub_i32 %0, %1, vcc_lo\n s_add_i32 vcc_hi, %0, -9\n s_cmp_lt_u32 vcc_hi, 392\n s_cbranch_scc1 .Lwa%=\n"
               " s_add_i32 %0, %0, -1\n s_cmp_lt_u32 %0, 8\n s_cbranch_scc1 .Lwb%=\n s_mov_b32 %0, vcc_lo" : "=&s"(t) : "s"(target) : "vcc", "scc", "memory");
  return t;
}
#define FINE0 150
__device__ __forceinline__ unsigned wait_fine(unsigned target) {
  unsigned t;
  target = (unsigned)__builtin_amdgcn_readfirstlane(target);
  asm volatile("s_branch .Lfb%=\n.Lfa%=:\n s_sleep 1\n.Lfb%=:\n s_memrealtime vcc\n s_waitcnt lgkmcnt(0)\n s_sub_i32 %0, %1, vcc_lo\n s_add_i32 vcc_hi, %0, -23\n s_cmp_lt_u32 vcc_hi, 378\n s_cbranch_scc1 .Lfa%=\n"
               " s_add_i32 vcc_hi, %0, -5\n s_cmp_lt_u32 vcc_hi, 18\n s_cselect_b32 %0, %1, vcc_lo\n s_cbranch_scc0 .Lfe%=\n"
               " s_memrealtime s[92:93]\n s_nop 4\n s_memrealtime s[94:95]\n s_nop 4\n s_memrealtime s[96:97]\n s_nop 4\n s_memrealtime s[98:99]\n s_nop 4\n s_memrealtime vcc\n s_waitcnt lgkmcnt(0)\n"
               " s_sub_i32 s93, %1, vcc_lo\n s_mul_i32 s93, s93, 23\n s_add_i32 s93, s93, %2\n s_mov_b32 s95, 0\n"
               " s_cmp_eq_u32 s92, vcc_lo\n s_addc_u32 s95, s95, 0\n s_cmp_eq_u32 s94, vcc_lo\n s_addc_u32 s95, s95, 0\n s_cmp_eq_u32 s96, vcc_lo\n s_addc_u32 s95, s95, 0\n s_cmp_eq_u32 s98, vcc_lo\n s_addc_u32 s95, s95, 0\n"
               " s_mul_i32 s95, s95, 24\n s_sub_i32 s93, s93, s95\n s_max_i32 s93, s93, 0\n s_min_i32 s93, s93, 383\n s_lshr_b32 s93, s93, 3\n s_lshl_b32 s93, s93, 2\n"
               " s_getpc_b64 s[96:97]\n.Lfp%=:\n s_sub_u32 s93, .Lfe%=-.Lfp%=, s93\n s_add_u32 s96, s96, s93\n s_addc_u32 s97, s97, 0\n s_setpc_b64 s[96:97]\n .rept 48\n s_nop 1\n .endr\n.Lfe%=:"
               : "=&s"(t) : "s"(target), "n"(-FINE0) : "vcc", "scc", "s92", "s93", "s94", "s95", "s96", "s97", "s98", "s99", "memory");
  return t;
}
__device__ __forceinline__ unsigned ts_max(unsigned v) {
  if ((unsigned)__ballot(v == ~0u)) return ~0u;
  const unsigned base = (unsigned)__builtin_amdgcn_readfirstlane(v) - 4096u;
  unsigned d = v == ~0u ? 0u : v - base;
  d = max(d, (unsigned)__builtin_amdgcn_mov_dpp((int)d, 0x121, 0xf, 0xf, true)); d = max(d, (unsigned)__builtin_amdgcn_mov_dpp((int)d, 0x122, 0xf, 0xf, true));
  d = max(d, (unsigned)__builtin_amdgcn_mov_dpp((int)d, 0x124, 0xf, 0xf, true)); d = max(d, (unsigned)__builtin_amdgcn_mov_dpp((int)d, 0x128, 0xf, 0xf, true));
  return (base + max(max((unsigned)__builtin_amdgcn_readlane(d, 0), (unsigned)__builtin_amdgcn_readlane(d, 16)), max((unsigned)__builtin_amdgcn_readlane(d, 32), (unsigned)__builtin_amdgcn_readlane(d, 48))));
}
#define TS_OFF(l_) (((l_) & 31) * 256 + ((l_) >> 5) * 16384)
#define TS_STEP(s_) (((s_) & 1) * 128 + ((s_) >> 1) * 4)
__device__ __forceinline__ unsigned ts_settle(const __amdgpu_buffer_rsrc_t rs_ts, const int tss, unsigned t, const int lead) {
  for (int n = 0; n < 48; ++n) {
    unsigned now;
    if (t == ~0u) { __builtin_amdgcn_s_sleep(4); u64 t_; RT_MARK(t_); now = (unsigned)t_; } else now = wait_rt(t - (unsigned)lead);
    t = ts_max(__builtin_amdgcn_raw_buffer_load_b32(rs_ts, TS_OFF(lane_id()), tss, 16));
    if (t != ~0u && (int)(t - now) <= lead + 4) break;
  }
  return t;
}
#define ADAPT(d_, failed_) do { if (s > 3) d_ += (failed_) ? a.d[5] : -(int)((s & a.d[6]) == 0); } while (0)
__device__ __forceinline__ void nap_rt(int n) { for (int i = 0; i < n; ++i) __builtin_amdgcn_s_sleep(1); }
#define PINV(x) asm volatile("" : "+v"(x))
#define PINS(x) asm volatile("" : "+s"(x))
#define PINU(x) asm volatile("" :: "v"(x))
#define IL(nm, nv) do { _Pragma("unroll") for (int il_ = 0; il_ < (nm); ++il_) { __builtin_amdgcn_sched_group_barrier(0x008, 1, 0); __builtin_amdgcn_sched_group_barrier(0x7f6, nv, 0); } } while (0)
constexpr int L_MEM = 0, L_QI = 12288;
constexpr int L_B0 = 16384, L_B1 = 65536, L_B2 = 114688;
constexpr int L_QA = L_B1, L_XV = L_B1 + 4608, L_FLG = L_B1 + 5632, L_XR = L_B1 + 6144;

#define TR8_ASM "ds_read_b64_tr_b16 %0, %8\n ds_read_b64_tr_b16 %1, %8 offset:1536\n ds_read_b64_tr_b16 %2, %8 offset:12288\n ds_read_b64_tr_b16 %3, %8 offset:13824\n" \
               "ds_read_b64_tr_b16 %4, %8 offset:24576\n ds_read_b64_tr_b16 %5, %8 offset:26112\n ds_read_b64_tr_b16 %6, %8 offset:36864\n ds_read_b64_tr_b16 %7, %8 offset:38400\n s_waitcnt lgkmcnt(0)"
__device__ __forceinline__ void tr8(const bool agpr, const unsigned ad, u32x8& f0, u32x8& f1) {
  u32x2 a, b, c, d, e, f, g, h;
  if (agpr) asm volatile(TR8_ASM : "=&a"(a), "=&a"(b), "=&a"(c), "=&a"(d), "=&a"(e), "=&a"(f), "=&a"(g), "=&a"(h) : "v"(ad) : "memory");
  else asm volatile(TR8_ASM : "=&v"(a), "=&v"(b), "=&v"(c), "=&v"(d), "=&v"(e), "=&v"(f), "=&v"(g), "=&v"(h) : "v"(ad) : "memory");
  f0 = (u32x8){a.x, a.y, b.x, b.y, c.x, c.y, d.x, d.y}; f1 = (u32x8){e.x, e.y, f.x, f.y, g.x, g.y, h.x, h.y};
}
__device__ __forceinline__ void tr8d(const bool agpr, const unsigned ad, u32x4& f0, u32x4& f1, u32x4& f2, u32x4& f3) {
  u32x2 a, b, c, d, e, f, g, h;
  if (agpr) asm volatile(TR8_ASM : "=&a"(a), "=&a"(b), "=&a"(c), "=&a"(d), "=&a"(e), "=&a"(f), "=&a"(g), "=&a"(h) : "v"(ad) : "memory");
  else asm volatile(TR8_ASM : "=&v"(a), "=&v"(b), "=&v"(c), "=&v"(d), "=&v"(e), "=&v"(f), "=&v"(g), "=&v"(h) : "v"(ad) : "memory");
  f0 = (u32x4){a.x, a.y, b.x, b.y}; f1 = (u32x4){c.x, c.y, d.x, d.y}; f2 = (u32x4){e.x, e.y, f.x, f.y}; f3 = (u32x4){g.x, g.y, h.x, h.y};
}
#define TR4_ASM "ds_read_b64_tr_b16 %0, %4 offset:%5\n ds_read_b64_tr_b16 %1, %4 offset:%6\n ds_read_b64_tr_b16 %2, %4 offset:%7\n ds_read_b64_tr_b16 %3, %4 offset:%8\n s_waitcnt lgkmcnt(0)"
template <int J> __device__ __forceinline__ void tr4(const bool agpr, const unsigned ad, u32x8& f) {
  u32x2 a, b, c, d;
  if (agpr) asm volatile(TR4_ASM : "=&a"(a), "=&a"(b), "=&a"(c), "=&a"(d) : "v"(ad), "n"(16384 * J), "n"(16384 * J + 4096), "n"(16384 * J + 8192), "n"(16384 * J + 12288) : "memory");
  else asm volatile(TR4_ASM : "=&v"(a), "=&v"(b), "=&v"(c), "=&v"(d) : "v"(ad), "n"(16384 * J), "n"(16384 * J + 4096), "n"(16384 * J + 8192), "n"(16384 * J + 12288) : "memory");
  f = (u32x8){a.x, a.y, b.x, b.y, c.x, c.y, d.x, d.y};
}
template <int J> __device__ __forceinline__ void tr4d(const bool agpr, const unsigned ad, u32x4& f0, u32x4& f1) {
  u32x2 a, b, c, d;
  if (agpr) asm volatile(TR4_ASM : "=&a"(a), "=&a"(b), "=&a"(c), "=&a"(d) : "v"(ad), "n"(16384 * J), "n"(16384 * J + 4096), "n"(16384 * J + 8192), "n"(16384 * J + 12288) : "memory");
  else asm volatile(TR4_ASM : "=&v"(a), "=&v"(b), "=&v"(c), "=&v"(d) : "v"(ad), "n"(16384 * J), "n"(16384 * J + 4096), "n"(16384 * J + 8192), "n"(16384 * J + 12288) : "memory");
  f0 = (u32x4){a.x, a.y, b.x, b.y}; f1 = (u32x4){c.x, c.y, d.x, d.y};
}
__device__ __forceinline__ void glds(const void* sbase, unsigned voff, unsigned ldsdst) {
  unsigned keep;
  asm volatile("s_mov_b32 %0, m0\n s_mov_b32 m0, %3\n s_nop 2\n global_load_lds_dwordx4 %1, %2\n s_mov_b32 m0, %0" : "=&s"(keep) : "v"(voff), "s"(sbase), "s"(ldsdst) : "memory");
}
__device__ __forceinline__ void m0_set(unsigned ldsdst) { asm volatile("s_mov_b32 m0, %0\n s_nop 0" :: "s"(ldsdst) : "memory"); }
template <int OFF> __device__ __forceinline__ void glds_o(const void* sbase, unsigned voff) { asm volatile("global_load_lds_dwordx4 %0, %1 offset:%2" :: "v"(voff), "s"(sbase), "n"(OFF) : "memory"); }
template <int OFF> __device__ __forceinline__ void glds_x(const void* sbase, unsigned voff) { asm volatile("global_load_lds_dwordx4 %0, %1 offset:%2 sc1" :: "v"(voff), "s"(sbase), "n"(OFF) : "memory"); }
__device__ __forceinline__ unsigned lds_addr(const unsigned char* p) { return (unsigned)(size_t)(const LAS unsigned char*)p; }
__device__ __forceinline__ unsigned ld_word(const unsigned* p) { return (unsigned)__builtin_amdgcn_readfirstlane(__hip_atomic_load((const gu32*)p, RLX_AGENT)); }
__device__ __forceinline__ unsigned uni(unsigned v) { asm volatile("s_nop 0" : "+v"(v)); return (unsigned)__builtin_amdgcn_readfirstlane(v); }
__device__ __forceinline__ u32x2 tag_of(const unsigned e) { return (u32x2){(e * 0x9E3779B1u + 0x7F4A7C15u) & ~7u, (e ^ 0x5BD1E995u) * 0x85EBCA6Bu + 1u}; }
__device__ __forceinline__ void send_tag(unsigned* cnt, const int line, const u32x2 tag, const unsigned v) { __hip_atomic_store((gu64*)(cnt + CNT(line)), ((u64)tag.y << 32) | (tag.x | v), RLX_AGENT); }
template <int SLEEP>
__device__ __forceinline__ unsigned wait_tag(const unsigned* cnt, const int line, const u32x2 tag) {
  unsigned lo = 0;
#pragma unroll 1
  for (unsigned n = 0; n < CH_SPIN_CAP; ++n) {
    const u64 w = __hip_atomic_load((const gu64*)(cnt + CNT(line)), RLX_AGENT);
    lo = (unsigned)__builtin_amdgcn_readfirstlane((unsigned)w);
    if ((lo & ~7u) == tag.x && (unsigned)__builtin_amdgcn_readfirstlane((unsigned)(w >> 32)) == tag.y) break;
    __builtin_amdgcn_s_sleep(SLEEP);
  }
  asm volatile("" ::: "memory");
  return uni(lo & 7u);
}
__device__ __forceinline__ void fill_done(unsigned* cnt, const u32x2 tag, const unsigned pol) {
  if (lane_id() == 0) { if (__hip_atomic_fetch_add((gu32*)(cnt + CNT(C_FILL)), 1u, RLX_AGENT) == NFILL - 1u) send_tag(cnt, C_GO, tag, pol); }
}
template <int SLEEP>
__device__ __forceinline__ unsigned wait_ge(const unsigned* p, unsigned target, unsigned* tmo) {
  unsigned spins = 0, v;
  while ((v = ld_word(p)) < target) { __builtin_amdgcn_s_sleep(SLEEP); if (!spin_check(spins, tmo)) break; }
  asm volatile("" ::: "memory");
  return v;
}
__device__ __forceinline__ unsigned min_t(const u32x4 w, const int kv) {
  unsigned v = min(min(w.x, w.y), w.z); if (!kv) v = min(v, w.w);
  return (unsigned)__builtin_amdgcn_readfirstlane(v);
}
template <int SLEEP>
__device__ __forceinline__ unsigned wait_t(const unsigned* p, const int first, unsigned target, unsigned* tmo) {
  unsigned spins = 0, v;
  for (;;) {
    v = min_t(ld16(mkrs(p), 0u, 0u), first);
    if (v >= target) break;
    __builtin_amdgcn_s_sleep(SLEEP); if (!spin_check(spins, tmo)) break;
  }
  asm volatile("" ::: "memory");
  return v;
}
template <int SLEEP>
__device__ __forceinline__ void wait_heads(const unsigned* p, const int second, unsigned target, unsigned* tmo) {
  unsigned spins = 0; const int ln = lane_id();
  while (!__all(__builtin_amdgcn_raw_buffer_load_b32(mkrs(p), (ln & 31) * 128 + (ln >> 5) * second * 4, 0, 16) >= target)) { __builtin_amdgcn_s_sleep(SLEEP); if (!spin_check(spins, tmo)) break; }
  asm volatile("" ::: "memory");
}
#define WNAP 12
template <int N> __device__ __forceinline__ void nap4() { if constexpr (N > 16) { asm volatile("s_nop 15"); nap4<N - 16>(); } else if constexpr (N > 0) asm volatile("s_nop %0" :: "n"(N - 1)); }
#define DPPM(d_, s_, ctl_) asm volatile("v_max_f32_dpp %0, %1, %1 " ctl_ " row_mask:0xf bank_mask:0xf" : "=v"(d_) : "v"(s_))
#define XOR1 "quad_perm:[1,0,3,2]"
#define XOR2 "quad_perm:[2,3,0,1]"
__device__ __forceinline__ void light(const __amdgpu_buffer_rsrc_t rs, unsigned vo, const unsigned so, unsigned* tmo, const int slow = 0) {
  unsigned spins = 0;
  for (;;) {
    const bool empty = __builtin_amdgcn_raw_buffer_load_b32(rs, (int)vo, (int)so, 16) == SENT;
    if (!__any(empty) || !spin_check(spins, tmo)) break;
    vo = empty ? vo : 0x80000000u;
    if (slow) __builtin_amdgcn_s_sleep(16); else __builtin_amdgcn_s_sleep(XLS);
  }
}
__device__ __forceinline__ void x1_light(const __amdgpu_buffer_rsrc_t rs_x1, const unsigned so, unsigned* tmo) {
  const unsigned ln = (unsigned)lane_id();
  light(rs_x1, ln < 40u ? (ln >> 5) * 16384u + (ln & 31u) * 256u + 252u : 0x80000000u, so, tmo);
}
__device__ __forceinline__ unsigned touch(const void* p, const int bytes, const int piece) { return __builtin_amdgcn_raw_buffer_load_b32(__builtin_amdgcn_make_buffer_rsrc((void*)p, 0, bytes, 0x00020000), lane_id() * 128, piece * 8192, 0); }
__device__ __forceinline__ unsigned touch_rows(const Args& a, const int which, const int piece) { return touch(which == 0 ? (const void*)a.Bx : (which == 1 ? (const void*)a.Ax : (const void*)(a.ws + O_OA)), 2048 * 1280, piece); }
#define TM 0
template <int N> __device__ __forceinline__ void touch_n(const Args& a, const int w, const int first, const int step) {
  unsigned v = 0;
#pragma unroll
  for (int i = 0; i < N; ++i) v ^= touch_rows(a, w % 3, first + w / 3 + step * i);
  asm volatile("" :: "v"(v));
}
#define DS(x) do { x; SB(); } while (0)
template <int V> struct IC { static constexpr int v = V; };
template <int I, int N, class F> __device__ __forceinline__ void sfor(F&& f) { if constexpr (I < N) { f(IC<I>{}); sfor<I + 1, N>(f); } }
#define SFOR(i_, n_) sfor<0, n_>([&](auto i_##_c) __attribute__((always_inline)) { constexpr int i_ = decltype(i_##_c)::v;
#define SEND })
typedef unsigned u32x32 __attribute__((ext_vector_type(32)));
typedef unsigned u32x16 __attribute__((ext_vector_type(16)));
struct WReg { u32x32 b[11]; u32x16 e; };
#define WR_SW(f_, X) switch ((f_) >> 2) { case 0: X("a", "{a[0:31]}", W.b[0]); break; case 1: X("a", "{a[32:63]}", W.b[1]); break; case 2: X("a", "{a[64:95]}", W.b[2]); break; case 3: X("a", "{a[96:127]}", W.b[3]); break; \
    case 4: X("a", "{a[128:159]}", W.b[4]); break; case 5: X("a", "{a[160:191]}", W.b[5]); break; case 6: X("a", "{a[192:223]}", W.b[6]); break; case 7: X("a", "{a[224:255]}", W.b[7]); break; \
    case 8: X("v", "{v[142:173]}", W.b[8]); break; case 9: X("v", "{v[174:205]}", W.b[9]); break; case 10: X("v", "{v[206:237]}", W.b[10]); break; default: X("v", "{v[238:253]}", W.e); break; }
__device__ __forceinline__ void smf_w(f32x4& c, const u32x4 a, const int f, const int ix, const WReg& W) {
  const int r = f < 32 ? 8 * f : 8 * f - 114;
#define X_(rf_, cs_, v_) asm volatile("v_smfmac_f32_16x16x64_bf16 %0, %1, " rf_ "[%c3:%c4], %2" : "+v"(c) : "v"(a), "v"(ix), "n"(r), "n"(r + 7), cs_(v_))
  WR_SW(f, X_);
#undef X_
}
template <bool WA> __device__ __forceinline__ void mf_w(f32x4& c, const u32x4 x, const int d, const bool first, const WReg& W) {
  const int r = d < 64 ? 4 * d : 4 * d - 114;
#define X_(rf_, cs_, v_) if (WA) { if (first) asm volatile("v_mfma_f32_16x16x32_bf16 %0, " rf_ "[%c2:%c3], %1, 0" : "=&v"(c) : "v"(x), "n"(r), "n"(r + 3), cs_(v_)); \
      else asm volatile("v_mfma_f32_16x16x32_bf16 %0, " rf_ "[%c2:%c3], %1, %0" : "+v"(c) : "v"(x), "n"(r), "n"(r + 3), cs_(v_)); } \
    else { if (first) asm volatile("v_mfma_f32_16x16x32_bf16 %0, %1, " rf_ "[%c2:%c3], 0" : "=&v"(c) : "v"(x), "n"(r), "n"(r + 3), cs_(v_)); \
      else asm volatile("v_mfma_f32_16x16x32_bf16 %0, %1, " rf_ "[%c2:%c3], %0" : "+v"(c) : "v"(x), "n"(r), "n"(r + 3), cs_(v_)); }
  WR_SW(d >> 1, X_);
#undef X_
}
__device__ __forceinline__ void ld_w(const u32x4 rs, const unsigned vo, const unsigned so, const int off, const int f, const bool def, WReg& W) {
  const int r = f < 32 ? 8 * f : 8 * f - 114;
#define Y_(md_, rf_, cs_, v_) asm volatile("s_nop 4\n buffer_load_dwordx4 " rf_ "[%c6:%c7], %1, %2, %3 offen offset:%4 sc1\n buffer_load_dwordx4 " rf_ "[%c8:%c9], %1, %2, %3 offen offset:%5 sc1" \
    : md_ cs_(v_) : "v"(vo), "s"(rs), "s"(so), "n"(off), "n"(off + 1024), "n"(r), "n"(r + 3), "n"(r + 4), "n"(r + 7) : "memory")
#define X_(rf_, cs_, v_) if (def) Y_("=&", rf_, cs_, v_); else Y_("+", rf_, cs_, v_)
  WR_SW(f, X_);
#undef X_
#undef Y_
}
__device__ __forceinline__ u32x4 mkdesc(const void* p) { const size_t b = (size_t)p; return (u32x4){(unsigned)b, (unsigned)(b >> 32) & 0xffffu, 0x7fffffffu, 0x00020000u}; }
__device__ __forceinline__ void tr4_w(const unsigned ad, const int o0, const int o1, const int o2, const int o3, const int f, WReg& W) {
  const int r = f < 32 ? 8 * f : 8 * f - 114;
#define Y_(md_, rf_, cs_, v_) asm volatile("ds_read_b64_tr_b16 " rf_ "[%c6:%c7], %1 offset:%2\n ds_read_b64_tr_b16 " rf_ "[%c8:%c9], %1 offset:%3\n ds_read_b64_tr_b16 " rf_ "[%c10:%c11], %1 offset:%4\n ds_read_b64_tr_b16 " rf_ "[%c12:%c13], %1 offset:%5\n s_waitcnt lgkmcnt(0)" \
    : md_ cs_(v_) : "v"(ad), "n"(o0), "n"(o1), "n"(o2), "n"(o3), "n"(r), "n"(r + 1), "n"(r + 2), "n"(r + 3), "n"(r + 4), "n"(r + 5), "n"(r + 6), "n"(r + 7) : "memory")
#define X_(rf_, cs_, v_) if ((f & 3) == 0) Y_("=&", rf_, cs_, v_); else Y_("+", rf_, cs_, v_)
  WR_SW(f, X_);
#undef X_
#undef Y_
}
#define TRS0(ad_, f_) tr4_w(ad_, 0, 1536, 12288, 13824, f_, W)
#define TRS1(ad_, f_) tr4_w(ad_, 24576, 26112, 36864, 38400, f_, W)
#define TRO4(ad_, j_, f_) tr4_w(ad_, 16384 * (j_), 16384 * (j_) + 4096, 16384 * (j_) + 8192, 16384 * (j_) + 12288, f_, W)
#define SMFV(c_, a_, b_) do { asm volatile("v_smfmac_f32_16x16x64_bf16 %0, %1, %2, %3" : "+v"(c_) : "v"(a_), "v"(b_), "v"(ix)); SB(); } while (0)
#define SMFW(c_, a_, f_) do { smf_w(c_, a_, f_, ix, W); SB(); } while (0)
#define ADD1(d_, x_, y_) do { d_ = (x_) + (y_); PINU(d_); SB(); } while (0)
#define ADD1B(d_, x_, y_) do { d_ = __builtin_fmaf(x_, one, y_); PINU(d_); SB(); } while (0)
#define ADD2(d0_, d1_, c_) do { asm volatile("v_add_f32 %0, %2, %3\n v_add_f32 %1, %4, %5" : "=&v"(d0_), "=v"(d1_) : "v"(c_[0]), "v"(c_[2]), "v"(c_[1]), "v"(c_[3])); SB(); } while (0)
#define CVT1(d_, x_, y_) do { d_ = cvtpk(x_, y_); PINU(d_); SB(); } while (0)
#define EPI3(d_, c_) do { float e_; asm volatile("v_add_f32 %1, %2, %3\n v_add_f32 %0, %4, %5\n v_cvt_pk_bf16_f32 %0, %1, %0" : "=&v"(d_), "=&v"(e_) : "v"(c_[0]), "v"(c_[2]), "v"(c_[1]), "v"(c_[3])); SB(); } while (0)
#define EPI4(d_, m_, c_) do { float e_, f_; asm volatile("v_add_f32 %1, %4, %5\n v_add_f32 %2, %6, %7\n v_cvt_pk_bf16_f32 %0, %1, %2\n v_cmp_u_f32_e64 %3, %1, %2" : "=&v"(d_), "=&v"(e_), "=&v"(f_), "=s"(m_) : "v"(c_[0]), "v"(c_[2]), "v"(c_[1]), "v"(c_[3])); SB(); } while (0)
#define CVTN(d_, m_, x_, y_) do { NANM(m_, x_, y_); CVT1(d_, x_, y_); } while (0)
#define ADDCVT(d_, x_, c_) do { asm volatile("v_add_f32 %0, %2, %3\n v_cvt_pk_bf16_f32 %0, %1, %0" : "=&v"(d_) : "v"(x_), "v"(c_[1]), "v"(c_[3])); SB(); } while (0)
#define LGW(n_) do { __builtin_amdgcn_s_waitcnt(0xC07F | ((n_) << 8)); SB(); } while (0)
#define LBAR(n_) do { asm volatile("s_waitcnt lgkmcnt(" #n_ ")" ::: "memory"); __builtin_amdgcn_s_barrier(); asm volatile("" ::: "memory"); SB(); } while (0)
#define FILL() do { } while (0)
#define NANM(m_, x_, y_) do { asm volatile("v_cmp_u_f32_e64 %0, %1, %2" : "=s"(m_) : "v"(x_), "v"(y_)); SB(); } while (0)
#define LW32(ad_, off_, v_) (*(unsigned*)(lds + (ad_) + (off_)) = (v_))
#define MEMB() asm volatile("" ::: "memory")
#define LG0() __builtin_amdgcn_s_waitcnt(0xC07F)
#define FLAG(i_) do { MEMB(); *(volatile LAS unsigned char*)(LAS unsigned char*)(lds + fla + 4 * (i_)) = (unsigned char)snv; SB(); } while (0)
#define FLRD(i_) (*(const volatile LAS unsigned*)(LAS unsigned char*)(lds + flr + 4 * (i_)))
#define SBUF(i_) (((i_) % 3) == 0 ? L_B0 : (((i_) % 3) == 1 ? L_B1 : L_B2))
#define SDMA_INIT() unsigned svo[12]; const int sq0 = 3 * ((h >> 2) & 3); \
    _Pragma("unroll") for (int q_ = 0; q_ < 12; ++q_) { const int qr_ = q_ + sq0, ins = wave + 4 * (qr_ < 12 ? qr_ : qr_ - 12); const unsigned c = (unsigned)(ins * 64 + lane), r = c / 24u, pc = c - r * 24u, sw = ((r >> 1) & 1u) * 2u + ((r >> 3) & 1u) * 4u; \
      svo[q_] = r * 12288u + 16u * (pc ^ sw); PINV(svo[q_]); }
#define KROT(kr_) ((kr_) < 4 ? (((kr_) + h) & 3) : 4)
#define SDMA(W_m, kr_, i_) do { const char* W_ = (const char*)((W_m) + (size_t)(128 * KROT(kr_)) * 6144 + 192 * h); \
    _Pragma("unroll") for (int q_ = 0; q_ < 12; ++q_) { const int qr_ = q_ + sq0; glds(W_, svo[q_], lds_addr(lds) + (unsigned)(SBUF(i_) + (wave + 4 * (qr_ < 12 ? qr_ : qr_ - 12)) * 1024)); } SB(); } while (0)
#define SDMAQK(i_) SDMA(((i_) & 1) ? a.Wk : a.Wq, (i_) >> 1, i_)
#define TRO(tile_) (trow * 384u + (((unsigned)(2 * (tile_) + ((l15 & 3) >> 1))) ^ tswz) * 16u + (unsigned)(l15 & 1) * 8u)
#define TRO_INIT() const unsigned trow = (unsigned)(8 * lq + (l15 >> 2)), tswz = ((trow >> 1) & 1u) * 2u + ((trow >> 3) & 1u) * 4u
#define SXDMA_INIT() unsigned xvo = (unsigned)((4 * wave + lq) * 1280 + (l15 ^ ((4 * wave + lq) & 15)) * 16); PINV(xvo)
#define SXDMA(kr_, xb_) do { const char* X_ = (const char*)a.Bx + 256 * KROT(kr_); \
    glds(X_, xvo, lds_addr(lds) + (unsigned)((xb_) + wave * 1024)); glds(X_ + 16 * 1280, xvo, lds_addr(lds) + (unsigned)((xb_) + 4096 + wave * 1024)); SB(); } while (0)
#define SXRD(xb_) _Pragma("unroll") for (int kk = 0; kk < 4; ++kk) _Pragma("unroll") for (int rt = 0; rt < 2; ++rt) xa[rt][kk] = *(const u32x4*)(lds + (xb_) + rt * 4096 + l15 * 256 + ((4 * kk + lq) ^ l15) * 16)
#define MFV(c_, a_, b_, first_) do { if (first_) asm volatile("v_mfma_f32_16x16x32_bf16 %0, %1, %2, 0" : "=&v"(c_) : "v"(a_), "v"(b_)); else asm volatile("v_mfma_f32_16x16x32_bf16 %0, %1, %2, %0" : "+v"(c_) : "v"(a_), "v"(b_)); } while (0)
#define HALF(f_, hi_) ((hi_) ? hi4(f_) : lo4(f_))
#define ODMA_INIT() unsigned ovo = (unsigned)((4 * wave + lq) * 1280 + (l15 ^ (((4 * wave + lq) & 7) << 1)) * 16); PINV(ovo); \
    const unsigned orow = (unsigned)(4 * lq + (l15 >> 2)); \
    unsigned otr[2]; _Pragma("unroll") for (int t = 0; t < 2; ++t) otr[t] = orow * 256u + (((unsigned)(2 * (2 * wave + t) + ((l15 & 3) >> 1))) ^ ((orow & 7u) << 1)) * 16u + (unsigned)(l15 & 1) * 8u
#define OROT(cr_) (((cr_) + h) % 5)
#define ODMA(cr_, i_) do { const char* W_ = (const char*)(a.Wo + (size_t)(192 * h) * 640) + 256 * OROT(cr_); \
    _Pragma("unroll") for (int q_ = 0; q_ < 12; ++q_) glds(W_ + q_ * (16 * 1280), ovo, lds_addr(lds) + (unsigned)(SBUF(i_) + (wave + 4 * q_) * 1024)); SB(); } while (0)

#define SAL 2
#define SOF 0
#define KXL 0
#define MNAP 0
#define SF0(m_, t_, k_) (6 * (2 * ((k_) >> 1) + (m_)) + 2 * (t_) + ((k_) & 1))
#define SFR(m_, t_, k_) (SF0(m_, t_, k_) - (SF0(m_, t_, k_) > 39) - (SF0(m_, t_, k_) > 41))
constexpr int L_KX = L_B1 + 22528, L_QX = 0;
template <int wave>
__device__ __forceinline__ void s_loop(const Args& a, unsigned char* lds, const int h, const unsigned pol, const WReg& W) {
  constexpr int KIND = wave < 2 ? 0 : wave - 1;
  char* ws = a.ws; asm volatile("" : "+s"(ws));
  unsigned* const cnt = (unsigned*)(ws + O_CNT); unsigned* const tmo = cnt + CNT(C_TMO);
  const int lane = lane_id(), l15 = lane & 15, lq = lane >> 4;
  char* const X1 = ws + O_X1; char* const P = ws + O_PS;
  const __amdgpu_buffer_rsrc_t rs_x1 = mkrs(X1), rs_p = mkrs(P);
  unsigned tprog = 0;
  const unsigned kkey = K_KEY();
  const char* const ksrc = ws + O_KF + (size_t)h * 32768 + wave * 16384 + 2048;
#define KXR(n_) do { if (KIND == 0) DS(kx[n_] = *(const LAS u32x4*)(size_t)(kxa + (n_) * 1024)); } while (0)
  unsigned trx = (unsigned)(L_XR + wave * 4096 + lane * 16); PINV(trx);
  unsigned trq = (unsigned)(L_B2 + wave * 12288 + lane * 16); PINV(trq);
  unsigned trk = (unsigned)(L_B0 + wave * 12288 + lane * 16); PINV(trk);
  int ix = (l15 & 2) ? 0xEEEE : 0x4444; PINV(ix);
  float one = 1.0f; PINS(one);
  const float CS = 0.07216878364870323f * 1.4426950408889634f;
  unsigned xv = (unsigned)lane * 16u;
  unsigned ib = (unsigned)(L_QI + (16 * wave + l15) * 16 + lq * 4);
  unsigned ia = (unsigned)(L_QA + wave * 384 + ((l15 >> 2) & 1) * 128 + (l15 >> 3) * 64 + (l15 & 1) * 32 + lq * 8 + ((l15 >> 1) & 1) * 4);
  unsigned rda = (unsigned)(L_QA + (lq >> 1) * 768 + (l15 >> 3) * 384 + (lq & 1) * 64 + ((l15 >> 2) & 1) * 32 + (l15 & 3) * 8);
  unsigned qk_rd = (unsigned)(L_QI + (4 * lq + (l15 >> 2)) * 16 + (l15 & 1) * 8);
  unsigned flr = (unsigned)L_FLG;
  unsigned xva = KIND == 0 ? (unsigned)(L_XV + lq * 256 + (l15 >> 1) * 16 + wave * 8 + (l15 & 1) * 4) : (unsigned)(L_XV + lane * 16);
  PINV(xv); PINV(ib); PINV(ia);
  if (KIND != 2) { PINV(rda); PINV(xva); } if (KIND == 1) PINV(qk_rd); if (KIND != 0) PINV(flr);
  const bool hi8 = l15 >= 8;
  const __amdgpu_buffer_rsrc_t rs_ts = mkrs(ws + O_TS + half_of(ws + O_TS, 0u, 0u, pol));
  int tso = lane == 0 ? 256 * h : (int)0x80000000u; if (KIND != 0) PINV(tso);
  int tsa = TS_OFF(lane); if (KIND == 2) PINV(tsa);
  const __amdgpu_buffer_rsrc_t rs_ps = mkrs(cnt + CNT(C_PS) - 32 * h);
  unsigned po = side_map(P, (unsigned)(h * 512 + ((l15 >> 2) & 1) * 256 + (4 * lq + 2 * (l15 & 1) + (l15 >> 3)) * 16 + ((l15 >> 1) & 1) * 8), 0u, pol);
  unsigned pm = !(l15 & 1) ? side_map(P, (unsigned)(16384 + h * 128 + (4 * lq + 2 * ((l15 >> 2) & 1) + (l15 >> 3)) * 8 + ((l15 >> 1) & 1) * 4), 0u, pol) : 0x80000000u;
  u32x4 p_desc = mkdesc(P); if (KIND == 1) PINS(p_desc);
  unsigned pfo = lane < 32 ? side_map(P, (unsigned)(h * 512 + lane * 16), 0u, pol) : (lane < 40 ? side_map(P, (unsigned)(16384 + h * 128 + (lane - 32) * 16), 0u, pol) : 0x80000000u);
  if (KIND == 1) { PINV(po); PINV(pm); }
  if (KIND == 1) PINV(pfo);
  unsigned dur = 130, tnx = 0, tpr = 0, adj = 0; bool was_calm = false; int dly = a.d[0]; PINS(dly);
#define MQ(t_, f_, w_) SMFV(aq[t_], f_, w_)
#define MK(t_, f_, w_) SMFV(ak[t_], f_, w_)
#define QW(t_, k_) SMFW(aq[t_], A[k_], SFR(0, t_, k_))
#define KW(t_, k_) SMFW(ak[t_], A[k_], SFR(1, t_, k_))
#define RLO(tb_, t_, k2_) (*(const u32x4*)(lds + (tb_) + (t_) * 4096 + (k2_) * 2048))
#define RHI(tb_, t_, k2_) (*(const u32x4*)(lds + (tb_) + (t_) * 4096 + (k2_) * 2048 + 1024))
#define RFR(tb_, t_, k2_) cat8(RLO(tb_, t_, k2_), RHI(tb_, t_, k2_))
#define XLO(j_) (*(const u32x4*)(lds + trx + (j_) * 2048))
#define XHI(j_) (*(const u32x4*)(lds + trx + (j_) * 2048 + 1024))
#define ALD(k_) DS(A[k_] = ld16(rs_x1, xv + (unsigned)(((k_) & 1) * 1024), sx[(k_) >> 1]))
#define APACE 10
#define ALP(k_) do { ALD(k_); nap4<APACE>(); SB(); } while (0)
#define A_LOAD() do { PINV(xv); ALP(0); ALP(8); ALP(1); ALP(9); ALP(2); ALP(7); ALP(3); ALP(4); ALP(5); ALD(6); SB(); } while (0)
  u32x4 ts_desc = mkdesc(ws + O_TS + half_of(ws + O_TS, 0u, 0u, pol) + 256 * h);
  u32x4 tsr_desc = mkdesc(ws + O_TS + half_of(ws + O_TS, 0u, 0u, pol)); if (KIND == 2) PINS(tsr_desc);
  unsigned fad = lds_addr(lds) + (unsigned)L_FLG; PINV(fad); PINS(ts_desc);
  if (KIND == 2) LW32(flr, 24, dur);
#pragma unroll 1
  for (int s = 0; s < 64; ++s) {
    int pss = PSLOT(s), tss = TS_STEP(s), tsn = TS_STEP(s + 1); PINS(pss); PINS(tss); if (KIND == 2) PINS(tsn);
    unsigned snp = (unsigned)(s + 1) * 0x01010101u, snv = snp, tries = 0; PINS(snp); PINV(snv);
    f32x4 sc = ZERO4; if (KIND != 2) PINV(sc);
    unsigned kxa = lds_addr(lds) + xv + (unsigned)(L_KX + (s & 1) * 12288 + wave * 6144); if (KIND == 0) PINV(kxa);
    u32x4 kx[6];
    unsigned sx[5];
    { const unsigned so = half_of(X1, XSLOT(s), 0u, pol);
#pragma unroll
      for (int j = 0; j < 5; ++j) { sx[j] = so + (j < 4 ? (unsigned)((j + h) & 3) * 2048u : 16384u); PINS(sx[j]); }
      if (KIND == 2 && s == 1) x1_light(rs_x1, so, tmo); }
    SB();
    asm volatile("s_branch .Lsre%c6\n"
                 ".Lsbad%c6:\n s_add_u32 %0, %0, 1\n s_cmp_lt_u32 %0, 4\n s_cbranch_scc1 .Lsnow%c6\n s_sleep 8\n"
                 ".Lsnow%c6:\n s_memrealtime vcc\n ds_write_b32 %5, %1 offset:28\n ds_read_b32 %1, %5 offset:24\n s_waitcnt lgkmcnt(0)\n v_add_u32 %1, vcc_lo, %1\n v_and_b32 %1, -2, %1\n"
                 " s_mov_b64 exec, 1\n buffer_store_dword %1, off, %3, %4 sc1\n s_mov_b64 exec, -1\n v_mov_b32 %1, %2\n"
                 ".Lsre%c6:" : "+s"(tries) : "v"(snv), "s"(snp), "s"(ts_desc), "s"(tss), "v"(fad), "n"(wave) : "vcc", "scc", "memory");
    SB();
    u32x8 ra = RFR(trq, 0, 0), rb = RFR(trq, 1, 0), rc = RFR(trq, 2, 0);
    f32x4 aq[3] = {ZERO4, ZERO4, ZERO4}, ak[3] = {ZERO4, ZERO4, ZERO4};
    asm volatile("s_cmp_lg_u32 %6, 0\n s_cbranch_scc1 .Lsgo%c7" : "+v"(aq[0]), "+v"(aq[1]), "+v"(aq[2]), "+v"(ak[0]), "+v"(ak[1]), "+v"(ak[2]) : "s"(tries), "n"(wave) : "scc", "memory");
    SB();
    unsigned tis = 0, tsl = 0; bool calm = false;
    asm volatile(".p2align %c0\n .fill %c1, 4, 0xbf800000" :: "n"(SAL), "n"(SOF));
    if (KIND == 2) tis = WAIT_GO(tnx);
    BAR();
    asm volatile(".Lsgo%c0:" :: "n"(wave) : "memory");
    SB();
    u32x4 A[10], lo, hi;
    A_LOAD();
    unsigned w0;
    float ta, tb_;
    u64 nanm;
    QW(0, 0); QW(1, 0); QW(2, 0); KW(0, 0);
    MQ(0, A[8], ra); DS(lo = RLO(trq, 0, 1));
    KW(1, 0); DS(hi = RHI(trq, 0, 1)); ra = cat8(lo, hi);
    MQ(1, A[8], rb); DS(lo = RLO(trq, 1, 1));
    KW(2, 0); DS(hi = RHI(trq, 1, 1)); rb = cat8(lo, hi);
    QW(0, 1);
    MQ(2, A[8], rc); DS(lo = RLO(trq, 2, 1));
    QW(1, 1); DS(hi = RHI(trq, 2, 1)); rc = cat8(lo, hi);
    QW(2, 1);
    MQ(0, A[9], ra); DS(lo = RLO(trk, 0, 0));
    KW(0, 1); DS(hi = RHI(trk, 0, 0)); ra = cat8(lo, hi);
    MQ(1, A[9], rb); DS(lo = XLO(0));
    KW(1, 1); DS(hi = XHI(0)); rb = cat8(lo, hi);
    QW(0, 2);
    MQ(2, A[9], rc); DS(lo = XLO(1));
    QW(1, 2); DS(hi = XHI(1)); rc = cat8(lo, hi);
    QW(2, 2);
    MK(0, A[8], ra); DS(lo = RLO(trk, 1, 0));
    QW(0, 7); DS(hi = RHI(trk, 1, 0)); ra = cat8(lo, hi);
    MQ(1, A[7], rb); DS(lo = RLO(trk, 0, 1));
    KW(0, 2); DS(hi = RHI(trk, 0, 1)); rb = cat8(lo, hi);
    QW(0, 3);
    MQ(2, A[7], rc); DS(lo = RLO(trk, 2, 0));
    QW(1, 3); DS(hi = RHI(trk, 2, 0)); rc = cat8(lo, hi);
    QW(2, 3); KXR(0);
    QW(0, 4);
    MK(1, A[8], ra); DS(lo = RLO(trk, 1, 1));
    QW(1, 4); DS(hi = RHI(trk, 1, 1)); ra = cat8(lo, hi);
    QW(2, 4); KXR(1);
    QW(0, 5);
    MK(0, A[9], rb); DS(lo = RLO(trk, 2, 1));
    QW(1, 5); DS(hi = RHI(trk, 2, 1)); rb = cat8(lo, hi);
    QW(2, 5); KXR(2);
    QW(0, 6);
    QW(1, 6); QW(2, 6); LGW(KIND == 0 ? 1 : 0);
    KW(0, 3); KXR(3); ADD1(ta, aq[0][0], aq[0][2]);
    KW(0, 4); KXR(4); ADD1B(tb_, aq[0][1], aq[0][3]);
    KW(0, 5); CVTN(w0, nanm, ta, tb_);
    KW(0, 6); DS(LW32(ia, 0, w0)); ADD1(ta, aq[1][0], aq[1][2]);
    KW(0, 7); KXR(5); ADD1B(tb_, aq[1][1], aq[1][3]);
    KW(1, 2);
    if (__builtin_expect(nanm != 0ull, 0)) {
      unsigned m = ~0u;
#define S_BACK() asm volatile("v_cmp_eq_u32 vcc, 0, %0\n s_cmp_lt_u32 %1, %3\n s_cselect_b64 vcc, vcc, 0\n s_cbranch_vccnz .Lsbad%c2" : "+v"(m) : "s"(tries), "n"(wave), "n"(CH_SPIN_CAP) : "vcc", "scc")
#pragma unroll
      for (int k = 0; k < 10; k += 2) m = sent_min(m, A[k].x, A[k + 1].x);
      S_BACK();
#pragma unroll
      for (int k = 0; k < 10; ++k) m = sent_min(m, A[k]);
      S_BACK();
    }
    asm volatile("" :: "s"(sx[0]), "s"(sx[1]), "s"(sx[2]), "s"(sx[3]), "s"(sx[4]), "s"(tries), "s"(tss), "s"(snp), "s"(ts_desc), "v"(fad), "v"(snv));
    if (KIND == 0) PINU(kxa);
    CVT1(w0, ta, tb_);
    KW(1, 3); DS(LW32(ia, 1536, w0)); ADD1(ta, aq[2][0], aq[2][2]);
    KW(1, 4); if (KIND == 2) { asm volatile("buffer_load_dword %0, %1, %2, %3 offen sc1" : "=v"(tsl) : "v"(tsa), "s"(tsr_desc), "s"(tss) : "memory"); SB(); } ADD1B(tb_, aq[2][1], aq[2][3]);
    KW(1, 5); CVT1(w0, ta, tb_);
    KW(1, 6); DS(LW32(ia, 3072, w0)); ADD1(ta, ak[0][0], ak[0][2]);
    KW(1, 7); ADD1B(tb_, ak[0][1], ak[0][3]);
    MK(1, A[9], ra); CVT1(w0, ta, tb_);
    KW(2, 1); DS(LW32(ib, 0, w0));
    u32x2 g[6];
#define QAR(i_) do { if (KIND != 2) DS(g[i_] = trrd(lds, rda + (unsigned)(((i_) >> 1) * 1536 + ((i_) & 1) * 128))); } while (0)
#define QA(t_) ((u32x4){g[2 * (t_)].x, g[2 * (t_)].y, g[2 * (t_) + 1].x, g[2 * (t_) + 1].y})
    KW(2, 2); LBAR(1); QAR(0);
    KW(2, 3); QAR(1); ADD1(ta, ak[1][0], ak[1][2]);
    KW(2, 4); QAR(2); ADD1B(tb_, ak[1][1], ak[1][3]);
    KW(2, 5); QAR(3); CVT1(w0, ta, tb_);
    KW(2, 6); DS(LW32(ib, 1024, w0));
    KW(2, 7); QAR(4); QAR(5);
    MK(2, A[8], rc);
    MK(2, A[9], rb);
    if (KIND == 0) {
      float la, lb;
      { const u32x4 q_ = QA(0); const u32x8 k_ = cat8(kx[0], kx[1]); SMFV(sc, q_, k_); } LBAR(2);
      { const u32x4 q_ = QA(1); const u32x8 k_ = cat8(kx[2], kx[3]); SMFV(sc, q_, k_); }
      { const u32x4 q_ = QA(2); const u32x8 k_ = cat8(kx[4], kx[5]); SMFV(sc, q_, k_); }
      EPI3(w0, ak[2]); DS(LW32(ib, 2048, w0));
      asm volatile("s_nop 3\n v_add_f32 %0, %2, %3\n v_add_f32 %1, %4, %5" : "=&v"(la), "=v"(lb) : "v"(sc[0]), "v"(sc[2]), "v"(sc[1]), "v"(sc[3])); SB();
      DS(LW32(xva, 0, __float_as_uint(la)); LW32(xva, 128, __float_as_uint(lb)));
      LBAR(1); LBAR(0);
    } else if (KIND == 2) {
      LBAR(0);
      asm volatile("s_nop 5"); SB();
      EPI3(w0, ak[2]); DS(LW32(ib, 2048, w0));
      LBAR(0); LBAR(0);
      asm volatile("s_waitcnt vmcnt(0)" : "+v"(tsl) :: "memory");
      tsl = ts_max(tsl);
      calm = s > 1 && tsl != ~0u && (int)(tsl - tpr) <= a.d[10];
      if (calm && s < 63) __builtin_amdgcn_raw_buffer_store_b32(min(tsl + (unsigned)dly + dur, ~2u), rs_ts, tso, tsn, 16);
    } else {
      u32x2 kb[12];
#define KBR(i_) DS(kb[i_] = trrd(lds, qk_rd + (unsigned)((i_) * 256)))
#define ML(t_) do { const u32x4 a_ = QA(t_); \
        const u32x8 b_ = {kb[4 * (t_)].x, kb[4 * (t_)].y, kb[4 * (t_) + 1].x, kb[4 * (t_) + 1].y, kb[4 * (t_) + 2].x, kb[4 * (t_) + 2].y, kb[4 * (t_) + 3].x, kb[4 * (t_) + 3].y}; SMFV(sc, a_, b_); } while (0)
      LBAR(2);
      KBR(0); KBR(1); KBR(2); KBR(3); KBR(4); KBR(5);
      EPI3(w0, ak[2]); DS(LW32(ib, 2048, w0));
      KBR(6); KBR(7);
      LBAR(2);
      KBR(8); KBR(9); KBR(10); KBR(11);
      ML(0);
      asm volatile("s_barrier" ::: "memory"); SB();
      f32x4 t; DS(t = *(const f32x4*)(lds + xva));
      ML(1); ML(2);
      float tl, nl, x, m, u, e, d;
      asm volatile("v_max3_f32 %4, %0, %1, %2\n v_max_f32 %4, %4, %3\n v_mul_f32 %5, %15, %4\n v_fma_f32 %0, %16, %0, %5\n v_fma_f32 %1, %16, %1, %5\n v_fma_f32 %2, %16, %2, %5\n v_fma_f32 %3, %16, %3, %5\n s_nop 0\n"
                   "v_add_f32_dpp %6, %11, %13 quad_perm:[0,1,2,3] row_mask:0xf bank_mask:0x3\n v_add_f32_dpp %6, %12, %14 quad_perm:[0,1,2,3] row_mask:0xf bank_mask:0xc\n v_max_f32 %7, %6, %4\n"
                   "v_exp_f32 %0, %0\n v_exp_f32 %1, %1\n v_max_f32_dpp %7, %7, %7 quad_perm:[1,0,3,2] row_mask:0xf bank_mask:0xf\n"
                   "v_exp_f32 %2, %2\n v_exp_f32 %3, %3\n v_max_f32_dpp %7, %7, %7 quad_perm:[2,3,0,1] row_mask:0xf bank_mask:0xf\n"
                   "v_add_f32 %8, %0, %1\n v_add_f32 %10, %2, %3\n v_max_f32_dpp %7, %7, %7 row_half_mirror row_mask:0xf bank_mask:0xf\n"
                   "v_mul_f32 %7, %15, %7\n v_sub_f32 %4, %7, %5\n v_fma_f32 %9, %16, %6, %7\n v_exp_f32 %4, %4\n v_exp_f32 %9, %9\n v_add_f32 %8, %8, %10\n v_fma_f32 %10, %4, %8, %9\n"
                   "v_mul_f32 %0, %0, %4\n v_mul_f32 %1, %1, %4\n v_add_f32_dpp %10, %10, %10 quad_perm:[1,0,3,2] row_mask:0xf bank_mask:0xf\n"
                   "v_mul_f32 %2, %2, %4\n v_mul_f32 %3, %3, %4\n v_add_f32_dpp %10, %10, %10 quad_perm:[2,3,0,1] row_mask:0xf bank_mask:0xf\n"
                   "v_mov_b32_dpp %6, %9 quad_perm:[1,0,3,2] row_mask:0xf bank_mask:0xf\n s_nop 0\n v_add_f32_dpp %10, %10, %10 row_half_mirror row_mask:0xf bank_mask:0xf\n v_rcp_f32 %10, %10\n s_nop 0\n"
                   "v_mul_f32 %0, %0, %10\n v_mul_f32 %1, %1, %10\n v_cvt_pk_bf16_f32 v0, %0, %1\n v_mul_f32 %2, %2, %10\n v_mul_f32 %3, %3, %10\n v_cvt_pk_bf16_f32 v1, %2, %3\n"
                   "buffer_store_dwordx2 v[0:1], %17, %19, %20 offen sc1\n v_mul_f32 %9, %9, %10\n v_mul_f32 %6, %6, %10\n v_cvt_pk_bf16_f32 %9, %9, %6\n buffer_store_dword %9, %18, %19, %20 offen sc1"
                   : "+v"(t[0]), "+v"(t[1]), "+v"(t[2]), "+v"(t[3]), "=&v"(tl), "=&v"(nl), "=&v"(x), "=&v"(m), "=&v"(u), "=&v"(e), "=&v"(d)
                   : "v"(sc[0]), "v"(sc[1]), "v"(sc[2]), "v"(sc[3]), "s"(-CS), "s"(CS), "v"(po), "v"(pm), "s"(p_desc), "s"(pss) : "v0", "v1", "memory");
      SB();
      { u64 t_; RT_MARK(t_); LW32(flr, 20, (unsigned)t_); }
    }
    BAR();
    if (KIND == 0 && s == 0) {
#pragma unroll
      for (int n = 0; n < 12; ++n) st16(mkrs(ws + (wave == 0 ? O_KF : O_QF)), xv + (unsigned)(n % 3 * 1024), KSLOT(0) + (unsigned)(h * 32768 + n / 6 * 16384 + n % 6 / 3 * 3072), *(const u32x4*)(lds + (wave == 0 ? L_KX : L_QX) + n * 1024 + xv));
    }
    if (KIND == 0 && s < 63) {
      nap_rt(KXL);
      if (tprog < (unsigned)(s + 1)) tprog = wait_t<4>(cnt + CNT(C_PT_ + h), 1, (unsigned)(s + 1), tmo);
      const char* kp = ksrc + KSLOT(s + 1); asm volatile("s_nop 4" : "+s"(kp));
      m0_set(lds_addr(lds) + (unsigned)(L_KX + ((s + 1) & 1) * 12288 + wave * 6144 + 2048));
      glds_x<-2048>(kp, xv); glds_x<-1024>(kp, xv); glds_x<0>(kp, xv); glds_x<1024>(kp, xv); glds_x<2048>(kp, xv); glds_x<3072>(kp, xv);
      if (tprog < (unsigned)(s + 2)) tprog = min_t(ld16(mkrs(cnt + CNT(C_PT_ + h)), 0u, 0u), 1);
    }
    if (KIND == 2 && s < 63) {
      const unsigned t2 = __builtin_amdgcn_raw_buffer_load_b32(rs_ts, tsa, tss, 16);
      const unsigned f7 = FLRD(7), f5 = FLRD(5);
      const unsigned tb2 = (unsigned)__builtin_amdgcn_readfirstlane(f5);
      adj = __builtin_amdgcn_readfirstlane(f7) == snp;
      const bool own = adj && was_calm;
      was_calm = calm && !adj && ts_max(t2) == tsl;
      if (__builtin_expect(!was_calm, 0)) {
        if (s < 2) __builtin_amdgcn_raw_buffer_store_b32(((unsigned)tb2 + (unsigned)a.d[11]) & ~1u, rs_ts, tso, tss, 16);
        tsl = ts_settle(rs_ts, tss, tsl, a.d[4]) + (unsigned)a.d[12];
        if (s > 0) __builtin_amdgcn_raw_buffer_store_b32(min(tsl + (unsigned)dly + dur, ~2u), rs_ts, tso, tsn, 16);
      }
      tnx = tsl + (unsigned)dly; tpr = tnx + dur;
      dur = min(dur + 1u, (unsigned)tb2 - tis); PINS(dur);
      LW32(flr, 24, dur + (unsigned)a.d[13]);
      ADAPT(dly, own);
    }
    if (KIND == 1) {
      const int ln = lane_id();
      const __amdgpu_buffer_rsrc_t rs_km = mkrs(ws + O_KM);
      if (s + PRE < 63) st16(rs_p, pfo, (unsigned)PSLOT(s + PRE), SENT4);
#pragma unroll
      for (int kk = 0; kk < 6; kk += 2) {
        const u32x4 v = trrd2(lds, qk_rd + kk * 512, 256u), v2 = trrd2(lds, qk_rd + (kk + 1) * 512, 256u);
        st16(rs_km, (unsigned)((kk + ((ln >> 3) & 1)) * 512 + (ln >> 4) * 128 + (ln & 7) * 16), MSLOT(s) + (unsigned)h * 3072u, hi8 ? v2 : v);
      }
      __builtin_amdgcn_raw_buffer_store_b32((unsigned)(s + 1), rs_ps, tso, 0, 16);
    }
  }
  VMW(0);
}
__device__ __forceinline__ void s_role(const Args& a, const u32x2 tag, unsigned char* lds, const int h, const int wave) {
  char* ws = a.ws; asm volatile("" : "+s"(ws));
  unsigned* const cnt = (unsigned*)(ws + O_CNT); unsigned* const tmo = cnt + CNT(C_TMO);
  const int lane = lane_id(), l15 = lane & 15, lq = lane >> 4;
  WReg W; u32x8 wx[3];
  unsigned tro[3];
  { TRO_INIT();
#pragma unroll
    for (int t = 0; t < 3; ++t) tro[t] = TRO(4 * t + wave); }
  SDMA_INIT();
  SXDMA_INIT();
  SXDMA(0, 0); SDMAQK(0); SDMAQK(1); SXDMA(1, 8192); SDMAQK(2);
  u32x2 pf;
  f32x4 pac[2][2][3];
#define kacc pac[1]
#define qacc pac[0]
  SFOR(i, 10)
    if (i < 2 || i == 3 || i == 5 || i == 7) VMW(26); else if (i < 8) VMW(24); else if (i == 8) VMW(12); else VMW(0);
    BAR();
    u32x8 res[3][2];
    SFOR(t, 3)
      const unsigned ad = lds_addr(lds) + tro[t] + (unsigned)SBUF(i);
      if (i < 8) { TRS0(ad, SFR(i & 1, t, i & 6)); if (i == 6 && t > 0) tr8(false, ad, res[t][0], wx[t]); else TRS1(ad, SFR(i & 1, t, (i & 6) + 1)); }
      else tr8(false, ad, res[t][0], res[t][1]);
    SEND;
    BAR();
    if (i == 2 || i == 4 || i == 6) SXDMA((i >> 1) + 1, (((i >> 1) + 1) & 1) * 8192);
    if (i + 3 < 10) SDMAQK(i + 3);
    if (i >= 8) {
#pragma unroll
      for (int t = 0; t < 3; ++t)
#pragma unroll
        for (int k = 0; k < 2; ++k) {
          unsigned char* dst = lds + SBUF(i) + wave * 12288 + t * 4096 + k * 2048 + lane * 16;
          *(u32x4*)dst = lo4(res[t][k]); *(u32x4*)(dst + 1024) = hi4(res[t][k]);
        }
    }
    if (i == 8) asm volatile("s_nop 4\n global_load_dwordx2 %0, %1, %2 sc1" : "=v"(pf) : "v"(0u), "s"(cnt + CNT(C_GO)) : "memory");
    if (i == 9) PINV(pf);
    if (i == 7) {
#pragma unroll
      for (int t = 1; t < 3; ++t) { unsigned char* dst = lds + L_XR + wave * 4096 + lane * 16 + (t - 1) * 2048; *(u32x4*)dst = lo4(wx[t]); *(u32x4*)(dst + 1024) = hi4(wx[t]); }
    }
    SB();
    SFOR(kk, 4)
      u32x4 xa[2];
#pragma unroll
      for (int rt = 0; rt < 2; ++rt) xa[rt] = *(const u32x4*)(lds + ((i >> 1) & 1) * 8192 + rt * 4096 + l15 * 256 + ((4 * kk + lq) ^ l15) * 16);
      SFOR(n, 6)
        constexpr int t = n >> 1, rt = n & 1;
        if (i >= 8) { const u32x4 w_ = HALF(res[t][kk >> 1], kk & 1); MFV(pac[i & 1][rt][t], w_, xa[rt], false); }
        else if (i == 6 && t > 0 && kk >= 2) { const u32x4 w_ = HALF(wx[t], kk & 1); MFV(pac[0][rt][t], w_, xa[rt], false); }
        else mf_w<true>(pac[i & 1][rt][t], xa[rt], 2 * SFR(i & 1, t, (i & 6) + (kk >> 1)) + (kk & 1), i < 2 && kk == 0, W);
      SEND;
    SEND;
    SB();
  SEND;
  unsigned pol;
  {
    asm volatile("s_nop 7\n s_nop 7" : "+v"(kacc[0][0]), "+v"(kacc[0][1]), "+v"(kacc[0][2]), "+v"(kacc[1][0]), "+v"(kacc[1][1]), "+v"(kacc[1][2]));
    pol = (unsigned)__builtin_amdgcn_readfirstlane(pf.x);
    if (__builtin_expect((pol & ~7u) != tag.x || (unsigned)__builtin_amdgcn_readfirstlane(pf.y) != tag.y, 0)) pol = wait_tag<8>(cnt, C_GO, tag);
    pol = uni(pol & 1u);
    BAR();
#pragma unroll
    for (int rt = 0; rt < 2; ++rt)
#pragma unroll
      for (int t = 0; t < 3; ++t) { const int ix = 4 * t + wave, o_ = rt * 6144 + (ix >> 1) * 1024;
        *(u32x2*)(lds + L_KX + o_ + lane * 16 + (ix & 1) * 8) = (u32x2){cvtpk(kacc[rt][t][0], kacc[rt][t][1]), cvtpk(kacc[rt][t][2], kacc[rt][t][3])};
        *(u32x2*)(lds + L_QX + o_ + ((ix >> 1) % 3 == 2 ? lane * 8 + (ix & 1) * 512 : lane * 16 + (ix & 1) * 8)) = (u32x2){cvtpk(qacc[rt][t][0], qacc[rt][t][1]), cvtpk(qacc[rt][t][2], qacc[rt][t][3])}; }
    if (wave == 2) { if (lane < 8) *(unsigned*)(lds + L_FLG + 4 * lane) = 0u; }
    BAR();
  }
  if (wave == 0) s_loop<0>(a, lds, h, pol, W); else if (wave == 1) s_loop<1>(a, lds, h, pol, W); else if (wave == 2) s_loop<2>(a, lds, h, pol, W); else s_loop<3>(a, lds, h, pol, W);
}
#undef kacc
#undef qacc

__device__ __forceinline__ void z_role(const Args& a, const u32x2 tag, unsigned char* lds, const int h, const int wave) {
  char* ws = a.ws; asm volatile("" : "+s"(ws));
  unsigned* const cnt = (unsigned*)(ws + O_CNT); unsigned* const tmo = cnt + CNT(C_TMO);
  char* const X1 = ws + O_X1;
#define ZFV(t_, k_) (8 * (t_) + (k_))
#define ZFO(n_, j_) ((n_) < 6 ? 24 + 3 * (n_) + (j_) : 30 + 2 * (n_) + ((j_) >> 1))
  WReg W;
  const __amdgpu_buffer_rsrc_t rs_u = mkrs(ws + O_UO + h * 1024);
  unsigned xv = (unsigned)lane_id() * 16u; PINV(xv);
  unsigned pkv, pk3, pk4; u32x2 pf;
  {
    const int lane = lane_id(), l15 = lane & 15, lq = lane >> 4;
    unsigned tro[3];
    { TRO_INIT();
#pragma unroll
      for (int t = 0; t < 3; ++t) tro[t] = TRO(4 * t + wave); }
    SDMA_INIT(); SXDMA_INIT(); ODMA_INIT();
    SDMA(a.Wv, 0, 0); SXDMA(0, 0); SDMA(a.Wv, 1, 1); SXDMA(1, 8192); SDMA(a.Wv, 2, 2);
    f32x4 kacc[2][3];
    u32x8 wl[3][2];
    SFOR(i, 5)
      if (i < 4) VMW(26); else VMW(24);
      BAR();
      u32x4 xa[2][4];
      SXRD((i & 1) * 8192);
      SFOR(t, 3)
        if (i < 4) { TRS0(lds_addr(lds) + tro[t] + (unsigned)SBUF(i), ZFV(t, 2 * (i & 3))); TRS1(lds_addr(lds) + tro[t] + (unsigned)SBUF(i), ZFV(t, 2 * (i & 3) + 1)); }
        else tr8(false, lds_addr(lds) + tro[t] + (unsigned)SBUF(i), wl[t][0], wl[t][1]);
      SEND;
#pragma unroll
      for (int n = 0; n < 8; ++n) PINV(xa[n >> 2][n & 3]);
      BAR();
      if (i + 2 < 5) SXDMA(i + 2, (i & 1) * 8192);
      if (i + 3 < 5) SDMA(a.Wv, i + 3, i + 3); else ODMA(i - 2, i + 3);
      SFOR(n, 24)
        constexpr int kk = n / 6, t = (n % 6) >> 1, rt = n & 1;
        if (i < 4) mf_w<true>(kacc[rt][t], xa[rt][kk], 2 * ZFV(t, 2 * (i & 3) + (kk >> 1)) + (kk & 1), i == 0 && kk == 0, W);
        else { const u32x4 w_ = HALF(wl[t][kk >> 1], kk & 1); MFV(kacc[rt][t], w_, xa[rt][kk], false); }
      SEND;
      SB();
    SEND;
    u32x4 vx[2][6];
    asm volatile("s_nop 7\n s_nop 7" : "+v"(kacc[0][0]), "+v"(kacc[0][1]), "+v"(kacc[0][2]), "+v"(kacc[1][0]), "+v"(kacc[1][1]), "+v"(kacc[1][2]));
#pragma unroll
    for (int rt = 0; rt < 2; ++rt)
#pragma unroll
      for (int t = 0; t < 3; ++t)
        *(u32x2*)(lds + rt * 6144 + (2 * t + (wave >> 1)) * 1024 + xv + (wave & 1) * 8) = (u32x2){cvtpk(kacc[rt][t][0], kacc[rt][t][1]), cvtpk(kacc[rt][t][2], kacc[rt][t][3])};
    BAR();
#pragma unroll
    for (int n = 0; n < 12; ++n) { vx[n / 6][n % 6] = *(const u32x4*)(lds + n * 1024 + xv); PINV(vx[n / 6][n % 6]); }
    SFOR(i5, 5)
      constexpr int i = i5 + 5;
      if (i == 5) VMW(24); else if (i == 6) VMW(26); else if (i == 7) VMW(28); else if (i == 8) VMW(18); else VMW(6);
      BAR();
      u32x8 res[2][3];
      SFOR(t, 2)
        const unsigned ad = lds_addr(lds) + otr[t] + (unsigned)SBUF(i);
        constexpr int n = 2 * i5 + t;
        if (n < 8) TRO4(ad, 0, ZFO(n & 7, 0)); else tr4<0>(false, ad, res[t][0]);
        if (n < 6) TRO4(ad, 1, ZFO(n & 7, 1)); else tr4<1>(false, ad, res[t][1]);
        if (n < 8) TRO4(ad, 2, ZFO(n & 7, 2)); else tr4<2>(false, ad, res[t][2]);
      SEND;
      BAR();
      if (i + 3 < 10) ODMA(i - 2, i + 3);
      if (i == 9) asm volatile("s_nop 4\n global_load_dwordx2 %0, %1, %2 sc1" : "=v"(pf) : "v"(0u), "s"(cnt + CNT(C_GO)) : "memory");
      f32x4 ua[2][2];
      SFOR(m, 24)
        constexpr int e = m >> 2, t = (m >> 1) & 1, rt = m & 1, n = 2 * i5 + t;
        if (n >= 8 || (n >= 6 && (e >> 1) == 1)) { const u32x4 w_ = HALF(res[t][e >> 1], e & 1); MFV(ua[t][rt], vx[rt][e], w_, e == 0); }
        else mf_w<false>(ua[t][rt], vx[rt][e], 2 * ZFO(n & 7, e >> 1) + (e & 1), e == 0, W);
      SEND;
      asm volatile("s_nop 7" : "+v"(ua[0][0]), "+v"(ua[0][1]), "+v"(ua[1][0]), "+v"(ua[1][1]));
#pragma unroll
      for (int t = 0; t < 2; ++t)
        st16(rs_u, xv, u_off(0u, (unsigned)(8 * OROT(i5) + 2 * wave + t)), (u32x4){cvtpk(ua[t][0][0], ua[t][0][1]), cvtpk(ua[t][0][2], ua[t][0][3]), cvtpk(ua[t][1][0], ua[t][1][1]), cvtpk(ua[t][1][2], ua[t][1][3])});
      SB();
      if (i == 7) {
        PINV(xv); pkv = xv + (unsigned)(L_B1 + wave * 12288);
#pragma unroll
        for (int n = 0; n < 6; ++n) { *(u32x4*)(lds + pkv + n * 2048) = lo4(wl[n >> 1][n & 1]); *(u32x4*)(lds + pkv + n * 2048 + 1024) = hi4(wl[n >> 1][n & 1]); }
      }
      if (i == 8) {
        PINV(xv); pk3 = xv + (unsigned)(L_B2 + wave * 12288);
#pragma unroll
        for (int t = 0; t < 2; ++t) { *(u32x4*)(lds + pk3 + t * 2048) = lo4(res[t][1]); *(u32x4*)(lds + pk3 + t * 2048 + 1024) = hi4(res[t][1]); }
      }
      if (i == 9) {
        PINV(xv); pk4 = xv + (unsigned)(L_B0 + wave * 12288);
#pragma unroll
        for (int n = 0; n < 6; ++n) { *(u32x4*)(lds + pk4 + n * 2048) = lo4(res[n & 1][n >> 1]); *(u32x4*)(lds + pk4 + n * 2048 + 1024) = hi4(res[n & 1][n >> 1]); }
      }
      SB();
    SEND;
  }
  const int lane = lane_id(), l15 = lane & 15, lq = lane >> 4;
  constexpr int L_VA = 10240;
  unsigned pol;
  {
    VMW(0); PINV(pf);
    pol = (unsigned)__builtin_amdgcn_readfirstlane(pf.x);
    if (__builtin_expect((pol & ~7u) != tag.x || (unsigned)__builtin_amdgcn_readfirstlane(pf.y) != tag.y, 0)) pol = wait_tag<8>(cnt, C_GO, tag);
    pol = uni(pol & 1u);
    __builtin_amdgcn_raw_buffer_store_b32(1u, mkrs(cnt + CNT(C_PZ + h)), lane == 0 ? 4 * wave : (int)0x80000000u, 0, 16);
  }
  const __amdgpu_buffer_rsrc_t rs_x1 = mkrs(X1), rs_z = mkrs(ws + O_ZB), rs_ts = mkrs(ws + O_TS + half_of(ws + O_TS, 0u, 0u, pol));
  int ix = (l15 & 2) ? 0xEEEE : 0x4444; PINV(ix);
  float one = 1.0f; PINS(one);
  unsigned ia = (unsigned)(L_VA + wave * 384 + ((l15 >> 2) & 1) * 128 + (l15 >> 3) * 64 + (l15 & 1) * 32 + lq * 8 + ((l15 >> 1) & 1) * 4);
  unsigned rda = (unsigned)(L_VA + (lq >> 1) * 768 + (l15 >> 3) * 384 + (lq & 1) * 64 + ((l15 >> 2) & 1) * 32 + (l15 & 3) * 8);
  unsigned zvo = (unsigned)((2 * wave + (lq >> 1)) * 16384 + h * 256 + l15 * 16 + (lq & 1) * 8);
  unsigned evo = (unsigned)((2 * wave + (lq & 1) + 8 * (lq >> 1)) * 16384 + h * 256 + l15 * 16);
  unsigned gvo = xv + (unsigned)(wave * 2048), gv2 = xv + 16384u + (unsigned)((wave >> 1) * 1024);
  u64 hmask = 0xffffffffull << (32 * (wave & 1)); PINS(hmask);
  const unsigned gm0 = lds_addr(lds) + (unsigned)(L_MEM + ((wave - h) & 3) * 2048), gm2 = lds_addr(lds) + (unsigned)(L_MEM + 8192 + (wave >> 1) * 1024);
  PINV(ia); PINV(rda); PINV(zvo); PINV(gvo); PINV(gv2); PINV(pkv); PINV(pk3); PINV(pk4);
  bool was_calm = false;
  unsigned tsv = ~0u, tries = 0; int dly = a.d[1]; PINS(dly);
#define PLO(b_, n_) (*(const u32x4*)(lds + (b_) + (n_) * 2048))
#define PHI(b_, n_) (*(const u32x4*)(lds + (b_) + (n_) * 2048 + 1024))
#define MV(t_, f_, w_) SMFV(av[t_], f_, w_)
#define VW(t_, k_) SMFW(av[t_], A[k_], ZFV(t_, k_))
#define MO(n_, j_, w_) SMFV(az[n_], va[j_], w_)
#define OW(n_, j_) SMFW(az[n_], va[j_], ZFO(n_, j_))
#define AFR(k_) (*(const u32x4*)(lds + afb + (k_) * 1024))
  unsigned afb = xv + L_MEM; PINV(afb);
#define G_LOAD() do { asm volatile("s_mov_b32 m0, %3\n s_nop 0\n global_load_lds_dwordx4 %0, %4 sc1\n global_load_lds_dwordx4 %0, %4 offset:1024 sc1\n s_mov_b32 m0, %2\n s_mov_b64 exec, %5\n global_load_lds_dwordx4 %1, %4 sc1\n s_mov_b64 exec, -1\n s_waitcnt vmcnt(2)\n s_barrier" \
      :: "v"(gvo), "v"(gv2), "s"(gm2), "s"(gm0), "s"(xs), "s"(hmask) : "memory"); SB(); } while (0)
  u32x4 ts_desc = mkdesc(ws + O_TS + half_of(ws + O_TS, 0u, 0u, pol) + 16384 + 256 * h);
  int w3m = wave, dzs = a.d[9]; PINS(w3m); PINS(dzs); PINS(ts_desc);
  u32x4 z_desc = mkdesc(ws + O_ZB); PINS(z_desc);
#pragma unroll 1
  for (int s = 0; s < 64; ++s) {
    f32x4 av[3], az[10];
#pragma unroll
    for (int n = 0; n < 10; ++n) { az[n] = ZERO4; PINV(az[n]); }
    unsigned snp = (unsigned)(s + 1) * 0x01010101u, snv = snp, adj = 0; PINS(snp); PINV(snv);
    const unsigned so = half_of(X1, XSLOT(s), 0u, pol);
    const char* xs = X1 + so; int tss = TS_STEP(s); PINS(xs); PINS(tss);
    unsigned zs[5], tnx = (unsigned)__builtin_amdgcn_readfirstlane(wave == 3 && tsv != ~0u ? tsv + (unsigned)dly : 0u); PINS(tnx);
    { const unsigned z0 = half_of(ws + O_ZB, ZSLOT(s), 0u, pol);
#pragma unroll
      for (int i = 0; i < 5; ++i) { zs[i] = z0 + (unsigned)OROT(i) * (8u * 16384u); PINS(zs[i]); } }
    if (s == 1 && wave == 3) x1_light(rs_x1, so, tmo);
    SB();
    asm volatile("s_branch .Lzre\n"
                 ".Lzbad:\n s_waitcnt lgkmcnt(0)\n s_add_u32 %0, %0, 1\n s_add_u32 %1, %1, 1\n s_cmp_lt_u32 %0, 4\n s_cbranch_scc1 .Lznow\n s_sleep 8\n"
                 ".Lznow:\n s_cmp_lg_u32 %6, 3\n s_cbranch_scc1 .Lzre\n s_memrealtime vcc\n s_waitcnt lgkmcnt(0)\n s_add_u32 vcc_lo, vcc_lo, %7\n s_and_b32 vcc_lo, vcc_lo, -2\n v_mov_b32 %2, vcc_lo\n"
                 " s_mov_b64 exec, 1\n buffer_store_dword %2, off, %4, %5 sc1\n s_mov_b64 exec, -1\n v_mov_b32 %2, %3\n"
                 ".Lzre:" : "+s"(adj), "+s"(tries) : "v"(snv), "s"(snp), "s"(ts_desc), "s"(tss), "s"(w3m), "s"(dzs) : "vcc", "scc", "memory");
    SB();
    u32x8 ra = cat8(PLO(pkv, 0), PHI(pkv, 0)), rb = cat8(PLO(pkv, 1), PHI(pkv, 1));
    av[0] = ZERO4; av[1] = ZERO4; av[2] = ZERO4;
    asm volatile("" : "+v"(av[0]), "+v"(av[1]), "+v"(av[2]));
    SB();
    WAIT_GO(tnx);
    BAR();
    G_LOAD();
    u32x4 A[10], lo, hi, va[3];
    unsigned w0, w1;
    float ta, tb_;
    u64 nanm;
    DS(A[0] = AFR(0)); DS(A[2] = AFR(2)); DS(A[4] = AFR(4)); DS(A[6] = AFR(6));
    VW(0, 0); VW(1, 0); asm volatile("s_waitcnt vmcnt(0)\n s_barrier" ::: "memory"); SB();
    VW(2, 0); DS(A[8] = AFR(8));
    VW(0, 2); DS(A[9] = AFR(9));
    VW(1, 2); DS(A[1] = AFR(1));
    VW(2, 2); DS(A[3] = AFR(3));
    VW(0, 4); DS(A[5] = AFR(5));
    VW(0, 6); DS(A[7] = AFR(7));
    MV(0, A[8], ra); DS(lo = PLO(pkv, 2));
    MV(0, A[9], rb); DS(hi = PHI(pkv, 2)); ra = cat8(lo, hi);
    VW(0, 1); DS(lo = PLO(pkv, 3));
    VW(0, 3); DS(hi = PHI(pkv, 3)); rb = cat8(lo, hi);
    VW(0, 5); VW(0, 7);
    VW(1, 4); VW(1, 6);
    MV(1, A[8], ra); DS(lo = PLO(pkv, 4)); ADD1(ta, av[0][0], av[0][2]);
    VW(1, 1); DS(hi = PHI(pkv, 4)); ra = cat8(lo, hi); ADD1B(tb_, av[0][1], av[0][3]);
    VW(1, 3); LGW(2); CVT1(w0, ta, tb_);
    MV(1, A[9], rb); DS(LW32(ia, 0, w0));
    VW(1, 5); DS(lo = PLO(pkv, 5)); NANM(nanm, ta, tb_);
    VW(1, 7); DS(hi = PHI(pkv, 5)); rb = cat8(lo, hi);
    VW(2, 4);
    if (__builtin_expect(nanm != 0ull, 0)) {
      unsigned m = ~0u;
#define Z_BACK() asm volatile("v_cmp_eq_u32 vcc, 0, %0\n s_cmp_lt_u32 %1, %2\n s_cselect_b64 vcc, vcc, 0\n s_cbranch_vccnz .Lzbad" : "+v"(m) : "s"(tries), "n"(CH_SPIN_CAP) : "vcc", "scc")
#pragma unroll
      for (int k = 0; k < 10; k += 2) m = sent_min(m, A[k].x, A[k + 1].x);
      Z_BACK();
#pragma unroll
      for (int k = 0; k < 10; ++k) m = sent_min(m, A[k]);
      Z_BACK();
    }
    asm volatile("" :: "s"(xs), "s"(tnx), "s"(tries), "s"(tss), "s"(snp), "s"(ts_desc), "s"(w3m), "s"(dzs), "v"(snv));
    PINS(adj);
    u32x2 g0, g1;
#define VR0(j_) DS(g0 = trrd(lds, rda + (unsigned)((j_) * 1536)))
#define VR1(j_) do { DS(g1 = trrd(lds, rda + (unsigned)((j_) * 1536 + 128))); va[j_] = (u32x4){g0.x, g0.y, g1.x, g1.y}; } while (0)
    VW(2, 6); LBAR(2);
    MV(2, A[8], ra); VR0(0); ADD1(ta, av[1][0], av[1][2]);
    VW(2, 1); VR1(0); ADD1B(tb_, av[1][1], av[1][3]);
    VW(2, 3); DS(lo = PLO(pk4, 0)); CVT1(w0, ta, tb_);
    VW(2, 5); DS(LW32(ia, 1536, w0));
    MV(2, A[9], rb); DS(hi = PHI(pk4, 0)); ra = cat8(lo, hi);
    VW(2, 7); DS(lo = PLO(pk4, 1));
    OW(0, 0); DS(hi = PHI(pk4, 1)); rb = cat8(lo, hi);
    OW(1, 0); LBAR(3);
    OW(2, 0); VR0(1); ADD1(ta, av[2][0], av[2][2]);
    OW(3, 0); VR1(1); ADD1B(tb_, av[2][1], av[2][3]);
    OW(4, 0); LGW(2); CVT1(w0, ta, tb_);
    MO(8, 0, ra); DS(LW32(ia, 3072, w0));
    MO(9, 0, rb); DS(lo = PLO(pk3, 0));
    OW(5, 0); DS(hi = PHI(pk3, 0)); ra = cat8(lo, hi);
    OW(6, 0); DS(lo = PLO(pk3, 1));
    OW(7, 0); LGW(3); MEMB(); __builtin_amdgcn_s_barrier(); MEMB(); SB();
    OW(0, 1); VR0(2); VR1(2);
    OW(1, 1); DS(hi = PHI(pk3, 1)); rb = cat8(lo, hi);
    OW(2, 1);
    MO(6, 1, ra); DS(lo = PLO(pk4, 2));
    OW(3, 1); DS(hi = PHI(pk4, 2)); ra = cat8(lo, hi);
#define ZSW() do { asm volatile("v_permlane32_swap_b32 %0, %1" : "+v"(w0), "+v"(w1)); SB(); } while (0)
#define ZST(i_) do { const u32x2 w_ = {w0, w1}; asm volatile("buffer_store_dwordx2 %0, %1, %2, %3 offen sc1" :: "v"(w_), "v"(zvo), "s"(z_desc), "s"(zs[i_]) : "memory"); SB(); } while (0)
    OW(0, 2);
    OW(1, 2);
    MO(7, 1, rb); DS(lo = PLO(pk4, 3));
    OW(2, 2); DS(hi = PHI(pk4, 3)); rb = cat8(lo, hi); ADD2(ta, tb_, az[0]);
    OW(3, 2); LGW(2); CVT1(w0, ta, tb_); ADD1(ta, az[1][0], az[1][2]);
    MO(8, 1, ra); DS(lo = PLO(pk4, 4)); ADDCVT(w1, ta, az[1]);
    OW(6, 2); DS(hi = PHI(pk4, 4)); ra = cat8(lo, hi); ZSW();
    OW(7, 2); ZST(0); ADD2(ta, tb_, az[2]);
    OW(4, 1); LGW(2); CVT1(w0, ta, tb_); ADD1(ta, az[3][0], az[3][2]);
    MO(9, 1, rb); DS(lo = PLO(pk4, 5)); ADDCVT(w1, ta, az[3]);
    OW(5, 1); DS(hi = PHI(pk4, 5)); rb = cat8(lo, hi); ZSW();
    OW(4, 2); ZST(1); ADD2(ta, tb_, az[6]);
    OW(5, 2); LGW(2); CVT1(w0, ta, tb_); ADD1(ta, az[7][0], az[7][2]);
    MO(8, 2, ra); LGW(0); ADDCVT(w1, ta, az[7]);
    MO(9, 2, rb);
    asm volatile("v_add_f32 v2, %2, %4\n v_permlane32_swap_b32 %0, %1\n v_add_f32 v0, %3, %5\n v_cvt_pk_bf16_f32 v0, v2, v0\n v_add_f32 v2, %6, %8\n v_add_f32 v1, %7, %9\n v_cvt_pk_bf16_f32 v1, v2, v1"
                 : "+v"(w0), "+v"(w1) : "v"(az[4][0]), "v"(az[4][1]), "v"(az[4][2]), "v"(az[4][3]), "v"(az[5][0]), "v"(az[5][1]), "v"(az[5][2]), "v"(az[5][3]) : "v0", "v1", "v2", "v3");
    { const u32x2 w_ = {w0, w1};
      asm volatile("buffer_store_dwordx2 %0, %1, %2, %3 offen sc1\n v_add_f32 v3, %6, %8\n v_permlane32_swap_b32 v0, v1\n v_add_f32 v2, %7, %9\n buffer_store_dwordx2 v[0:1], %1, %2, %4 offen sc1\n v_cvt_pk_bf16_f32 v2, v3, v2\n"
                   "v_add_f32 %10, %10, %12\n v_add_f32 v3, %11, %13\n v_cvt_pk_bf16_f32 v3, %10, v3\n s_nop 1\n v_permlane32_swap_b32 v2, v3\n buffer_store_dwordx2 v[2:3], %1, %2, %5 offen sc1"
                   :: "v"(w_), "v"(zvo), "s"(z_desc), "s"(zs[3]), "s"(zs[2]), "s"(zs[4]), "v"(az[8][0]), "v"(az[8][1]), "v"(az[8][2]), "v"(az[8][3]), "v"(az[9][0]), "v"(az[9][1]), "v"(az[9][2]), "v"(az[9][3]) : "v0", "v1", "v2", "v3", "memory"); }
    SB();
    ADAPT(dly, adj && was_calm);
    if (s < 63) {
      if (s + PRE < 64) {
        const unsigned e0 = half_of(ws + O_ZB, ZSLOT((s + PRE)), 0u, pol);
        st16(rs_z, evo, e0, SENT4); st16(rs_z, evo, e0 + 16u * 16384u, SENT4); st16(rs_z, lane < 32 ? evo : 0x80000000u, e0 + 32u * 16384u, SENT4);
      }
      if (wave == 3) { tsv = ts_settle(rs_ts, tss, 0u, a.d[4]); was_calm = !(tsv != ~0u && (int)(tsv - tnx) > dzs + a.d[10]); if (!was_calm) tsv += (unsigned)a.d[12]; }
    }
  }
}

#define WCK 3
#define DIAG 0
#define UNAP 0
__device__ __forceinline__ void r_role(const Args& a, const u32x2 tag, unsigned char* lds, const int c, const int wave) {
  char* ws = a.ws; asm volatile("" : "+s"(ws));
  unsigned* const cnt = (unsigned*)(ws + O_CNT); unsigned* const tmo = cnt + CNT(C_TMO);
  char* const X1 = ws + O_X1; char* const P = ws + O_PS;
  const int lane = lane_id(), l15 = lane & 15, lq = lane >> 4;
  const unsigned pol = wait_tag<8>(cnt, C_POL, tag);
  const __amdgpu_buffer_rsrc_t rs_x1 = mkrs(X1 + (c >> 5) * 16384 + (c & 31) * 256), rs_p = mkrs(P + half_of(P, 0u, 0u, pol)), rs_z = mkrs(ws + O_ZB + (size_t)c * 16384);
  const __amdgpu_buffer_rsrc_t rs_ts = mkrs(ws + O_TS + half_of(ws + O_TS, 0u, 0u, pol));
  unsigned xv = (unsigned)lane * 16u; PINV(xv);
  {
    const u32x4 ones = {~0u, ~0u, ~0u, ~0u};
    static_assert(PRE == 4, "");
#pragma unroll
    for (int i = 0; i < 8; ++i) st16(rs_z, xv + (unsigned)((i & 3) * 1024), half_of(ws + O_ZB, ZSLOT(wave), 0u, pol) + (unsigned)((i >> 2) * 4096), SENT4);
    st16(rs_p, lane < 32 ? xv : 0x80000000u, (unsigned)(PSLOT(wave) + (c >> 4) * 16384 + (c & 15) * 512), SENT4);
    st16(rs_x1, lane < 16 ? xv : 0x80000000u, half_of(X1, XSLOT((wave + 1)), 0u, pol), SENT4);
    if (c < 4) st16(rs_ts, xv, (unsigned)((c >> 1) * 16384 + (c & 1) * 4096 + wave * 1024), ones);
    if (wave == 0) {
      const int q = lane >> 2, r = (q >> 2) * 2 + (q & 1);
      const unsigned v = *(const unsigned*)(a.mem0 + r * 640 + 16 * c + 4 * (lane & 3) + (q & 2));
      __builtin_amdgcn_raw_buffer_store_b32(v == SENT ? ~0u : v, rs_x1, lane * 4, (int)half_of(X1, 0u, 0u, pol), 16);
    }
    VMW(0);
    BAR();
    if (wave != 0) return;
    fill_done(cnt, tag, pol);
  }
  const u32x4 rs_u = mkdesc(ws + O_UO);
  const __amdgpu_buffer_rsrc_t rs_pw = mkrs(cnt + CNT(C_PW) + ((c >> 1) & 3));
  int ix = (l15 & 2) ? 0xEEEE : 0x4444; PINV(ix);
  unsigned pm = (unsigned)(lq * 256 + l15 * 8);
  int xo = (4 * lq + (l15 & 3)) * 16 + (l15 >> 2) * 4; unsigned sel = (l15 & 1) ? 0x07060302u : 0x01000504u;
  int tsa = (lane & 31) * 4;
  PINV(pm); PINV(xo); PINV(sel); PINV(tsa);
  WReg W;
#define U_LOAD(sg_, def_) SFOR(i, 16) ld_w(rs_u, xv, u_off((unsigned)(sg_), (unsigned)c) + (unsigned)((i >> 1) * 4096), (i & 1) * 2048, i, (def_) && (i & 3) == 0, W); nap4<UNAP>(); SEND
  wait_tag<8>(cnt, C_GO, tag);
  wait_heads<8>(cnt + CNT(C_PZ) + ((c >> 1) & 3), 0, 1u, tmo);
  unsigned wl = __builtin_amdgcn_raw_buffer_load_b32(rs_pw, tsa * 32, 0, 16), tsl = 0; int tso = TS_OFF(lane), two = tsa * 32; PINV(tso); PINV(two); PINV(wl);
  const u32x4 ts_desc = mkdesc(ws + O_TS + half_of(ws + O_TS, 0u, 0u, pol));
  U_LOAD(0, true);
  unsigned tsv = ~0u, tpub; int dlp = a.d[2]; PINS(dlp);
  { u64 t_; RT_MARK(t_); tpub = (unsigned)t_ - 4096u; }
#pragma unroll 1
  for (int s = 0; s < 63; ++s) {
    unsigned ps[5], zs[2], xs;
#pragma unroll
    for (int j = 0; j < 5; ++j) { ps[j] = (unsigned)PSLOT(s) + (unsigned)((j >> 1) * 16384 + (j & 1) * 4096); PINS(ps[j]); }
    zs[0] = half_of(ws + O_ZB, ZSLOT(s), 0u, pol); zs[1] = zs[0] + 4096u; xs = half_of(X1, XSLOT((s + 1)), 0u, pol); PINS(zs[0]); PINS(zs[1]); PINS(xs);
    int tss = TS_STEP(s); PINS(tss);
    asm volatile("s_waitcnt vmcnt(32)" : "+v"(tsl) :: "memory");
    tsv = s < 2 ? ~0u : ts_max(tsl);
    if (__builtin_expect(s >= 2 && (tsv == ~0u || (int)(tsv - tpub) < a.d[8]), 0)) { tsv = ts_settle(rs_ts, tss, tsv, a.d[4]); }
    int wo = __all(wl >= (unsigned)(s + 2)) ? (int)0x80000000u : two; PINV(wo);
    f32x4 o = ZERO4; PINV(o);
    unsigned tis = tsv + (unsigned)dlp; (void)tis;
#define R_LIGHT() do { const unsigned ln = (unsigned)lane_id(); light(mkrs(ws + O_ZB), ln < 32u ? zs[0] + (unsigned)c * 16384u + ln * 256u : (unsigned)(O_PS - O_ZB) + half_of(P, 0u, 0u, pol) + ps[4] + (ln - 32u) * 128u, 0u, tmo); } while (0)
    if (__builtin_expect(tsv == ~0u, 0)) { R_LIGHT(); }
    tis = WAIT_GO(tsv == ~0u ? tpub : tsv + (unsigned)dlp);
    unsigned adj = 0, pk, tsx, wn; bool own = false; u64 nanm;
    u32x4 z[8], p[16]; u32x2 m[8];
#define R_LOAD() do { PINV(xv); PINV(pm); \
      _Pragma("unroll") for (int i = 0; i < 8; ++i) DS(z[i] = ld16(rs_z, xv + (unsigned)((i & 3) * 1024), zs[i >> 2])); \
      _Pragma("unroll") for (int i = 0; i < 16; ++i) DS(p[i] = ld16(rs_p, xv + (unsigned)((i & 3) * 1024), ps[i >> 2])); \
      _Pragma("unroll") for (int i = 0; i < 8; ++i) DS(m[i] = ld8(rs_p, pm + (unsigned)((i >> 1) * 1024 + (i & 1) * 128), ps[4])); \
      DS(tsx = __builtin_amdgcn_raw_buffer_load_b32(rs_ts, tso, tss, 16));        \
      DS(wn = __builtin_amdgcn_raw_buffer_load_b32(rs_pw, wo, 0, 16)); } while (0)
#define R_PROD() do { \
      SFOR(i, 16) SMFW(o, p[i], i); SEND; \
      _Pragma("unroll") for (int i = 0; i < 4; ++i) { const u32x4 a_ = {m[2 * i].x, m[2 * i].y, m[2 * i + 1].x, m[2 * i + 1].y}; const u32x8 b_ = cat8(z[2 * i], z[2 * i + 1]); SMFV(o, a_, b_); } \
      float ta, tb_; unsigned q; \
      asm volatile("s_nop 7\n v_add_f32 %2, %5, %7\n v_add_f32 %3, %6, %8\n v_cvt_pk_bf16_f32 %0, %2, %3\n v_cmp_u_f32_e64 %1, %2, %3\n s_nop 0\n" \
                   "v_mov_b32_dpp %4, %0 quad_perm:[1,0,3,2] row_mask:0xf bank_mask:0xf\n v_perm_b32 %0, %0, %4, %9" \
                   : "=&v"(pk), "=&s"(nanm), "=&v"(ta), "=&v"(tb_), "=&v"(q) : "v"(o[0]), "v"(o[1]), "v"(o[2]), "v"(o[3]), "v"(sel)); } while (0)
    R_LOAD();
    R_PROD();
    if (__builtin_expect(nanm != 0ull, 0)) {
      own = tsv != ~0u && ts_max(tsx) == tsv;
      while (spin_check(adj, tmo)) {
        if (adj > 2u) R_LIGHT();
        else { const unsigned t_ = ts_max(tsx); if (t_ != ~0u) wait_rt(t_ + (unsigned)dlp); }
        R_LOAD();
        o = ZERO4; asm volatile("s_nop 1" : "+v"(o)); SB();
        R_PROD();
        if (nanm == 0ull) break;
        if ((adj & 7u) == 0u) {
          unsigned e = ~0u;
#pragma unroll
          for (int i = 0; i < 16; ++i) e = sent_min(e, p[i]);
#pragma unroll
          for (int i = 0; i < 8; ++i) e = sent_min(sent_min(e, z[i]), m[i].x, m[i].y);
          if (!__any(e == 0u)) break;
        }
      }
    }
    __builtin_amdgcn_raw_buffer_store_b32(pk, rs_x1, xo, (int)xs, 16);
    SB();
    PINU(tsx);
    { u64 t_; RT_MARK(t_); tpub = (unsigned)t_; }
    ADAPT(dlp, own);
    if (s + PRE + 1 < 64) st16(rs_x1, lane < 16 ? xv : 0x80000000u, half_of(X1, XSLOT((s + PRE + 1)), 0u, pol), SENT4);
    {
      wl = max(wl, wn);
      if (__builtin_expect(!__all(wl >= (unsigned)(s + 2)), 0)) { wait_heads<4>(cnt + CNT(C_PW) + ((c >> 1) & 3), 0, (unsigned)(s + 2), tmo); }
      wait_rt(tpub + (unsigned)a.d[3]);
      asm volatile("s_nop 4\n buffer_load_dword %0, %1, %2, %3 offen sc1" : "=v"(tsl) : "v"(tso), "s"(ts_desc), "s"(TS_STEP(s + 1)) : "memory");
      U_LOAD(s + 1, false);
    }
  }
  VMW(0);
}

__device__ __forceinline__ void w_role(const Args& a, const u32x2 tag, unsigned char* lds, const int h, const int wave) {
  char* ws = a.ws; asm volatile("" : "+s"(ws));
  unsigned* const cnt = (unsigned*)(ws + O_CNT); unsigned* const tmo = cnt + CNT(C_TMO);
  const int lane = lane_id(), l15 = lane & 15, lq = lane >> 4;
  WReg W;
  {
    ODMA_INIT();
    ODMA(0, 0); ODMA(1, 1); ODMA(2, 2);
#pragma unroll
    for (int i = 0; i < 5; ++i) {
      if (i < 3) VMW(24); else if (i == 3) VMW(12); else VMW(0);
      BAR();
#pragma unroll
      for (int t = 0; t < 2; ++t) {
        const unsigned ad = lds_addr(lds) + otr[t] + (unsigned)SBUF(i);
        TRO4(ad, 0, 6 * i + 3 * t); TRO4(ad, 1, 6 * i + 3 * t + 1); TRO4(ad, 2, 6 * i + 3 * t + 2);
      }
      if (i + 3 < 5) { BAR(); ODMA(i + 3, i + 3); }
    }
  }
  const __amdgpu_buffer_rsrc_t rs_u = mkrs(ws + O_UO + h * 1024);
  const __amdgpu_buffer_rsrc_t rs_pv = mkrs(cnt + CNT(C_PV + h)), rs_pw = mkrs(cnt + CNT(C_PW + h));
  unsigned xv = (unsigned)lane * 16u; int flo = lane == 0 ? 4 * wave : (int)0x80000000u; PINV(xv); PINV(flo);
  unsigned uo[10];
#pragma unroll
  for (int i = 0; i < 10; ++i) { uo[i] = (unsigned)(8 * OROT(i >> 1) + 2 * wave + (i & 1)) << 15; PINS(uo[i]); }
  const __amdgpu_buffer_rsrc_t rs_po = mkrs(cnt + CNT(C_PO)); int poo = wave == 0 ? (lane % 24) / 6 * 128 + (lane % 6) * 4 : (int)0x80000000u; PINV(poo);
  const unsigned pol = wait_tag<8>(cnt, C_POL, tag), kkey = K_KEY();
  const char* vsrc = ws + O_VF + (size_t)h * 32768 + (wave >> 1) * 16384 + (wave & 1) * 3072;
#define V_DMA(sg_) do { m0_set(lds_addr(lds) + (unsigned)(((sg_) % 3) * 12288 + wave * 3072)); const char* p_ = vsrc + KSLOT(sg_); asm volatile("s_nop 4" : "+s"(p_)); glds_x<0>(p_, xv); glds_x<1024>(p_, xv); glds_x<2048>(p_, xv); } while (0)
  if (TM == 0) touch_n<5>(a, 4 * h + wave, 0, 64);
  if (TM == 3) touch_n<4>(a, 4 * h + wave, 0, 96);
  BAR();
  wait_t<2>(cnt + CNT(C_PV + h), 1, 2u, tmo);
  V_DMA(1); V_DMA(2);
  VMW(3);
  int nxt = 3;
#pragma unroll 1
  for (int s = 1; s < 64; ++s) {
    unsigned ub = u_off((unsigned)s, 0u), sv = (unsigned)s, va = lds_addr(lds) + (unsigned)((s % 3) * 12288) + xv; PINS(ub); PINV(sv); PINV(va);
    f32x4 acc[2][2]; u32x4 vx[12], pw, w; unsigned sp; int vprog; bool late, far;
    BAR();
#pragma unroll
    for (int n = 0; n < 12; ++n) DS(vx[(n >> 1) + 6 * (n & 1)] = *(const LAS u32x4*)(size_t)(va + ((n >> 1) + 6 * (n & 1)) * 1024));
    SFOR(t, 11)
      SFOR(m, 12)
        constexpr int e = m >> 1, rt = m & 1, c = (t + 1) & 1;
        if (t < 10) { mf_w<false>(acc[t & 1][rt], vx[6 * rt + e], 6 * (t % 10) + e, e == 0, W); SB(); }
        else if (m == 0) { asm volatile("s_nop 7" : "+v"(acc[1][0]), "+v"(acc[1][1])); SB(); }
        if (t > 0 && m >= 2 && m < 6) { if (t < 10) FILL(); CVT1(w[(m + 2) & 3], acc[c][(m >> 1) - 1 & 1][2 * (m & 1)], acc[c][(m >> 1) - 1 & 1][2 * (m & 1) + 1]); }
        if (t > 0 && m == 6) { if (t < 10) __builtin_amdgcn_raw_buffer_store_b128(w, rs_u, (int)xv, (int)(ub + uo[(t + 9) % 10]), 16); else st16(rs_u, xv, ub + uo[9], w); SB(); }
        if (t == 0 && m == 1) { pw = ld16(rs_pv, 0u, 0u); SB(); }
        if (t == 0 && m == 4) { sp = __builtin_amdgcn_raw_buffer_load_b32(rs_po, poo, 0, 16); SB(); }
        if (t == WCK && m == 8) { vprog = (int)min_t(pw, 1); PINS(vprog); far = wave == 0 && __any((int)sp < s + 2 - WGATE); MEMB(); SB(); }
        if (t == WCK && m == 10) { __builtin_amdgcn_raw_buffer_store_b32(sv, rs_pw, flo, 0, 16); SB(); }
        if (t == WCK + 1 && m == 0) {
          late = nxt <= s + 1;
          const int upto = min(min(vprog, s + 2), 63);
          while (nxt <= upto) { V_DMA(nxt); ++nxt; }
          SB(); }
        if (t == 6 && m == 8) { pw = ld16(rs_pv, 0u, 0u); SB(); }
      SEND;
    SEND;
    if (__builtin_expect(nxt <= s + 1 || far, 0)) {
      const int upto = min(min((int)min_t(pw, 1), s + 2), 63);
      while (nxt <= upto) { V_DMA(nxt); ++nxt; }
      VMW(0); __builtin_amdgcn_raw_buffer_store_b32((unsigned)(s + 1), rs_pw, flo, 0, 16);
      if (s < 63) {
        if (far) { unsigned spins = 0; while (__any((int)__builtin_amdgcn_raw_buffer_load_b32(rs_po, poo, 0, 16) < s + 2 - WGATE) && spin_check(spins, tmo)) __builtin_amdgcn_s_sleep(24); MEMB(); }
        if (nxt <= s + 1) { wait_t<1>(cnt + CNT(C_PV + h), 1, (unsigned)(s + 1), tmo); V_DMA(nxt); ++nxt; VMW(0); }
      }
    } else if (late) asm volatile("s_waitcnt vmcnt(%0)" :: "n"(11 - WCK) : "memory");
  }
}

#define TPF 0
#define SAUX 16
#define TLAG 5
#define TGATE NK
#define TSTRICT 24
#define LDS16(ad_) (*(LAS u32x4*)(size_t)(ad_))
struct TReg { u32x32 b[12]; u32x16 e; };
#define TR_SW(g_, X) switch (g_) { case 0: X("a", "{a[0:31]}", T.b[0]); break; case 1: X("a", "{a[32:63]}", T.b[1]); break; case 2: X("a", "{a[64:95]}", T.b[2]); break; case 3: X("a", "{a[96:127]}", T.b[3]); break; \
    case 4: X("a", "{a[128:159]}", T.b[4]); break; case 5: X("a", "{a[160:191]}", T.b[5]); break; case 6: X("a", "{a[192:223]}", T.b[6]); break; case 7: X("a", "{a[224:255]}", T.b[7]); break; \
    case 8: X("v", "{v[110:141]}", T.b[8]); break; case 9: X("v", "{v[142:173]}", T.b[9]); break; case 10: X("v", "{v[174:205]}", T.b[10]); break; case 11: X("v", "{v[206:237]}", T.b[11]); break; default: X("v", "{v[238:253]}", T.e); break; }
__device__ __forceinline__ void mf_t(f32x4& c, const u32x4 x, const int d, const bool first, const TReg& T) {
  const int r = d < 64 ? 4 * d : 4 * d - 146;
#define X_(rf_, cs_, v_) if (first) asm volatile("v_mfma_f32_16x16x32_bf16 %0, " rf_ "[%c2:%c3], %1, 0" : "=&v"(c) : "v"(x), "n"(r), "n"(r + 3), cs_(v_)); \
      else asm volatile("v_mfma_f32_16x16x32_bf16 %0, " rf_ "[%c2:%c3], %1, %0" : "+v"(c) : "v"(x), "n"(r), "n"(r + 3), cs_(v_))
  TR_SW(d >> 3, X_);
#undef X_
}
__device__ __forceinline__ void tr_t(const unsigned ad0, const unsigned ad1, const int off, const int d, TReg& T) {
  const int r = d < 64 ? 4 * d : 4 * d - 146;
#define Y_(md_, rf_, cs_, v_) asm volatile("ds_read_b64_tr_b16 " rf_ "[%c5:%c6], %1 offset:%3\n ds_read_b64_tr_b16 " rf_ "[%c7:%c8], %1 offset:%4\n ds_read_b64_tr_b16 " rf_ "[%c9:%c10], %2 offset:%3\n ds_read_b64_tr_b16 " rf_ "[%c11:%c12], %2 offset:%4\n s_waitcnt lgkmcnt(0)" \
    : md_ cs_(v_) : "v"(ad0), "v"(ad1), "n"(off), "n"(off + 1536), "n"(r), "n"(r + 1), "n"(r + 2), "n"(r + 3), "n"(r + 4), "n"(r + 5), "n"(r + 6), "n"(r + 7) : "memory")
#define X_(rf_, cs_, v_) if ((d & 7) == 0) Y_("=&", rf_, cs_, v_); else Y_("+", rf_, cs_, v_)
  TR_SW(d >> 3, X_);
#undef X_
#undef Y_
}
__device__ __forceinline__ void t_role(const Args& a, const u32x2 tag, unsigned char* lds, const int h, const int isv, const int wave) {
  char* ws = a.ws; asm volatile("" : "+s"(ws));
  unsigned* const cnt = (unsigned*)(ws + O_CNT); unsigned* const tmo = cnt + CNT(C_TMO);
  const int lane = lane_id(), l15 = lane & 15, lq = lane >> 4;
  TReg T;
#define TBUF(j_) (24576u + (unsigned)(((j_) + 3) % 6) * 20480u)
#define TSRC(c_, r_) ((((c_) - (r_) * 80u < 64u ? (r_) * 80u + (((c_) - (r_) * 80u + 16u * (unsigned)h) & 63u) : (c_)) ^ ((r_) & 15u)) * 16u)
#define TDMA1(src_, dst_, i_) do { const unsigned c = (unsigned)((wave + 4 * (i_)) * 64 + lane_id()), r = c / 80u; glds(src_, TSRC(c, r), (dst_) + (i_) * 4096u); } while (0)
#define TROW(j_) (16 * ((j_) + 6))
#define TDMA(j_) do { _Pragma("unroll") for (int xi_ = 0; xi_ < 5; ++xi_) TDMA1(a.Bx + (size_t)TROW(j_) * 640, lds_addr(lds) + TBUF(j_) + (unsigned)wave * 1024u, xi_); } while (0)
  const __amdgpu_buffer_rsrc_t rs_fl = mkrs(cnt + CNT((isv ? C_PV : C_PT_) + h)), rs_o = mkrs(ws + O_KF + (size_t)h * 32768);
  unsigned vo[2], kkey;
  {
    unsigned ad0[5], ad1[5], mvo[6], qvo[3], xvo, xr, x5;
    { TRO_INIT();
#pragma unroll
      for (int t = 0; t < 5; ++t) { const bool isq = t == 4 || wave == 3; const unsigned tile = t == 4 ? 4u + (unsigned)(wave >> 1) : (wave == 3 ? (unsigned)t : (unsigned)(4 * wave + t));
        ad0[t] = lds_addr(lds) + 32768u + TRO(tile) + (isq ? 24576u : 0u); ad1[t] = lds_addr(lds) + 32768u + (isq ? TRO(tile + 6u) + 24576u : TRO(tile) + 12288u); PINV(ad0[t]); PINV(ad1[t]); } }
#pragma unroll
    for (int q = 0; q < 6; ++q) { const unsigned c = (unsigned)((wave + 4 * q) * 64 + lane), r = c / 24u, pc = (c - r * 24u) ^ (((r >> 1) & 1u) * 2u + ((r >> 3) & 1u) * 4u);
      mvo[q] = r * 12288u + 16u * pc; PINV(mvo[q]);
      if (q < 3) { qvo[q] = pc < 12u ? mvo[q] : mvo[q] + 32u * 12288u - 192u; PINV(qvo[q]); } }
    xvo = (unsigned)((8 * wave + (lane >> 3)) * 1280 + ((lane & 7) ^ (lane >> 3)) * 16);
    xr = lds_addr(lds) + (unsigned)(l15 * 128 + (lq ^ (l15 & 7)) * 16); x5 = xr + (unsigned)((wave & 1) * 2048);
    PINV(xvo); PINV(xr); PINV(x5);
    const u16* Wm = (isv ? a.Wv : a.Wk) + 192 * h; const u16* Wh = a.Wq + 192 * h + 96 * isv;
#define TW_DMA(i_) do { const int r_ = 128 * KROT((i_) >> 1) + 64 * ((i_) & 1); const unsigned d_ = lds_addr(lds) + (unsigned)(32768 + ((i_) % 3) * 36864 + wave * 1024); \
      _Pragma("unroll") for (int q_ = 0; q_ < 2; ++q_) glds(a.Bx + (32 + 32 * q_) * 640 + r_, xvo, lds_addr(lds) + (unsigned)(((i_) & 3) * 8192 + q_ * 4096 + wave * 1024)); \
      _Pragma("unroll") for (int q_ = 0; q_ < 6; ++q_) glds(Wm + (size_t)r_ * 6144, mvo[q_], d_ + (unsigned)(q_ * 4096)); \
      _Pragma("unroll") for (int q_ = 0; q_ < 3; ++q_) glds(Wh + (size_t)r_ * 6144, qvo[q_], d_ + (unsigned)(24576 + q_ * 4096)); SB(); } while (0)
    TW_DMA(0); TW_DMA(1); TW_DMA(2);
    f32x4 pa[4][4], p5[2];
    SFOR(i, 10)
      if (i < 8) VMW(22); else if (i == 8) VMW(16); else VMW(10);
      BAR();
      SFOR(t, 5)
        if (i % 3 < 2) tr_t(ad0[t], ad1[t], (i % 3) * 36864, 10 * i + 2 * t, T); else tr_t(ad0[t] + 73728u, ad1[t] + 73728u, 0, 10 * i + 2 * t, T);
      SEND;
      if (i < 9) BAR();
      if (i + 3 < 10) TW_DMA(i + 3);
      if (i == 7) TDMA(0);
      if (i == 8) TDMA(1);
      SFOR(kk, 2)
        u32x4 x_[4], y_[2];
#pragma unroll
        for (int rt = 0; rt < 4; ++rt) x_[rt] = LDS16((xr ^ (unsigned)(kk * 64)) + (i & 3) * 8192 + rt * 2048);
#pragma unroll
        for (int g = 0; g < 2; ++g) y_[g] = LDS16((x5 ^ (unsigned)(kk * 64)) + (i & 3) * 8192 + g * 4096);
        SFOR(n, 16) mf_t(pa[n >> 2][n & 3], x_[n >> 2], 10 * i + 2 * (n & 3) + kk, i == 0 && kk == 0, T); SEND;
        SFOR(g, 2) mf_t(p5[g], y_[g], 10 * i + 8 + kk, i == 0 && kk == 0, T); SEND;
        SB();
      SEND;
    SEND;
    { const unsigned pol = wait_tag<8>(cnt, C_POL, tag); kkey = K_KEY(); PINS(kkey); }
    const int ln = lane_id();
    vo[0] = (unsigned)ln * 16u + (wave == 3 ? (unsigned)(O_QF - O_KF) + (unsigned)(3 * isv) * 1024u : (isv ? (unsigned)(O_VF - O_KF) : 0u) + (unsigned)wave * 2048u);
    vo[1] = (unsigned)ln * 8u + (unsigned)(O_QF - O_KF) + (unsigned)(3 * isv + 2) * 1024u + (unsigned)(wave >> 1) * 512u;
    PINV(vo[0]); PINV(vo[1]);
    asm volatile("s_nop 7" : "+v"(pa[3][0]), "+v"(pa[3][1]), "+v"(pa[3][2]), "+v"(pa[3][3]), "+v"(p5[0]), "+v"(p5[1]));
#pragma unroll
    for (int g = 0; g < 2; ++g) {
#pragma unroll
      for (int n = 0; n < 4; ++n) { const f32x4 c0 = pa[2 * g + (n >> 1)][2 * (n & 1)], c1 = pa[2 * g + (n >> 1)][2 * (n & 1) + 1];
        st16(rs_o, vo[0] + (unsigned)((n & 1) * 1024), KSLOT(1 + g) + (unsigned)((n >> 1) * 16384), (u32x4){cvtpk(c0[0], c0[1]), cvtpk(c0[2], c0[3]), cvtpk(c1[0], c1[1]), cvtpk(c1[2], c1[3])}); }
      st8(rs_o, vo[1], KSLOT(1 + g) + (unsigned)((wave & 1) * 16384), cvtpk(p5[g][0], p5[g][1]), cvtpk(p5[g][2], p5[g][3]));
    }
  }
  const int lane2 = lane_id();
#define lane lane2
#define l15 (lane2 & 15)
#define lq (lane2 >> 4)
  unsigned tvo[5];
#pragma unroll
  for (int i = 0; i < 5; ++i) { const unsigned c = (unsigned)((5 * wave + i) * 64 + lane), r = c / 80u; tvo[i] = TSRC(c, r) + (unsigned)(4096 - (i - 2) * 1024); PINV(tvo[i]); }
  int flo = lane == 0 ? wave * 4 : (int)0x80000000u; PINV(flo);
  unsigned xq[4];
#pragma unroll
  for (int q = 0; q < 4; ++q) { xq[q] = lds_addr(lds) + (unsigned)TBUF(0) + (unsigned)(l15 * 1280 + ((4 * q + lq) ^ l15) * 16); PINV(xq[q]); }
#define XFRT(k_) LDS16(xq[(k_) & 3] + ((k_) >> 2) * 256)
  const unsigned fla = lds_addr(lds) + 147456u;
  unsigned flw = fla + 4u * (unsigned)lane, one = 1u; PINV(flw); PINV(one);
  if (wave == 0) *(volatile LAS unsigned*)(size_t)flw = 0u;
  unsigned gw = flw + 256u * (unsigned)(wave + 1), go = (unsigned)((lane % 24) / 6 * 128 + (lane % 6) * 4), gm = fla + 256u * (unsigned)(wave + 1), gl; PINV(gw); PINV(go); PINS(gm);
  const unsigned* gsrc = cnt + CNT(C_PO); PINS(gsrc);
  *(volatile LAS unsigned*)(size_t)gw = 0u;
  u64 gb; int gt;
  unsigned po = lane < 32 ? (isv && wave < 3 ? 0x80000000u : (vo[0] & ~1023u) + (unsigned)((lane >> 4) * 16384 + (lane & 15) * 128)) : (lane < 36 ? (vo[1] & ~511u) + (unsigned)((wave & 1) * 16384 + (lane - 32) * 128) : 0x80000000u), pso = 0u, pm0 = fla + 2048u;
  if (TPF == 0 || (TPF == 2 && !isv && wave < 3)) po = 0x80000000u;
  u32x4 o_desc = mkdesc(ws + O_KF + (size_t)h * 32768); PINV(po); PINS(o_desc); PINS(pm0);
  u32x4 xa[4]; u32x2 cv[5]; unsigned fl;
  f32x4 acc[5];
#pragma unroll
  for (int t = 0; t < 5; ++t) { acc[t] = ZERO4; cv[t] = (u32x2){0u, 0u}; }
  VMW(0);
  __builtin_amdgcn_raw_buffer_store_b32(2u, rs_fl, flo, 0, 16);
  BAR();
  TDMA(2); TDMA(3);
  xa[0] = XFRT(0); xa[1] = XFRT(1);
#define T_CVT(t_) do { unsigned c0_, c1_; \
      asm volatile("v_cvt_pk_bf16_f32 %0, %2, %3\n v_cvt_pk_bf16_f32 %1, %4, %5" : "=&v"(c0_), "=&v"(c1_) : "v"(acc[t_][0]), "v"(acc[t_][1]), "v"(acc[t_][2]), "v"(acc[t_][3])); \
      cv[t_] = (u32x2){c0_, c1_}; SB(); } while (0)
#define T_ST(i_) do { PINV(vo[0]); if ((i_) < 2) __builtin_amdgcn_raw_buffer_store_b128((u32x4){cv[2 * (i_)].x, cv[2 * (i_)].y, cv[(2 * (i_) + 1) % 5].x, cv[(2 * (i_) + 1) % 5].y}, rs_o, (int)vo[0] + (i_) * 1024, (int)sof, SAUX); \
      else __builtin_amdgcn_raw_buffer_store_b64(cv[4], rs_o, (int)vo[1], (int)sof, SAUX); SB(); } while (0)
#define T_REST(five_) do { asm volatile("s_nop 7\n s_nop 7"); T_CVT(1); T_CVT(2); T_CVT(3); T_CVT(4); T_ST(0); T_ST(1); if (five_) T_ST(2); VMW(0); } while (0)
  const char* dsrc = (const char*)(a.Bx + (size_t)TROW(4) * 640) - 4096;
  unsigned tb = TBUF(4) - 24576u, rb = TBUF(0) - 24576u;
  unsigned sol = 3u * K_STEP + 16384u, sof = k_fix(sol, kkey), sinc = 0u - 16384u, tgt = 4u, dstep = 20480u, fv = 0u, dq = 0u;
  const unsigned dd0 = lds_addr(lds) + 24576u + (unsigned)(5 * wave + 2) * 1024u;
#define T_MF(t_) do { mf_t(acc[t_], xa[k & 3], 10 * (k >> 1) + 2 * (t_) + (k & 1), k == 0, T); SB(); } while (0)
#define T_TILE(STRICT, FIVE) do { \
    SFOR(k, 20) \
      constexpr int k2 = k + 2, kn = k2 < 20 ? k2 : k2 - 20; \
      T_MF(0); \
      if (k != 18) { xa[k2 & 3] = XFRT(kn); SB(); } \
      if (k == 0) T_CVT(1); \
      if (k == 18) { xq[0] += dq; PINV(xq[0]); SB(); }        \
      T_MF(1); \
      if (k == 0) T_CVT(2); \
      if (k == 2) { asm volatile("s_mov_b32 m0, %0" :: "s"(FIVE ? gm : pm0) : "memory"); SB(); } \
      if (k == 14 && FIVE) { gl = *(const volatile LAS unsigned*)(size_t)gw; SB(); } \
      if (k == 17 && FIVE) {        \
        if (__builtin_expect(gb != 0ull, 0)) { unsigned spins = 0; while (__any((int)__hip_atomic_load((const gu32*)gsrc + (go >> 2), RLX_AGENT) < gt) && spin_check(spins, tmo)) __builtin_amdgcn_s_sleep(24); } \
        MEMB(); SB(); } \
      if (k == 3) { unsigned m_ = dd0 + tb; PINS(m_); asm volatile("s_mov_b32 m0, %0" :: "s"(m_) : "memory"); SB(); } \
      if (k == 16) { fl = *(const volatile LAS unsigned*)(size_t)flw; SB(); } \
      if (k == 18) {        \
        if (__builtin_expect(fs < tgt, 0)) { do fl = *(const volatile LAS unsigned*)(size_t)flw; while ((unsigned)__builtin_amdgcn_readfirstlane(fl) < tgt); } \
        MEMB(); SB(); } \
      if (k == 9) { PINS(tb); tb += 20480u; PINS(tb); SB(); } \
      if (k == 11) { PINS(rb); rb += 20480u; PINS(rb); SB(); } \
      if (k == 12) { PINS(rb); rb = rb == 6u * 20480u ? 0u : rb; PINS(rb); SB(); } \
      if (k == 13) { PINS(sol); PINS(sinc); sol = (sol + sinc) & (NK * K_STEP - 1u); PINS(sol); SB(); } \
      if (k == 15) { PINS(dstep); dstep = j < 116 ? 20480u : 0u; PINS(dstep); SB(); } \
      if (k == 10) { fv = (unsigned)(STRICT ? (j < 1 ? 4 : j + 3) : j + 2) >> 1; PINV(fv); SB(); } \
      if (k == 19) { xq[2] += dq; PINV(xq[2]); SB(); } \
      T_MF(2); \
      if (k == 0) T_CVT(3); \
      if (k == 1 || k == 2 || (k == 3 && !FIVE)) T_ST(k >= 1 && k <= 3 ? k - 1 : 0); \
      if (k == 4) glds_o<-2048>(dsrc, tvo[0]); else if (k == 5) glds_o<-1024>(dsrc, tvo[1]); else if (k == 6) glds_o<0>(dsrc, tvo[2]); else if (k == 7) glds_o<1024>(dsrc, tvo[3]); else if (k == 8) glds_o<2048>(dsrc, tvo[4]); \
      if (k == 9) { PINS(dsrc); dsrc += dstep; PINS(dsrc); SB(); } \
      if (k == 11) { PINS(tb); tb = tb == 6u * 20480u ? 0u : tb; PINS(tb); SB(); } \
      if (k == 14) { PINS(rb); dq = rb == 0u ? 0u - 5u * 20480u : 20480u; PINS(dq); SB(); } \
      if (k == 15) { PINS(sinc); sinc = sinc == 16384u ? K_STEP - 16384u : 16384u; PINS(sinc); SB(); } \
      if (k == 12) {        \
        asm volatile("s_waitcnt vmcnt(%0)" :: "n"(STRICT ? 14 : 24) : "memory"); \
        __hip_atomic_fetch_add((LAS unsigned*)(size_t)flw, one, __ATOMIC_RELAXED, __HIP_MEMORY_SCOPE_WORKGROUP); SB(); \
      } \
      if (k == 13) { __builtin_amdgcn_raw_buffer_store_b32(fv, rs_fl, flo, 0, 16); SB(); } \
      if (k == 17) { fs = (unsigned)__builtin_amdgcn_readfirstlane(fl); PINS(fs); SB(); } \
      if (k == 18) { xa[0] = XFRT(0); SB(); xq[1] += dq; PINV(xq[1]); SB(); } \
      if (k == 19) { PINS(tgt); tgt += 4u; PINS(tgt); xq[3] += dq; PINV(xq[3]); SB(); } \
      T_MF(3); \
      if (k == 0 && !FIVE) T_CVT(4); \
      if (k == 13) { sof = (sol >> 8) & 0x2000u; PINS(sof); SB(); } \
      if (k == 16) { PINS(sof); sof ^= sol ^ kkey; PINS(sof); SB(); } \
      if (k == 2 && FIVE) { asm volatile("global_load_lds_dword %0, %1 sc1" :: "v"(go), "s"(gsrc) : "memory"); SB(); } \
      if (k == 2 && !FIVE) { asm volatile("buffer_load_dword %0, %1, %2 offen lds" :: "v"(po), "s"(o_desc), "s"(pso) : "memory"); SB(); } \
      if (k == 9 && FIVE) { pso = ((unsigned)((j >> 1) + 3 - TLAG) & (NK - 1u)) * K_STEP; PINS(pso); SB(); } \
      if (k == 10 && FIVE) { PINS(pso); pso ^= kkey; PINS(pso); SB(); } \
      if (k == 11 && FIVE) { PINS(pso); pso ^= (pso >> 8) & 0x2000u; PINS(pso); SB(); } \
      if (k == 14 && FIVE) { gt = (j >> 1) + 5 - TGATE; PINS(gt); SB(); } \
      if (k == 15 && FIVE) { asm volatile("v_cmp_gt_i32_e64 %0, %1, %2" : "=s"(gb) : "s"(gt), "v"(gl)); SB(); } \
      if (k == 19) T_CVT(0); \
      if (FIVE) T_MF(4); \
    SEND; } while (0)
  unsigned fs;
  static_assert(TSTRICT % 2 == 0, "");
#define T_ALL(ODD) do { \
    if (ODD) { const int j = 0; T_TILE(true, false); } \
    _Pragma("unroll 1") for (int j2 = ODD; j2 < TSTRICT; j2 += 2) { \
      { const int j = j2; T_TILE(true, true); } \
      { const int j = j2 + 1; T_TILE(true, false); } } \
    _Pragma("unroll 1") for (int j2 = TSTRICT + ODD; j2 < 121; j2 += 2) { \
      const int s = j2 >> 1; (void)s; \
      { const int j = j2; T_TILE(false, true); } \
      { const int j = j2 + 1; T_TILE(false, false); } } \
    if (ODD) { const int j = 121; T_TILE(false, true); } \
    T_REST(ODD); \
    __builtin_amdgcn_raw_buffer_store_b32(64u, rs_fl, flo, 0, 16); } while (0)
  if (wave & 1) T_ALL(1); else T_ALL(0);
}
#undef lane
#undef l15
#undef lq

#define KPF 2
#define K_KEY2() ((((unsigned)((size_t)(ws + O_KF) >> 21) ^ KSIDE ^ pol) & 1u) << 13)
#define XPO 0
#define OAUX 0
__device__ __forceinline__ void o_role(const Args& a, const u32x2 tag, unsigned char* lds, const int j, const int wave) {
  char* ws = a.ws; asm volatile("" : "+s"(ws));
  unsigned* const cnt = (unsigned*)(ws + O_CNT); unsigned* const tmo = cnt + CNT(C_TMO);
  const int lane = lane_id(), l15 = lane & 15, lq = lane >> 4;
  const unsigned pol = wait_tag<32>(cnt, C_POL, tag);
  const int xq = j / 6, c0 = 10 * xq + 5 * (j & 1), s0 = (j - 6 * xq) >> 1;
  const u32x4 rs_u = mkdesc(ws + O_UO + wave * 8192);
  const __amdgpu_buffer_rsrc_t rs_z = mkrs(ws + O_ZB + (size_t)c0 * 16384 + wave * 2048), rs_p = mkrs(ws + O_PT + wave * 16384), rs_m = mkrs(ws + O_PT + 65536 + wave * 4096), rs_out = mkrs(a.out + 640 + 16 * c0);
  unsigned xv = (unsigned)lane * 16u, xh = (unsigned)lane * 8u;
  unsigned wa = (unsigned)(wave * 10880 + lq * 272 + l15 * 16), ra[2], oo[2];
#pragma unroll
  for (int i = 0; i < 2; ++i) {
    const int n = i == 0 ? lane : 64 + l15, tok = 8 * wave + n / 10, oc = n % 10;
    ra[i] = (unsigned)((((oc >> 1) * 2 + (tok >> 4)) * 4 + 2 * (oc & 1)) * 272 + (tok & 15) * 16);
    oo[i] = i == 0 || lane < 16 ? (unsigned)((tok * 1280 + 8 * oc) * 2) : 0x80000000u;
    PINV(ra[i]); PINV(oo[i]);
  }
  PINV(xv); PINV(xh); PINV(wa);
  WReg W;
  if (TM == 2) touch_n<10>(a, 4 * j + wave, 0, 32);
  if (TM == 3) touch_n<4>(a, 192 + 4 * j + wave, 0, 96);
  if (TM == 1) {
    const int w = 4 * j + wave;
    wait_ge<32>(cnt + CNT(C_PZ + 8 * xq + (w & 7)), 1u, tmo);
    unsigned v = 0;
#pragma unroll
    for (int i = 0; i < 8; ++i) v ^= touch_rows(a, w % 3, 64 + w / 3 + 32 * i);
    v ^= touch(a.Awo, 640 * 1280, w) ^ touch(a.Awo, 640 * 1280, 96 + w);
    asm volatile("" :: "v"(v));
  }
  u32x2 wp = {0u, 0u};
  const u32x4 k_desc = mkdesc(ws + O_KF + (size_t)(8 * xq + 4 * (j & 1) + wave) * 32768); int ko = lane < 48 ? lane * 128 : (int)0x80000000u; PINV(ko);
  wait_tag<32>(cnt, C_GO, tag);
#pragma unroll 1
  for (int s = s0; s < 64; s += 3) {
    if (wave == 0) {
      const unsigned tk = __hip_atomic_load((const gu32*)(cnt + CNT(C_TOK + 4 * s + xq)), RLX_AGENT), sp = __hip_atomic_load((const gu32*)(cnt + CNT(C_PS + j)), RLX_AGENT);
      if (__any(min(wp.x, wp.y) < (unsigned)(s + 1))) {
        unsigned spins = 0;
        do wp = ld8(mkrs(cnt + CNT(s == 0 ? C_PZ : C_PW)), (unsigned)((lane & 31) * 128 + (lane >> 5) * 8), 0u); while (__any(min(wp.x, wp.y) < (unsigned)(s + 1)) && (__builtin_amdgcn_s_sleep(16), spin_check(spins, tmo)));
      }
      if ((unsigned)__builtin_amdgcn_readfirstlane(sp) < (unsigned)s) { wait_ge<8>(cnt + CNT(C_PS + j), (unsigned)s, tmo); nap_rt(a.d[14]); }
      *(volatile LAS unsigned*)(lds + 65536) = tk;
    }
    BAR();
    const bool behind = (unsigned)__builtin_amdgcn_readfirstlane(*(volatile LAS unsigned*)(lds + 65536)) >= 32u;
    SFOR(i, 20) ld_w(rs_u, xv, u_off((unsigned)s, (unsigned)(c0 + (i >> 2))) + (unsigned)(((i >> 1) & 1) * 4096), (i & 1) * 2048, i, (i & 3) == 0, W); SEND;
    {
      const unsigned kkey = K_KEY2(), so = KSLOT(s + KPF);
      asm volatile("s_mov_b32 m0, %4\n s_nop 4\n buffer_load_dword %0, %1, %2 offen sc1 lds\n buffer_load_dword %0, %1, %3 offen sc1 lds" :: "v"(ko), "s"(k_desc), "s"(so), "s"(so + 16384u), "s"(lds_addr(lds) + 66560u) : "memory");
    }
    if (!behind) {
      if (wave == 0) { if (s >= 60) wait_ge<1>(cnt + CNT(C_TOK + 4 * s + xq), 32u, tmo); else wait_ge<8>(cnt + CNT(C_TOK + 4 * s + xq), 32u, tmo); }
      BAR();
    }
    u32x4 z[5][2], p1[8], pm[2][2]; u32x2 p0[8];
    const unsigned zs = half_of(ws + O_ZB, ZSLOT(s), 0u, pol), ps = TSLOT(s);
    PINV(xv); PINV(xh);
#pragma unroll
    for (int n = 0; n < 8; ++n) { p1[n] = ld16(rs_p, xv + (unsigned)((n & 1) * 2048 + 1024), ps + (unsigned)((n >> 1) * 4096)); p0[n] = ld8(rs_p, xh + (unsigned)((n & 1) * 2048), ps + (unsigned)((n >> 1) * 4096)); SB(); }
#pragma unroll
    for (int n = 0; n < 4; ++n) pm[n >> 1][n & 1] = ld16(rs_m, xv + (unsigned)(n * 1024), ps);
#define O_ZLOAD() do { PINV(xv); _Pragma("unroll") for (int n = 0; n < 10; ++n) z[n >> 1][n & 1] = ld16(rs_z, xv + (unsigned)((n & 1) * 1024), zs + (unsigned)((n >> 1) * 16384)); } while (0)
    O_ZLOAD();
    SB();
    f32x4 acc[5][2];
    SFOR(n, 8)
      u32x4 q0 = {p0[n].x, p0[n].y, 0u, 0u}; PINV(q0);
      SFOR(t, 5) mf_w<true>(acc[t][1], p1[n], 8 * t + n, n == 0, W); SB(); SEND;
      SFOR(t, 5) mf_w<true>(acc[t][0], q0, 8 * t + n, n == 0, W); SB(); SEND;
    SEND;
    for (unsigned spins = 0;;) {
      unsigned e = ~0u;
#pragma unroll
      for (int n = 0; n < 10; ++n) e = sent_min(e, z[n >> 1][n & 1]);
      if (__builtin_expect(!__any(e == 0u), 1) || !spin_check(spins, tmo)) break;
      O_ZLOAD();
    }
#pragma unroll
    for (int n = 0; n < 4; ++n)
#pragma unroll
      for (int t = 0; t < 5; ++t) asm volatile("v_mfma_f32_16x16x32_bf16 %0, %1, %2, %0" : "+v"(acc[t][n & 1]) : "v"(z[t][n >> 1]), "v"(pm[n >> 1][n & 1]));
    asm volatile("s_nop 7" : "+v"(acc[0][1]), "+v"(acc[1][1]), "+v"(acc[2][1]), "+v"(acc[3][1]), "+v"(acc[4][1]));
#pragma unroll
    for (int n = 0; n < 10; ++n) *(f32x4*)(lds + wa + (n % 5 * 2 + n / 5) * 1088) = acc[n % 5][n / 5];
    BAR();
    if (wave == 0 && lane == 0) __hip_atomic_store((gu32*)(cnt + CNT(C_PO + xq) + j - 6 * xq), (unsigned)(s + 3), RLX_AGENT);
#pragma unroll
    for (int i = 0; i < 2; ++i) {
      f32x4 v0 = *(const f32x4*)(lds + ra[i]), v1 = *(const f32x4*)(lds + ra[i] + 272);
#pragma unroll
      for (int w = 1; w < 4; ++w) { v0 += *(const f32x4*)(lds + ra[i] + w * 10880); v1 += *(const f32x4*)(lds + ra[i] + w * 10880 + 272); }
      const u32x4 w_ = {cvtpk(v0[0], v0[1]), cvtpk(v0[2], v0[3]), cvtpk(v1[0], v1[1]), cvtpk(v1[2], v1[3])};
      __builtin_amdgcn_raw_buffer_store_b128(w_, rs_out, (int)oo[i], 32 * s * 1280 * 2, OAUX);
      asm volatile("s_nop 1" :: "v"(w_) : "memory");
    }
  }
  VMW(0);
}

#define LAUX 16
#define XPF 0
__device__ __forceinline__ void att_role(const Args& a, const u32x2 tag, unsigned char* lds, const int h, const int wave) {
  char* ws = a.ws;
  unsigned* const cnt = (unsigned*)(ws + O_CNT); unsigned* const tmo = cnt + CNT(C_TMO);
  const int lane = lane_id(), l15 = lane & 15, lq = lane >> 4, par = wave >> 1;
  const __amdgpu_buffer_rsrc_t rs_q = mkrs(ws + O_QF + (size_t)h * 32768), rs_k = mkrs(ws + O_KF + (size_t)h * 32768), rs_km = mkrs(ws + O_KM + (size_t)h * 3072), rs_pt = mkrs(ws + O_PT);
  const float scale = 0.07216878364870323f, LOG2E = 1.4426950408889634f;
  unsigned xv = (unsigned)lane * 16u; PINV(xv);
#define KM_EMPTY(s_) do { _Pragma("unroll") for (int i_ = 0; i_ < 3; ++i_) st16(rs_km, xv + (unsigned)(i_ * 1024), MSLOT(s_), SENT4); } while (0)
#pragma unroll 1
  for (int s = par; s < NZ; s += 2) KM_EMPTY(s);
  VMW(0);
  const unsigned pol = wait_tag<16>(cnt, C_POL, tag), kkey = K_KEY();
  fill_done(cnt, tag, pol);
  nap_rt(XPF);
  if (TM == 0) touch_n<5>(a, 128 + 2 * h + par, 0, 64);
  if (TM == 3) touch_n<4>(a, 128 + 2 * h + par, 0, 96);
  if (TM != 1) { const unsigned v = touch(a.Awo, 640 * 1280, 2 * h + par) ^ touch(a.Awo, 640 * 1280, 64 + 2 * h + par); asm volatile("" :: "v"(v)); }
  if (TM == 1) { const unsigned v = touch_rows(a, 1, 2 * h + par) ^ touch_rows(a, 0, 2 * h + par) ^ touch_rows(a, 2, 2 * h + par); asm volatile("" :: "v"(v)); }
  u32x4 qf[2][6], kf[2][6];
  unsigned xh = (unsigned)lane * 8u;
#define QK_LOAD(s_) do { PINV(xv); PINV(xh); _Pragma("unroll") for (int i = 0; i < 12; ++i) { const unsigned so = KSLOT(s_) + (unsigned)(i / 6 * 16384 + i % 6 / 3 * 3072), vo = xv + (unsigned)(i % 3 * 1024); \
      kf[i / 6][i % 6] = __builtin_amdgcn_raw_buffer_load_b128(rs_k, (int)vo, (int)so, LAUX); \
      if (i % 3 < 2) qf[i / 6][i % 6] = __builtin_amdgcn_raw_buffer_load_b128(rs_q, (int)vo, (int)so, LAUX); else { const u32x2 l_ = __builtin_amdgcn_raw_buffer_load_b64(rs_q, (int)(xh + 2048u), (int)so, LAUX), h_ = __builtin_amdgcn_raw_buffer_load_b64(rs_q, (int)(xh + 2560u), (int)so, LAUX); qf[i / 6][i % 6] = (u32x4){l_.x, l_.y, h_.x, h_.y}; } } SB(); } while (0)
  const __amdgpu_buffer_rsrc_t rs_tv = mkrs(cnt + CNT(C_PT_ + h));
  int tvo = (lane & 3) * 4 + ((lane >> 2) & 1) * 4 * CNT(C_PV - C_PT_); PINV(tvo);
#define TV_WAIT(s_) do { unsigned spins = 0; while (__any(__builtin_amdgcn_raw_buffer_load_b32(rs_tv, tvo, 0, 16) < (unsigned)(s_)) && spin_check(spins, tmo)) __builtin_amdgcn_s_sleep(16); MEMB(); } while (0)
  unsigned kmo = (unsigned)(lq * 128 + ((l15 & 1) + (l15 & 2) * 2 + ((l15 >> 1) & 2)) * 16), tlast = 0; PINV(kmo);
  if (par == 0) wait_ge<32>(cnt + CNT(C_PS + h), 2u, tmo); else TV_WAIT(1);
  QK_LOAD(par);
#pragma unroll 1
  for (int s = par; s < 64; s += 2) {
    f32x4 sc[2][3];
    float mx[2];
    {
#pragma unroll
      for (int q = 0; q < 2; ++q) {
        mx[q] = -3.0e38f;
#pragma unroll
        for (int t = 0; t <= q; ++t) {
          sc[q][t] = ZERO4;
#pragma unroll
          for (int kk = 0; kk < 6; ++kk) sc[q][t] = mfma(kf[t][kk], qf[q][kk], sc[q][t]);
#pragma unroll
          for (int r = 0; r < 4; ++r) {
            const float v = rbf(rbf(sc[q][t][r]) * scale);
            sc[q][t][r] = (t < q || 4 * lq + r <= l15) ? v : -3.0e38f;
            mx[q] = fmaxf(mx[q], sc[q][t][r]);
          }
        }
        PINV(mx[q]);
      }
    }
    SB();
    unsigned tv;
    {
      u32x4 km[6];
      unsigned spins = 0;
      const unsigned t0 = wait_rt(tlast + (unsigned)a.d[15]);
      for (;;) {
        unsigned m = ~0u;
        PINV(kmo);
        tv = __builtin_amdgcn_raw_buffer_load_b32(rs_tv, tvo, 0, 16);
#pragma unroll
        for (int kk = 0; kk < 6; ++kk) { km[kk] = ld16(rs_km, kmo + (unsigned)(kk * 512), MSLOT(s)); m = sent_min(m, km[kk]); }
        if (!__any(m == 0u) || !spin_check(spins, tmo)) break;
        if (spins > 4u) __builtin_amdgcn_s_sleep(12); else __builtin_amdgcn_s_sleep(2);
      }
      tlast = t0 - 100u;
      if (spins) { u64 t_; RT_MARK(t_); tlast = (unsigned)t_; }
#pragma unroll
      for (int q = 0; q < 2; ++q) {
        sc[q][2] = ZERO4;
#pragma unroll
        for (int kk = 0; kk < 6; ++kk) sc[q][2] = mfma(km[kk], qf[q][kk], sc[q][2]);
      }
    }
    asm volatile("" : "+v"(sc[0][2]), "+v"(sc[1][2]));
    const bool ahead = s + 2 < 64 && !__any(tv < (unsigned)(s + 2));
#pragma unroll
    for (int q = 0; q < 2; ++q) {
#pragma unroll
      for (int r = 0; r < 4; ++r) { sc[q][2][r] = rbf(rbf(sc[q][2][r]) * scale); mx[q] = fmaxf(mx[q], sc[q][2][r]); }
      const float m = max32(max16(mx[q])), nml = -m * LOG2E;
      float sum = 0.f, sm = 0.f;
#pragma unroll
      for (int t = 0; t <= q; ++t)
#pragma unroll
        for (int r = 0; r < 4; ++r) { const float e = (t < q || 4 * lq + r <= l15) ? __builtin_amdgcn_exp2f(__builtin_fmaf(sc[q][t][r], LOG2E, nml)) : 0.f; sc[q][t][r] = e; sum += e; }
#pragma unroll
      for (int r = 0; r < 4; ++r) { sc[q][2][r] = __builtin_amdgcn_exp2f(__builtin_fmaf(sc[q][2][r], LOG2E, nml)); sm += sc[q][2][r]; }
      sum = sum32(sum16(__builtin_fmaf(sm, 0.5f, sum)));
      const float inv = 1.0f / sum;
      if (q == 0) st8(rs_pt, (unsigned)(h * 2048 + lane * 8), TSLOT(s), cvtpk(sc[0][0][0] * inv, sc[0][0][1] * inv), cvtpk(sc[0][0][2] * inv, sc[0][0][3] * inv));
      else st16(rs_pt, (unsigned)(h * 2048 + 1024 + lane * 16), TSLOT(s), (u32x4){cvtpk(sc[1][0][0] * inv, sc[1][0][1] * inv), cvtpk(sc[1][0][2] * inv, sc[1][0][3] * inv), cvtpk(sc[1][1][0] * inv, sc[1][1][1] * inv), cvtpk(sc[1][1][2] * inv, sc[1][1][3] * inv)});
      st8(rs_pt, lq < 2 ? (unsigned)(65536 + (h >> 2) * 2048 + q * 1024 + (h & 3) * 256 + l15 * 16 + lq * 8) : 0x80000000u, TSLOT(s), cvtpk(sc[q][2][0] * inv, sc[q][2][1] * inv), cvtpk(sc[q][2][2] * inv, sc[q][2][3] * inv));
    }
    if (s + NZ < 64) KM_EMPTY(s);
    if (ahead) { QK_LOAD(s + 2); VMW(28); } else VMW(0);
    if (lane < 4) __hip_atomic_fetch_add((gu32*)(cnt + CNT(C_TOK + 4 * s + lane)), 1u, RLX_AGENT);
    if (!ahead && s + 2 < 64) { TV_WAIT(s + 2); QK_LOAD(s + 2); }
  }
}

__device__ __forceinline__ float dot2bf(unsigned x, unsigned y, float c) { return __builtin_amdgcn_fdot2_f32_bf16(__builtin_bit_cast(bf16x2_t, x), __builtin_bit_cast(bf16x2_t, y), c, false); }
__device__ __forceinline__ void a_role(const Args& a, const u32x2 tag, unsigned char* lds, const int j, const int wave) {
  char* ws = a.ws; asm volatile("" : "+s"(ws));
  unsigned* const cnt = (unsigned*)(ws + O_CNT); unsigned* const tmo = cnt + CNT(C_TMO);
  const int lane = lane_id(), l15 = lane & 15, lq = lane >> 4;
  const int c0 = 40 * j - 8 * (j & 1);
  u32x4 wt[3][20];
#define A_WDMA(src_, ld_, off_) do { const int ln_ = lane_id(); _Pragma("unroll") for (int q_ = 0; q_ < 15; ++q_) { const int ins = wave + 4 * q_; const unsigned c = (unsigned)(ins * 64 + ln_), r = c / 6u, pc = c - r * 6u; \
      glds(src_, r * (unsigned)((ld_) * 2) + 16u * pc, lds_addr(lds) + (unsigned)((off_) + ins * 1024)); } } while (0)
#define A_WREAD(off_) do { unsigned o_ = (unsigned)((off_) + (8 * lq + (l15 >> 2)) * 96 + 8 * (l15 & 3)); PINV(o_); \
    _Pragma("unroll") for (int t = 0; t < 3; ++t) { _Pragma("unroll") for (int kk = 0; kk < 20; ++kk) wt[t][kk] = trrd2(lds, o_ + (unsigned)(kk * 3072 + 32 * t), 384u); SB(); } } while (0)
  A_WDMA(a.Awqkv + c0, 1920, 0); A_WDMA(a.Awqkv + 640 + c0, 1920, 61440);
  VMW(0); BAR();
  if (wave < 2) A_WREAD(wave * 61440);
  BAR();
  A_WDMA(a.Awqkv + 1280 + c0, 1920, 0);
  VMW(0); BAR();
  if (wave == 2) A_WREAD(0);
  if (wave == 3) wait_tag<8>(cnt, C_POL, tag);
  BAR();
  constexpr int LX = 0, LR = 122880, RS = 304, NR = 128;
  unsigned dvo[10];
#pragma unroll
  for (int i = 0; i < 10; ++i) { const unsigned c = (unsigned)((wave + 4 * i) * 64 + lane), r = c / 80u; dvo[i] = (c ^ (r & 15u)) * 16u; PINV(dvo[i]); }
#define A_XDMA(src_, b_) do { _Pragma("unroll") for (int xi_ = 0; xi_ < 10; ++xi_) glds(src_, dvo[xi_], lds_addr(lds) + (unsigned)(LX + ((b_) & 1) * 40960 + wave * 1024 + xi_ * 4096)); } while (0)
  unsigned xr[4];
#pragma unroll
  for (int q = 0; q < 4; ++q) xr[q] = (unsigned)(LX + l15 * 1280 + ((4 * q + lq) ^ l15) * 16);
  unsigned dv3[14];
#pragma unroll
  for (int i = 0; i < 14; ++i) { const unsigned c = (unsigned)((13 * (wave < 3 ? wave : 0) + i) * 64 + lane), r = c / 80u; dv3[i] = (c ^ (r & 15u)) * 16u + (unsigned)(4096 - (i % 7 - 3) * 1024); PINV(dv3[i]); }
  const unsigned am0 = lds_addr(lds) + (unsigned)(LX + (13 * wave + 3) * 1024);
#define A_XD3(src_, lb_, i_) do { if ((i_) % 7 == 0) m0_set(am0 + (lb_) + (unsigned)((i_) / 7 * 7168)); \
    switch ((i_) % 7) { case 0: glds_o<-3072>(src_, dv3[i_]); break; case 1: glds_o<-2048>(src_, dv3[i_]); break; case 2: glds_o<-1024>(src_, dv3[i_]); break; case 3: glds_o<0>(src_, dv3[i_]); break; \
      case 4: glds_o<1024>(src_, dv3[i_]); break; case 5: glds_o<2048>(src_, dv3[i_]); break; default: glds_o<3072>(src_, dv3[i_]); break; } } while (0)
  A_XDMA(a.Ax, 0);
  A_XDMA(a.Ax + 32 * 640, 1);
  unsigned wo_[3];
#pragma unroll
  for (int t = 0; t < 3; ++t) { const int v = 4 * t + lq - 2 * (j & 1); wo_[t] = (unsigned)(LR + l15 * RS + wave * 96 + (v < 0 ? 40 + (v + 2) * 48 : (v >= 10 ? 40 + (v - 10) * 48 : (v >= 5 ? 8 : 0) + v * 8))); }
  const __amdgpu_buffer_rsrc_t rs_oa = mkrs(ws + O_OA);
#pragma unroll 1
  for (int b = 0; b <= 64; ++b) {
    const int s = b; (void)s;
    if (b == 0) VMW(0); else if (wave < 3) VMW(14);
    BAR();
    const char* xsrc = (const char*)(a.Ax + (size_t)(32 * (b < 62 ? b + 2 : 63)) * 640) - 4096;
    const unsigned xb = (unsigned)(b % 3) * 40960u, xb2 = (unsigned)((b + 2) % 3) * 40960u;
    if (wave < 3) {
      if (b < 64) {
        unsigned xq[4];
#pragma unroll
        for (int q = 0; q < 4; ++q) { xq[q] = xr[q] + xb; PINV(xq[q]); }
#define A_XF(rt_, k_) (*(const u32x4*)(lds + xq[(k_) & 3] + (rt_) * 20480 + ((k_) >> 2) * 256))
        f32x4 acc[2][3]; u32x4 xa[3][2];
#pragma unroll
        for (int k = 0; k < 2; ++k) { xa[k][0] = A_XF(0, k); xa[k][1] = A_XF(1, k); }
#pragma unroll
        for (int k = 0; k < 20; ++k) {
          if (k + 2 < 20) { xa[(k + 2) % 3][0] = A_XF(0, k + 2); xa[(k + 2) % 3][1] = A_XF(1, k + 2); }
#pragma unroll
          for (int t = 0; t < 3; ++t) { acc[0][t] = mfma(wt[t][k], xa[k % 3][0], k == 0 ? ZERO4 : acc[0][t]); acc[1][t] = mfma(wt[t][k], xa[k % 3][1], k == 0 ? ZERO4 : acc[1][t]);
            if (t == 1 && k < 14) { asm volatile("" : "+a"(acc[0][0]), "+a"(acc[0][1]), "+a"(acc[1][0]), "+a"(acc[1][1])); A_XD3(xsrc, xb2, k < 14 ? k : 0); SB(); } }
          if (k >= 14) IL(6, 1);
          asm volatile("" : "+a"(acc[0][0]), "+a"(acc[0][1]), "+a"(acc[0][2]), "+a"(acc[1][0]), "+a"(acc[1][1]), "+a"(acc[1][2])); SB();
        }
        const unsigned ro = (unsigned)(((32 * b) & (NR - 1)) * RS);
#pragma unroll
        for (int t = 0; t < 3; ++t)
#pragma unroll
          for (int rt = 0; rt < 2; ++rt) *(u32x2*)(lds + wo_[t] + ro + rt * 16 * RS) = (u32x2){cvtpk(acc[rt][t][0], acc[rt][t][1]), cvtpk(acc[rt][t][2], acc[rt][t][3])};
      }
    } else {
      const int row = 32 * (b > 0 ? b - 1 : 0) + (lane & 31), hh = lane >> 5;
      const unsigned ho = (unsigned)(LR + 48 * hh);
      unsigned ra[8];
#pragma unroll
      for (int w = 0; w < 8; ++w) { const int p = row - 2 * w; ra[w] = ho + (unsigned)(((p > 0 ? p : 0) & (NR - 1)) * RS); }
#define A_LD10(d_, ad_) do { const u32x4 x0 = *(const u32x4*)(lds + (ad_)), x1 = *(const u32x4*)(lds + (ad_) + 16); const u32x2 x2 = *(const u32x2*)(lds + (ad_) + 32); \
        d_[0] = x0.x; d_[1] = x0.y; d_[2] = x0.z; d_[3] = x0.w; d_[4] = x1.x; d_[5] = x1.y; d_[6] = x1.z; d_[7] = x1.w; d_[8] = x2.x; d_[9] = x2.y; } while (0)
      unsigned q[10], kv[8][10], vv[8][10];
      A_LD10(q, ra[0]);
#pragma unroll
      for (int w = 0; w < 8; ++w) A_LD10(kv[w], ra[w] + 96);
#pragma unroll
      for (int w = 0; w < 8; ++w) A_LD10(vv[w], ra[w] + 192);
      SB();
      float lg[8], mx = -3.0e38f;
#pragma unroll
      for (int w = 0; w < 8; ++w) lg[w] = 0.f;
#pragma unroll
      for (int i = 0; i < 10; ++i)
#pragma unroll
        for (int w = 0; w < 8; ++w) lg[w] = dot2bf(q[i], kv[w][i], lg[w]);
#pragma unroll
      for (int w = 0; w < 8; ++w) { lg[w] = row - 2 * w >= 0 ? rbf(rbf(lg[w]) * 0.22360679774997896f) : rbf(-1000000000.0f); mx = fmaxf(mx, lg[w]); }
      float sum = 0.f;
#pragma unroll
      for (int w = 0; w < 8; ++w) { lg[w] = __expf(lg[w] - mx); sum += lg[w]; }
      const float inv = __builtin_amdgcn_rcpf(sum);
      float ov[20];
#pragma unroll
      for (int d = 0; d < 20; ++d) ov[d] = 0.f;
#pragma unroll
      for (int w = 0; w < 8; w += 2) {
        const unsigned pw = cvtpk(lg[w] * inv, lg[w + 1] * inv);
#pragma unroll
        for (int i = 0; i < 10; ++i) {
          ov[2 * i] = dot2bf(__builtin_amdgcn_perm(vv[w + 1][i], vv[w][i], 0x05040100u), pw, ov[2 * i]);
          ov[2 * i + 1] = dot2bf(__builtin_amdgcn_perm(vv[w + 1][i], vv[w][i], 0x07060302u), pw, ov[2 * i + 1]);
        }
      }
      const unsigned oo = b > 0 ? (unsigned)((row * 640 + 40 * j + 20 * hh) * 2) : 0x80000000u;
#pragma unroll
      for (int i = 0; i < 5; ++i) st8(rs_oa, oo + 8u * i, 0u, cvtpk(ov[4 * i], ov[4 * i + 1]), cvtpk(ov[4 * i + 2], ov[4 * i + 3]));
      if (b > 0 && (b & 15) == 0) { VMW(0); if (lane == 0) __hip_atomic_fetch_add((gu32*)(cnt + CNT(C_AO + (b >> 4) - 1)), 1u, RLX_AGENT); }
    }
  }
  VMW(0);
}
__device__ __forceinline__ void a2_role(const Args& a, unsigned char* lds, const int j, const int wave) {
  char* ws = a.ws; asm volatile("" : "+s"(ws));
  unsigned* const cnt = (unsigned*)(ws + O_CNT); unsigned* const tmo = cnt + CNT(C_TMO);
  VMW(0);
  BAR();
  {
    const int ln = lane_id(), m15 = ln & 15, mq = ln >> 4, cg = j & 3, rg = j >> 2;
    WReg W;
    {
      unsigned wv[10];
#pragma unroll
      for (int q = 0; q < 10; ++q) { const unsigned c = (unsigned)((wave + 4 * q) * 64 + ln), r = c / 20u; wv[q] = r * 1280u + (c - r * 20u) * 16u; PINV(wv[q]); }
#define A2_WDMA(i_) do { _Pragma("unroll") for (int q_ = 0; q_ < 10; ++q_) glds(a.Awo + (size_t)(128 * (i_)) * 640 + 160 * cg, wv[q_], lds_addr(lds) + (unsigned)(((i_) & 3) * 40960 + (wave + 4 * q_) * 1024)); } while (0)
      A2_WDMA(0); A2_WDMA(1); A2_WDMA(2); A2_WDMA(3);
      const unsigned rb = lds_addr(lds) + (unsigned)((8 * mq + (m15 >> 2)) * 320);
      const unsigned ad[3] = {rb + (unsigned)(64 * wave + 16 * (m15 & 3)), rb + (unsigned)(64 * wave + 16 * (m15 & 3) + 8), rb + (unsigned)(256 + 32 * (wave >> 1) + 8 * (m15 & 3))};
      SFOR(i, 5)
        if (i < 2) VMW(30); else if (i == 2) VMW(20); else if (i == 3) VMW(10); else VMW(0);
        BAR();
        SFOR(n, 6) tr4_w(ad[n >> 1] + (unsigned)((i & 3) * 40960), (n & 1) * 20480, (n & 1) * 20480 + 1280, (n & 1) * 20480 + 10240, (n & 1) * 20480 + 11520, 6 * i + n, W); SEND;
        if (i == 0) { BAR(); A2_WDMA(4); }
      SEND;
    }
    unsigned dv[10], xq[4], x5[4];
#pragma unroll
    for (int i = 0; i < 10; ++i) { const unsigned c = (unsigned)((10 * wave + i) * 64 + ln), r = c / 80u; dv[i] = (c ^ (r & 15u)) * 16u + (unsigned)(4096 - (i % 5 - 2) * 1024); PINV(dv[i]); }
    const char* src = ws + O_OA + (size_t)(128 * rg) * 1280 - 4096;
    const unsigned dm = lds_addr(lds) + (unsigned)((10 * wave + 2) * 1024);
#define A2_X(p_, lb_, i_) do { if ((i_) % 5 == 0) m0_set(dm + (lb_) + (unsigned)((i_) / 5 * 5120)); \
      switch ((i_) % 5) { case 0: glds_x<-2048>(p_, dv[i_]); break; case 1: glds_x<-1024>(p_, dv[i_]); break; case 2: glds_x<0>(p_, dv[i_]); break; case 3: glds_x<1024>(p_, dv[i_]); break; default: glds_x<2048>(p_, dv[i_]); break; } } while (0)
    const __amdgpu_buffer_rsrc_t rs_out = mkrs(a.out + (size_t)(128 * rg) * 1280 + 160 * cg);
    unsigned o2 = (unsigned)((m15 * 1280 + 32 * wave + 8 * mq) * 2), o5 = (unsigned)(((16 * (wave & 1) + m15) * 1280 + 128 + 16 * (wave >> 1) + 4 * mq) * 2); PINV(o2); PINV(o5);
    if (wave == 0) wait_ge<16>(cnt + CNT(C_AO + (rg >> 2)), 16u, tmo);
    BAR();
#pragma unroll
    for (int b = 0; b < 4; ++b) { const char* p_ = src + b * 40960; asm volatile("s_nop 4" : "+s"(p_));
#pragma unroll
      for (int i = 0; i < 10; ++i) A2_X(p_, (unsigned)(b * 40960), i); }
#pragma unroll 1
    for (int b = 0; b < 4; ++b) {
      if (b == 0) VMW(30); else if (b == 1) VMW(23); else if (b == 2) VMW(16); else VMW(9);
      BAR();
#pragma unroll
      for (int q = 0; q < 4; ++q) { xq[q] = lds_addr(lds) + (unsigned)((b & 3) * 40960 + m15 * 1280 + ((4 * q + mq) ^ m15) * 16); x5[q] = xq[q] + (unsigned)((wave & 1) * 20480); PINV(xq[q]); PINV(x5[q]); }
      f32x4 acc[5]; u32x4 xa[3][3];
#define A2_XF(n_, k_) LDS16(((n_) == 2 ? x5[(k_) & 3] : xq[(k_) & 3] + (n_) * 20480) + ((k_) >> 2) * 256)
#pragma unroll
      for (int n = 0; n < 6; ++n) xa[n / 3][n % 3] = A2_XF(n % 3, n / 3);
      SFOR(k, 20)
        SFOR(n, 5)
          mf_w<true>(acc[n], xa[k % 3][n >> 1], 12 * (k >> 2) + 4 * (n == 4 ? 2 : (n & 1)) + (k & 3), k == 0, W); SB();
          if (n < 3 && k + 2 < 20) { xa[(k + 2) % 3][n % 3] = A2_XF(n, k + 2); SB(); }
        SEND;
      SEND;
      asm volatile("s_nop 7" : "+v"(acc[0]), "+v"(acc[1]), "+v"(acc[2]), "+v"(acc[3]), "+v"(acc[4]));
#pragma unroll
      for (int rt = 0; rt < 2; ++rt) { const u32x4 w = {cvtpk(acc[2 * rt][0], acc[2 * rt][1]), cvtpk(acc[2 * rt][2], acc[2 * rt][3]), cvtpk(acc[2 * rt + 1][0], acc[2 * rt + 1][1]), cvtpk(acc[2 * rt + 1][2], acc[2 * rt + 1][3])};
        __builtin_amdgcn_raw_buffer_store_b128(w, rs_out, (int)o2, (32 * b + 16 * rt) * 2560, OAUX); asm volatile("s_nop 1" :: "v"(w) : "memory"); }
      __builtin_amdgcn_raw_buffer_store_b64((u32x2){cvtpk(acc[4][0], acc[4][1]), cvtpk(acc[4][2], acc[4][3])}, rs_out, (int)o5, 32 * b * 2560, OAUX);
    }
    VMW(0);
  }
}

#define CALN 6
__device__ __forceinline__ void calibrate(char* ws, const u32x2 tag, int* vote, const int wave) {
  {
    const int ln = lane_id();
#pragma unroll
    for (int i = 0; i < 15; ++i) st16(mkrs(ws + O_CNT), wave == 0 && i == 0 && ln < 32 ? 0x80000000u : (unsigned)(ln * 16), (unsigned)((4 * i + wave) * 1024), (u32x4){0u, 0u, 0u, 0u});
    VMW(0);
  }
  char* cal = ws + O_CAL + wave * 16384;
  unsigned best0 = ~0u, best1 = ~0u, acc = 0;
  const int lane = lane_id();
  for (int r = 0; r < CALN; ++r) {
#pragma unroll
    for (int hf = 0; hf < 2; ++hf) {
      const unsigned long long t0 = __builtin_amdgcn_s_memtime();
      if (lane == 0) acc += __hip_atomic_fetch_add((gu32*)(cal + hf * 8192 + (acc & 0u)), 1u, RLX_AGENT);
      asm volatile("s_waitcnt vmcnt(0)" ::: "memory");
      const unsigned d = (unsigned)(__builtin_amdgcn_s_memtime() - t0);
      if (hf == 0) best0 = d < best0 ? d : best0; else best1 = d < best1 ? d : best1;
    }
  }
  int dl = (int)best1 - (int)best0 + (int)(acc & 0u);
  dl = dl > 400 ? 400 : (dl < -400 ? -400 : dl);
  if (((unsigned)((size_t)cal >> 21) & 1u) != 0u) dl = -dl;
  if (lane == 0) vote[wave] = dl;
  __syncthreads();
  if (wave == 0 && lane == 0) send_tag((unsigned*)(ws + O_CNT), C_POL, tag, vote[0] + vote[1] + vote[2] + vote[3] < 0 ? 1u : 0u);
  __syncthreads();
}

#define XOFF 0
__global__ __launch_bounds__(256, 1) void k_mega(Args a) {
  extern __shared__ __attribute__((aligned(16))) unsigned char shm[];
  unsigned* const cnt = (unsigned*)(a.ws + O_CNT);
  const int wave = __builtin_amdgcn_readfirstlane(threadIdx.x >> 6);
  if (wave != 0) {
    u64 pc; unsigned off, len;
    asm volatile("s_getpc_b64 %0\n.Lcpf_mega:\n s_mov_b32 %1, .Lcpf_mega-_Z6k_mega4Args\n s_mov_b32 %2, _Z5k_endv-_Z6k_mega4Args" : "=s"(pc), "=s"(off), "=s"(len));
    u32x4 rc = mkdesc((const void*)(pc - off)), rw = mkdesc(a.ws + O_X1); rc.z = len; rw.z = (unsigned)(O_END - O_X1);
    asm volatile("s_nop 4\n buffer_load_dword a255, %0, %1, 0 offen\n buffer_load_dword a254, %2, %3, 0 offen"
                 :: "v"(((blockIdx.x >> 3) * 192u + threadIdx.x - 64u) * 64u), "s"(rc), "v"((threadIdx.x - 64u) * (unsigned)(2 * MB) + (blockIdx.x >> 3) * 2048u), "s"(rw) : "a254", "a255", "memory");
  }
  volatile LAS unsigned* sl = (volatile LAS unsigned*)shm;
  if (wave == 0) {
    const unsigned e = ld_word(cnt + CNT(C_EPOCH));
    const u32x2 tg = tag_of(e);
    unsigned r = (unsigned)__builtin_amdgcn_s_getreg((3 << 11) | 20) & 3u;
    if (blockIdx.x == 0) send_tag(cnt, C_ROT, tg, r); else r = wait_tag<1>(cnt, C_ROT, tg);
    sl[0] = r; sl[1] = e;
  }
  BAR();
  const unsigned epoch = (unsigned)__builtin_amdgcn_readfirstlane(sl[1]);
  const int slot = (int)uni(((blockIdx.x + sl[0]) & 7u) * 32u + (blockIdx.x >> 3));
  const u32x2 tag = tag_of(epoch);
  BAR();
  const int xq = (slot >> 5) & 3, kq = slot & 31;
#define ENTER LG0()
  const int h = xq * 8 + (kq & 7);
  if (slot < 128) {
    if (kq < 8) { ENTER; s_role(a, tag, shm, h, wave);
    } else if (kq < 16) { ENTER; z_role(a, tag, shm, h, wave); }
    else if (kq < 26) { ENTER; r_role(a, tag, shm, xq * 10 + kq - 16, wave); }
    else { if (slot == 26) calibrate(a.ws, tag, (int*)shm, wave); ENTER; if (!(XOFF & 1)) o_role(a, tag, shm, 6 * xq + kq - 26, wave); }
  } else {
    if (kq < 16) { ENTER; t_role(a, tag, shm, h, kq >> 3, wave); ENTER; if (!(XOFF & 4)) a2_role(a, shm, (int)uni((unsigned)(16 * xq + kq)), wave);
      __builtin_amdgcn_raw_buffer_store_b32(epoch + 1u, mkrs(cnt + CNT(C_EPOCH)), slot == 239 && wave == 0 && lane_id() == 0 ? 0 : (int)0x80000000u, 0, 16); }
    else if (kq < 24) { ENTER; w_role(a, tag, shm, h, wave); }
    else if (kq < 28) { ENTER; if (!(XOFF & 4)) a_role(a, tag, shm, xq * 4 + kq - 24, wave); }
    else { ENTER; if (!(XOFF & 2)) att_role(a, tag, shm, xq * 8 + (kq - 28) * 2 + (wave & 1), wave); }
  }
}

__global__ void k_end() {}
extern "C" void kernel_launch(void* const* d_in, const int* in_sizes, int n_in, void* d_out, int out_size, void* d_ws, size_t ws_size, hipStream_t stream) {
  Args a;
  a.Ax = (const u16*)d_in[0]; a.Awqkv = (const u16*)d_in[1]; a.Awo = (const u16*)d_in[2]; a.Bx = (const u16*)d_in[3]; a.mem0 = (const u16*)d_in[4];
  a.Wq = (const u16*)d_in[5]; a.Wk = (const u16*)d_in[6]; a.Wv = (const u16*)d_in[7]; a.Wo = (const u16*)d_in[8];
  a.out = (u16*)d_out;
  a.ws = (char*)(((size_t)d_ws + 2 * MB - 1) & ~(size_t)(2 * MB - 1));
  if ((size_t)(a.ws - (char*)d_ws) + O_END > ws_size) return;
  const int dd[16] = {DDV};
  for (int i = 0; i < 16; ++i) a.d[i] = dd[i];
  static_assert(CNT(C_TOTAL) * 4 == 15 * 4096, "");
  (void)hipFuncSetAttribute((const void*)k_mega, hipFuncAttributeMaxDynamicSharedMemorySize, 163840);
  k_mega<<<256, 256, 163840, stream>>>(a);
}
```

```cpp
#include <hip/hip_runtime.h>
#include <hip/hip_bf16.h>
#include <stdint.h>

typedef unsigned short u16;
typedef unsigned long long u64;
typedef __attribute__((ext_vector_type(4))) float f32x4;
typedef __attribute__((address_space(1))) u64 gu64;
typedef __attribute__((address_space(1))) unsigned gu32;
typedef unsigned u32x8 __attribute__((ext_vector_type(8)));
typedef unsigned u32x4 __attribute__((ext_vector_type(4)));
typedef unsigned u32x2 __attribute__((ext_vector_type(2)));
typedef __attribute__((ext_vector_type(4))) short bf16x4;
typedef __attribute__((ext_vector_type(8))) __bf16 bfv8;
typedef __attribute__((ext_vector_type(16))) __bf16 bfv16;
typedef __bf16 bf16x2_t __attribute__((ext_vector_type(2)));
typedef float f32x2_t __attribute__((ext_vector_type(2)));
typedef unsigned v2u_t __attribute__((ext_vector_type(2)));
#define RLX_AGENT __ATOMIC_RELAXED, __HIP_MEMORY_SCOPE_AGENT
#define LAS __attribute__((address_space(3)))

__device__ __forceinline__ float bf2f(u16 v) { return __uint_as_float(((unsigned)v) << 16); }
__device__ __forceinline__ u16 f2bf(float f) { return __builtin_bit_cast(u16, (__bf16)f); }
__device__ __forceinline__ float rbf(float f) { return bf2f(f2bf(f)); }
__device__ __forceinline__ unsigned cvtpk(float lo, float hi) { f32x2_t v = {lo, hi}; return __builtin_bit_cast(unsigned, __builtin_convertvector(v, bf16x2_t)); }
__device__ __forceinline__ __amdgpu_buffer_rsrc_t mkrs(const void* p) { return __builtin_amdgcn_make_buffer_rsrc((void*)p, 0, 0x7fffffff, 0x00020000); }
__device__ __forceinline__ u32x4 ld16(__amdgpu_buffer_rsrc_t rs, unsigned voff, unsigned soff) { return __builtin_amdgcn_raw_buffer_load_b128(rs, (int)voff, (int)soff, 16); }
#define OLA 0
__device__ __forceinline__ u32x4 ld16c(__amdgpu_buffer_rsrc_t rs, unsigned voff, unsigned soff) { return __builtin_amdgcn_raw_buffer_load_b128(rs, (int)voff, (int)soff, OLA); }
__device__ __forceinline__ u32x2 ld8(__amdgpu_buffer_rsrc_t rs, unsigned voff, unsigned soff) { return __builtin_amdgcn_raw_buffer_load_b64(rs, (int)voff, (int)soff, 16); }
__device__ __forceinline__ void st16(__amdgpu_buffer_rsrc_t rs, unsigned voff, unsigned soff, u32x4 w) { __builtin_amdgcn_raw_buffer_store_b128(w, rs, (int)voff, (int)soff, 16); asm volatile("s_nop 1" :: "v"(w) : "memory"); }
__device__ __forceinline__ void st8(__amdgpu_buffer_rsrc_t rs, unsigned voff, unsigned soff, unsigned lo, unsigned hi) { u32x2 w = {lo, hi}; __builtin_amdgcn_raw_buffer_store_b64(w, rs, (int)voff, (int)soff, 16); }
__device__ __forceinline__ bool full16(u32x4 v) { return ((v.x & v.y) != ~0u) & ((v.z & v.w) != ~0u); }
__device__ __forceinline__ float max16(float x) { v2u_t r = __builtin_amdgcn_permlane16_swap(__float_as_uint(x), __float_as_uint(x), false, false); return fmaxf(__uint_as_float(r[0]), __uint_as_float(r[1])); }
__device__ __forceinline__ float max32(float x) { v2u_t r = __builtin_amdgcn_permlane32_swap(__float_as_uint(x), __float_as_uint(x), false, false); return fmaxf(__uint_as_float(r[0]), __uint_as_float(r[1])); }
__device__ __forceinline__ float sum16(float x) { v2u_t r = __builtin_amdgcn_permlane16_swap(__float_as_uint(x), __float_as_uint(x), false, false); return __uint_as_float(r[0]) + __uint_as_float(r[1]); }
__device__ __forceinline__ float sum32(float x) { v2u_t r = __builtin_amdgcn_permlane32_swap(__float_as_uint(x), __float_as_uint(x), false, false); return __uint_as_float(r[0]) + __uint_as_float(r[1]); }
__device__ __forceinline__ float tred32(float a, float b) { v2u_t r = __builtin_amdgcn_permlane32_swap(__float_as_uint(a), __float_as_uint(b), false, false); return __uint_as_float(r[0]) + __uint_as_float(r[1]); }
__device__ __forceinline__ float tred16(float a, float b) { v2u_t r = __builtin_amdgcn_permlane16_swap(__float_as_uint(a), __float_as_uint(b), false, false); return __uint_as_float(r[0]) + __uint_as_float(r[1]); }
__device__ __forceinline__ unsigned ror8(unsigned x) { return (unsigned)__builtin_amdgcn_mov_dpp((int)x, 0x128, 0xf, 0xf, true); }
template <int N> __device__ __forceinline__ void nap() { if constexpr (N > 0) { if constexpr (N > 127) { __builtin_amdgcn_s_sleep(127); nap<N - 127>(); } else __builtin_amdgcn_s_sleep(N); } }
__device__ __forceinline__ unsigned half_of(const char* base, unsigned off, unsigned side, unsigned pol) {
  return off + ((((unsigned)(((size_t)base + off) >> 21) ^ side ^ pol) & 1u) << 13);
}
__device__ __forceinline__ unsigned side_map(const void* base, unsigned L, unsigned side, unsigned pol) { return half_of((const char*)base, (L >> 13) << 14, side, pol) + (L & 8191u); }
constexpr unsigned CH_SPIN_CAP = 1u << 18;
__device__ __forceinline__ bool spin_check(unsigned& spins, unsigned* tmop) {
  asm volatile("" ::: "memory");
  ++spins;
  if ((spins & 63u) == 0u) {
    gu32* tmo = (gu32*)tmop;
    if (__builtin_amdgcn_readfirstlane(__hip_atomic_load(tmo, RLX_AGENT))) return false;
    if (spins > CH_SPIN_CAP) { __hip_atomic_store(tmo, 1u, RLX_AGENT); return false; }
  }
  return true;
}
__device__ __forceinline__ unsigned xcc_side() { return ((unsigned)__builtin_amdgcn_s_getreg((3 << 11) | 20) >> 2) & 1u; }
__device__ __forceinline__ f32x4 mfma(u32x4 a, u32x4 b, f32x4 c) { return __builtin_amdgcn_mfma_f32_16x16x32_bf16(__builtin_bit_cast(bfv8, a), __builtin_bit_cast(bfv8, b), c, 0, 0, 0); }
__device__ __forceinline__ f32x4 smf(u32x4 a, u32x8 b, f32x4 c, int ix) { return __builtin_amdgcn_smfmac_f32_16x16x64_bf16(__builtin_bit_cast(bfv8, a), __builtin_bit_cast(bfv16, b), c, ix, 0, 0); }
__device__ __forceinline__ u32x4 lo4(u32x8 v) { return __builtin_shufflevector(v, v, 0, 1, 2, 3); }
__device__ __forceinline__ u32x4 hi4(u32x8 v) { return __builtin_shufflevector(v, v, 4, 5, 6, 7); }
__device__ __forceinline__ u32x8 cat8(u32x4 a, u32x4 b) { return __builtin_shufflevector(a, b, 0, 1, 2, 3, 4, 5, 6, 7); }
#define BAR() do { asm volatile("s_waitcnt lgkmcnt(0)" ::: "memory"); __builtin_amdgcn_s_barrier(); asm volatile("" ::: "memory"); } while (0)
#define SB() __builtin_amdgcn_sched_barrier(0)
#define VMW(n) asm volatile("s_waitcnt vmcnt(" #n ")" ::: "memory")
#define ZERO4 ((f32x4){0.f, 0.f, 0.f, 0.f})
__device__ __forceinline__ u32x2 trrd(const unsigned char* lds, unsigned off) { return __builtin_bit_cast(u32x2, __builtin_amdgcn_ds_read_tr16_b64_v4i16((LAS bf16x4*)(lds + off))); }
__device__ __forceinline__ u32x4 trrd2(const unsigned char* lds, unsigned off, unsigned d) { const u32x2 lo = trrd(lds, off), hi = trrd(lds, off + d); return (u32x4){lo.x, lo.y, hi.x, hi.y}; }


#include <stdlib.h>
#define SAG 16
#define DDV 81,81,13,0,30,0,127,40,90,110,25,30,20,12,20,345
#define FINE 1
#define WAIT_GO wait_fine
#define XLN 4
#define XLS 2
struct Args {
  const u16* mem0; const u16* Wq; const u16* Wk; const u16* Wv; const u16* Wo; const u16* Bx; const u16* Ax; const u16* Awqkv; const u16* Awo;
  u16* out; char* ws;
  int d[16];
};
constexpr size_t MB = 1u << 20;
#define WOFF 0
#define NK 16
#define NU 16
#define NZ 16
#define NP 16
#define CSIDE 0u
#define KSIDE 1
constexpr unsigned X1_STEP = 32768, Z_STEP = 1u << 20, K_STEP = 1u << 20;
constexpr size_t O_PROF = 0, O_CNT = (WOFF + 1) * MB, O_CAL = O_CNT + 131072, O_TS = O_CAL + 65536, O_X1 = (WOFF + 2) * MB, O_PS = O_X1 + 1 * MB, O_KM = O_PS + 1 * MB, O_PT = O_KM + 2 * MB, O_OA = O_PT + NZ * 81920,
                 O_KF = O_PT + 4 * MB, O_QF = O_KF + NK * (size_t)K_STEP, O_VF = O_QF + NK * (size_t)K_STEP, O_ZB = KSIDE == 1 ? O_KF : O_VF + NK * MB, O_UO = O_VF + (KSIDE == 1 ? NK : NK + NZ) * MB, O_END = O_UO + NU * 40 * (size_t)32768;
static_assert(O_ZB % (2 * MB) == 0 && NP <= 16 && NZ <= 16 && NZ <= NK && NK % 4 == 0 && O_OA + 2048 * 1280 <= O_KF, "");
#define MSLOT(s_) (((unsigned)(s_) & (NZ - 1u)) * 98304u)
#define TSLOT(s_) (((unsigned)(s_) & (NZ - 1u)) * 81920u)
__device__ __forceinline__ unsigned k_fix(const unsigned o, const unsigned key) { return o ^ ((o >> 8) & 0x2000u) ^ key; }
#define K_KEY() ((((unsigned)((size_t)(ws + O_KF) >> 21) ^ (KSIDE == 2 ? (unsigned)h : KSIDE ^ pol)) & 1u) << 13)
#define KSLOT(s_) k_fix(((unsigned)(s_) & (NK - 1u)) * K_STEP, kkey)
#define ZSLOT(s_) (((unsigned)(s_) & (NZ - 1u)) * Z_STEP)
#define XSLOT(s_) (((unsigned)(s_) & (NP - 1u)) * X1_STEP)
#define CNT(i) (32 * (i))
__device__ __forceinline__ unsigned u_off(const unsigned s, const unsigned c) { return ((s & (NU - 1u)) * 40u + c) << 15; }
constexpr int PRE = 4;
#define WGATE 10
static_assert(WGATE + 3 <= NK && WGATE <= NU && WGATE + PRE + 2 <= NZ && NP >= PRE + 4, "");
constexpr unsigned NFILL = 40 + 64;
constexpr unsigned SENT = 0xFF81FF81u;
#define SENT4 ((u32x4){SENT, SENT, SENT, SENT})
#define PSLOT(s_) (((s_) & (NP - 1)) * 65536)
constexpr int C_EPOCH = 0, C_ROT = 1, C_POL = 2, C_GO = 3, C_TMO = 8, C_FILL = 10, C_AO = 12;
constexpr int C_PW = 128, C_PZ = 160, C_PT_ = 256, C_PS = 288, C_TOK = 384, C_PV = 128, C_PO = 160, C_TOTAL = 896;
#define TOKL(s_) (C_TOK + ((s_) >> 4) * 128 + ((s_) & 15) * 4)
#define SIDES() unsigned* const cn = cnt + (half_of(ws + O_CNT, 0u, CSIDE, pol) >> 2); unsigned* const cf = cnt + (half_of(ws + O_CNT, 0u, 1u - CSIDE, pol) >> 2); (void)cn; (void)cf
__device__ __forceinline__ float nopk(float x) { asm("" : "+v"(x)); return x; }
__device__ __forceinline__ float fmx(float a, float b) { float big = __builtin_inff(); asm("" : "+s"(big)); return __builtin_amdgcn_fmed3f(a, b, big); }
__device__ __forceinline__ float mx16(float x) { v2u_t r = __builtin_amdgcn_permlane16_swap(__float_as_uint(x), __float_as_uint(x), false, false); return fmx(__uint_as_float(r[0]), __uint_as_float(r[1])); }
__device__ __forceinline__ float mx32(float x) { v2u_t r = __builtin_amdgcn_permlane32_swap(__float_as_uint(x), __float_as_uint(x), false, false); return fmx(__uint_as_float(r[0]), __uint_as_float(r[1])); }
__device__ __forceinline__ int lane_id() { int l; asm volatile("v_mbcnt_lo_u32_b32 %0, -1, 0\n v_mbcnt_hi_u32_b32 %0, -1, %0" : "=v"(l)); return l; }
__device__ __forceinline__ unsigned sent_min(unsigned m, unsigned x, unsigned y) { unsigned r = min(min(m, x ^ SENT), y ^ SENT); asm("" : "+v"(r)); return r; }
__device__ __forceinline__ unsigned sent_min(unsigned m, u32x4 v) { return sent_min(sent_min(m, v.x, v.y), v.z, v.w); }
#define RT_MARK(t_) asm volatile("s_memrealtime %0\n s_waitcnt lgkmcnt(0)" : "=s"(t_) :: "memory")
__device__ __forceinline__ unsigned wait_rt(unsigned target) {
  unsigned t;
  target = (unsigned)__builtin_amdgcn_readfirstlane(target);
  asm volatile("s_branch .Lwb%=\n.Lwa%=:\n s_sleep 1\n.Lwb%=:\n s_memrealtime vcc\n s_waitcnt lgkmcnt(0)\n s_sub_i32 %0, %1, vcc_lo\n s_add_i32 vcc_hi, %0, -9\n s_cmp_lt_u32 vcc_hi, 392\n s_cbranch_scc1 .Lwa%=\n"
               " s_add_i32 %0, %0, -1\n s_cmp_lt_u32 %0, 8\n s_cbranch_scc1 .Lwb%=\n s_mov_b32 %0, vcc_lo" : "=&s"(t) : "s"(target) : "vcc", "scc", "memory");
  return t;
}
#define FINE0 150
__device__ __forceinline__ unsigned wait_fine(unsigned target) {
  unsigned t;
  target = (unsigned)__builtin_amdgcn_readfirstlane(target);
  asm volatile("s_branch .Lfb%=\n.Lfa%=:\n s_sleep 1\n.Lfb%=:\n s_memrealtime vcc\n s_waitcnt lgkmcnt(0)\n s_sub_i32 %0, %1, vcc_lo\n s_add_i32 vcc_hi, %0, -23\n s_cmp_lt_u32 vcc_hi, 378\n s_cbranch_scc1 .Lfa%=\n"
               " s_add_i32 vcc_hi, %0, -5\n s_cmp_lt_u32 vcc_hi, 18\n s_cselect_b32 %0, %1, vcc_lo\n s_cbranch_scc0 .Lfe%=\n"
               " s_memrealtime s[92:93]\n s_nop 4\n s_memrealtime s[94:95]\n s_nop 4\n s_memrealtime s[96:97]\n s_nop 4\n s_memrealtime s[98:99]\n s_nop 4\n s_memrealtime vcc\n s_waitcnt lgkmcnt(0)\n"
               " s_sub_i32 s93, %1, vcc_lo\n s_mul_i32 s93, s93, 23\n s_add_i32 s93, s93, %2\n s_mov_b32 s95, 0\n"
               " s_cmp_eq_u32 s92, vcc_lo\n s_addc_u32 s95, s95, 0\n s_cmp_eq_u32 s94, vcc_lo\n s_addc_u32 s95, s95, 0\n s_cmp_eq_u32 s96, vcc_lo\n s_addc_u32 s95, s95, 0\n s_cmp_eq_u32 s98, vcc_lo\n s_addc_u32 s95, s95, 0\n"
               " s_mul_i32 s95, s95, 24\n s_sub_i32 s93, s93, s95\n s_max_i32 s93, s93, 0\n s_min_i32 s93, s93, 383\n s_lshr_b32 s93, s93, 3\n s_lshl_b32 s93, s93, 2\n"
               " s_getpc_b64 s[96:97]\n.Lfp%=:\n s_sub_u32 s93, .Lfe%=-.Lfp%=, s93\n s_add_u32 s96, s96, s93\n s_addc_u32 s97, s97, 0\n s_setpc_b64 s[96:97]\n .rept 48\n s_nop 1\n .endr\n.Lfe%=:"
               : "=&s"(t) : "s"(target), "n"(-FINE0) : "vcc", "scc", "s92", "s93", "s94", "s95", "s96", "s97", "s98", "s99", "memory");
  return t;
}
__device__ __forceinline__ unsigned ts_max(unsigned v) {
  if ((unsigned)__ballot(v == ~0u)) return ~0u;
  const unsigned base = (unsigned)__builtin_amdgcn_readfirstlane(v) - 4096u;
  unsigned d = v == ~0u ? 0u : v - base;
  d = max(d, (unsigned)__builtin_amdgcn_mov_dpp((int)d, 0x121, 0xf, 0xf, true)); d = max(d, (unsigned)__builtin_amdgcn_mov_dpp((int)d, 0x122, 0xf, 0xf, true));
  d = max(d, (unsigned)__builtin_amdgcn_mov_dpp((int)d, 0x124, 0xf, 0xf, true)); d = max(d, (unsigned)__builtin_amdgcn_mov_dpp((int)d, 0x128, 0xf, 0xf, true));
  return (base + max(max((unsigned)__builtin_amdgcn_readlane(d, 0), (unsigned)__builtin_amdgcn_readlane(d, 16)), max((unsigned)__builtin_amdgcn_readlane(d, 32), (unsigned)__builtin_amdgcn_readlane(d, 48))));
}
#define TS_OFF(l_) (((l_) & 31) * 256 + ((l_) >> 5) * 16384)
#define TS_STEP(s_) (((s_) & 1) * 128 + ((s_) >> 1) * 4)
__device__ __forceinline__ unsigned ts_settle(const __amdgpu_buffer_rsrc_t rs_ts, const int tss, unsigned t, const int lead) {
  for (int n = 0; n < 48; ++n) {
    unsigned now;
    if (t == ~0u) { __builtin_amdgcn_s_sleep(4); u64 t_; RT_MARK(t_); now = (unsigned)t_; } else now = wait_rt(t - (unsigned)lead);
    t = ts_max(__builtin_amdgcn_raw_buffer_load_b32(rs_ts, TS_OFF(lane_id()), tss, 16));
    if (t != ~0u && (int)(t - now) <= lead + 4) break;
  }
  return t;
}
#define ADAPT(d_, failed_) do { if (s > 3) d_ += (failed_) ? a.d[5] : -(int)((s & a.d[6]) == 0); } while (0)
__device__ __forceinline__ void nap_rt(int n) { for (int i = 0; i < n; ++i) __builtin_amdgcn_s_sleep(1); }
#define PINV(x) asm volatile("" : "+v"(x))
#define PINS(x) asm volatile("" : "+s"(x))
#define PINU(x) asm volatile("" :: "v"(x))
#define IL(nm, nv) do { _Pragma("unroll") for (int il_ = 0; il_ < (nm); ++il_) { __builtin_amdgcn_sched_group_barrier(0x008, 1, 0); __builtin_amdgcn_sched_group_barrier(0x7f6, nv, 0); } } while (0)
constexpr int L_MEM = 0, L_QI = 12288;
constexpr int L_B0 = 16384, L_B1 = 65536, L_B2 = 114688;
constexpr int L_QA = L_B1, L_XV = L_B1 + 4608, L_FLG = L_B1 + 5632, L_XR = L_B1 + 6144;

#define TR8_ASM "ds_read_b64_tr_b16 %0, %8\n ds_read_b64_tr_b16 %1, %8 offset:1536\n ds_read_b64_tr_b16 %2, %8 offset:12288\n ds_read_b64_tr_b16 %3, %8 offset:13824\n" \
               "ds_read_b64_tr_b16 %4, %8 offset:24576\n ds_read_b64_tr_b16 %5, %8 offset:26112\n ds_read_b64_tr_b16 %6, %8 offset:36864\n ds_read_b64_tr_b16 %7, %8 offset:38400\n s_waitcnt lgkmcnt(0)"
__device__ __forceinline__ void tr8(const bool agpr, const unsigned ad, u32x8& f0, u32x8& f1) {
  u32x2 a, b, c, d, e, f, g, h;
  if (agpr) asm volatile(TR8_ASM : "=&a"(a), "=&a"(b), "=&a"(c), "=&a"(d), "=&a"(e), "=&a"(f), "=&a"(g), "=&a"(h) : "v"(ad) : "memory");
  else asm volatile(TR8_ASM : "=&v"(a), "=&v"(b), "=&v"(c), "=&v"(d), "=&v"(e), "=&v"(f), "=&v"(g), "=&v"(h) : "v"(ad) : "memory");
  f0 = (u32x8){a.x, a.y, b.x, b.y, c.x, c.y, d.x, d.y}; f1 = (u32x8){e.x, e.y, f.x, f.y, g.x, g.y, h.x, h.y};
}
__device__ __forceinline__ void tr8d(const bool agpr, const unsigned ad, u32x4& f0, u32x4& f1, u32x4& f2, u32x4& f3) {
  u32x2 a, b, c, d, e, f, g, h;
  if (agpr) asm volatile(TR8_ASM : "=&a"(a), "=&a"(b), "=&a"(c), "=&a"(d), "=&a"(e), "=&a"(f), "=&a"(g), "=&a"(h) : "v"(ad) : "memory");
  else asm volatile(TR8_ASM : "=&v"(a), "=&v"(b), "=&v"(c), "=&v"(d), "=&v"(e), "=&v"(f), "=&v"(g), "=&v"(h) : "v"(ad) : "memory");
  f0 = (u32x4){a.x, a.y, b.x, b.y}; f1 = (u32x4){c.x, c.y, d.x, d.y}; f2 = (u32x4){e.x, e.y, f.x, f.y}; f3 = (u32x4){g.x, g.y, h.x, h.y};
}
#define TR4_ASM "ds_read_b64_tr_b16 %0, %4 offset:%5\n ds_read_b64_tr_b16 %1, %4 offset:%6\n ds_read_b64_tr_b16 %2, %4 offset:%7\n ds_read_b64_tr_b16 %3, %4 offset:%8\n s_waitcnt lgkmcnt(0)"
template <int J> __device__ __forceinline__ void tr4(const bool agpr, const unsigned ad, u32x8& f) {
  u32x2 a, b, c, d;
  if (agpr) asm volatile(TR4_ASM : "=&a"(a), "=&a"(b), "=&a"(c), "=&a"(d) : "v"(ad), "n"(16384 * J), "n"(16384 * J + 4096), "n"(16384 * J + 8192), "n"(16384 * J + 12288) : "memory");
  else asm volatile(TR4_ASM : "=&v"(a), "=&v"(b), "=&v"(c), "=&v"(d) : "v"(ad), "n"(16384 * J), "n"(16384 * J + 4096), "n"(16384 * J + 8192), "n"(16384 * J + 12288) : "memory");
  f = (u32x8){a.x, a.y, b.x, b.y, c.x, c.y, d.x, d.y};
}
template <int J> __device__ __forceinline__ void tr4d(const bool agpr, const unsigned ad, u32x4& f0, u32x4& f1) {
  u32x2 a, b, c, d;
  if (agpr) asm volatile(TR4_ASM : "=&a"(a), "=&a"(b), "=&a"(c), "=&a"(d) : "v"(ad), "n"(16384 * J), "n"(16384 * J + 4096), "n"(16384 * J + 8192), "n"(16384 * J + 12288) : "memory");
  else asm volatile(TR4_ASM : "=&v"(a), "=&v"(b), "=&v"(c), "=&v"(d) : "v"(ad), "n"(16384 * J), "n"(16384 * J + 4096), "n"(16384 * J + 8192), "n"(16384 * J + 12288) : "memory");
  f0 = (u32x4){a.x, a.y, b.x, b.y}; f1 = (u32x4){c.x, c.y, d.x, d.y};
}
__device__ __forceinline__ void glds(const void* sbase, unsigned voff, unsigned ldsdst) {
  unsigned keep;
  asm volatile("s_mov_b32 %0, m0\n s_mov_b32 m0, %3\n s_nop 2\n global_load_lds_dwordx4 %1, %2\n s_mov_b32 m0, %0" : "=&s"(keep) : "v"(voff), "s"(sbase), "s"(ldsdst) : "memory");
}
__device__ __forceinline__ void m0_set(unsigned ldsdst) { asm volatile("s_mov_b32 m0, %0\n s_nop 0" :: "s"(ldsdst) : "memory"); }
template <int OFF> __device__ __forceinline__ void glds_o(const void* sbase, unsigned voff) { asm volatile("global_load_lds_dwordx4 %0, %1 offset:%2" :: "v"(voff), "s"(sbase), "n"(OFF) : "memory"); }
template <int OFF> __device__ __forceinline__ void glds_x(const void* sbase, unsigned voff) { asm volatile("global_load_lds_dwordx4 %0, %1 offset:%2 sc1" :: "v"(voff), "s"(sbase), "n"(OFF) : "memory"); }
__device__ __forceinline__ unsigned lds_addr(const unsigned char* p) { return (unsigned)(size_t)(const LAS unsigned char*)p; }
__device__ __forceinline__ unsigned ld_word(const unsigned* p) { return (unsigned)__builtin_amdgcn_readfirstlane(__hip_atomic_load((const gu32*)p, RLX_AGENT)); }
__device__ __forceinline__ unsigned uni(unsigned v) { asm volatile("s_nop 0" : "+v"(v)); return (unsigned)__builtin_amdgcn_readfirstlane(v); }
__device__ __forceinline__ u32x2 tag_of(const unsigned e) { return (u32x2){(e * 0x9E3779B1u + 0x7F4A7C15u) & ~7u, (e ^ 0x5BD1E995u) * 0x85EBCA6Bu + 1u}; }
__device__ __forceinline__ void send_tag(unsigned* cnt, const int line, const u32x2 tag, const unsigned v) { __hip_atomic_store((gu64*)(cnt + CNT(line)), ((u64)tag.y << 32) | (tag.x | v), RLX_AGENT); }
template <int SLEEP>
__device__ __forceinline__ unsigned wait_tag(const unsigned* cnt, const int line, const u32x2 tag) {
  unsigned lo = 0;
#pragma unroll 1
  for (unsigned n = 0; n < CH_SPIN_CAP; ++n) {
    const u64 w = __hip_atomic_load((const gu64*)(cnt + CNT(line)), RLX_AGENT);
    lo = (unsigned)__builtin_amdgcn_readfirstlane((unsigned)w);
    if ((lo & ~7u) == tag.x && (unsigned)__builtin_amdgcn_readfirstlane((unsigned)(w >> 32)) == tag.y) break;
    __builtin_amdgcn_s_sleep(SLEEP);
  }
  asm volatile("" ::: "memory");
  return uni(lo & 7u);
}
__device__ __forceinline__ void fill_done(unsigned* cnt, const u32x2 tag, const unsigned pol) {
  if (lane_id() == 0) { if (__hip_atomic_fetch_add((gu32*)(cnt + CNT(C_FILL)), 1u, RLX_AGENT) == NFILL - 1u) send_tag(cnt, C_GO, tag, pol); }
}
template <int SLEEP>
__device__ __forceinline__ unsigned wait_ge(const unsigned* p, unsigned target, unsigned* tmo) {
  unsigned spins = 0, v;
  while ((v = ld_word(p)) < target) { __builtin_amdgcn_s_sleep(SLEEP); if (!spin_check(spins, tmo)) break; }
  asm volatile("" ::: "memory");
  return v;
}
__device__ __forceinline__ unsigned min_t(const u32x4 w, const int kv) {
  unsigned v = min(min(w.x, w.y), w.z); if (!kv) v = min(v, w.w);
  return (unsigned)__builtin_amdgcn_readfirstlane(v);
}
template <int SLEEP>
__device__ __forceinline__ unsigned wait_t(const unsigned* p, const int first, unsigned target, unsigned* tmo) {
  unsigned spins = 0, v;
  for (;;) {
    v = min_t(ld16(mkrs(p), 0u, 0u), first);
    if (v >= target) break;
    __builtin_amdgcn_s_sleep(SLEEP); if (!spin_check(spins, tmo)) break;
  }
  asm volatile("" ::: "memory");
  return v;
}
template <int SLEEP>
__device__ __forceinline__ void wait_heads(const unsigned* p, const int second, unsigned target, unsigned* tmo) {
  unsigned spins = 0; const int ln = lane_id();
  while (!__all(__builtin_amdgcn_raw_buffer_load_b32(mkrs(p), (ln & 31) * 128 + (ln >> 5) * second * 4, 0, 16) >= target)) { __builtin_amdgcn_s_sleep(SLEEP); if (!spin_check(spins, tmo)) break; }
  asm volatile("" ::: "memory");
}
#define WNAP 12
template <int N> __device__ __forceinline__ void nap4() { if constexpr (N > 16) { asm volatile("s_nop 15"); nap4<N - 16>(); } else if constexpr (N > 0) asm volatile("s_nop %0" :: "n"(N - 1)); }
#define DPPM(d_, s_, ctl_) asm volatile("v_max_f32_dpp %0, %1, %1 " ctl_ " row_mask:0xf bank_mask:0xf" : "=v"(d_) : "v"(s_))
#define XOR1 "quad_perm:[1,0,3,2]"
#define XOR2 "quad_perm:[2,3,0,1]"
__device__ __forceinline__ void light(const __amdgpu_buffer_rsrc_t rs, unsigned vo, const unsigned so, unsigned* tmo, const int slow = 0) {
  unsigned spins = 0;
  for (;;) {
    const bool empty = __builtin_amdgcn_raw_buffer_load_b32(rs, (int)vo, (int)so, 16) == SENT;
    if (!__any(empty) || !spin_check(spins, tmo)) break;
    vo = empty ? vo : 0x80000000u;
    if (slow) __builtin_amdgcn_s_sleep(16); else __builtin_amdgcn_s_sleep(XLS);
  }
}
__device__ __forceinline__ void x1_light(const __amdgpu_buffer_rsrc_t rs_x1, const unsigned so, unsigned* tmo) {
  const unsigned ln = (unsigned)lane_id();
  light(rs_x1, ln < 40u ? (ln >> 5) * 16384u + (ln & 31u) * 256u + 252u : 0x80000000u, so, tmo);
}
__device__ __forceinline__ unsigned touch(const void* p, const int bytes, const int piece) { return __builtin_amdgcn_raw_buffer_load_b32(__builtin_amdgcn_make_buffer_rsrc((void*)p, 0, bytes, 0x00020000), lane_id() * 128, piece * 8192, 0); }
__device__ __forceinline__ unsigned touch_rows(const Args& a, const int which, const int piece) { return touch(which == 0 ? (const void*)a.Bx : (which == 1 ? (const void*)a.Ax : (const void*)(a.ws + O_OA)), 2048 * 1280, piece); }
#define PFK 0
#define NOROT 0
__device__ __forceinline__ void touch_w(const Args& a, const int w) {
  unsigned v = 0;
#pragma unroll
  for (int i = 0; i < 14; ++i) {
    const int n = w + 280 * i, m = n < 2880 ? n % 3 : 3, pc = n < 2880 ? n / 3 : n - 2880;
    v ^= touch(m == 0 ? (const void*)a.Wq : (m == 1 ? (const void*)a.Wk : (m == 2 ? (const void*)a.Wv : (const void*)a.Wo)), 640 * 12288, pc);
  }
  asm volatile("" :: "v"(v));
}
#define TM 0
template <int N> __device__ __forceinline__ void touch_n(const Args& a, const int w, const int first, const int step) {
  unsigned v = 0;
#pragma unroll
  for (int i = 0; i < N; ++i) v ^= touch_rows(a, w % 3, first + w / 3 + step * i);
  asm volatile("" :: "v"(v));
}
#define DS(x) do { x; SB(); } while (0)
template <int V> struct IC { static constexpr int v = V; };
template <int I, int N, class F> __device__ __forceinline__ void sfor(F&& f) { if constexpr (I < N) { f(IC<I>{}); sfor<I + 1, N>(f); } }
#define SFOR(i_, n_) sfor<0, n_>([&](auto i_##_c) __attribute__((always_inline)) { constexpr int i_ = decltype(i_##_c)::v;
#define SEND })
typedef unsigned u32x32 __attribute__((ext_vector_type(32)));
typedef unsigned u32x16 __attribute__((ext_vector_type(16)));
struct WReg { u32x32 b[11]; u32x16 e; };
#define WR_SW(f_, X) switch ((f_) >> 2) { case 0: X("a", "{a[0:31]}", W.b[0]); break; case 1: X("a", "{a[32:63]}", W.b[1]); break; case 2: X("a", "{a[64:95]}", W.b[2]); break; case 3: X("a", "{a[96:127]}", W.b[3]); break; \
    case 4: X("a", "{a[128:159]}", W.b[4]); break; case 5: X("a", "{a[160:191]}", W.b[5]); break; case 6: X("a", "{a[192:223]}", W.b[6]); break; case 7: X("a", "{a[224:255]}", W.b[7]); break; \
    case 8: X("v", "{v[142:173]}", W.b[8]); break; case 9: X("v", "{v[174:205]}", W.b[9]); break; case 10: X("v", "{v[206:237]}", W.b[10]); break; default: X("v", "{v[238:253]}", W.e); break; }
__device__ __forceinline__ void smf_w(f32x4& c, const u32x4 a, const int f, const int ix, const WReg& W) {
  const int r = f < 32 ? 8 * f : 8 * f - 114;
#define X_(rf_, cs_, v_) asm volatile("v_smfmac_f32_16x16x64_bf16 %0, %1, " rf_ "[%c3:%c4], %2" : "+v"(c) : "v"(a), "v"(ix), "n"(r), "n"(r + 7), cs_(v_))
  WR_SW(f, X_);
#undef X_
}
template <bool WA> __device__ __forceinline__ void mf_w(f32x4& c, const u32x4 x, const int d, const bool first, const WReg& W) {
  const int r = d < 64 ? 4 * d : 4 * d - 114;
#define X_(rf_, cs_, v_) if (WA) { if (first) asm volatile("v_mfma_f32_16x16x32_bf16 %0, " rf_ "[%c2:%c3], %1, 0" : "=&v"(c) : "v"(x), "n"(r), "n"(r + 3), cs_(v_)); \
      else asm volatile("v_mfma_f32_16x16x32_bf16 %0, " rf_ "[%c2:%c3], %1, %0" : "+v"(c) : "v"(x), "n"(r), "n"(r + 3), cs_(v_)); } \
    else { if (first) asm volatile("v_mfma_f32_16x16x32_bf16 %0, %1, " rf_ "[%c2:%c3], 0" : "=&v"(c) : "v"(x), "n"(r), "n"(r + 3), cs_(v_)); \
      else asm volatile("v_mfma_f32_16x16x32_bf16 %0, %1, " rf_ "[%c2:%c3], %0" : "+v"(c) : "v"(x), "n"(r), "n"(r + 3), cs_(v_)); }
  WR_SW(d >> 1, X_);
#undef X_
}
__device__ __forceinline__ void ld_w(const u32x4 rs, const unsigned vo, const unsigned so, const int off, const int f, const bool def, WReg& W) {
  const int r = f < 32 ? 8 * f : 8 * f - 114;
#define Y_(md_, rf_, cs_, v_) asm volatile("s_nop 4\n buffer_load_dwordx4 " rf_ "[%c6:%c7], %1, %2, %3 offen offset:%4 sc1\n buffer_load_dwordx4 " rf_ "[%c8:%c9], %1, %2, %3 offen offset:%5 sc1" \
    : md_ cs_(v_) : "v"(vo), "s"(rs), "s"(so), "n"(off), "n"(off + 1024), "n"(r), "n"(r + 3), "n"(r + 4), "n"(r + 7) : "memory")
#define X_(rf_, cs_, v_) if (def) Y_("=&", rf_, cs_, v_); else Y_("+", rf_, cs_, v_)
  WR_SW(f, X_);
#undef X_
#undef Y_
}
__device__ __forceinline__ u32x4 mkdesc(const void* p) { const size_t b = (size_t)p; return (u32x4){(unsigned)b, (unsigned)(b >> 32) & 0xffffu, 0x7fffffffu, 0x00020000u}; }
__device__ __forceinline__ void tr4_w(const unsigned ad, const int o0, const int o1, const int o2, const int o3, const int f, WReg& W) {
  const int r = f < 32 ? 8 * f : 8 * f - 114;
#define Y_(md_, rf_, cs_, v_) asm volatile("ds_read_b64_tr_b16 " rf_ "[%c6:%c7], %1 offset:%2\n ds_read_b64_tr_b16 " rf_ "[%c8:%c9], %1 offset:%3\n ds_read_b64_tr_b16 " rf_ "[%c10:%c11], %1 offset:%4\n ds_read_b64_tr_b16 " rf_ "[%c12:%c13], %1 offset:%5\n s_waitcnt lgkmcnt(0)" \
    : md_ cs_(v_) : "v"(ad), "n"(o0), "n"(o1), "n"(o2), "n"(o3), "n"(r), "n"(r + 1), "n"(r + 2), "n"(r + 3), "n"(r + 4), "n"(r + 5), "n"(r + 6), "n"(r + 7) : "memory")
#define X_(rf_, cs_, v_) if ((f & 3) == 0) Y_("=&", rf_, cs_, v_); else Y_("+", rf_, cs_, v_)
  WR_SW(f, X_);
#undef X_
#undef Y_
}
#define TRS0(ad_, f_) tr4_w(ad_, 0, 1536, 12288, 13824, f_, W)
#define TRS1(ad_, f_) tr4_w(ad_, 24576, 26112, 36864, 38400, f_, W)
#define TRO4(ad_, j_, f_) tr4_w(ad_, 16384 * (j_), 16384 * (j_) + 4096, 16384 * (j_) + 8192, 16384 * (j_) + 12288, f_, W)
#define SMFV(c_, a_, b_) do { asm volatile("v_smfmac_f32_16x16x64_bf16 %0, %1, %2, %3" : "+v"(c_) : "v"(a_), "v"(b_), "v"(ix)); SB(); } while (0)
#define SMFW(c_, a_, f_) do { smf_w(c_, a_, f_, ix, W); SB(); } while (0)
#define ADD1(d_, x_, y_) do { d_ = (x_) + (y_); PINU(d_); SB(); } while (0)
#define ADD1B(d_, x_, y_) do { d_ = __builtin_fmaf(x_, one, y_); PINU(d_); SB(); } while (0)
#define ADD2(d0_, d1_, c_) do { asm volatile("v_add_f32 %0, %2, %3\n v_add_f32 %1, %4, %5" : "=&v"(d0_), "=v"(d1_) : "v"(c_[0]), "v"(c_[2]), "v"(c_[1]), "v"(c_[3])); SB(); } while (0)
#define CVT1(d_, x_, y_) do { d_ = cvtpk(x_, y_); PINU(d_); SB(); } while (0)
#define EPI3(d_, c_) do { float e_; asm volatile("v_add_f32 %1, %2, %3\n v_add_f32 %0, %4, %5\n v_cvt_pk_bf16_f32 %0, %1, %0" : "=&v"(d_), "=&v"(e_) : "v"(c_[0]), "v"(c_[2]), "v"(c_[1]), "v"(c_[3])); SB(); } while (0)
#define EPI4(d_, m_, c_) do { float e_, f_; asm volatile("v_add_f32 %1, %4, %5\n v_add_f32 %2, %6, %7\n v_cvt_pk_bf16_f32 %0, %1, %2\n v_cmp_u_f32_e64 %3, %1, %2" : "=&v"(d_), "=&v"(e_), "=&v"(f_), "=s"(m_) : "v"(c_[0]), "v"(c_[2]), "v"(c_[1]), "v"(c_[3])); SB(); } while (0)
#define CVTN(d_, m_, x_, y_) do { NANM(m_, x_, y_); CVT1(d_, x_, y_); } while (0)
#define ADDCVT(d_, x_, c_) do { asm volatile("v_add_f32 %0, %2, %3\n v_cvt_pk_bf16_f32 %0, %1, %0" : "=&v"(d_) : "v"(x_), "v"(c_[1]), "v"(c_[3])); SB(); } while (0)
#define LGW(n_) do { __builtin_amdgcn_s_waitcnt(0xC07F | ((n_) << 8)); SB(); } while (0)
#define LBAR(n_) do { asm volatile("s_waitcnt lgkmcnt(" #n_ ")" ::: "memory"); __builtin_amdgcn_s_barrier(); asm volatile("" ::: "memory"); SB(); } while (0)
#define FILL() do { } while (0)
#define NANM(m_, x_, y_) do { asm volatile("v_cmp_u_f32_e64 %0, %1, %2" : "=s"(m_) : "v"(x_), "v"(y_)); SB(); } while (0)
#define LW32(ad_, off_, v_) (*(unsigned*)(lds + (ad_) + (off_)) = (v_))
#define MEMB() asm volatile("" ::: "memory")
#define LG0() __builtin_amdgcn_s_waitcnt(0xC07F)
#define FLAG(i_) do { MEMB(); *(volatile LAS unsigned char*)(LAS unsigned char*)(lds + fla + 4 * (i_)) = (unsigned char)snv; SB(); } while (0)
#define FLRD(i_) (*(const volatile LAS unsigned*)(LAS unsigned char*)(lds + flr + 4 * (i_)))
#define SBUF(i_) (((i_) % 3) == 0 ? L_B0 : (((i_) % 3) == 1 ? L_B1 : L_B2))
#define SDMA_INIT() unsigned svo[12]; const int sq0 = 3 * ((h >> 2) & 3); \
    _Pragma("unroll") for (int q_ = 0; q_ < 12; ++q_) { const int qr_ = q_ + sq0, ins = wave + 4 * (qr_ < 12 ? qr_ : qr_ - 12); const unsigned c = (unsigned)(ins * 64 + lane), r = c / 24u, pc = c - r * 24u, sw = ((r >> 1) & 1u) * 2u + ((r >> 3) & 1u) * 4u; \
      svo[q_] = r * 12288u + 16u * (pc ^ sw); PINV(svo[q_]); }
#define HROT (NOROT ? 0 : h)
#define KROT(kr_) ((kr_) < 4 ? (((kr_) + HROT) & 3) : 4)
#define SDMA(W_m, kr_, i_) do { const char* W_ = (const char*)((W_m) + (size_t)(128 * KROT(kr_)) * 6144 + 192 * h); \
    _Pragma("unroll") for (int q_ = 0; q_ < 12; ++q_) { const int qr_ = q_ + sq0; glds(W_, svo[q_], lds_addr(lds) + (unsigned)(SBUF(i_) + (wave + 4 * (qr_ < 12 ? qr_ : qr_ - 12)) * 1024)); } SB(); } while (0)
#define SDMAQK(i_) SDMA(((i_) & 1) ? a.Wk : a.Wq, (i_) >> 1, i_)
#define TRO(tile_) (trow * 384u + (((unsigned)(2 * (tile_) + ((l15 & 3) >> 1))) ^ tswz) * 16u + (unsigned)(l15 & 1) * 8u)
#define TRO_INIT() const unsigned trow = (unsigned)(8 * lq + (l15 >> 2)), tswz = ((trow >> 1) & 1u) * 2u + ((trow >> 3) & 1u) * 4u
#define SXDMA_INIT() unsigned xvo = (unsigned)((4 * wave + lq) * 1280 + (l15 ^ ((4 * wave + lq) & 15)) * 16); PINV(xvo)
#define SXDMA(kr_, xb_) do { const char* X_ = (const char*)a.Bx + 256 * KROT(kr_); \
    glds(X_, xvo, lds_addr(lds) + (unsigned)((xb_) + wave * 1024)); glds(X_ + 16 * 1280, xvo, lds_addr(lds) + (unsigned)((xb_) + 4096 + wave * 1024)); SB(); } while (0)
#define SXRD(xb_) _Pragma("unroll") for (int kk = 0; kk < 4; ++kk) _Pragma("unroll") for (int rt = 0; rt < 2; ++rt) xa[rt][kk] = *(const u32x4*)(lds + (xb_) + rt * 4096 + l15 * 256 + ((4 * kk + lq) ^ l15) * 16)
#define MFV(c_, a_, b_, first_) do { if (first_) asm volatile("v_mfma_f32_16x16x32_bf16 %0, %1, %2, 0" : "=&v"(c_) : "v"(a_), "v"(b_)); else asm volatile("v_mfma_f32_16x16x32_bf16 %0, %1, %2, %0" : "+v"(c_) : "v"(a_), "v"(b_)); } while (0)
#define HALF(f_, hi_) ((hi_) ? hi4(f_) : lo4(f_))
#define ODMA_INIT() unsigned ovo = (unsigned)((4 * wave + lq) * 1280 + (l15 ^ (((4 * wave + lq) & 7) << 1)) * 16); PINV(ovo); \
    const unsigned orow = (unsigned)(4 * lq + (l15 >> 2)); \
    unsigned otr[2]; _Pragma("unroll") for (int t = 0; t < 2; ++t) otr[t] = orow * 256u + (((unsigned)(2 * (2 * wave + t) + ((l15 & 3) >> 1))) ^ ((orow & 7u) << 1)) * 16u + (unsigned)(l15 & 1) * 8u
#define OROT(cr_) (((cr_) + h) % 5)
#define ODMA(cr_, i_) do { const char* W_ = (const char*)(a.Wo + (size_t)(192 * h) * 640) + 256 * OROT(cr_); \
    _Pragma("unroll") for (int q_ = 0; q_ < 12; ++q_) glds(W_ + q_ * (16 * 1280), ovo, lds_addr(lds) + (unsigned)(SBUF(i_) + (wave + 4 * q_) * 1024)); SB(); } while (0)

#define SAL 2
#define SOF 0
#define KXL 0
#define SLATE 0
#define SMID 1
#define MNAP 0
#define SF0(m_, t_, k_) (6 * (2 * ((k_) >> 1) + (m_)) + 2 * (t_) + ((k_) & 1))
#define SFR(m_, t_, k_) (SF0(m_, t_, k_) - (SF0(m_, t_, k_) > 39) - (SF0(m_, t_, k_) > 41))
constexpr int L_KX = L_B1 + 22528, L_QX = 0;
template <int wave>
__device__ __forceinline__ void s_loop(const Args& a, unsigned char* lds, const int h, const unsigned pol, const WReg& W) {
  constexpr int KIND = wave < 2 ? 0 : wave - 1;
  char* ws = a.ws; asm volatile("" : "+s"(ws));
  unsigned* const cnt = (unsigned*)(ws + O_CNT); unsigned* const tmo = cnt + CNT(C_TMO);
  const int lane = lane_id(), l15 = lane & 15, lq = lane >> 4;
  char* const X1 = ws + O_X1; char* const P = ws + O_PS;
  SIDES();
  const __amdgpu_buffer_rsrc_t rs_x1 = mkrs(X1), rs_p = mkrs(P);
  unsigned tprog = 0;
  const unsigned kkey = K_KEY();
  const char* const ksrc = ws + O_KF + (size_t)h * 32768 + wave * 16384 + 2048;
#define KXR(n_) do { if (KIND == 0) DS(kx[n_] = *(const LAS u32x4*)(size_t)(kxa + (n_) * 1024)); } while (0)
  unsigned trx = (unsigned)(L_XR + wave * 4096 + lane * 16); PINV(trx);
  unsigned trq = (unsigned)(L_B2 + wave * 12288 + lane * 16); PINV(trq);
  unsigned trk = (unsigned)(L_B0 + wave * 12288 + lane * 16); PINV(trk);
  int ix = (l15 & 2) ? 0xEEEE : 0x4444; PINV(ix);
  float one = 1.0f; PINS(one);
  const float CS = 0.07216878364870323f * 1.4426950408889634f;
  unsigned xv = (unsigned)lane * 16u;
  unsigned ib = (unsigned)(L_QI + (16 * wave + l15) * 16 + lq * 4);
  unsigned ia = (unsigned)(L_QA + wave * 384 + ((l15 >> 2) & 1) * 128 + (l15 >> 3) * 64 + (l15 & 1) * 32 + lq * 8 + ((l15 >> 1) & 1) * 4);
  unsigned rda = (unsigned)(L_QA + (lq >> 1) * 768 + (l15 >> 3) * 384 + (lq & 1) * 64 + ((l15 >> 2) & 1) * 32 + (l15 & 3) * 8);
  unsigned qk_rd = (unsigned)(L_QI + (4 * lq + (l15 >> 2)) * 16 + (l15 & 1) * 8);
  unsigned flr = (unsigned)L_FLG;
  unsigned xva = KIND == 0 ? (unsigned)(L_XV + lq * 256 + (l15 >> 1) * 16 + wave * 8 + (l15 & 1) * 4) : (unsigned)(L_XV + lane * 16);
  PINV(xv); PINV(ib); PINV(ia);
  if (KIND != 2) { PINV(rda); PINV(xva); } if (KIND == 1) PINV(qk_rd); if (KIND != 0) PINV(flr);
  const bool hi8 = l15 >= 8;
  const __amdgpu_buffer_rsrc_t rs_ts = mkrs(ws + O_TS + half_of(ws + O_TS, 0u, 0u, pol));
  int tso = lane == 0 ? 256 * h : (int)0x80000000u; if (KIND != 0) PINV(tso);
  int tsa = TS_OFF(lane); if (KIND == 2) PINV(tsa);
  const __amdgpu_buffer_rsrc_t rs_ps = mkrs(cn + CNT(C_PS) - 32 * h);
  unsigned po = side_map(P, (unsigned)(h * 512 + ((l15 >> 2) & 1) * 256 + (4 * lq + 2 * (l15 & 1) + (l15 >> 3)) * 16 + ((l15 >> 1) & 1) * 8), 0u, pol);
  unsigned pm = !(l15 & 1) ? side_map(P, (unsigned)(16384 + (h >> 1) * 256 + (4 * lq + 2 * ((l15 >> 2) & 1) + (l15 >> 3)) * 16 + (h & 1) * 8 + ((l15 >> 1) & 1) * 4), 0u, pol) : 0x80000000u;
  u32x4 p_desc = mkdesc(P); if (KIND == 1) PINS(p_desc);
  unsigned pfo = lane < 32 ? side_map(P, (unsigned)(h * 512 + lane * 16), 0u, pol) : (lane < 40 ? side_map(P, (unsigned)(16384 + h * 128 + (lane - 32) * 16), 0u, pol) : 0x80000000u);
  if (KIND == 1) { PINV(po); PINV(pm); }
  if (KIND == 1) PINV(pfo);
  unsigned dur = 130, tnx = 0, tpr = 0, adj = 0; bool was_calm = false; int dly = a.d[0]; PINS(dly);
#define MQ(t_, f_, w_) SMFV(aq[t_], f_, w_)
#define MK(t_, f_, w_) SMFV(ak[t_], f_, w_)
#define QW(t_, k_) SMFW(aq[t_], A[k_], SFR(0, t_, k_))
#define KW(t_, k_) SMFW(ak[t_], A[k_], SFR(1, t_, k_))
#define RLO(tb_, t_, k2_) (*(const u32x4*)(lds + (tb_) + (t_) * 4096 + (k2_) * 2048))
#define RHI(tb_, t_, k2_) (*(const u32x4*)(lds + (tb_) + (t_) * 4096 + (k2_) * 2048 + 1024))
#define RFR(tb_, t_, k2_) cat8(RLO(tb_, t_, k2_), RHI(tb_, t_, k2_))
#define XLO(j_) (*(const u32x4*)(lds + trx + (j_) * 2048))
#define XHI(j_) (*(const u32x4*)(lds + trx + (j_) * 2048 + 1024))
#define ALD(k_) DS(A[k_] = ld16(rs_x1, xv + (unsigned)(((k_) & 1) * 1024), sx[(k_) >> 1]))
#define APACE 10
#define ALP(k_) do { ALD(k_); nap4<APACE>(); SB(); } while (0)
#define A_LOAD() do { PINV(xv); ALP(0); ALP(8); ALP(1); ALP(9); ALP(2); ALP(7); ALP(3); ALP(4); ALP(5); ALD(6); SB(); } while (0)
  u32x4 ts_desc = mkdesc(ws + O_TS + half_of(ws + O_TS, 0u, 0u, pol) + 256 * h);
  u32x4 tsr_desc = mkdesc(ws + O_TS + half_of(ws + O_TS, 0u, 0u, pol)); if (KIND == 2) PINS(tsr_desc);
  u32x4 ex[3], sent4 = SENT4, km_desc = mkdesc(ws + O_KM + h * 3072); unsigned kmo = (unsigned)(((lane >> 3) & 1) * 512 + lq * 128 + (lane & 7) * 16);
  if (KIND == 1) { PINV(kmo); PINV(sent4); PINS(km_desc); ex[0] = ex[1] = ex[2] = sent4; }
  unsigned fad = lds_addr(lds) + (unsigned)L_FLG; PINV(fad); PINS(ts_desc);
  if (KIND == 2) LW32(flr, 24, dur);
#pragma unroll 1
  for (int s = 0; s < 64; ++s) {
    int pss = PSLOT(s), tss = TS_STEP(s), tsn = TS_STEP(s + 1); PINS(pss); PINS(tss); if (KIND == 2) PINS(tsn);
    unsigned snp = (unsigned)(s + 1) * 0x01010101u, snv = snp, tries = 0; PINS(snp); PINV(snv);
    f32x4 sc = ZERO4; if (KIND != 2) PINV(sc);
    unsigned kxa = lds_addr(lds) + xv + (unsigned)(L_KX + (s & 1) * 12288 + wave * 6144); if (KIND == 0) PINV(kxa);
    u32x4 kx[6];
    unsigned xo = s > 0 && s <= 61 ? kmo : 0x80000000u, xs = MSLOT(s - 1), pe = (unsigned)PSLOT(s + PRE - 1); if (KIND == 1 && SMID) { PINV(xo); PINS(xs); PINS(pe); }
#define EXP(i_) do { if (KIND == 1 && SMID) { if ((i_) < 3) asm volatile("buffer_store_dwordx4 %0, %1, %2, %3 offen offset:%4 sc1" :: "v"(ex[(i_) % 3]), "v"(xo), "s"(km_desc), "s"(xs), "n"((i_) % 3 * 1024) : "memory"); \
      else asm volatile("buffer_store_dwordx4 %0, %1, %2, %3 offen sc1" :: "v"(sent4), "v"(pfo), "s"(p_desc), "s"(pe) : "memory"); SB(); } } while (0)
    unsigned sx[5];
    { const unsigned so = half_of(X1, XSLOT(s), 0u, pol);
#pragma unroll
      for (int j = 0; j < 5; ++j) { sx[j] = so + (j < 4 ? (unsigned)((j + HROT) & 3) * 2048u : 16384u); PINS(sx[j]); }
      if (KIND == 2 && s == 1) x1_light(rs_x1, so, tmo); }
    SB();
    asm volatile("s_branch .Lsre%c6\n"
                 ".Lsbad%c6:\n s_add_u32 %0, %0, 1\n s_cmp_lt_u32 %0, 4\n s_cbranch_scc1 .Lsnow%c6\n s_sleep 8\n"
                 ".Lsnow%c6:\n s_memrealtime vcc\n ds_write_b32 %5, %1 offset:28\n ds_read_b32 %1, %5 offset:24\n s_waitcnt lgkmcnt(0)\n v_add_u32 %1, vcc_lo, %1\n v_and_b32 %1, -2, %1\n"
                 " s_mov_b64 exec, 1\n buffer_store_dword %1, off, %3, %4 sc1\n s_mov_b64 exec, -1\n v_mov_b32 %1, %2\n"
                 ".Lsre%c6:" : "+s"(tries) : "v"(snv), "s"(snp), "s"(ts_desc), "s"(tss), "v"(fad), "n"(wave) : "vcc", "scc", "s92", "s93", "s94", "s95", "s96", "s97", "s98", "s99", "memory");
    SB();
    u32x8 ra = RFR(trq, 0, 0), rb = RFR(trq, 1, 0), rc = RFR(trq, 2, 0);
    f32x4 aq[3] = {ZERO4, ZERO4, ZERO4}, ak[3] = {ZERO4, ZERO4, ZERO4};
    asm volatile("s_cmp_lg_u32 %6, 0\n s_cbranch_scc1 .Lsgo%c7" : "+v"(aq[0]), "+v"(aq[1]), "+v"(aq[2]), "+v"(ak[0]), "+v"(ak[1]), "+v"(ak[2]) : "s"(tries), "n"(wave) : "scc", "memory");
    SB();
    unsigned tis = 0, tsl = 0; bool calm = false;
    asm volatile(".p2align %c0\n .fill %c1, 4, 0xbf800000" :: "n"(SAL), "n"(SOF));
    if (KIND == 2) tis = WAIT_GO(tnx);
    BAR();
    asm volatile(".Lsgo%c0:" :: "n"(wave) : "memory");
    SB();
    u32x4 A[10], lo, hi;
    A_LOAD();
    unsigned w0;
    float ta, tb_;
    u64 nanm;
    QW(0, 0); QW(1, 0); QW(2, 0); KW(0, 0);
    MQ(0, A[8], ra); DS(lo = RLO(trq, 0, 1));
    KW(1, 0); DS(hi = RHI(trq, 0, 1)); ra = cat8(lo, hi);
    MQ(1, A[8], rb); DS(lo = RLO(trq, 1, 1));
    KW(2, 0); DS(hi = RHI(trq, 1, 1)); rb = cat8(lo, hi);
    QW(0, 1);
    MQ(2, A[8], rc); DS(lo = RLO(trq, 2, 1));
    QW(1, 1); DS(hi = RHI(trq, 2, 1)); rc = cat8(lo, hi);
    QW(2, 1);
    MQ(0, A[9], ra); DS(lo = RLO(trk, 0, 0));
    KW(0, 1); DS(hi = RHI(trk, 0, 0)); ra = cat8(lo, hi);
    MQ(1, A[9], rb); DS(lo = XLO(0));
    KW(1, 1); DS(hi = XHI(0)); rb = cat8(lo, hi);
    QW(0, 2);
    MQ(2, A[9], rc); DS(lo = XLO(1));
    QW(1, 2); DS(hi = XHI(1)); rc = cat8(lo, hi);
    QW(2, 2);
    MK(0, A[8], ra); DS(lo = RLO(trk, 1, 0));
    QW(0, 7); DS(hi = RHI(trk, 1, 0)); ra = cat8(lo, hi);
    MQ(1, A[7], rb); DS(lo = RLO(trk, 0, 1));
    KW(0, 2); DS(hi = RHI(trk, 0, 1)); rb = cat8(lo, hi);
    QW(0, 3);
    MQ(2, A[7], rc); DS(lo = RLO(trk, 2, 0));
    QW(1, 3); DS(hi = RHI(trk, 2, 0)); rc = cat8(lo, hi);
    QW(2, 3); KXR(0);
    QW(0, 4);
    MK(1, A[8], ra); DS(lo = RLO(trk, 1, 1));
    QW(1, 4); DS(hi = RHI(trk, 1, 1)); ra = cat8(lo, hi);
    QW(2, 4); KXR(1);
    QW(0, 5);
    MK(0, A[9], rb); DS(lo = RLO(trk, 2, 1));
    QW(1, 5); DS(hi = RHI(trk, 2, 1)); rb = cat8(lo, hi);
    QW(2, 5); KXR(2);
    QW(0, 6); EXP(0);
    QW(1, 6); EXP(1); QW(2, 6); LGW(KIND == 0 ? 1 : 0);
    KW(0, 3); KXR(3); EXP(2); ADD1(ta, aq[0][0], aq[0][2]);
    KW(0, 4); KXR(4); EXP(3); ADD1B(tb_, aq[0][1], aq[0][3]);
    KW(0, 5); CVTN(w0, nanm, ta, tb_);
    KW(0, 6); DS(LW32(ia, 0, w0)); ADD1(ta, aq[1][0], aq[1][2]);
    KW(0, 7); KXR(5); ADD1B(tb_, aq[1][1], aq[1][3]);
    KW(1, 2);
    if (__builtin_expect(nanm != 0ull, 0)) {
      if (KIND == 1 && SMID) asm volatile("v_bfrev_b32 %0, 1" :: "v"(xo));
      unsigned m = ~0u;
#define S_BACK() asm volatile("v_cmp_eq_u32 vcc, 0, %0\n s_cmp_lt_u32 %1, %3\n s_cselect_b64 vcc, vcc, 0\n s_cbranch_vccnz .Lsbad%c2" : "+v"(m) : "s"(tries), "n"(wave), "n"(CH_SPIN_CAP) : "vcc", "scc")
#pragma unroll
      for (int k = 0; k < 10; k += 2) m = sent_min(m, A[k].x, A[k + 1].x);
      S_BACK();
#pragma unroll
      for (int k = 0; k < 10; ++k) m = sent_min(m, A[k]);
      S_BACK();
    }
    asm volatile("" :: "s"(sx[0]), "s"(sx[1]), "s"(sx[2]), "s"(sx[3]), "s"(sx[4]), "s"(tries), "s"(tss), "s"(snp), "s"(ts_desc), "v"(fad), "v"(snv));
    if (KIND == 0) PINU(kxa);
    if (KIND == 1 && SMID) asm volatile("" :: "v"(ex[0]), "v"(ex[1]), "v"(ex[2]), "v"(xo), "s"(xs), "s"(pe));
    CVT1(w0, ta, tb_);
    KW(1, 3); DS(LW32(ia, 1536, w0)); ADD1(ta, aq[2][0], aq[2][2]);
    KW(1, 4); if (KIND == 2) { asm volatile("buffer_load_dword %0, %1, %2, %3 offen sc1" : "=v"(tsl) : "v"(tsa), "s"(tsr_desc), "s"(tss) : "memory"); SB(); } ADD1B(tb_, aq[2][1], aq[2][3]);
    KW(1, 5); CVT1(w0, ta, tb_);
    KW(1, 6); DS(LW32(ia, 3072, w0)); ADD1(ta, ak[0][0], ak[0][2]);
    KW(1, 7); ADD1B(tb_, ak[0][1], ak[0][3]);
    MK(1, A[9], ra); CVT1(w0, ta, tb_);
    KW(2, 1); DS(LW32(ib, 0, w0));
    u32x2 g[6];
#define QAR(i_) do { if (KIND != 2) DS(g[i_] = trrd(lds, rda + (unsigned)(((i_) >> 1) * 1536 + ((i_) & 1) * 128))); } while (0)
#define QA(t_) ((u32x4){g[2 * (t_)].x, g[2 * (t_)].y, g[2 * (t_) + 1].x, g[2 * (t_) + 1].y})
    KW(2, 2); LBAR(1); QAR(0);
    KW(2, 3); QAR(1); ADD1(ta, ak[1][0], ak[1][2]);
    KW(2, 4); QAR(2); ADD1B(tb_, ak[1][1], ak[1][3]);
    KW(2, 5); QAR(3); CVT1(w0, ta, tb_);
    KW(2, 6); DS(LW32(ib, 1024, w0));
    KW(2, 7); QAR(4); QAR(5);
    MK(2, A[8], rc);
    MK(2, A[9], rb);
    if (KIND == 0) {
      float la, lb;
      { const u32x4 q_ = QA(0); const u32x8 k_ = cat8(kx[0], kx[1]); SMFV(sc, q_, k_); } LBAR(2);
      { const u32x4 q_ = QA(1); const u32x8 k_ = cat8(kx[2], kx[3]); SMFV(sc, q_, k_); }
      { const u32x4 q_ = QA(2); const u32x8 k_ = cat8(kx[4], kx[5]); SMFV(sc, q_, k_); }
      EPI3(w0, ak[2]); DS(LW32(ib, 2048, w0));
      asm volatile("s_nop 3\n v_add_f32 %0, %2, %3\n v_add_f32 %1, %4, %5" : "=&v"(la), "=v"(lb) : "v"(sc[0]), "v"(sc[2]), "v"(sc[1]), "v"(sc[3])); SB();
      DS(LW32(xva, 0, __float_as_uint(la)); LW32(xva, 128, __float_as_uint(lb)));
      LBAR(1); LBAR(0);
    } else if (KIND == 2) {
      LBAR(0);
      asm volatile("s_nop 5"); SB();
      EPI3(w0, ak[2]); DS(LW32(ib, 2048, w0));
      LBAR(0); LBAR(0);
      asm volatile("s_waitcnt vmcnt(0)" : "+v"(tsl) :: "memory");
      tsl = ts_max(tsl);
      calm = s > 1 && tsl != ~0u && (int)(tsl - tpr) <= a.d[10];
      if (calm && s < 63) __builtin_amdgcn_raw_buffer_store_b32(min(tsl + (unsigned)dly + dur, ~2u), rs_ts, tso, tsn, 16);
    } else {
      u32x2 kb[12];
#define KBR(i_) DS(kb[i_] = trrd(lds, qk_rd + (unsigned)((i_) * 256)))
#define ML(t_) do { const u32x4 a_ = QA(t_); \
        const u32x8 b_ = {kb[4 * (t_)].x, kb[4 * (t_)].y, kb[4 * (t_) + 1].x, kb[4 * (t_) + 1].y, kb[4 * (t_) + 2].x, kb[4 * (t_) + 2].y, kb[4 * (t_) + 3].x, kb[4 * (t_) + 3].y}; SMFV(sc, a_, b_); } while (0)
      LBAR(2);
      KBR(0); KBR(1); KBR(2); KBR(3); KBR(4); KBR(5);
      EPI3(w0, ak[2]); DS(LW32(ib, 2048, w0));
      KBR(6); KBR(7);
      LBAR(2);
      KBR(8); KBR(9); KBR(10); KBR(11);
      ML(0);
      asm volatile("s_barrier" ::: "memory"); SB();
      f32x4 t; DS(t = *(const f32x4*)(lds + xva));
      ML(1); ML(2);
      float tl, nl, x, m, u, e, d;
      asm volatile("v_max3_f32 %4, %0, %1, %2\n v_max_f32 %4, %4, %3\n v_mul_f32 %5, %15, %4\n v_fma_f32 %0, %16, %0, %5\n v_fma_f32 %1, %16, %1, %5\n v_fma_f32 %2, %16, %2, %5\n v_fma_f32 %3, %16, %3, %5\n s_nop 0\n"
                   "v_add_f32_dpp %6, %11, %13 quad_perm:[0,1,2,3] row_mask:0xf bank_mask:0x3\n v_add_f32_dpp %6, %12, %14 quad_perm:[0,1,2,3] row_mask:0xf bank_mask:0xc\n v_max_f32 %7, %6, %4\n"
                   "v_exp_f32 %0, %0\n v_exp_f32 %1, %1\n v_max_f32_dpp %7, %7, %7 quad_perm:[1,0,3,2] row_mask:0xf bank_mask:0xf\n"
                   "v_exp_f32 %2, %2\n v_exp_f32 %3, %3\n v_max_f32_dpp %7, %7, %7 quad_perm:[2,3,0,1] row_mask:0xf bank_mask:0xf\n"
                   "v_add_f32 %8, %0, %1\n v_add_f32 %10, %2, %3\n v_max_f32_dpp %7, %7, %7 row_half_mirror row_mask:0xf bank_mask:0xf\n"
                   "v_mul_f32 %7, %15, %7\n v_sub_f32 %4, %7, %5\n v_fma_f32 %9, %16, %6, %7\n v_exp_f32 %4, %4\n v_exp_f32 %9, %9\n v_add_f32 %8, %8, %10\n v_fma_f32 %10, %4, %8, %9\n"
                   "v_mul_f32 %0, %0, %4\n v_mul_f32 %1, %1, %4\n v_add_f32_dpp %10, %10, %10 quad_perm:[1,0,3,2] row_mask:0xf bank_mask:0xf\n"
                   "v_mul_f32 %2, %2, %4\n v_mul_f32 %3, %3, %4\n v_add_f32_dpp %10, %10, %10 quad_perm:[2,3,0,1] row_mask:0xf bank_mask:0xf\n"
                   "v_mov_b32_dpp %6, %9 quad_perm:[1,0,3,2] row_mask:0xf bank_mask:0xf\n s_nop 0\n v_add_f32_dpp %10, %10, %10 row_half_mirror row_mask:0xf bank_mask:0xf\n v_rcp_f32 %10, %10\n s_nop 0\n"
                   "v_mul_f32 %0, %0, %10\n v_mul_f32 %1, %1, %10\n v_cvt_pk_bf16_f32 v0, %0, %1\n v_mul_f32 %2, %2, %10\n v_mul_f32 %3, %3, %10\n v_cvt_pk_bf16_f32 v1, %2, %3\n"
                   "buffer_store_dwordx2 v[0:1], %17, %19, %20 offen sc1\n v_mul_f32 %9, %9, %10\n v_mul_f32 %6, %6, %10\n v_cvt_pk_bf16_f32 %9, %9, %6\n buffer_store_dword %9, %18, %19, %20 offen sc1"
                   : "+v"(t[0]), "+v"(t[1]), "+v"(t[2]), "+v"(t[3]), "=&v"(tl), "=&v"(nl), "=&v"(x), "=&v"(m), "=&v"(u), "=&v"(e), "=&v"(d)
                   : "v"(sc[0]), "v"(sc[1]), "v"(sc[2]), "v"(sc[3]), "s"(-CS), "s"(CS), "v"(po), "v"(pm), "s"(p_desc), "s"(pss) : "v0", "v1", "memory");
      SB();
      { u64 t_; RT_MARK(t_); LW32(flr, 20, (unsigned)t_); }
    }
    BAR();
    if (KIND == 0 && s == 0) {
#pragma unroll
      for (int n = 0; n < 12; ++n) st16(mkrs(ws + (wave == 0 ? O_KF : O_QF)), xv + (unsigned)(n % 3 * 1024), KSLOT(0) + (unsigned)(h * 32768 + n / 6 * 16384 + n % 6 / 3 * 3072), *(const u32x4*)(lds + (wave == 0 ? L_KX : L_QX) + n * 1024 + xv));
    }
    if (KIND == 0 && s < 63) {
      nap_rt(KXL);
      if (tprog < (unsigned)(s + 1)) tprog = wait_t<4>(cn + CNT(C_PT_ + h), 1, (unsigned)(s + 1), tmo);
      const char* kp = ksrc + KSLOT(s + 1); asm volatile("s_nop 4" : "+s"(kp));
      m0_set(lds_addr(lds) + (unsigned)(L_KX + ((s + 1) & 1) * 12288 + wave * 6144 + 2048));
      glds_x<-2048>(kp, xv); glds_x<-1024>(kp, xv); glds_x<0>(kp, xv); glds_x<1024>(kp, xv); glds_x<2048>(kp, xv); glds_x<3072>(kp, xv);
      if (tprog < (unsigned)(s + 2)) tprog = min_t(ld16(mkrs(cn + CNT(C_PT_ + h)), 0u, 0u), 1);
    }
    if (KIND == 2 && s < 63) {
      const unsigned t2 = __builtin_amdgcn_raw_buffer_load_b32(rs_ts, tsa, tss, 16);
      const unsigned f7 = FLRD(7), f5 = FLRD(5);
      const unsigned tb2 = (unsigned)__builtin_amdgcn_readfirstlane(f5);
      adj = __builtin_amdgcn_readfirstlane(f7) == snp;
      const bool own = adj && was_calm;
      was_calm = calm && !adj && ts_max(t2) == tsl;
      if (__builtin_expect(!was_calm, 0)) {
        if (s < 2) __builtin_amdgcn_raw_buffer_store_b32(((unsigned)tb2 + (unsigned)a.d[11]) & ~1u, rs_ts, tso, tss, 16);
        tsl = ts_settle(rs_ts, tss, tsl, a.d[4]) + (unsigned)a.d[12];
        if (s > 0) __builtin_amdgcn_raw_buffer_store_b32(min(tsl + (unsigned)dly + dur, ~2u), rs_ts, tso, tsn, 16);
      }
      tnx = tsl + (unsigned)dly; tpr = tnx + dur;
      dur = min(dur + 1u, (unsigned)tb2 - tis); PINS(dur);
      LW32(flr, 24, dur + (unsigned)a.d[13]);
      ADAPT(dly, own);
    }
    if (KIND == 1) {
      const int ln = lane_id();
#pragma unroll
      for (int kk = 0; kk < 6; kk += 2) { const u32x4 v = trrd2(lds, qk_rd + kk * 512, 256u), v2 = trrd2(lds, qk_rd + (kk + 1) * 512, 256u); ex[kk >> 1] = hi8 ? v2 : v; PINV(ex[kk >> 1]); }
      if (!SMID || s >= 61) {
        if (!SMID && s + PRE < 63) st16(rs_p, pfo, (unsigned)PSLOT(s + PRE), SENT4);
#pragma unroll
        for (int i = 0; i < 3; ++i) st16(mkrs(ws + O_KM), kmo + (unsigned)(i * 1024), MSLOT(s) + (unsigned)h * 3072u, ex[i]);
      }
      __builtin_amdgcn_raw_buffer_store_b32((unsigned)(s + 1), rs_ps, tso, 0, 16);
    }
  }
  VMW(0);
}
__device__ __forceinline__ void s_role(const Args& a, const u32x2 tag, unsigned char* lds, const int h, const int wave) {
  char* ws = a.ws; asm volatile("" : "+s"(ws));
  unsigned* const cnt = (unsigned*)(ws + O_CNT); unsigned* const tmo = cnt + CNT(C_TMO);
  const int lane = lane_id(), l15 = lane & 15, lq = lane >> 4;
  WReg W; u32x8 wx[3];
  unsigned tro[3];
  { TRO_INIT();
#pragma unroll
    for (int t = 0; t < 3; ++t) tro[t] = TRO(4 * t + wave); }
  SDMA_INIT();
  SXDMA_INIT();
  SXDMA(0, 0); SDMAQK(0); SDMAQK(1); SXDMA(1, 8192); SDMAQK(2);
  u32x2 pf;
  f32x4 pac[2][2][3];
#define kacc pac[1]
#define qacc pac[0]
  SFOR(i, 10)
    if (i < 2 || i == 3 || i == 5 || i == 7) VMW(26); else if (i < 8) VMW(24); else if (i == 8) VMW(12); else VMW(0);
    BAR();
    u32x8 res[3][2];
    SFOR(t, 3)
      const unsigned ad = lds_addr(lds) + tro[t] + (unsigned)SBUF(i);
      if (i < 8) { TRS0(ad, SFR(i & 1, t, i & 6)); if (i == 6 && t > 0) tr8(false, ad, res[t][0], wx[t]); else TRS1(ad, SFR(i & 1, t, (i & 6) + 1)); }
      else tr8(false, ad, res[t][0], res[t][1]);
    SEND;
    BAR();
    if (i == 2 || i == 4 || i == 6) SXDMA((i >> 1) + 1, (((i >> 1) + 1) & 1) * 8192);
    if (i + 3 < 10) SDMAQK(i + 3);
    if (i >= 8) {
#pragma unroll
      for (int t = 0; t < 3; ++t)
#pragma unroll
        for (int k = 0; k < 2; ++k) {
          unsigned char* dst = lds + SBUF(i) + wave * 12288 + t * 4096 + k * 2048 + lane * 16;
          *(u32x4*)dst = lo4(res[t][k]); *(u32x4*)(dst + 1024) = hi4(res[t][k]);
        }
    }
    if (i == 8) asm volatile("s_nop 4\n global_load_dwordx2 %0, %1, %2 sc1" : "=v"(pf) : "v"(0u), "s"(cnt + CNT(C_GO)) : "memory");
    if (i == 9) PINV(pf);
    if (i == 7) {
#pragma unroll
      for (int t = 1; t < 3; ++t) { unsigned char* dst = lds + L_XR + wave * 4096 + lane * 16 + (t - 1) * 2048; *(u32x4*)dst = lo4(wx[t]); *(u32x4*)(dst + 1024) = hi4(wx[t]); }
    }
    SB();
    SFOR(kk, 4)
      u32x4 xa[2];
#pragma unroll
      for (int rt = 0; rt < 2; ++rt) xa[rt] = *(const u32x4*)(lds + ((i >> 1) & 1) * 8192 + rt * 4096 + l15 * 256 + ((4 * kk + lq) ^ l15) * 16);
      SFOR(n, 6)
        constexpr int t = n >> 1, rt = n & 1;
        if (i >= 8) { const u32x4 w_ = HALF(res[t][kk >> 1], kk & 1); MFV(pac[i & 1][rt][t], w_, xa[rt], false); }
        else if (i == 6 && t > 0 && kk >= 2) { const u32x4 w_ = HALF(wx[t], kk & 1); MFV(pac[0][rt][t], w_, xa[rt], false); }
        else mf_w<true>(pac[i & 1][rt][t], xa[rt], 2 * SFR(i & 1, t, (i & 6) + (kk >> 1)) + (kk & 1), i < 2 && kk == 0, W);
      SEND;
    SEND;
    SB();
  SEND;
  unsigned pol;
  {
    asm volatile("s_nop 7\n s_nop 7" : "+v"(kacc[0][0]), "+v"(kacc[0][1]), "+v"(kacc[0][2]), "+v"(kacc[1][0]), "+v"(kacc[1][1]), "+v"(kacc[1][2]));
    pol = (unsigned)__builtin_amdgcn_readfirstlane(pf.x);
    if (__builtin_expect((pol & ~7u) != tag.x || (unsigned)__builtin_amdgcn_readfirstlane(pf.y) != tag.y, 0)) pol = wait_tag<8>(cnt, C_GO, tag);
    pol = uni(pol & 1u);
    BAR();
#pragma unroll
    for (int rt = 0; rt < 2; ++rt)
#pragma unroll
      for (int t = 0; t < 3; ++t) { const int ix = 4 * t + wave, o_ = rt * 6144 + (ix >> 1) * 1024;
        *(u32x2*)(lds + L_KX + o_ + lane * 16 + (ix & 1) * 8) = (u32x2){cvtpk(kacc[rt][t][0], kacc[rt][t][1]), cvtpk(kacc[rt][t][2], kacc[rt][t][3])};
        *(u32x2*)(lds + L_QX + o_ + ((ix >> 1) % 3 == 2 ? lane * 8 + (ix & 1) * 512 : lane * 16 + (ix & 1) * 8)) = (u32x2){cvtpk(qacc[rt][t][0], qacc[rt][t][1]), cvtpk(qacc[rt][t][2], qacc[rt][t][3])}; }
    if (wave == 2) { if (lane < 8) *(unsigned*)(lds + L_FLG + 4 * lane) = 0u; }
    BAR();
  }
  if (wave == 0) s_loop<0>(a, lds, h, pol, W); else if (wave == 1) s_loop<1>(a, lds, h, pol, W); else if (wave == 2) s_loop<2>(a, lds, h, pol, W); else s_loop<3>(a, lds, h, pol, W);
}
#undef kacc
#undef qacc

#define ZMID 2
__device__ __forceinline__ void z_role(const Args& a, const u32x2 tag, unsigned char* lds, const int h, const int wave) {
  char* ws = a.ws; asm volatile("" : "+s"(ws));
  unsigned* const cnt = (unsigned*)(ws + O_CNT); unsigned* const tmo = cnt + CNT(C_TMO);
  char* const X1 = ws + O_X1;
#define ZFV(t_, k_) (8 * (t_) + (k_))
#define ZFO(n_, j_) ((n_) < 6 ? 24 + 3 * (n_) + (j_) : 30 + 2 * (n_) + ((j_) >> 1))
  WReg W;
  const __amdgpu_buffer_rsrc_t rs_u = mkrs(ws + O_UO + h * 1024);
  unsigned xv = (unsigned)lane_id() * 16u; PINV(xv);
  unsigned pkv, pk3, pk4; u32x2 pf;
  {
    const int lane = lane_id(), l15 = lane & 15, lq = lane >> 4;
    unsigned tro[3];
    { TRO_INIT();
#pragma unroll
      for (int t = 0; t < 3; ++t) tro[t] = TRO(4 * t + wave); }
    SDMA_INIT(); SXDMA_INIT(); ODMA_INIT();
    SDMA(a.Wv, 0, 0); SXDMA(0, 0); SDMA(a.Wv, 1, 1); SXDMA(1, 8192); SDMA(a.Wv, 2, 2);
    f32x4 kacc[2][3];
    u32x8 wl[3][2];
    SFOR(i, 5)
      if (i < 4) VMW(26); else VMW(24);
      BAR();
      u32x4 xa[2][4];
      SXRD((i & 1) * 8192);
      SFOR(t, 3)
        if (i < 4) { TRS0(lds_addr(lds) + tro[t] + (unsigned)SBUF(i), ZFV(t, 2 * (i & 3))); TRS1(lds_addr(lds) + tro[t] + (unsigned)SBUF(i), ZFV(t, 2 * (i & 3) + 1)); }
        else tr8(false, lds_addr(lds) + tro[t] + (unsigned)SBUF(i), wl[t][0], wl[t][1]);
      SEND;
#pragma unroll
      for (int n = 0; n < 8; ++n) PINV(xa[n >> 2][n & 3]);
      BAR();
      if (i + 2 < 5) SXDMA(i + 2, (i & 1) * 8192);
      if (i + 3 < 5) SDMA(a.Wv, i + 3, i + 3); else ODMA(i - 2, i + 3);
      SFOR(n, 24)
        constexpr int kk = n / 6, t = (n % 6) >> 1, rt = n & 1;
        if (i < 4) mf_w<true>(kacc[rt][t], xa[rt][kk], 2 * ZFV(t, 2 * (i & 3) + (kk >> 1)) + (kk & 1), i == 0 && kk == 0, W);
        else { const u32x4 w_ = HALF(wl[t][kk >> 1], kk & 1); MFV(kacc[rt][t], w_, xa[rt][kk], false); }
      SEND;
      SB();
    SEND;
    u32x4 vx[2][6];
    asm volatile("s_nop 7\n s_nop 7" : "+v"(kacc[0][0]), "+v"(kacc[0][1]), "+v"(kacc[0][2]), "+v"(kacc[1][0]), "+v"(kacc[1][1]), "+v"(kacc[1][2]));
#pragma unroll
    for (int rt = 0; rt < 2; ++rt)
#pragma unroll
      for (int t = 0; t < 3; ++t)
        *(u32x2*)(lds + rt * 6144 + (2 * t + (wave >> 1)) * 1024 + xv + (wave & 1) * 8) = (u32x2){cvtpk(kacc[rt][t][0], kacc[rt][t][1]), cvtpk(kacc[rt][t][2], kacc[rt][t][3])};
    BAR();
#pragma unroll
    for (int n = 0; n < 12; ++n) { vx[n / 6][n % 6] = *(const u32x4*)(lds + n * 1024 + xv); PINV(vx[n / 6][n % 6]); }
    SFOR(i5, 5)
      constexpr int i = i5 + 5;
      if (i == 5) VMW(24); else if (i == 6) VMW(26); else if (i == 7) VMW(28); else if (i == 8) VMW(18); else VMW(6);
      BAR();
      u32x8 res[2][3];
      SFOR(t, 2)
        const unsigned ad = lds_addr(lds) + otr[t] + (unsigned)SBUF(i);
        constexpr int n = 2 * i5 + t;
        if (n < 8) TRO4(ad, 0, ZFO(n & 7, 0)); else tr4<0>(false, ad, res[t][0]);
        if (n < 6) TRO4(ad, 1, ZFO(n & 7, 1)); else tr4<1>(false, ad, res[t][1]);
        if (n < 8) TRO4(ad, 2, ZFO(n & 7, 2)); else tr4<2>(false, ad, res[t][2]);
      SEND;
      BAR();
      if (i + 3 < 10) ODMA(i - 2, i + 3);
      if (i == 9) asm volatile("s_nop 4\n global_load_dwordx2 %0, %1, %2 sc1" : "=v"(pf) : "v"(0u), "s"(cnt + CNT(C_GO)) : "memory");
      f32x4 ua[2][2];
      SFOR(m, 24)
        constexpr int e = m >> 2, t = (m >> 1) & 1, rt = m & 1, n = 2 * i5 + t;
        if (n >= 8 || (n >= 6 && (e >> 1) == 1)) { const u32x4 w_ = HALF(res[t][e >> 1], e & 1); MFV(ua[t][rt], vx[rt][e], w_, e == 0); }
        else mf_w<false>(ua[t][rt], vx[rt][e], 2 * ZFO(n & 7, e >> 1) + (e & 1), e == 0, W);
      SEND;
      asm volatile("s_nop 7" : "+v"(ua[0][0]), "+v"(ua[0][1]), "+v"(ua[1][0]), "+v"(ua[1][1]));
#pragma unroll
      for (int t = 0; t < 2; ++t)
        st16(rs_u, xv, u_off(0u, (unsigned)(8 * OROT(i5) + 2 * wave + t)), (u32x4){cvtpk(ua[t][0][0], ua[t][0][1]), cvtpk(ua[t][0][2], ua[t][0][3]), cvtpk(ua[t][1][0], ua[t][1][1]), cvtpk(ua[t][1][2], ua[t][1][3])});
      SB();
      if (i == 7) {
        PINV(xv); pkv = xv + (unsigned)(L_B1 + wave * 12288);
#pragma unroll
        for (int n = 0; n < 6; ++n) { *(u32x4*)(lds + pkv + n * 2048) = lo4(wl[n >> 1][n & 1]); *(u32x4*)(lds + pkv + n * 2048 + 1024) = hi4(wl[n >> 1][n & 1]); }
      }
      if (i == 8) {
        PINV(xv); pk3 = xv + (unsigned)(L_B2 + wave * 12288);
#pragma unroll
        for (int t = 0; t < 2; ++t) { *(u32x4*)(lds + pk3 + t * 2048) = lo4(res[t][1]); *(u32x4*)(lds + pk3 + t * 2048 + 1024) = hi4(res[t][1]); }
      }
      if (i == 9) {
        PINV(xv); pk4 = xv + (unsigned)(L_B0 + wave * 12288);
#pragma unroll
        for (int n = 0; n < 6; ++n) { *(u32x4*)(lds + pk4 + n * 2048) = lo4(res[n & 1][n >> 1]); *(u32x4*)(lds + pk4 + n * 2048 + 1024) = hi4(res[n & 1][n >> 1]); }
      }
      SB();
    SEND;
  }
  const int lane = lane_id(), l15 = lane & 15, lq = lane >> 4;
  constexpr int L_VA = 10240;
  unsigned pol;
  {
    VMW(0); PINV(pf);
    pol = (unsigned)__builtin_amdgcn_readfirstlane(pf.x);
    if (__builtin_expect((pol & ~7u) != tag.x || (unsigned)__builtin_amdgcn_readfirstlane(pf.y) != tag.y, 0)) pol = wait_tag<8>(cnt, C_GO, tag);
    pol = uni(pol & 1u);
    SIDES();
    __builtin_amdgcn_raw_buffer_store_b32(1u, mkrs(cn + CNT(C_PZ + h)), lane == 0 ? 4 * wave : (int)0x80000000u, 0, 16);
  }
  const __amdgpu_buffer_rsrc_t rs_x1 = mkrs(X1), rs_z = mkrs(ws + O_ZB), rs_ts = mkrs(ws + O_TS + half_of(ws + O_TS, 0u, 0u, pol));
  int ix = (l15 & 2) ? 0xEEEE : 0x4444; PINV(ix);
  float one = 1.0f; PINS(one);
  unsigned ia = (unsigned)(L_VA + wave * 384 + ((l15 >> 2) & 1) * 128 + (l15 >> 3) * 64 + (l15 & 1) * 32 + lq * 8 + ((l15 >> 1) & 1) * 4);
  unsigned rda = (unsigned)(L_VA + (lq >> 1) * 768 + (l15 >> 3) * 384 + (lq & 1) * 64 + ((l15 >> 2) & 1) * 32 + (l15 & 3) * 8);
  unsigned zvo = (unsigned)((2 * wave + (lq >> 1)) * 16384 + h * 256 + l15 * 16 + (lq & 1) * 8);
  unsigned evo = (unsigned)((2 * wave + (lq & 1) + 8 * (lq >> 1)) * 16384 + h * 256 + l15 * 16);
  unsigned gvo = xv + (unsigned)(wave * 2048), gv2 = xv + 16384u + (unsigned)((wave >> 1) * 1024);
  u64 hmask = 0xffffffffull << (32 * (wave & 1)); PINS(hmask);
  const unsigned gm0 = lds_addr(lds) + (unsigned)(L_MEM + ((wave - HROT) & 3) * 2048), gm2 = lds_addr(lds) + (unsigned)(L_MEM + 8192 + (wave >> 1) * 1024);
  PINV(ia); PINV(rda); PINV(zvo); PINV(gvo); PINV(gv2); PINV(pkv); PINV(pk3); PINV(pk4);
  bool was_calm = false;
  unsigned tsv = ~0u, tries = 0; int dly = a.d[1]; PINS(dly);
#define PLO(b_, n_) (*(const u32x4*)(lds + (b_) + (n_) * 2048))
#define PHI(b_, n_) (*(const u32x4*)(lds + (b_) + (n_) * 2048 + 1024))
#define MV(t_, f_, w_) SMFV(av[t_], f_, w_)
#define VW(t_, k_) SMFW(av[t_], A[k_], ZFV(t_, k_))
#define MO(n_, j_, w_) SMFV(az[n_], va[j_], w_)
#define OW(n_, j_) SMFW(az[n_], va[j_], ZFO(n_, j_))
#define AFR(k_) (*(const u32x4*)(lds + afb + (k_) * 1024))
  unsigned afb = xv + L_MEM; PINV(afb);
#define G_LOAD() do { asm volatile("s_mov_b32 m0, %3\n s_nop 0\n global_load_lds_dwordx4 %0, %4 sc1\n global_load_lds_dwordx4 %0, %4 offset:1024 sc1\n s_mov_b32 m0, %2\n s_mov_b64 exec, %5\n global_load_lds_dwordx4 %1, %4 sc1\n s_mov_b64 exec, -1\n s_waitcnt vmcnt(2)\n s_barrier" \
      :: "v"(gvo), "v"(gv2), "s"(gm2), "s"(gm0), "s"(xs), "s"(hmask) : "memory"); SB(); } while (0)
  u32x4 ts_desc = mkdesc(ws + O_TS + half_of(ws + O_TS, 0u, 0u, pol) + 16384 + 256 * h);
  int w3m = wave, dzs = a.d[9]; PINS(w3m); PINS(dzs); PINS(ts_desc);
  u32x4 sent4 = SENT4; unsigned ev2 = lane < 32 ? evo : 0x80000000u; if (ZMID == 1) { PINV(sent4); PINV(evo); PINV(ev2); }
  u32x4 z_desc = mkdesc(ws + O_ZB); PINS(z_desc);
#pragma unroll 1
  for (int s = 0; s < 64; ++s) {
    f32x4 av[3], az[10];
#pragma unroll
    for (int n = 0; n < 10; ++n) { az[n] = ZERO4; PINV(az[n]); }
    unsigned snp = (unsigned)(s + 1) * 0x01010101u, snv = snp, adj = 0; PINS(snp); PINV(snv);
    const unsigned so = half_of(X1, XSLOT(s), 0u, pol);
    const char* xs = X1 + so; int tss = TS_STEP(s); PINS(xs); PINS(tss);
    unsigned zs[5], tnx = (unsigned)__builtin_amdgcn_readfirstlane(wave == 3 && tsv != ~0u ? tsv + (unsigned)dly : 0u); PINS(tnx);
    { const unsigned z0 = half_of(ws + O_ZB, ZSLOT(s), 0u, pol);
#pragma unroll
      for (int i = 0; i < 5; ++i) { zs[i] = z0 + (unsigned)OROT(i) * (8u * 16384u); PINS(zs[i]); } }
    unsigned es[3]; es[0] = half_of(ws + O_ZB, ZSLOT(s + PRE), 0u, pol); es[1] = es[0] + 16u * 16384u; es[2] = es[0] + 32u * 16384u; if (ZMID == 1) { PINS(es[0]); PINS(es[1]); PINS(es[2]); }
    if (s == 1 && wave == 3) x1_light(rs_x1, so, tmo);
    SB();
    asm volatile("s_branch .Lzre\n"
                 ".Lzbad:\n s_waitcnt lgkmcnt(0)\n s_add_u32 %0, %0, 1\n s_add_u32 %1, %1, 1\n s_cmp_lt_u32 %0, 4\n s_cbranch_scc1 .Lznow\n s_sleep 8\n"
                 ".Lznow:\n s_cmp_lg_u32 %6, 3\n s_cbranch_scc1 .Lzre\n s_memrealtime vcc\n s_waitcnt lgkmcnt(0)\n s_add_u32 vcc_lo, vcc_lo, %7\n s_and_b32 vcc_lo, vcc_lo, -2\n v_mov_b32 %2, vcc_lo\n"
                 " s_mov_b64 exec, 1\n buffer_store_dword %2, off, %4, %5 sc1\n s_mov_b64 exec, -1\n v_mov_b32 %2, %3\n"
                 ".Lzre:" : "+s"(adj), "+s"(tries) : "v"(snv), "s"(snp), "s"(ts_desc), "s"(tss), "s"(w3m), "s"(dzs) : "vcc", "scc", "s92", "s93", "s94", "s95", "s96", "s97", "s98", "s99", "memory");
    SB();
    u32x8 ra = cat8(PLO(pkv, 0), PHI(pkv, 0)), rb = cat8(PLO(pkv, 1), PHI(pkv, 1));
    av[0] = ZERO4; av[1] = ZERO4; av[2] = ZERO4;
    asm volatile("" : "+v"(av[0]), "+v"(av[1]), "+v"(av[2]));
    SB();
    WAIT_GO(tnx);
    BAR();
    G_LOAD();
    u32x4 A[10], lo, hi, va[3];
    unsigned w0, w1;
    float ta, tb_;
    u64 nanm;
    DS(A[0] = AFR(0)); DS(A[2] = AFR(2)); DS(A[4] = AFR(4)); DS(A[6] = AFR(6));
    VW(0, 0); VW(1, 0); asm volatile("s_waitcnt vmcnt(0)\n s_barrier" ::: "memory"); SB();
    VW(2, 0); DS(A[8] = AFR(8));
    VW(0, 2); DS(A[9] = AFR(9));
    VW(1, 2); DS(A[1] = AFR(1));
    VW(2, 2); DS(A[3] = AFR(3));
    VW(0, 4); DS(A[5] = AFR(5));
    VW(0, 6); DS(A[7] = AFR(7));
    MV(0, A[8], ra); DS(lo = PLO(pkv, 2));
    MV(0, A[9], rb); DS(hi = PHI(pkv, 2)); ra = cat8(lo, hi);
    VW(0, 1); DS(lo = PLO(pkv, 3));
    VW(0, 3); DS(hi = PHI(pkv, 3)); rb = cat8(lo, hi);
#define ZEM(v_, i_) do { if (ZMID == 1) { asm volatile("buffer_store_dwordx4 %0, %1, %2, %3 offen sc1" :: "v"(sent4), "v"(v_), "s"(z_desc), "s"(es[i_]) : "memory"); SB(); } } while (0)
    VW(0, 5); ZEM(evo, 0); VW(0, 7); ZEM(evo, 1);
    VW(1, 4); ZEM(ev2, 2); VW(1, 6);
    MV(1, A[8], ra); DS(lo = PLO(pkv, 4)); ADD1(ta, av[0][0], av[0][2]);
    VW(1, 1); DS(hi = PHI(pkv, 4)); ra = cat8(lo, hi); ADD1B(tb_, av[0][1], av[0][3]);
    VW(1, 3); LGW(2); CVT1(w0, ta, tb_);
    MV(1, A[9], rb); DS(LW32(ia, 0, w0));
    VW(1, 5); DS(lo = PLO(pkv, 5)); NANM(nanm, ta, tb_);
    VW(1, 7); DS(hi = PHI(pkv, 5)); rb = cat8(lo, hi);
    VW(2, 4);
    if (__builtin_expect(nanm != 0ull, 0)) {
      unsigned m = ~0u;
#define Z_BACK() asm volatile("v_cmp_eq_u32 vcc, 0, %0\n s_cmp_lt_u32 %1, %2\n s_cselect_b64 vcc, vcc, 0\n s_cbranch_vccnz .Lzbad" : "+v"(m) : "s"(tries), "n"(CH_SPIN_CAP) : "vcc", "scc")
#pragma unroll
      for (int k = 0; k < 10; k += 2) m = sent_min(m, A[k].x, A[k + 1].x);
      Z_BACK();
#pragma unroll
      for (int k = 0; k < 10; ++k) m = sent_min(m, A[k]);
      Z_BACK();
    }
    asm volatile("" :: "s"(xs), "s"(tnx), "s"(tries), "s"(tss), "s"(snp), "s"(ts_desc), "s"(w3m), "s"(dzs), "v"(snv));
    if (ZMID == 1) asm volatile("" :: "s"(es[0]), "s"(es[1]), "s"(es[2]));
    PINS(adj);
    u32x2 g0, g1;
#define VR0(j_) DS(g0 = trrd(lds, rda + (unsigned)((j_) * 1536)))
#define VR1(j_) do { DS(g1 = trrd(lds, rda + (unsigned)((j_) * 1536 + 128))); va[j_] = (u32x4){g0.x, g0.y, g1.x, g1.y}; } while (0)
    VW(2, 6); LBAR(2);
    MV(2, A[8], ra); VR0(0); ADD1(ta, av[1][0], av[1][2]);
    VW(2, 1); VR1(0); ADD1B(tb_, av[1][1], av[1][3]);
    VW(2, 3); DS(lo = PLO(pk4, 0)); CVT1(w0, ta, tb_);
    VW(2, 5); DS(LW32(ia, 1536, w0));
    MV(2, A[9], rb); DS(hi = PHI(pk4, 0)); ra = cat8(lo, hi);
    VW(2, 7); DS(lo = PLO(pk4, 1));
    OW(0, 0); DS(hi = PHI(pk4, 1)); rb = cat8(lo, hi);
    OW(1, 0); LBAR(3);
    OW(2, 0); VR0(1); ADD1(ta, av[2][0], av[2][2]);
    OW(3, 0); VR1(1); ADD1B(tb_, av[2][1], av[2][3]);
    OW(4, 0); LGW(2); CVT1(w0, ta, tb_);
    MO(8, 0, ra); DS(LW32(ia, 3072, w0));
    MO(9, 0, rb); DS(lo = PLO(pk3, 0));
    OW(5, 0); DS(hi = PHI(pk3, 0)); ra = cat8(lo, hi);
    OW(6, 0); DS(lo = PLO(pk3, 1));
    OW(7, 0); LGW(3); MEMB(); __builtin_amdgcn_s_barrier(); MEMB(); SB();
    OW(0, 1); VR0(2); VR1(2);
    OW(1, 1); DS(hi = PHI(pk3, 1)); rb = cat8(lo, hi);
    OW(2, 1);
    MO(6, 1, ra); DS(lo = PLO(pk4, 2));
    OW(3, 1); DS(hi = PHI(pk4, 2)); ra = cat8(lo, hi);
#define ZSW() do { asm volatile("v_permlane32_swap_b32 %0, %1" : "+v"(w0), "+v"(w1)); SB(); } while (0)
#define ZST(i_) do { const u32x2 w_ = {w0, w1}; asm volatile("buffer_store_dwordx2 %0, %1, %2, %3 offen sc1" :: "v"(w_), "v"(zvo), "s"(z_desc), "s"(zs[i_]) : "memory"); SB(); } while (0)
    OW(0, 2);
    OW(1, 2);
    MO(7, 1, rb); DS(lo = PLO(pk4, 3));
    OW(2, 2); DS(hi = PHI(pk4, 3)); rb = cat8(lo, hi); ADD2(ta, tb_, az[0]);
    OW(3, 2); LGW(2); CVT1(w0, ta, tb_); ADD1(ta, az[1][0], az[1][2]);
    MO(8, 1, ra); DS(lo = PLO(pk4, 4)); ADDCVT(w1, ta, az[1]);
    OW(6, 2); DS(hi = PHI(pk4, 4)); ra = cat8(lo, hi); ZSW();
    OW(7, 2); ZST(0); ADD2(ta, tb_, az[2]);
    OW(4, 1); LGW(2); CVT1(w0, ta, tb_); ADD1(ta, az[3][0], az[3][2]);
    MO(9, 1, rb); DS(lo = PLO(pk4, 5)); ADDCVT(w1, ta, az[3]);
    OW(5, 1); DS(hi = PHI(pk4, 5)); rb = cat8(lo, hi); ZSW();
    OW(4, 2); ZST(1); ADD2(ta, tb_, az[6]);
    OW(5, 2); LGW(2); CVT1(w0, ta, tb_); ADD1(ta, az[7][0], az[7][2]);
    MO(8, 2, ra); LGW(0); ADDCVT(w1, ta, az[7]);
    MO(9, 2, rb);
    asm volatile("v_add_f32 v2, %2, %4\n v_permlane32_swap_b32 %0, %1\n v_add_f32 v0, %3, %5\n v_cvt_pk_bf16_f32 v0, v2, v0\n v_add_f32 v2, %6, %8\n v_add_f32 v1, %7, %9\n v_cvt_pk_bf16_f32 v1, v2, v1"
                 : "+v"(w0), "+v"(w1) : "v"(az[4][0]), "v"(az[4][1]), "v"(az[4][2]), "v"(az[4][3]), "v"(az[5][0]), "v"(az[5][1]), "v"(az[5][2]), "v"(az[5][3]) : "v0", "v1", "v2", "v3");
    { const u32x2 w_ = {w0, w1};
      asm volatile("buffer_store_dwordx2 %0, %1, %2, %3 offen sc1\n v_add_f32 v3, %6, %8\n v_permlane32_swap_b32 v0, v1\n v_add_f32 v2, %7, %9\n buffer_store_dwordx2 v[0:1], %1, %2, %4 offen sc1\n v_cvt_pk_bf16_f32 v2, v3, v2\n"
                   "v_add_f32 %10, %10, %12\n v_add_f32 v3, %11, %13\n v_cvt_pk_bf16_f32 v3, %10, v3\n s_nop 1\n v_permlane32_swap_b32 v2, v3\n buffer_store_dwordx2 v[2:3], %1, %2, %5 offen sc1"
                   :: "v"(w_), "v"(zvo), "s"(z_desc), "s"(zs[3]), "s"(zs[2]), "s"(zs[4]), "v"(az[8][0]), "v"(az[8][1]), "v"(az[8][2]), "v"(az[8][3]), "v"(az[9][0]), "v"(az[9][1]), "v"(az[9][2]), "v"(az[9][3]) : "v0", "v1", "v2", "v3", "memory"); }
    SB();
    ADAPT(dly, adj && was_calm);
    if (s < 63) {
      if (ZMID == 0 && s + PRE < 64) {
        const unsigned e0 = half_of(ws + O_ZB, ZSLOT((s + PRE)), 0u, pol);
        st16(rs_z, evo, e0, SENT4); st16(rs_z, evo, e0 + 16u * 16384u, SENT4); st16(rs_z, lane < 32 ? evo : 0x80000000u, e0 + 32u * 16384u, SENT4);
      }
      if (wave == 3) { tsv = ts_settle(rs_ts, tss, 0u, a.d[4]); was_calm = !(tsv != ~0u && (int)(tsv - tnx) > dzs + a.d[10]); if (!was_calm) tsv += (unsigned)a.d[12]; }
    }
  }
}

#define WCK 3
#define DIAG 0
#define RROT 1
#define UNAP 0
__device__ __forceinline__ void r_role(const Args& a, const u32x2 tag, unsigned char* lds, const int c, const int wave) {
  char* ws = a.ws; asm volatile("" : "+s"(ws));
  unsigned* const cnt = (unsigned*)(ws + O_CNT); unsigned* const tmo = cnt + CNT(C_TMO);
  char* const X1 = ws + O_X1; char* const P = ws + O_PS;
  const int lane = lane_id(), l15 = lane & 15, lq = lane >> 4;
  const unsigned pol = wait_tag<8>(cnt, C_POL, tag);
  SIDES();
  const __amdgpu_buffer_rsrc_t rs_x1 = mkrs(X1 + (c >> 5) * 16384 + (c & 31) * 256), rs_p = mkrs(P + half_of(P, 0u, 0u, pol)), rs_z = mkrs(ws + O_ZB + (size_t)c * 16384);
  const __amdgpu_buffer_rsrc_t rs_ts = mkrs(ws + O_TS + half_of(ws + O_TS, 0u, 0u, pol));
  unsigned xv = (unsigned)lane * 16u; PINV(xv);
  {
    const u32x4 ones = {~0u, ~0u, ~0u, ~0u};
    static_assert(PRE == 4, "");
#pragma unroll
    for (int i = 0; i < 8; ++i) st16(rs_z, xv + (unsigned)((i & 3) * 1024), half_of(ws + O_ZB, ZSLOT(wave), 0u, pol) + (unsigned)((i >> 2) * 4096), SENT4);
    st16(rs_p, lane < 32 ? xv : 0x80000000u, (unsigned)(PSLOT(wave) + (c >> 4) * 16384 + (c & 15) * 512), SENT4);
    st16(rs_x1, lane < 16 ? xv : 0x80000000u, half_of(X1, XSLOT((wave + 1)), 0u, pol), SENT4);
    if (c < 4) st16(rs_ts, xv, (unsigned)((c >> 1) * 16384 + (c & 1) * 4096 + wave * 1024), ones);
    if (wave == 0) {
      const int q = lane >> 2, r = (q >> 2) * 2 + (q & 1);
      const unsigned v = *(const unsigned*)(a.mem0 + r * 640 + 16 * c + 4 * (lane & 3) + (q & 2));
      __builtin_amdgcn_raw_buffer_store_b32(v == SENT ? ~0u : v, rs_x1, lane * 4, (int)half_of(X1, 0u, 0u, pol), 16);
    }
    VMW(0);
    BAR();
    if (wave != 0) return;
    fill_done(cnt, tag, pol);
  }
  const u32x4 rs_u = mkdesc(ws + O_UO);
  const __amdgpu_buffer_rsrc_t rs_pw = mkrs(cn + CNT(C_PW) + ((c >> 1) & 3));
  int ix = (l15 & 2) ? 0xEEEE : 0x4444; PINV(ix);
  int xo = (4 * lq + (l15 & 3)) * 16 + (l15 >> 2) * 4; unsigned sel = (l15 & 1) ? 0x07060302u : 0x01000504u;
  int tsa = (lane & 31) * 4;
  PINV(xo); PINV(sel); PINV(tsa);
#define RG(j_) (((unsigned)((j_) + RROT) + (RROT ? (unsigned)c / 10u : 0u)) & 3u)
  WReg W;
#define U_LOAD(sg_, def_) SFOR(i, 16) ld_w(rs_u, xv, u_off((unsigned)(sg_), (unsigned)c) + RG(i >> 2) * 8192u + (unsigned)(((i >> 1) & 1) * 4096), (i & 1) * 2048, i, (def_) && (i & 3) == 0, W); nap4<UNAP>(); SEND
  wait_tag<8>(cnt, C_GO, tag);
  wait_heads<8>(cn + CNT(C_PZ) + ((c >> 1) & 3), 0, 1u, tmo);
  unsigned wl = __builtin_amdgcn_raw_buffer_load_b32(rs_pw, tsa * 32, 0, 16), tsl = 0; int tso = TS_OFF(lane), two = tsa * 32; PINV(tso); PINV(two); PINV(wl);
  const u32x4 ts_desc = mkdesc(ws + O_TS + half_of(ws + O_TS, 0u, 0u, pol));
  U_LOAD(0, true);
  unsigned tsv = ~0u, tpub; int dlp = a.d[2]; PINS(dlp);
  { u64 t_; RT_MARK(t_); tpub = (unsigned)t_ - 4096u; }
#pragma unroll 1
  for (int s = 0; s < 63; ++s) {
    unsigned ps[4], pq[4], zs[4], xs;
    const unsigned z0 = half_of(ws + O_ZB, ZSLOT(s), 0u, pol);
#pragma unroll
    for (int j = 0; j < 4; ++j) { const unsigned g = RG(j); ps[j] = (unsigned)PSLOT(s) + (g >> 1) * 16384u + (g & 1u) * 4096u; pq[j] = (unsigned)PSLOT(s) + 32768u + g * 1024u; zs[j] = z0 + g * 2048u; PINS(ps[j]); PINS(pq[j]); PINS(zs[j]); }
    xs = half_of(X1, XSLOT((s + 1)), 0u, pol); PINS(xs);
    int tss = TS_STEP(s); PINS(tss);
    asm volatile("s_waitcnt vmcnt(32)" : "+v"(tsl) :: "memory");
    tsv = s < 2 ? ~0u : ts_max(tsl);
    if (__builtin_expect(s >= 2 && (tsv == ~0u || (int)(tsv - tpub) < a.d[8]), 0)) { tsv = ts_settle(rs_ts, tss, tsv, a.d[4]); }
    if (ZMID == 2 && s > 0 && s + PRE - 1 < 64) {
      wait_rt(tpub + (unsigned)a.d[7]);
      const unsigned e0 = half_of(ws + O_ZB, ZSLOT(s + PRE - 1), 0u, pol);
      PINV(xv);
#pragma unroll
      for (int i = 0; i < 8; ++i) st16(rs_z, xv + (unsigned)((i & 3) * 1024), e0 + (unsigned)((i >> 2) * 4096), SENT4);
    }
    int wo = __all(wl >= (unsigned)(s + 2)) ? (int)0x80000000u : two; PINV(wo);
    f32x4 o = ZERO4; PINV(o);
    unsigned tis = tsv + (unsigned)dlp; (void)tis;
#define R_LIGHT() do { const unsigned ln = (unsigned)lane_id(); light(mkrs(ws + O_ZB), ln < 32u ? z0 + (unsigned)c * 16384u + ln * 256u : (unsigned)(O_PS - O_ZB) + half_of(P, 0u, 0u, pol) + (unsigned)PSLOT(s) + 32768u + ((ln - 32u) >> 1) * 256u + (ln & 1u) * 8u, 0u, tmo); } while (0)
    if (__builtin_expect(tsv == ~0u, 0)) { R_LIGHT(); }
    tis = WAIT_GO(tsv == ~0u ? tpub : tsv + (unsigned)dlp);
    unsigned adj = 0, pk, tsx, wn; bool own = false; u64 nanm;
    u32x4 z[8], p[16], m[4];
#define RQ(i_)
#define R_LOAD() do { PINV(xv); \
      _Pragma("unroll") for (int i = 0; i < 8; ++i) DS(z[i] = ld16(rs_z, xv + (unsigned)((i & 1) * 1024), zs[i >> 1])); \
      _Pragma("unroll") for (int i = 0; i < 16; ++i) DS(p[i] = ld16(rs_p, xv + (unsigned)((i & 3) * 1024), ps[i >> 2])); \
      _Pragma("unroll") for (int i = 0; i < 4; ++i) DS(m[i] = ld16(rs_p, xv, pq[i])); \
      DS(tsx = __builtin_amdgcn_raw_buffer_load_b32(rs_ts, tso, tss, 16));        \
      DS(wn = __builtin_amdgcn_raw_buffer_load_b32(rs_pw, wo, 0, 16)); } while (0)
#define R_PROD() do { \
      SFOR(i, 16) SMFW(o, p[i], i); RQ(i); SEND; \
      _Pragma("unroll") for (int i = 0; i < 4; ++i) { const u32x8 b_ = cat8(z[2 * i], z[2 * i + 1]); SMFV(o, m[i], b_); } \
      float ta, tb_; unsigned q; \
      asm volatile("s_nop 6\n v_add_f32 %2, %5, %7\n v_add_f32 %3, %6, %8\n v_cvt_pk_bf16_f32 %0, %2, %3\n v_cmp_u_f32_e64 %1, %2, %3\n s_nop 0\n" \
                   "v_mov_b32_dpp %4, %0 quad_perm:[1,0,3,2] row_mask:0xf bank_mask:0xf\n v_perm_b32 %0, %0, %4, %9" \
                   : "=&v"(pk), "=&s"(nanm), "=&v"(ta), "=&v"(tb_), "=&v"(q) : "v"(o[0]), "v"(o[1]), "v"(o[2]), "v"(o[3]), "v"(sel)); } while (0)
    R_LOAD();
    R_PROD();
    if (__builtin_expect(nanm != 0ull, 0)) {
      own = tsv != ~0u && ts_max(tsx) == tsv;
      while (spin_check(adj, tmo)) {
        if (adj > 2u) R_LIGHT();
        else { const unsigned t_ = ts_max(tsx); if (t_ != ~0u) wait_rt(t_ + (unsigned)dlp); }
        R_LOAD();
        o = ZERO4; asm volatile("s_nop 1" : "+v"(o)); SB();
        R_PROD();
        if (nanm == 0ull) break;
        if ((adj & 7u) == 0u) {
          unsigned e = ~0u;
#pragma unroll
          for (int i = 0; i < 16; ++i) e = sent_min(e, p[i]);
#pragma unroll
          for (int i = 0; i < 8; ++i) e = sent_min(sent_min(e, z[i]), m[i >> 1]);
          if (!__any(e == 0u)) break;
        }
      }
    }
    __builtin_amdgcn_raw_buffer_store_b32(pk, rs_x1, xo, (int)xs, 16);
    SB();
    PINU(tsx);
    { u64 t_; RT_MARK(t_); tpub = (unsigned)t_; }
    ADAPT(dlp, own);
    if (s + PRE + 1 < 64) st16(rs_x1, lane < 16 ? xv : 0x80000000u, half_of(X1, XSLOT((s + PRE + 1)), 0u, pol), SENT4);
    {
      wl = max(wl, wn);
      if (__builtin_expect(!__all(wl >= (unsigned)(s + 2)), 0)) { wait_heads<4>(cn + CNT(C_PW) + ((c >> 1) & 3), 0, (unsigned)(s + 2), tmo); }
      wait_rt(tpub + (unsigned)a.d[3]);
      asm volatile("s_nop 4\n buffer_load_dword %0, %1, %2, %3 offen sc1" : "=v"(tsl) : "v"(tso), "s"(ts_desc), "s"(TS_STEP(s + 1)) : "memory");
      U_LOAD(s + 1, false);
    }
  }
  VMW(0);
}

__device__ __forceinline__ void w_role(const Args& a, const u32x2 tag, unsigned char* lds, const int h, const int wave) {
  char* ws = a.ws; asm volatile("" : "+s"(ws));
  unsigned* const cnt = (unsigned*)(ws + O_CNT); unsigned* const tmo = cnt + CNT(C_TMO);
  const int lane = lane_id(), l15 = lane & 15, lq = lane >> 4;
  WReg W;
  {
    ODMA_INIT();
    ODMA(0, 0); ODMA(1, 1); ODMA(2, 2);
#pragma unroll
    for (int i = 0; i < 5; ++i) {
      if (i < 3) VMW(24); else if (i == 3) VMW(12); else VMW(0);
      BAR();
#pragma unroll
      for (int t = 0; t < 2; ++t) {
        const unsigned ad = lds_addr(lds) + otr[t] + (unsigned)SBUF(i);
        TRO4(ad, 0, 6 * i + 3 * t); TRO4(ad, 1, 6 * i + 3 * t + 1); TRO4(ad, 2, 6 * i + 3 * t + 2);
      }
      if (i + 3 < 5) { BAR(); ODMA(i + 3, i + 3); }
    }
  }
  const __amdgpu_buffer_rsrc_t rs_u = mkrs(ws + O_UO + h * 1024);
  if (TM == 0) touch_n<5>(a, 4 * h + wave, 0, 64);
  if (TM == 3) touch_n<4>(a, 4 * h + wave, 0, 96);
  const unsigned pol = wait_tag<8>(cnt, C_POL, tag), kkey = K_KEY();
  SIDES();
  const __amdgpu_buffer_rsrc_t rs_pv = mkrs(cf + CNT(C_PV + h)), rs_pw = mkrs(cn + CNT(C_PW + h));
  unsigned xv = (unsigned)lane * 16u; int flo = lane == 0 ? 4 * wave : (int)0x80000000u; PINV(xv); PINV(flo);
  unsigned uo[10];
#pragma unroll
  for (int i = 0; i < 10; ++i) { uo[i] = (unsigned)(8 * OROT(i >> 1) + 2 * wave + (i & 1)) << 15; PINS(uo[i]); }
  const __amdgpu_buffer_rsrc_t rs_po = mkrs(cf + CNT(C_PO)); int poo = wave == 0 ? (lane % 24) / 6 * 128 + (lane % 6) * 4 : (int)0x80000000u; PINV(poo);
  const char* vsrc = ws + O_VF + (size_t)h * 32768 + (wave >> 1) * 16384 + (wave & 1) * 3072;
#define V_DMA(sg_) do { m0_set(lds_addr(lds) + (unsigned)(((sg_) % 3) * 12288 + wave * 3072)); const char* p_ = vsrc + KSLOT(sg_); asm volatile("s_nop 4" : "+s"(p_)); glds_x<0>(p_, xv); glds_x<1024>(p_, xv); glds_x<2048>(p_, xv); } while (0)
  BAR();
  wait_t<2>(cf + CNT(C_PV + h), 1, 2u, tmo);
  V_DMA(1); V_DMA(2);
  VMW(3);
  int nxt = 3;
#pragma unroll 1
  for (int s = 1; s < 64; ++s) {
    unsigned ub = u_off((unsigned)s, 0u), sv = (unsigned)s, va = lds_addr(lds) + (unsigned)((s % 3) * 12288) + xv; PINS(ub); PINV(sv); PINV(va);
    f32x4 acc[2][2]; u32x4 vx[12], pw, w; unsigned sp; int vprog; bool late, far;
    BAR();
#pragma unroll
    for (int n = 0; n < 12; ++n) DS(vx[(n >> 1) + 6 * (n & 1)] = *(const LAS u32x4*)(size_t)(va + ((n >> 1) + 6 * (n & 1)) * 1024));
    SFOR(t, 11)
      SFOR(m, 12)
        constexpr int e = m >> 1, rt = m & 1, c = (t + 1) & 1;
        if (t < 10) { mf_w<false>(acc[t & 1][rt], vx[6 * rt + e], 6 * (t % 10) + e, e == 0, W); SB(); }
        else if (m == 0) { asm volatile("s_nop 7" : "+v"(acc[1][0]), "+v"(acc[1][1])); SB(); }
        if (t > 0 && m >= 2 && m < 6) { if (t < 10) FILL(); CVT1(w[(m + 2) & 3], acc[c][(m >> 1) - 1 & 1][2 * (m & 1)], acc[c][(m >> 1) - 1 & 1][2 * (m & 1) + 1]); }
        if (t > 0 && m == 6) { if (t < 10) __builtin_amdgcn_raw_buffer_store_b128(w, rs_u, (int)xv, (int)(ub + uo[(t + 9) % 10]), 16); else st16(rs_u, xv, ub + uo[9], w); SB(); }
        if (t == 0 && m == 1) { pw = ld16(rs_pv, 0u, 0u); SB(); }
        if (t == 0 && m == 4) { sp = __builtin_amdgcn_raw_buffer_load_b32(rs_po, poo, 0, 16); SB(); }
        if (t == WCK && m == 8) { vprog = (int)min_t(pw, 1); PINS(vprog); far = wave == 0 && __any((int)sp < s + 2 - WGATE); MEMB(); SB(); }
        if (t == WCK && m == 10) { __builtin_amdgcn_raw_buffer_store_b32(sv, rs_pw, flo, 0, 16); SB(); }
        if (t == WCK + 1 && m == 0) {
          late = nxt <= s + 1;
          const int upto = min(min(vprog, s + 2), 63);
          while (nxt <= upto) { V_DMA(nxt); ++nxt; }
          SB(); }
        if (t == 6 && m == 8) { pw = ld16(rs_pv, 0u, 0u); SB(); }
      SEND;
    SEND;
    if (__builtin_expect(nxt <= s + 1 || far, 0)) {
      const int upto = min(min((int)min_t(pw, 1), s + 2), 63);
      while (nxt <= upto) { V_DMA(nxt); ++nxt; }
      VMW(0); __builtin_amdgcn_raw_buffer_store_b32((unsigned)(s + 1), rs_pw, flo, 0, 16);
      if (s < 63) {
        if (far) { unsigned spins = 0; while (__any((int)__builtin_amdgcn_raw_buffer_load_b32(rs_po, poo, 0, 16) < s + 2 - WGATE) && spin_check(spins, tmo)) __builtin_amdgcn_s_sleep(24); MEMB(); }
        if (nxt <= s + 1) { wait_t<1>(cf + CNT(C_PV + h), 1, (unsigned)(s + 1), tmo); V_DMA(nxt); ++nxt; VMW(0); }
      }
    } else if (late) asm volatile("s_waitcnt vmcnt(%0)" :: "n"(11 - WCK) : "memory");
  }
}

#define TPF 0
#define SAUX 16
#define TLAG 5
#define TGATE NK
#define TSTRICT 24
#define LDS16(ad_) (*(LAS u32x4*)(size_t)(ad_))
struct TReg { u32x32 b[12]; u32x16 e; };
#define TR_SW(g_, X) switch (g_) { case 0: X("a", "{a[0:31]}", T.b[0]); break; case 1: X("a", "{a[32:63]}", T.b[1]); break; case 2: X("a", "{a[64:95]}", T.b[2]); break; case 3: X("a", "{a[96:127]}", T.b[3]); break; \
    case 4: X("a", "{a[128:159]}", T.b[4]); break; case 5: X("a", "{a[160:191]}", T.b[5]); break; case 6: X("a", "{a[192:223]}", T.b[6]); break; case 7: X("a", "{a[224:255]}", T.b[7]); break; \
    case 8: X("v", "{v[110:141]}", T.b[8]); break; case 9: X("v", "{v[142:173]}", T.b[9]); break; case 10: X("v", "{v[174:205]}", T.b[10]); break; case 11: X("v", "{v[206:237]}", T.b[11]); break; default: X("v", "{v[238:253]}", T.e); break; }
__device__ __forceinline__ void mf_t(f32x4& c, const u32x4 x, const int d, const bool first, const TReg& T) {
  const int r = d < 64 ? 4 * d : 4 * d - 146;
#define X_(rf_, cs_, v_) if (first) asm volatile("v_mfma_f32_16x16x32_bf16 %0, " rf_ "[%c2:%c3], %1, 0" : "=&v"(c) : "v"(x), "n"(r), "n"(r + 3), cs_(v_)); \
      else asm volatile("v_mfma_f32_16x16x32_bf16 %0, " rf_ "[%c2:%c3], %1, %0" : "+v"(c) : "v"(x), "n"(r), "n"(r + 3), cs_(v_))
  TR_SW(d >> 3, X_);
#undef X_
}
__device__ __forceinline__ void tr_t(const unsigned ad0, const unsigned ad1, const int off, const int d, TReg& T) {
  const int r = d < 64 ? 4 * d : 4 * d - 146;
#define Y_(md_, rf_, cs_, v_) asm volatile("ds_read_b64_tr_b16 " rf_ "[%c5:%c6], %1 offset:%3\n ds_read_b64_tr_b16 " rf_ "[%c7:%c8], %1 offset:%4\n ds_read_b64_tr_b16 " rf_ "[%c9:%c10], %2 offset:%3\n ds_read_b64_tr_b16 " rf_ "[%c11:%c12], %2 offset:%4\n s_waitcnt lgkmcnt(0)" \
    : md_ cs_(v_) : "v"(ad0), "v"(ad1), "n"(off), "n"(off + 1536), "n"(r), "n"(r + 1), "n"(r + 2), "n"(r + 3), "n"(r + 4), "n"(r + 5), "n"(r + 6), "n"(r + 7) : "memory")
#define X_(rf_, cs_, v_) if ((d & 7) == 0) Y_("=&", rf_, cs_, v_); else Y_("+", rf_, cs_, v_)
  TR_SW(d >> 3, X_);
#undef X_
#undef Y_
}
__device__ __forceinline__ void t_role(const Args& a, const u32x2 tag, unsigned char* lds, const int h, const int isv, const int wave) {
  char* ws = a.ws; asm volatile("" : "+s"(ws));
  unsigned* const cnt = (unsigned*)(ws + O_CNT); unsigned* const tmo = cnt + CNT(C_TMO);
  const int lane = lane_id(), l15 = lane & 15, lq = lane >> 4;
  TReg T;
#define TBUF(j_) (24576u + (unsigned)(((j_) + 3) % 6) * 20480u)
#define TSRC(c_, r_) ((((c_) - (r_) * 80u < 64u ? (r_) * 80u + (((c_) - (r_) * 80u + 16u * (unsigned)HROT) & 63u) : (c_)) ^ ((r_) & 15u)) * 16u)
#define TDMA1(src_, dst_, i_) do { const unsigned c = (unsigned)((wave + 4 * (i_)) * 64 + lane_id()), r = c / 80u; glds(src_, TSRC(c, r), (dst_) + (i_) * 4096u); } while (0)
#define TROW(j_) (16 * ((j_) + 6))
#define TDMA(j_) do { _Pragma("unroll") for (int xi_ = 0; xi_ < 5; ++xi_) TDMA1(a.Bx + (size_t)TROW(j_) * 640, lds_addr(lds) + TBUF(j_) + (unsigned)wave * 1024u, xi_); } while (0)
  const __amdgpu_buffer_rsrc_t rs_o = mkrs(ws + O_KF + (size_t)h * 32768);
  unsigned vo[2], kkey, pol;
  {
    unsigned ad0[5], ad1[5], mvo[6], qvo[3], xvo, xr, x5;
    { TRO_INIT();
#pragma unroll
      for (int t = 0; t < 5; ++t) { const bool isq = t == 4 || wave == 3; const unsigned tile = t == 4 ? 4u + (unsigned)(wave >> 1) : (wave == 3 ? (unsigned)t : (unsigned)(4 * wave + t));
        ad0[t] = lds_addr(lds) + 32768u + TRO(tile) + (isq ? 24576u : 0u); ad1[t] = lds_addr(lds) + 32768u + (isq ? TRO(tile + 6u) + 24576u : TRO(tile) + 12288u); PINV(ad0[t]); PINV(ad1[t]); } }
#pragma unroll
    for (int q = 0; q < 6; ++q) { const unsigned c = (unsigned)((wave + 4 * q) * 64 + lane), r = c / 24u, pc = (c - r * 24u) ^ (((r >> 1) & 1u) * 2u + ((r >> 3) & 1u) * 4u);
      mvo[q] = r * 12288u + 16u * pc; PINV(mvo[q]);
      if (q < 3) { qvo[q] = pc < 12u ? mvo[q] : mvo[q] + 32u * 12288u - 192u; PINV(qvo[q]); } }
    xvo = (unsigned)((8 * wave + (lane >> 3)) * 1280 + ((lane & 7) ^ (lane >> 3)) * 16);
    xr = lds_addr(lds) + (unsigned)(l15 * 128 + (lq ^ (l15 & 7)) * 16); x5 = xr + (unsigned)((wave & 1) * 2048);
    PINV(xvo); PINV(xr); PINV(x5);
    const u16* Wm = (isv ? a.Wv : a.Wk) + 192 * h; const u16* Wh = a.Wq + 192 * h + 96 * isv;
#define TW_DMA(i_) do { const int r_ = 128 * KROT((i_) >> 1) + 64 * ((i_) & 1); const unsigned d_ = lds_addr(lds) + (unsigned)(32768 + ((i_) % 3) * 36864 + wave * 1024); \
      _Pragma("unroll") for (int q_ = 0; q_ < 2; ++q_) glds(a.Bx + (32 + 32 * q_) * 640 + r_, xvo, lds_addr(lds) + (unsigned)(((i_) & 3) * 8192 + q_ * 4096 + wave * 1024)); \
      _Pragma("unroll") for (int q_ = 0; q_ < 6; ++q_) glds(Wm + (size_t)r_ * 6144, mvo[q_], d_ + (unsigned)(q_ * 4096)); \
      _Pragma("unroll") for (int q_ = 0; q_ < 3; ++q_) glds(Wh + (size_t)r_ * 6144, qvo[q_], d_ + (unsigned)(24576 + q_ * 4096)); SB(); } while (0)
    TW_DMA(0); TW_DMA(1); TW_DMA(2);
    f32x4 pa[4][4], p5[2];
    SFOR(i, 10)
      if (i < 8) VMW(22); else if (i == 8) VMW(16); else VMW(10);
      BAR();
      SFOR(t, 5)
        if (i % 3 < 2) tr_t(ad0[t], ad1[t], (i % 3) * 36864, 10 * i + 2 * t, T); else tr_t(ad0[t] + 73728u, ad1[t] + 73728u, 0, 10 * i + 2 * t, T);
      SEND;
      if (i < 9) BAR();
      if (i + 3 < 10) TW_DMA(i + 3);
      if (i == 7) TDMA(0);
      if (i == 8) TDMA(1);
      SFOR(kk, 2)
        u32x4 x_[4], y_[2];
#pragma unroll
        for (int rt = 0; rt < 4; ++rt) x_[rt] = LDS16((xr ^ (unsigned)(kk * 64)) + (i & 3) * 8192 + rt * 2048);
#pragma unroll
        for (int g = 0; g < 2; ++g) y_[g] = LDS16((x5 ^ (unsigned)(kk * 64)) + (i & 3) * 8192 + g * 4096);
        SFOR(n, 16) mf_t(pa[n >> 2][n & 3], x_[n >> 2], 10 * i + 2 * (n & 3) + kk, i == 0 && kk == 0, T); SEND;
        SFOR(g, 2) mf_t(p5[g], y_[g], 10 * i + 8 + kk, i == 0 && kk == 0, T); SEND;
        SB();
      SEND;
    SEND;
    pol = wait_tag<8>(cnt, C_POL, tag); kkey = K_KEY(); PINS(kkey);
    const int ln = lane_id();
    vo[0] = (unsigned)ln * 16u + (wave == 3 ? (unsigned)(O_QF - O_KF) + (unsigned)(3 * isv) * 1024u : (isv ? (unsigned)(O_VF - O_KF) : 0u) + (unsigned)wave * 2048u);
    vo[1] = (unsigned)ln * 8u + (unsigned)(O_QF - O_KF) + (unsigned)(3 * isv + 2) * 1024u + (unsigned)(wave >> 1) * 512u;
    PINV(vo[0]); PINV(vo[1]);
    asm volatile("s_nop 7" : "+v"(pa[3][0]), "+v"(pa[3][1]), "+v"(pa[3][2]), "+v"(pa[3][3]), "+v"(p5[0]), "+v"(p5[1]));
#pragma unroll
    for (int g = 0; g < 2; ++g) {
#pragma unroll
      for (int n = 0; n < 4; ++n) { const f32x4 c0 = pa[2 * g + (n >> 1)][2 * (n & 1)], c1 = pa[2 * g + (n >> 1)][2 * (n & 1) + 1];
        st16(rs_o, vo[0] + (unsigned)((n & 1) * 1024), KSLOT(1 + g) + (unsigned)((n >> 1) * 16384), (u32x4){cvtpk(c0[0], c0[1]), cvtpk(c0[2], c0[3]), cvtpk(c1[0], c1[1]), cvtpk(c1[2], c1[3])}); }
      st8(rs_o, vo[1], KSLOT(1 + g) + (unsigned)((wave & 1) * 16384), cvtpk(p5[g][0], p5[g][1]), cvtpk(p5[g][2], p5[g][3]));
    }
  }
  SIDES();
  const __amdgpu_buffer_rsrc_t rs_fl = mkrs(isv ? cf + CNT(C_PV + h) : cn + CNT(C_PT_ + h));
  const int lane2 = lane_id();
#define lane lane2
#define l15 (lane2 & 15)
#define lq (lane2 >> 4)
  unsigned tvo[5];
#pragma unroll
  for (int i = 0; i < 5; ++i) { const unsigned c = (unsigned)((5 * wave + i) * 64 + lane), r = c / 80u; tvo[i] = TSRC(c, r) + (unsigned)(4096 - (i - 2) * 1024); PINV(tvo[i]); }
  int flo = lane == 0 ? wave * 4 : (int)0x80000000u; PINV(flo);
  unsigned xq[4];
#pragma unroll
  for (int q = 0; q < 4; ++q) { xq[q] = lds_addr(lds) + (unsigned)TBUF(0) + (unsigned)(l15 * 1280 + ((4 * q + lq) ^ l15) * 16); PINV(xq[q]); }
#define XFRT(k_) LDS16(xq[(k_) & 3] + ((k_) >> 2) * 256)
  const unsigned fla = lds_addr(lds) + 147456u;
  unsigned flw = fla + 4u * (unsigned)lane, one = 1u; PINV(flw); PINV(one);
  if (wave == 0) *(volatile LAS unsigned*)(size_t)flw = 0u;
  unsigned gw = flw + 256u * (unsigned)(wave + 1), go = (unsigned)((lane % 24) / 6 * 128 + (lane % 6) * 4), gm = fla + 256u * (unsigned)(wave + 1), gl; PINV(gw); PINV(go); PINS(gm);
  const unsigned* gsrc = cf + CNT(C_PO); PINS(gsrc);
  *(volatile LAS unsigned*)(size_t)gw = 0u;
  u64 gb; int gt;
  unsigned po = lane < 32 ? (isv && wave < 3 ? 0x80000000u : (vo[0] & ~1023u) + (unsigned)((lane >> 4) * 16384 + (lane & 15) * 128)) : (lane < 36 ? (vo[1] & ~511u) + (unsigned)((wave & 1) * 16384 + (lane - 32) * 128) : 0x80000000u), pso = 0u, pm0 = fla + 2048u;
  if (TPF == 0 || (TPF == 2 && !isv && wave < 3)) po = 0x80000000u;
  u32x4 o_desc = mkdesc(ws + O_KF + (size_t)h * 32768); PINV(po); PINS(o_desc); PINS(pm0);
  u32x4 xa[4]; u32x2 cv[5]; unsigned fl;
  f32x4 acc[5];
#pragma unroll
  for (int t = 0; t < 5; ++t) { acc[t] = ZERO4; cv[t] = (u32x2){0u, 0u}; }
  VMW(0);
  __builtin_amdgcn_raw_buffer_store_b32(2u, rs_fl, flo, 0, 16);
  BAR();
  TDMA(2); TDMA(3);
  xa[0] = XFRT(0); xa[1] = XFRT(1);
#define T_CVT(t_) do { unsigned c0_, c1_; \
      asm volatile("v_cvt_pk_bf16_f32 %0, %2, %3\n v_cvt_pk_bf16_f32 %1, %4, %5" : "=&v"(c0_), "=&v"(c1_) : "v"(acc[t_][0]), "v"(acc[t_][1]), "v"(acc[t_][2]), "v"(acc[t_][3])); \
      cv[t_] = (u32x2){c0_, c1_}; SB(); } while (0)
#define T_ST(i_) do { PINV(vo[0]); if ((i_) < 2) __builtin_amdgcn_raw_buffer_store_b128((u32x4){cv[2 * (i_)].x, cv[2 * (i_)].y, cv[(2 * (i_) + 1) % 5].x, cv[(2 * (i_) + 1) % 5].y}, rs_o, (int)vo[0] + (i_) * 1024, (int)sof, SAUX); \
      else __builtin_amdgcn_raw_buffer_store_b64(cv[4], rs_o, (int)vo[1], (int)sof, SAUX); SB(); } while (0)
#define T_REST(five_) do { asm volatile("s_nop 7\n s_nop 7"); T_CVT(1); T_CVT(2); T_CVT(3); T_CVT(4); T_ST(0); T_ST(1); if (five_) T_ST(2); VMW(0); } while (0)
  const char* dsrc = (const char*)(a.Bx + (size_t)TROW(4) * 640) - 4096;
  unsigned tb = TBUF(4) - 24576u, rb = TBUF(0) - 24576u;
  unsigned sol = 3u * K_STEP + 16384u, sof = k_fix(sol, kkey), sinc = 0u - 16384u, tgt = 4u, dstep = 20480u, fv = 0u, dq = 0u;
  const unsigned dd0 = lds_addr(lds) + 24576u + (unsigned)(5 * wave + 2) * 1024u;
#define T_MF(t_) do { mf_t(acc[t_], xa[k & 3], 10 * (k >> 1) + 2 * (t_) + (k & 1), k == 0, T); SB(); } while (0)
#define T_TILE(STRICT, FIVE) do { \
    SFOR(k, 20) \
      constexpr int k2 = k + 2, kn = k2 < 20 ? k2 : k2 - 20; \
      T_MF(0); \
      if (k != 18) { xa[k2 & 3] = XFRT(kn); SB(); } \
      if (k == 0) T_CVT(1); \
      if (k == 18) { xq[0] += dq; PINV(xq[0]); SB(); }        \
      T_MF(1); \
      if (k == 0) T_CVT(2); \
      if (k == 2) { asm volatile("s_mov_b32 m0, %0" :: "s"(FIVE ? gm : pm0) : "memory"); SB(); } \
      if (k == 14 && FIVE) { gl = *(const volatile LAS unsigned*)(size_t)gw; SB(); } \
      if (k == 17 && FIVE) {        \
        if (__builtin_expect(gb != 0ull, 0)) { unsigned spins = 0; while (__any((int)__hip_atomic_load((const gu32*)gsrc + (go >> 2), RLX_AGENT) < gt) && spin_check(spins, tmo)) __builtin_amdgcn_s_sleep(24); } \
        MEMB(); SB(); } \
      if (k == 3) { unsigned m_ = dd0 + tb; PINS(m_); asm volatile("s_mov_b32 m0, %0" :: "s"(m_) : "memory"); SB(); } \
      if (k == 16) { fl = *(const volatile LAS unsigned*)(size_t)flw; SB(); } \
      if (k == 18) {        \
        if (__builtin_expect(fs < tgt, 0)) { do fl = *(const volatile LAS unsigned*)(size_t)flw; while ((unsigned)__builtin_amdgcn_readfirstlane(fl) < tgt); } \
        MEMB(); SB(); } \
      if (k == 9) { PINS(tb); tb += 20480u; PINS(tb); SB(); } \
      if (k == 11) { PINS(rb); rb += 20480u; PINS(rb); SB(); } \
      if (k == 12) { PINS(rb); rb = rb == 6u * 20480u ? 0u : rb; PINS(rb); SB(); } \
      if (k == 13) { PINS(sol); PINS(sinc); sol = (sol + sinc) & (NK * K_STEP - 1u); PINS(sol); SB(); } \
      if (k == 15) { PINS(dstep); dstep = j < 116 ? 20480u : 0u; PINS(dstep); SB(); } \
      if (k == 10) { fv = (unsigned)(STRICT ? (j < 1 ? 4 : j + 3) : j + 2) >> 1; PINV(fv); SB(); } \
      if (k == 19) { xq[2] += dq; PINV(xq[2]); SB(); } \
      T_MF(2); \
      if (k == 0) T_CVT(3); \
      if (k == 1 || k == 2 || (k == 3 && !FIVE)) T_ST(k >= 1 && k <= 3 ? k - 1 : 0); \
      if (k == 4) glds_o<-2048>(dsrc, tvo[0]); else if (k == 5) glds_o<-1024>(dsrc, tvo[1]); else if (k == 6) glds_o<0>(dsrc, tvo[2]); else if (k == 7) glds_o<1024>(dsrc, tvo[3]); else if (k == 8) glds_o<2048>(dsrc, tvo[4]); \
      if (k == 9) { PINS(dsrc); dsrc += dstep; PINS(dsrc); SB(); } \
      if (k == 11) { PINS(tb); tb = tb == 6u * 20480u ? 0u : tb; PINS(tb); SB(); } \
      if (k == 14) { PINS(rb); dq = rb == 0u ? 0u - 5u * 20480u : 20480u; PINS(dq); SB(); } \
      if (k == 15) { PINS(sinc); sinc = sinc == 16384u ? K_STEP - 16384u : 16384u; PINS(sinc); SB(); } \
      if (k == 12) {        \
        asm volatile("s_waitcnt vmcnt(%0)" :: "n"(STRICT ? 14 : 24) : "memory"); \
        __hip_atomic_fetch_add((LAS unsigned*)(size_t)flw, one, __ATOMIC_RELAXED, __HIP_MEMORY_SCOPE_WORKGROUP); SB(); \
      } \
      if (k == 13) { __builtin_amdgcn_raw_buffer_store_b32(fv, rs_fl, flo, 0, 16); SB(); } \
      if (k == 17) { fs = (unsigned)__builtin_amdgcn_readfirstlane(fl); PINS(fs); SB(); } \
      if (k == 18) { xa[0] = XFRT(0); SB(); xq[1] += dq; PINV(xq[1]); SB(); } \
      if (k == 19) { PINS(tgt); tgt += 4u; PINS(tgt); xq[3] += dq; PINV(xq[3]); SB(); } \
      T_MF(3); \
      if (k == 0 && !FIVE) T_CVT(4); \
      if (k == 13) { sof = (sol >> 8) & 0x2000u; PINS(sof); SB(); } \
      if (k == 16) { PINS(sof); sof ^= sol ^ kkey; PINS(sof); SB(); } \
      if (k == 2 && FIVE) { asm volatile("global_load_lds_dword %0, %1 sc1" :: "v"(go), "s"(gsrc) : "memory"); SB(); } \
      if (k == 2 && !FIVE) { asm volatile("buffer_load_dword %0, %1, %2 offen lds" :: "v"(po), "s"(o_desc), "s"(pso) : "memory"); SB(); } \
      if (k == 9 && FIVE) { pso = ((unsigned)((j >> 1) + 3 - TLAG) & (NK - 1u)) * K_STEP; PINS(pso); SB(); } \
      if (k == 10 && FIVE) { PINS(pso); pso ^= kkey; PINS(pso); SB(); } \
      if (k == 11 && FIVE) { PINS(pso); pso ^= (pso >> 8) & 0x2000u; PINS(pso); SB(); } \
      if (k == 14 && FIVE) { gt = (j >> 1) + 5 - TGATE; PINS(gt); SB(); } \
      if (k == 15 && FIVE) { asm volatile("v_cmp_gt_i32_e64 %0, %1, %2" : "=s"(gb) : "s"(gt), "v"(gl)); SB(); } \
      if (k == 19) T_CVT(0); \
      if (FIVE) T_MF(4); \
    SEND; } while (0)
  unsigned fs;
  static_assert(TSTRICT % 2 == 0, "");
#define T_ALL(ODD) do { \
    if (ODD) { const int j = 0; T_TILE(true, false); } \
    _Pragma("unroll 1") for (int j2 = ODD; j2 < TSTRICT; j2 += 2) { \
      { const int j = j2; T_TILE(true, true); } \
      { const int j = j2 + 1; T_TILE(true, false); } } \
    _Pragma("unroll 1") for (int j2 = TSTRICT + ODD; j2 < 121; j2 += 2) { \
      const int s = j2 >> 1; (void)s; \
      { const int j = j2; T_TILE(false, true); } \
      { const int j = j2 + 1; T_TILE(false, false); } } \
    if (ODD) { const int j = 121; T_TILE(false, true); } \
    T_REST(ODD); \
    __builtin_amdgcn_raw_buffer_store_b32(64u, rs_fl, flo, 0, 16); } while (0)
  if (wave & 1) T_ALL(1); else T_ALL(0);
}
#undef lane
#undef l15
#undef lq

#define KPF 2
#define K_KEY2() ((((unsigned)((size_t)(ws + O_KF) >> 21) ^ KSIDE ^ pol) & 1u) << 13)
#define XPO 0
#define OAUX 0
__device__ __forceinline__ void o_role(const Args& a, const u32x2 tag, unsigned char* lds, const int j, const int wave) {
  char* ws = a.ws; asm volatile("" : "+s"(ws));
  unsigned* const cnt = (unsigned*)(ws + O_CNT); unsigned* const tmo = cnt + CNT(C_TMO);
  const int lane = lane_id(), l15 = lane & 15, lq = lane >> 4;
  const unsigned pol = wait_tag<32>(cnt, C_POL, tag);
  SIDES();
  const int xq = j / 6, c0 = 10 * xq + 5 * (j & 1), s0 = (j - 6 * xq) >> 1;
  const u32x4 rs_u = mkdesc(ws + O_UO + wave * 8192);
  const __amdgpu_buffer_rsrc_t rs_z = mkrs(ws + O_ZB + (size_t)c0 * 16384 + wave * 2048), rs_p = mkrs(ws + O_PT + wave * 16384), rs_m = mkrs(ws + O_PT + 65536 + wave * 4096), rs_out = mkrs(a.out + 640 + 16 * c0);
  unsigned xv = (unsigned)lane * 16u, xh = (unsigned)lane * 8u;
  unsigned wa = (unsigned)(wave * 10880 + lq * 272 + l15 * 16), ra[2], oo[2];
#pragma unroll
  for (int i = 0; i < 2; ++i) {
    const int n = i == 0 ? lane : 64 + l15, tok = 8 * wave + n / 10, oc = n % 10;
    ra[i] = (unsigned)((((oc >> 1) * 2 + (tok >> 4)) * 4 + 2 * (oc & 1)) * 272 + (tok & 15) * 16);
    oo[i] = i == 0 || lane < 16 ? (unsigned)((tok * 1280 + 8 * oc) * 2) : 0x80000000u;
    PINV(ra[i]); PINV(oo[i]);
  }
  PINV(xv); PINV(xh); PINV(wa);
  WReg W;
  if (TM == 2) touch_n<10>(a, 4 * j + wave, 0, 32);
  if (TM == 3) touch_n<4>(a, 192 + 4 * j + wave, 0, 96);
  if (TM == 1) {
    const int w = 4 * j + wave;
    wait_ge<32>(cn + CNT(C_PZ + 8 * xq + (w & 7)), 1u, tmo);
    unsigned v = 0;
#pragma unroll
    for (int i = 0; i < 8; ++i) v ^= touch_rows(a, w % 3, 64 + w / 3 + 32 * i);
    v ^= touch(a.Awo, 640 * 1280, w) ^ touch(a.Awo, 640 * 1280, 96 + w);
    asm volatile("" :: "v"(v));
  }
  u32x2 wp = {0u, 0u};
  const u32x4 k_desc = mkdesc(ws + O_KF + (size_t)(8 * xq + 4 * (j & 1) + wave) * 32768); int ko = lane < 48 ? lane * 128 : (int)0x80000000u; PINV(ko);
  wait_tag<32>(cnt, C_GO, tag);
#pragma unroll 1
  for (int s = s0; s < 64; s += 3) {
    if (wave == 0) {
      const unsigned tk = __hip_atomic_load((const gu32*)(cn + CNT(TOKL(s) + xq)), RLX_AGENT), sp = __hip_atomic_load((const gu32*)(cn + CNT(C_PS + j)), RLX_AGENT);
      if (__any(min(wp.x, wp.y) < (unsigned)(s + 1))) {
        unsigned spins = 0;
        do wp = ld8(mkrs(cn + CNT(s == 0 ? C_PZ : C_PW)), (unsigned)((lane & 31) * 128 + (lane >> 5) * 8), 0u); while (__any(min(wp.x, wp.y) < (unsigned)(s + 1)) && (__builtin_amdgcn_s_sleep(16), spin_check(spins, tmo)));
      }
      if ((unsigned)__builtin_amdgcn_readfirstlane(sp) < (unsigned)s) { wait_ge<8>(cn + CNT(C_PS + j), (unsigned)s, tmo); nap_rt(a.d[14]); }
      *(volatile LAS unsigned*)(lds + 65536) = tk;
    }
    BAR();
    const bool behind = (unsigned)__builtin_amdgcn_readfirstlane(*(volatile LAS unsigned*)(lds + 65536)) >= 32u;
    SFOR(i, 20) ld_w(rs_u, xv, u_off((unsigned)s, (unsigned)(c0 + (i >> 2))) + (unsigned)(((i >> 1) & 1) * 4096), (i & 1) * 2048, i, (i & 3) == 0, W); SEND;
    {
      const unsigned kkey = K_KEY2(), so = KSLOT(s + KPF);
      asm volatile("s_mov_b32 m0, %4\n s_nop 4\n buffer_load_dword %0, %1, %2 offen sc1 lds\n buffer_load_dword %0, %1, %3 offen sc1 lds" :: "v"(ko), "s"(k_desc), "s"(so), "s"(so + 16384u), "s"(lds_addr(lds) + 66560u) : "memory");
    }
    if (!behind) {
      if (wave == 0) { if (s >= 60) wait_ge<1>(cn + CNT(TOKL(s) + xq), 32u, tmo); else wait_ge<8>(cn + CNT(TOKL(s) + xq), 32u, tmo); }
      BAR();
    }
    u32x4 z[5][2], p1[8], pm[2][2]; u32x2 p0[8];
    const unsigned zs = half_of(ws + O_ZB, ZSLOT(s), 0u, pol), ps = TSLOT(s);
    PINV(xv); PINV(xh);
#pragma unroll
    for (int n = 0; n < 8; ++n) { p1[n] = ld16(rs_p, xv + (unsigned)((n & 1) * 2048 + 1024), ps + (unsigned)((n >> 1) * 4096)); p0[n] = ld8(rs_p, xh + (unsigned)((n & 1) * 2048), ps + (unsigned)((n >> 1) * 4096)); SB(); }
#pragma unroll
    for (int n = 0; n < 4; ++n) pm[n >> 1][n & 1] = ld16(rs_m, xv + (unsigned)(n * 1024), ps);
#define O_ZLOAD() do { PINV(xv); _Pragma("unroll") for (int n = 0; n < 10; ++n) z[n >> 1][n & 1] = ld16(rs_z, xv + (unsigned)((n & 1) * 1024), zs + (unsigned)((n >> 1) * 16384)); } while (0)
    O_ZLOAD();
    SB();
    f32x4 acc[5][2];
    SFOR(n, 8)
      u32x4 q0 = {p0[n].x, p0[n].y, 0u, 0u}; PINV(q0);
      SFOR(t, 5) mf_w<true>(acc[t][1], p1[n], 8 * t + n, n == 0, W); SB(); SEND;
      SFOR(t, 5) mf_w<true>(acc[t][0], q0, 8 * t + n, n == 0, W); SB(); SEND;
    SEND;
    for (unsigned spins = 0;;) {
      unsigned e = ~0u;
#pragma unroll
      for (int n = 0; n < 10; ++n) e = sent_min(e, z[n >> 1][n & 1]);
      if (__builtin_expect(!__any(e == 0u), 1) || !spin_check(spins, tmo)) break;
      O_ZLOAD();
    }
#pragma unroll
    for (int n = 0; n < 4; ++n)
#pragma unroll
      for (int t = 0; t < 5; ++t) asm volatile("v_mfma_f32_16x16x32_bf16 %0, %1, %2, %0" : "+v"(acc[t][n & 1]) : "v"(z[t][n >> 1]), "v"(pm[n >> 1][n & 1]));
    asm volatile("s_nop 7" : "+v"(acc[0][1]), "+v"(acc[1][1]), "+v"(acc[2][1]), "+v"(acc[3][1]), "+v"(acc[4][1]));
#pragma unroll
    for (int n = 0; n < 10; ++n) *(f32x4*)(lds + wa + (n % 5 * 2 + n / 5) * 1088) = acc[n % 5][n / 5];
    BAR();
    if (wave == 0 && lane == 0) __hip_atomic_store((gu32*)(cf + CNT(C_PO + xq) + j - 6 * xq), (unsigned)(s + 3), RLX_AGENT);
#pragma unroll
    for (int i = 0; i < 2; ++i) {
      f32x4 v0 = *(const f32x4*)(lds + ra[i]), v1 = *(const f32x4*)(lds + ra[i] + 272);
#pragma unroll
      for (int w = 1; w < 4; ++w) { v0 += *(const f32x4*)(lds + ra[i] + w * 10880); v1 += *(const f32x4*)(lds + ra[i] + w * 10880 + 272); }
      const u32x4 w_ = {cvtpk(v0[0], v0[1]), cvtpk(v0[2], v0[3]), cvtpk(v1[0], v1[1]), cvtpk(v1[2], v1[3])};
      __builtin_amdgcn_raw_buffer_store_b128(w_, rs_out, (int)oo[i], 32 * s * 1280 * 2, OAUX);
      asm volatile("s_nop 1" :: "v"(w_) : "memory");
    }
  }
  VMW(0);
}

#define LAUX 16
#define XPF 0
__device__ __forceinline__ void att_role(const Args& a, const u32x2 tag, unsigned char* lds, const int h, const int wave) {
  char* ws = a.ws;
  unsigned* const cnt = (unsigned*)(ws + O_CNT); unsigned* const tmo = cnt + CNT(C_TMO);
  const int lane = lane_id(), l15 = lane & 15, lq = lane >> 4, par = wave >> 1;
  const __amdgpu_buffer_rsrc_t rs_q = mkrs(ws + O_QF + (size_t)h * 32768), rs_k = mkrs(ws + O_KF + (size_t)h * 32768), rs_km = mkrs(ws + O_KM + (size_t)h * 3072), rs_pt = mkrs(ws + O_PT);
  const float scale = 0.07216878364870323f, LOG2E = 1.4426950408889634f;
  unsigned xv = (unsigned)lane * 16u; PINV(xv);
#define KM_EMPTY(s_) do { _Pragma("unroll") for (int i_ = 0; i_ < 3; ++i_) st16(rs_km, xv + (unsigned)(i_ * 1024), MSLOT(s_), SENT4); } while (0)
#pragma unroll 1
  for (int s = par; s < NZ; s += 2) KM_EMPTY(s);
  VMW(0);
  const unsigned pol = wait_tag<16>(cnt, C_POL, tag), kkey = K_KEY();
  SIDES();
  fill_done(cnt, tag, pol);
  nap_rt(XPF);
  if (TM == 0) touch_n<5>(a, 128 + 2 * h + par, 0, 64);
  if (TM == 3) touch_n<4>(a, 128 + 2 * h + par, 0, 96);
  if (TM != 1) { const unsigned v = touch(a.Awo, 640 * 1280, 2 * h + par) ^ touch(a.Awo, 640 * 1280, 64 + 2 * h + par); asm volatile("" :: "v"(v)); }
  if (TM == 1) { const unsigned v = touch_rows(a, 1, 2 * h + par) ^ touch_rows(a, 0, 2 * h + par) ^ touch_rows(a, 2, 2 * h + par); asm volatile("" :: "v"(v)); }
  u32x4 qf[2][6], kf[2][6];
  unsigned xh = (unsigned)lane * 8u;
#define QK_LOAD(s_) do { PINV(xv); PINV(xh); _Pragma("unroll") for (int i = 0; i < 12; ++i) { const unsigned so = KSLOT(s_) + (unsigned)(i / 6 * 16384 + i % 6 / 3 * 3072), vo = xv + (unsigned)(i % 3 * 1024); \
      kf[i / 6][i % 6] = __builtin_amdgcn_raw_buffer_load_b128(rs_k, (int)vo, (int)so, LAUX); \
      if (i % 3 < 2) qf[i / 6][i % 6] = __builtin_amdgcn_raw_buffer_load_b128(rs_q, (int)vo, (int)so, LAUX); else { const u32x2 l_ = __builtin_amdgcn_raw_buffer_load_b64(rs_q, (int)(xh + 2048u), (int)so, LAUX), h_ = __builtin_amdgcn_raw_buffer_load_b64(rs_q, (int)(xh + 2560u), (int)so, LAUX); qf[i / 6][i % 6] = (u32x4){l_.x, l_.y, h_.x, h_.y}; } } SB(); } while (0)
  const __amdgpu_buffer_rsrc_t rs_tv = mkrs(cnt);
  int tvo = (lane & 3) * 4 + 4 * (int)((lane & 4) ? cf + CNT(C_PV + h) - cnt : cn + CNT(C_PT_ + h) - cnt); PINV(tvo);
#define TV_WAIT(s_) do { unsigned spins = 0; while (__any(__builtin_amdgcn_raw_buffer_load_b32(rs_tv, tvo, 0, 16) < (unsigned)(s_)) && spin_check(spins, tmo)) __builtin_amdgcn_s_sleep(16); MEMB(); } while (0)
  unsigned kmo = (unsigned)(lq * 128 + ((l15 & 1) + (l15 & 2) * 2 + ((l15 >> 1) & 2)) * 16), tlast = 0; PINV(kmo);
  if (par == 0) wait_ge<32>(cn + CNT(C_PS + h), 2u, tmo); else TV_WAIT(1);
  QK_LOAD(par);
#pragma unroll 1
  for (int s = par; s < 64; s += 2) {
    f32x4 sc[2][3];
    float mx[2];
    {
#pragma unroll
      for (int q = 0; q < 2; ++q) {
        mx[q] = -3.0e38f;
#pragma unroll
        for (int t = 0; t <= q; ++t) {
          sc[q][t] = ZERO4;
#pragma unroll
          for (int kk = 0; kk < 6; ++kk) sc[q][t] = mfma(kf[t][kk], qf[q][kk], sc[q][t]);
#pragma unroll
          for (int r = 0; r < 4; ++r) {
            const float v = rbf(rbf(sc[q][t][r]) * scale);
            sc[q][t][r] = (t < q || 4 * lq + r <= l15) ? v : -3.0e38f;
            mx[q] = fmaxf(mx[q], sc[q][t][r]);
          }
        }
        PINV(mx[q]);
      }
    }
    SB();
    unsigned tv;
    {
      u32x4 km[6];
      unsigned spins = 0;
      const unsigned t0 = wait_rt(tlast + (unsigned)a.d[15]);
      for (;;) {
        unsigned m = ~0u;
        PINV(kmo);
        tv = __builtin_amdgcn_raw_buffer_load_b32(rs_tv, tvo, 0, 16);
#pragma unroll
        for (int kk = 0; kk < 6; ++kk) { km[kk] = ld16(rs_km, kmo + (unsigned)(kk * 512), MSLOT(s)); m = sent_min(m, km[kk]); }
        if (!__any(m == 0u) || !spin_check(spins, tmo)) break;
        if (spins > 4u) __builtin_amdgcn_s_sleep(12); else __builtin_amdgcn_s_sleep(2);
      }
      tlast = t0 - 100u;
      if (spins) { u64 t_; RT_MARK(t_); tlast = (unsigned)t_; }
#pragma unroll
      for (int q = 0; q < 2; ++q) {
        sc[q][2] = ZERO4;
#pragma unroll
        for (int kk = 0; kk < 6; ++kk) sc[q][2] = mfma(km[kk], qf[q][kk], sc[q][2]);
      }
    }
    asm volatile("" : "+v"(sc[0][2]), "+v"(sc[1][2]));
    const bool ahead = s + 2 < 64 && !__any(tv < (unsigned)(s + 2));
#pragma unroll
    for (int q = 0; q < 2; ++q) {
#pragma unroll
      for (int r = 0; r < 4; ++r) { sc[q][2][r] = rbf(rbf(sc[q][2][r]) * scale); mx[q] = fmaxf(mx[q], sc[q][2][r]); }
      const float m = max32(max16(mx[q])), nml = -m * LOG2E;
      float sum = 0.f, sm = 0.f;
#pragma unroll
      for (int t = 0; t <= q; ++t)
#pragma unroll
        for (int r = 0; r < 4; ++r) { const float e = (t < q || 4 * lq + r <= l15) ? __builtin_amdgcn_exp2f(__builtin_fmaf(sc[q][t][r], LOG2E, nml)) : 0.f; sc[q][t][r] = e; sum += e; }
#pragma unroll
      for (int r = 0; r < 4; ++r) { sc[q][2][r] = __builtin_amdgcn_exp2f(__builtin_fmaf(sc[q][2][r], LOG2E, nml)); sm += sc[q][2][r]; }
      sum = sum32(sum16(__builtin_fmaf(sm, 0.5f, sum)));
      const float inv = 1.0f / sum;
      if (q == 0) st8(rs_pt, (unsigned)(h * 2048 + lane * 8), TSLOT(s), cvtpk(sc[0][0][0] * inv, sc[0][0][1] * inv), cvtpk(sc[0][0][2] * inv, sc[0][0][3] * inv));
      else st16(rs_pt, (unsigned)(h * 2048 + 1024 + lane * 16), TSLOT(s), (u32x4){cvtpk(sc[1][0][0] * inv, sc[1][0][1] * inv), cvtpk(sc[1][0][2] * inv, sc[1][0][3] * inv), cvtpk(sc[1][1][0] * inv, sc[1][1][1] * inv), cvtpk(sc[1][1][2] * inv, sc[1][1][3] * inv)});
      st8(rs_pt, lq < 2 ? (unsigned)(65536 + (h >> 2) * 2048 + q * 1024 + (h & 3) * 256 + l15 * 16 + lq * 8) : 0x80000000u, TSLOT(s), cvtpk(sc[q][2][0] * inv, sc[q][2][1] * inv), cvtpk(sc[q][2][2] * inv, sc[q][2][3] * inv));
    }
    if (s + NZ < 64) KM_EMPTY(s);
    if (ahead) { QK_LOAD(s + 2); VMW(28); } else VMW(0);
    if (lane < 4) __hip_atomic_fetch_add((gu32*)(cn + CNT(TOKL(s) + lane)), 1u, RLX_AGENT);
    if (!ahead && s + 2 < 64) { TV_WAIT(s + 2); QK_LOAD(s + 2); }
  }
}

__device__ __forceinline__ float dot2bf(unsigned x, unsigned y, float c) { return __builtin_amdgcn_fdot2_f32_bf16(__builtin_bit_cast(bf16x2_t, x), __builtin_bit_cast(bf16x2_t, y), c, false); }
__device__ __forceinline__ void a_role(const Args& a, const u32x2 tag, unsigned char* lds, const int j, const int wave) {
  char* ws = a.ws; asm volatile("" : "+s"(ws));
  unsigned* const cnt = (unsigned*)(ws + O_CNT); unsigned* const tmo = cnt + CNT(C_TMO);
  const int lane = lane_id(), l15 = lane & 15, lq = lane >> 4;
  const int c0 = 40 * j - 8 * (j & 1);
  u32x4 wt[3][20];
#define A_WDMA(src_, ld_, off_) do { const int ln_ = lane_id(); _Pragma("unroll") for (int q_ = 0; q_ < 15; ++q_) { const int ins = wave + 4 * q_; const unsigned c = (unsigned)(ins * 64 + ln_), r = c / 6u, pc = c - r * 6u; \
      glds(src_, r * (unsigned)((ld_) * 2) + 16u * pc, lds_addr(lds) + (unsigned)((off_) + ins * 1024)); } } while (0)
#define A_WREAD(off_) do { unsigned o_ = (unsigned)((off_) + (8 * lq + (l15 >> 2)) * 96 + 8 * (l15 & 3)); PINV(o_); \
    _Pragma("unroll") for (int t = 0; t < 3; ++t) { _Pragma("unroll") for (int kk = 0; kk < 20; ++kk) wt[t][kk] = trrd2(lds, o_ + (unsigned)(kk * 3072 + 32 * t), 384u); SB(); } } while (0)
  A_WDMA(a.Awqkv + c0, 1920, 0); A_WDMA(a.Awqkv + 640 + c0, 1920, 61440);
  VMW(0); BAR();
  if (wave < 2) A_WREAD(wave * 61440);
  BAR();
  A_WDMA(a.Awqkv + 1280 + c0, 1920, 0);
  VMW(0); BAR();
  if (wave == 2) A_WREAD(0);
  if (wave == 3) wait_tag<8>(cnt, C_POL, tag);
  BAR();
  constexpr int LX = 0, LR = 122880, RS = 304, NR = 128;
  unsigned dvo[10];
#pragma unroll
  for (int i = 0; i < 10; ++i) { const unsigned c = (unsigned)((wave + 4 * i) * 64 + lane), r = c / 80u; dvo[i] = (c ^ (r & 15u)) * 16u; PINV(dvo[i]); }
#define A_XDMA(src_, b_) do { _Pragma("unroll") for (int xi_ = 0; xi_ < 10; ++xi_) glds(src_, dvo[xi_], lds_addr(lds) + (unsigned)(LX + ((b_) & 1) * 40960 + wave * 1024 + xi_ * 4096)); } while (0)
  unsigned xr[4];
#pragma unroll
  for (int q = 0; q < 4; ++q) xr[q] = (unsigned)(LX + l15 * 1280 + ((4 * q + lq) ^ l15) * 16);
  unsigned dv3[14];
#pragma unroll
  for (int i = 0; i < 14; ++i) { const unsigned c = (unsigned)((13 * (wave < 3 ? wave : 0) + i) * 64 + lane), r = c / 80u; dv3[i] = (c ^ (r & 15u)) * 16u + (unsigned)(4096 - (i % 7 - 3) * 1024); PINV(dv3[i]); }
  const unsigned am0 = lds_addr(lds) + (unsigned)(LX + (13 * wave + 3) * 1024);
#define A_XD3(src_, lb_, i_) do { if ((i_) % 7 == 0) m0_set(am0 + (lb_) + (unsigned)((i_) / 7 * 7168)); \
    switch ((i_) % 7) { case 0: glds_o<-3072>(src_, dv3[i_]); break; case 1: glds_o<-2048>(src_, dv3[i_]); break; case 2: glds_o<-1024>(src_, dv3[i_]); break; case 3: glds_o<0>(src_, dv3[i_]); break; \
      case 4: glds_o<1024>(src_, dv3[i_]); break; case 5: glds_o<2048>(src_, dv3[i_]); break; default: glds_o<3072>(src_, dv3[i_]); break; } } while (0)
  A_XDMA(a.Ax, 0);
  A_XDMA(a.Ax + 32 * 640, 1);
  unsigned wo_[3];
#pragma unroll
  for (int t = 0; t < 3; ++t) { const int v = 4 * t + lq - 2 * (j & 1); wo_[t] = (unsigned)(LR + l15 * RS + wave * 96 + (v < 0 ? 40 + (v + 2) * 48 : (v >= 10 ? 40 + (v - 10) * 48 : (v >= 5 ? 8 : 0) + v * 8))); }
  const __amdgpu_buffer_rsrc_t rs_oa = mkrs(ws + O_OA);
#pragma unroll 1
  for (int b = 0; b <= 64; ++b) {
    const int s = b; (void)s;
    if (b == 0) VMW(0); else if (wave < 3) VMW(14);
    BAR();
    const char* xsrc = (const char*)(a.Ax + (size_t)(32 * (b < 62 ? b + 2 : 63)) * 640) - 4096;
    const unsigned xb = (unsigned)(b % 3) * 40960u, xb2 = (unsigned)((b + 2) % 3) * 40960u;
    if (wave < 3) {
      if (b < 64) {
        unsigned xq[4];
#pragma unroll
        for (int q = 0; q < 4; ++q) { xq[q] = xr[q] + xb; PINV(xq[q]); }
#define A_XF(rt_, k_) (*(const u32x4*)(lds + xq[(k_) & 3] + (rt_) * 20480 + ((k_) >> 2) * 256))
        f32x4 acc[2][3]; u32x4 xa[3][2];
#pragma unroll
        for (int k = 0; k < 2; ++k) { xa[k][0] = A_XF(0, k); xa[k][1] = A_XF(1, k); }
#pragma unroll
        for (int k = 0; k < 20; ++k) {
          if (k + 2 < 20) { xa[(k + 2) % 3][0] = A_XF(0, k + 2); xa[(k + 2) % 3][1] = A_XF(1, k + 2); }
#pragma unroll
          for (int t = 0; t < 3; ++t) { acc[0][t] = mfma(wt[t][k], xa[k % 3][0], k == 0 ? ZERO4 : acc[0][t]); acc[1][t] = mfma(wt[t][k], xa[k % 3][1], k == 0 ? ZERO4 : acc[1][t]);
            if (t == 1 && k < 14) { asm volatile("" : "+a"(acc[0][0]), "+a"(acc[0][1]), "+a"(acc[1][0]), "+a"(acc[1][1])); A_XD3(xsrc, xb2, k < 14 ? k : 0); SB(); } }
          if (k >= 14) IL(6, 1);
          asm volatile("" : "+a"(acc[0][0]), "+a"(acc[0][1]), "+a"(acc[0][2]), "+a"(acc[1][0]), "+a"(acc[1][1]), "+a"(acc[1][2])); SB();
        }
        const unsigned ro = (unsigned)(((32 * b) & (NR - 1)) * RS);
#pragma unroll
        for (int t = 0; t < 3; ++t)
#pragma unroll
          for (int rt = 0; rt < 2; ++rt) *(u32x2*)(lds + wo_[t] + ro + rt * 16 * RS) = (u32x2){cvtpk(acc[rt][t][0], acc[rt][t][1]), cvtpk(acc[rt][t][2], acc[rt][t][3])};
      }
    } else {
      const int row = 32 * (b > 0 ? b - 1 : 0) + (lane & 31), hh = lane >> 5;
      const unsigned ho = (unsigned)(LR + 48 * hh);
      unsigned ra[8];
#pragma unroll
      for (int w = 0; w < 8; ++w) { const int p = row - 2 * w; ra[w] = ho + (unsigned)(((p > 0 ? p : 0) & (NR - 1)) * RS); }
#define A_LD10(d_, ad_) do { const u32x4 x0 = *(const u32x4*)(lds + (ad_)), x1 = *(const u32x4*)(lds + (ad_) + 16); const u32x2 x2 = *(const u32x2*)(lds + (ad_) + 32); \
        d_[0] = x0.x; d_[1] = x0.y; d_[2] = x0.z; d_[3] = x0.w; d_[4] = x1.x; d_[5] = x1.y; d_[6] = x1.z; d_[7] = x1.w; d_[8] = x2.x; d_[9] = x2.y; } while (0)
      unsigned q[10], kv[8][10], vv[8][10];
      A_LD10(q, ra[0]);
#pragma unroll
      for (int w = 0; w < 8; ++w) A_LD10(kv[w], ra[w] + 96);
#pragma unroll
      for (int w = 0; w < 8; ++w) A_LD10(vv[w], ra[w] + 192);
      SB();
      float lg[8], mx = -3.0e38f;
#pragma unroll
      for (int w = 0; w < 8; ++w) lg[w] = 0.f;
#pragma unroll
      for (int i = 0; i < 10; ++i)
#pragma unroll
        for (int w = 0; w < 8; ++w) lg[w] = dot2bf(q[i], kv[w][i], lg[w]);
#pragma unroll
      for (int w = 0; w < 8; ++w) { lg[w] = row - 2 * w >= 0 ? rbf(rbf(lg[w]) * 0.22360679774997896f) : rbf(-1000000000.0f); mx = fmaxf(mx, lg[w]); }
      float sum = 0.f;
#pragma unroll
      for (int w = 0; w < 8; ++w) { lg[w] = __expf(lg[w] - mx); sum += lg[w]; }
      const float inv = __builtin_amdgcn_rcpf(sum);
      float ov[20];
#pragma unroll
      for (int d = 0; d < 20; ++d) ov[d] = 0.f;
#pragma unroll
      for (int w = 0; w < 8; w += 2) {
        const unsigned pw = cvtpk(lg[w] * inv, lg[w + 1] * inv);
#pragma unroll
        for (int i = 0; i < 10; ++i) {
          ov[2 * i] = dot2bf(__builtin_amdgcn_perm(vv[w + 1][i], vv[w][i], 0x05040100u), pw, ov[2 * i]);
          ov[2 * i + 1] = dot2bf(__builtin_amdgcn_perm(vv[w + 1][i], vv[w][i], 0x07060302u), pw, ov[2 * i + 1]);
        }
      }
      const unsigned oo = b > 0 ? (unsigned)((row * 640 + 40 * j + 20 * hh) * 2) : 0x80000000u;
#pragma unroll
      for (int i = 0; i < 5; ++i) st8(rs_oa, oo + 8u * i, 0u, cvtpk(ov[4 * i], ov[4 * i + 1]), cvtpk(ov[4 * i + 2], ov[4 * i + 3]));
      if (b > 0 && (b & 15) == 0) { VMW(0); if (lane == 0) __hip_atomic_fetch_add((gu32*)(cnt + CNT(C_AO + (b >> 4) - 1)), 1u, RLX_AGENT); }
    }
  }
  VMW(0);
}
__device__ __forceinline__ void a2_role(const Args& a, unsigned char* lds, const int j, const int wave) {
  char* ws = a.ws; asm volatile("" : "+s"(ws));
  unsigned* const cnt = (unsigned*)(ws + O_CNT); unsigned* const tmo = cnt + CNT(C_TMO);
  VMW(0);
  BAR();
  {
    const int ln = lane_id(), m15 = ln & 15, mq = ln >> 4, cg = j & 3, rg = j >> 2;
    WReg W;
    {
      unsigned wv[10];
#pragma unroll
      for (int q = 0; q < 10; ++q) { const unsigned c = (unsigned)((wave + 4 * q) * 64 + ln), r = c / 20u; wv[q] = r * 1280u + (c - r * 20u) * 16u; PINV(wv[q]); }
#define A2_WDMA(i_) do { _Pragma("unroll") for (int q_ = 0; q_ < 10; ++q_) glds(a.Awo + (size_t)(128 * (i_)) * 640 + 160 * cg, wv[q_], lds_addr(lds) + (unsigned)(((i_) & 3) * 40960 + (wave + 4 * q_) * 1024)); } while (0)
      A2_WDMA(0); A2_WDMA(1); A2_WDMA(2); A2_WDMA(3);
      const unsigned rb = lds_addr(lds) + (unsigned)((8 * mq + (m15 >> 2)) * 320);
      const unsigned ad[3] = {rb + (unsigned)(64 * wave + 16 * (m15 & 3)), rb + (unsigned)(64 * wave + 16 * (m15 & 3) + 8), rb + (unsigned)(256 + 32 * (wave >> 1) + 8 * (m15 & 3))};
      SFOR(i, 5)
        if (i < 2) VMW(30); else if (i == 2) VMW(20); else if (i == 3) VMW(10); else VMW(0);
        BAR();
        SFOR(n, 6) tr4_w(ad[n >> 1] + (unsigned)((i & 3) * 40960), (n & 1) * 20480, (n & 1) * 20480 + 1280, (n & 1) * 20480 + 10240, (n & 1) * 20480 + 11520, 6 * i + n, W); SEND;
        if (i == 0) { BAR(); A2_WDMA(4); }
      SEND;
    }
    unsigned dv[10], xq[4], x5[4];
#pragma unroll
    for (int i = 0; i < 10; ++i) { const unsigned c = (unsigned)((10 * wave + i) * 64 + ln), r = c / 80u; dv[i] = (c ^ (r & 15u)) * 16u + (unsigned)(4096 - (i % 5 - 2) * 1024); PINV(dv[i]); }
    const char* src = ws + O_OA + (size_t)(128 * rg) * 1280 - 4096;
    const unsigned dm = lds_addr(lds) + (unsigned)((10 * wave + 2) * 1024);
#define A2_X(p_, lb_, i_) do { if ((i_) % 5 == 0) m0_set(dm + (lb_) + (unsigned)((i_) / 5 * 5120)); \
      switch ((i_) % 5) { case 0: glds_x<-2048>(p_, dv[i_]); break; case 1: glds_x<-1024>(p_, dv[i_]); break; case 2: glds_x<0>(p_, dv[i_]); break; case 3: glds_x<1024>(p_, dv[i_]); break; default: glds_x<2048>(p_, dv[i_]); break; } } while (0)
    const __amdgpu_buffer_rsrc_t rs_out = mkrs(a.out + (size_t)(128 * rg) * 1280 + 160 * cg);
    unsigned o2 = (unsigned)((m15 * 1280 + 32 * wave + 8 * mq) * 2), o5 = (unsigned)(((16 * (wave & 1) + m15) * 1280 + 128 + 16 * (wave >> 1) + 4 * mq) * 2); PINV(o2); PINV(o5);
    if (wave == 0) wait_ge<16>(cnt + CNT(C_AO + (rg >> 2)), 16u, tmo);
    BAR();
#pragma unroll
    for (int b = 0; b < 4; ++b) { const char* p_ = src + b * 40960; asm volatile("s_nop 4" : "+s"(p_));
#pragma unroll
      for (int i = 0; i < 10; ++i) A2_X(p_, (unsigned)(b * 40960), i); }
#pragma unroll 1
    for (int b = 0; b < 4; ++b) {
      if (b == 0) VMW(30); else if (b == 1) VMW(23); else if (b == 2) VMW(16); else VMW(9);
      BAR();
#pragma unroll
      for (int q = 0; q < 4; ++q) { xq[q] = lds_addr(lds) + (unsigned)((b & 3) * 40960 + m15 * 1280 + ((4 * q + mq) ^ m15) * 16); x5[q] = xq[q] + (unsigned)((wave & 1) * 20480); PINV(xq[q]); PINV(x5[q]); }
      f32x4 acc[5]; u32x4 xa[3][3];
#define A2_XF(n_, k_) LDS16(((n_) == 2 ? x5[(k_) & 3] : xq[(k_) & 3] + (n_) * 20480) + ((k_) >> 2) * 256)
#pragma unroll
      for (int n = 0; n < 6; ++n) xa[n / 3][n % 3] = A2_XF(n % 3, n / 3);
      SFOR(k, 20)
        SFOR(n, 5)
          mf_w<true>(acc[n], xa[k % 3][n >> 1], 12 * (k >> 2) + 4 * (n == 4 ? 2 : (n & 1)) + (k & 3), k == 0, W); SB();
          if (n < 3 && k + 2 < 20) { xa[(k + 2) % 3][n % 3] = A2_XF(n, k + 2); SB(); }
        SEND;
      SEND;
      asm volatile("s_nop 7" : "+v"(acc[0]), "+v"(acc[1]), "+v"(acc[2]), "+v"(acc[3]), "+v"(acc[4]));
#pragma unroll
      for (int rt = 0; rt < 2; ++rt) { const u32x4 w = {cvtpk(acc[2 * rt][0], acc[2 * rt][1]), cvtpk(acc[2 * rt][2], acc[2 * rt][3]), cvtpk(acc[2 * rt + 1][0], acc[2 * rt + 1][1]), cvtpk(acc[2 * rt + 1][2], acc[2 * rt + 1][3])};
        __builtin_amdgcn_raw_buffer_store_b128(w, rs_out, (int)o2, (32 * b + 16 * rt) * 2560, OAUX); asm volatile("s_nop 1" :: "v"(w) : "memory"); }
      __builtin_amdgcn_raw_buffer_store_b64((u32x2){cvtpk(acc[4][0], acc[4][1]), cvtpk(acc[4][2], acc[4][3])}, rs_out, (int)o5, 32 * b * 2560, OAUX);
    }
    VMW(0);
  }
}

#define CALN 6
__device__ __forceinline__ void calibrate(char* ws, const u32x2 tag, int* vote, const int wave) {
  {
    const int ln = lane_id();
#pragma unroll
    for (int i = 0; i < 28; ++i) st16(mkrs(ws + O_CNT), wave == 0 && i == 0 && ln < 32 ? 0x80000000u : (unsigned)(ln * 16), (unsigned)((4 * i + wave) * 1024), (u32x4){0u, 0u, 0u, 0u});
    VMW(0);
  }
  char* cal = ws + O_CAL + wave * 16384;
  unsigned best0 = ~0u, best1 = ~0u, acc = 0;
  const int lane = lane_id();
  for (int r = 0; r < CALN; ++r) {
#pragma unroll
    for (int hf = 0; hf < 2; ++hf) {
      const unsigned long long t0 = __builtin_amdgcn_s_memtime();
      if (lane == 0) acc += __hip_atomic_fetch_add((gu32*)(cal + hf * 8192 + (acc & 0u)), 1u, RLX_AGENT);
      asm volatile("s_waitcnt vmcnt(0)" ::: "memory");
      const unsigned d = (unsigned)(__builtin_amdgcn_s_memtime() - t0);
      if (hf == 0) best0 = d < best0 ? d : best0; else best1 = d < best1 ? d : best1;
    }
  }
  int dl = (int)best1 - (int)best0 + (int)(acc & 0u);
  dl = dl > 400 ? 400 : (dl < -400 ? -400 : dl);
  if (((unsigned)((size_t)cal >> 21) & 1u) != 0u) dl = -dl;
  if (lane == 0) vote[wave] = dl;
  __syncthreads();
  if (wave == 0 && lane == 0) send_tag((unsigned*)(ws + O_CNT), C_POL, tag, vote[0] + vote[1] + vote[2] + vote[3] < 0 ? 1u : 0u);
  __syncthreads();
}

#define SEP 0
#define XOFF 0
__global__ __launch_bounds__(256, 1) void k_mega(Args a) {
  extern __shared__ __attribute__((aligned(16))) unsigned char shm[];
  unsigned* const cnt = (unsigned*)(a.ws + O_CNT);
  const int wave = __builtin_amdgcn_readfirstlane(threadIdx.x >> 6);
  if (wave != 0) {
    u64 pc; unsigned off, len;
    asm volatile("s_getpc_b64 %0\n.Lcpf_mega:\n s_mov_b32 %1, .Lcpf_mega-_Z6k_mega4Args\n s_mov_b32 %2, _Z5k_endv-_Z6k_mega4Args" : "=s"(pc), "=s"(off), "=s"(len));
    u32x4 rc = mkdesc((const void*)(pc - off)), rw = mkdesc(a.ws + O_X1); rc.z = len; rw.z = (unsigned)(O_END - O_X1);
    asm volatile("s_nop 4\n buffer_load_dword a255, %0, %1, 0 offen\n buffer_load_dword a254, %2, %3, 0 offen"
                 :: "v"(((blockIdx.x >> 3) * 192u + threadIdx.x - 64u) * 64u), "s"(rc), "v"((threadIdx.x - 64u) * (unsigned)(2 * MB) + (blockIdx.x >> 3) * 2048u), "s"(rw) : "a254", "a255", "memory");
  }
  volatile LAS unsigned* sl = (volatile LAS unsigned*)shm;
  if (wave == 0) {
    const unsigned e = ld_word(cnt + CNT(C_EPOCH));
    const u32x2 tg = tag_of(e);
    unsigned r = (unsigned)__builtin_amdgcn_s_getreg((3 << 11) | 20) & 3u;
    if (blockIdx.x == 0) send_tag(cnt, C_ROT, tg, r); else r = wait_tag<1>(cnt, C_ROT, tg);
    sl[0] = r; sl[1] = e;
  }
  BAR();
  const unsigned epoch = (unsigned)__builtin_amdgcn_readfirstlane(sl[1]);
  const int slot0 = (int)uni(((blockIdx.x + sl[0]) & 7u) * 32u + (blockIdx.x >> 3));
  const int slot = slot0;
  const u32x2 tag = tag_of(epoch);
  BAR();
  const int xq = (slot >> 5) & 3, kq = slot & 31;
#define ENTER LG0()
  const int h = xq * 8 + (kq & 7);
  if (slot < 128) {
    if (kq < 8) { ENTER; s_role(a, tag, shm, h, wave);
    } else if (kq < 16) { ENTER; z_role(a, tag, shm, h, wave); }
    else if (kq < 26) { ENTER; r_role(a, tag, shm, xq * 10 + kq - 16, wave); }
    else { if (slot == 26) calibrate(a.ws, tag, (int*)shm, wave); ENTER; if (!(XOFF & 1)) o_role(a, tag, shm, 6 * xq + kq - 26, wave); }
  } else {
    if (kq < 16) { ENTER; t_role(a, tag, shm, h, kq >> 3, wave); ENTER; if (!(XOFF & 4)) a2_role(a, shm, (int)uni((unsigned)(16 * xq + kq)), wave);
      __builtin_amdgcn_raw_buffer_store_b32(epoch + 1u, mkrs(cnt + CNT(C_EPOCH)), slot == 239 && wave == 0 && lane_id() == 0 ? 0 : (int)0x80000000u, 0, 16); }
    else if (kq < 24) { ENTER; w_role(a, tag, shm, h, wave); }
    else if (kq < 28) { ENTER; if (!(XOFF & 4)) a_role(a, tag, shm, xq * 4 + kq - 24, wave); }
    else { ENTER; if (!(XOFF & 2)) att_role(a, tag, shm, xq * 8 + (kq - 28) * 2 + (wave & 1), wave); }
  }
}

__global__ void k_end() {}
extern "C" void kernel_launch(void* const* d_in, const int* in_sizes, int n_in, void* d_out, int out_size, void* d_ws, size_t ws_size, hipStream_t stream) {
  Args a;
  a.Ax = (const u16*)d_in[0]; a.Awqkv = (const u16*)d_in[1]; a.Awo = (const u16*)d_in[2]; a.Bx = (const u16*)d_in[3]; a.mem0 = (const u16*)d_in[4];
  a.Wq = (const u16*)d_in[5]; a.Wk = (const u16*)d_in[6]; a.Wv = (const u16*)d_in[7]; a.Wo = (const u16*)d_in[8];
  a.out = (u16*)d_out;
  a.ws = (char*)(((size_t)d_ws + 2 * MB - 1) & ~(size_t)(2 * MB - 1));
  if ((size_t)(a.ws - (char*)d_ws) + O_END > ws_size) return;
  const int dd[16] = {DDV};
  for (int i = 0; i < 16; ++i) a.d[i] = dd[i];
  static_assert(CNT(C_TOTAL) * 4 == 28 * 4096, "");
  (void)hipFuncSetAttribute((const void*)k_mega, hipFuncAttributeMaxDynamicSharedMemorySize, 163840);
  k_mega<<<256, 256, 163840, stream>>>(a);
}
```
